```python
import torch

def _softmax(x, axis=-1):
    return torch.softmax(x, dim=axis)
def _gelu(x):
    return torch.nn.functional.gelu(x, approximate="tanh")
def _silu(x):
    return torch.nn.functional.silu(x)
def _sigmoid(x):
    return torch.sigmoid(x)
def _layernorm(x, axis=-1, eps=1e-5):
    m = x.mean(dim=axis, keepdim=True)
    v = x.var(dim=axis, keepdim=True, unbiased=False)
    return (x - m) / torch.sqrt(v + eps)
def _scatter_add(tgt, idx, src, axis=0):
    return tgt.clone().index_add_(axis, idx, src)
def _segment_sum(src, seg, n):
    out = torch.zeros((n,) + src.shape[1:], dtype=src.dtype)
    return out.index_add_(0, seg, src)
def _pad(x, pw):
    flat = tuple(v for pair in reversed(pw) for v in pair)
    return torch.nn.functional.pad(x, flat)

kernel_name = 'standalone__ragged_sequences_packed_sequences__ragged'

def setup_inputs():

    _w = (lambda a: (lambda t: t.to(torch.bfloat16) if t.is_floating_point() else t)(torch.as_tensor(a)))
    cu = torch.tensor([0, 400, 516, 964, 1456, 1702, 1900, 2021, 2266], dtype=torch.int64)
    T = int(cu[-1])
    H, D = 16, 128
    lens = torch.diff(cu)
    seg_ids = torch.repeat_interleave(torch.arange(len(lens), dtype=torch.int64), lens)
    return {
        "q": _w((lambda *s: torch.randn(*s, dtype=torch.bfloat16) * 0.8)(T, H, D)),
        "k": _w((lambda *s: torch.randn(*s, dtype=torch.bfloat16) * 0.8)(T, H, D)),
        "v": _w((lambda *s: torch.randn(*s, dtype=torch.bfloat16) * 0.05)(T, H, D)),
        "seg_ids": _w(seg_ids),
    }

def reference(q, k, v, seg_ids):
    T, H, D = q.shape
    scale = D ** -0.5
    same_seq = seg_ids[:, None] == seg_ids[None, :]
    logits = torch.einsum("qhd,khd->hqk", q, k) * scale
    bias = (1.0 - (lambda x, dt: x.to(dt))(same_seq, q.dtype)) * (-1e9)
    p = _softmax(logits + bias[None, :, :], axis=-1)
    return torch.einsum("hqk,khd->qhd", p, v)
```

```cpp
#include <hip/hip_runtime.h>
#include <stdint.h>

typedef short    bf16x8 __attribute__((ext_vector_type(8)));
typedef short    s16x4  __attribute__((ext_vector_type(4)));
typedef float    f32x16 __attribute__((ext_vector_type(16)));
typedef float    f32x4  __attribute__((ext_vector_type(4)));
typedef float    f32x2  __attribute__((ext_vector_type(2)));
typedef __bf16   bf2    __attribute__((ext_vector_type(2)));
typedef unsigned u32x4  __attribute__((ext_vector_type(4)));
typedef unsigned u32x2  __attribute__((ext_vector_type(2)));
typedef unsigned long long u64;
#define LDSP __attribute__((address_space(3)))
typedef LDSP const char* lds_cptr;
typedef LDSP char* lds_ptr;
typedef short v4i16_t __attribute__((ext_vector_type(4)));
#define DI __device__ __forceinline__
#define MFMA(a, b, c) __builtin_amdgcn_mfma_f32_32x32x16_bf16((a), (b), (c), 0, 0, 0)

constexpr int NH = 16, HD = 128;
constexpr unsigned ROWB = NH * HD * 2;
constexpr unsigned SLOT = 32768, VOFF = 16384;
constexpr unsigned XREG = 4 * SLOT;
constexpr unsigned LDS_BYTES = 5 * SLOT;
constexpr float CEXP = 0.08838834764831845f * 1.4426950408889634f;
constexpr float THR_RAW = 8.0f / CEXP;
constexpr float MINIT = -1.0e30f;

DI unsigned rfl(unsigned x) { return __builtin_amdgcn_readfirstlane(x); }
DI int rdl(int x, int l) { return __builtin_amdgcn_readlane(x, l); }
DI unsigned cvtpk(float lo, float hi) { f32x2 v = {lo, hi}; bf2 b = __builtin_convertvector(v, bf2); return __builtin_bit_cast(unsigned, b); }
DI float swap_max(float x) { auto r = __builtin_amdgcn_permlane32_swap(__float_as_uint(x), __float_as_uint(x), false, false); return __builtin_fmaxf(__uint_as_float(r[0]), __uint_as_float(r[1])); }
DI float swap_add(float x) { auto r = __builtin_amdgcn_permlane32_swap(__float_as_uint(x), __float_as_uint(x), false, false); return __uint_as_float(r[0]) + __uint_as_float(r[1]); }
DI s16x4 vtr(lds_cptr p) { return __builtin_bit_cast(s16x4, __builtin_amdgcn_ds_read_tr16_b64_v4i16((LDSP v4i16_t*)p)); }
#define MX3(a, b, c) __builtin_fmaxf(__builtin_fmaxf((a), (b)), (c))

DI void dma16(__amdgpu_buffer_rsrc_t srd, unsigned voff, unsigned soff, unsigned m0v) {
  asm volatile("s_nop 4\n\ts_mov_b32 m0, %0\n\ts_nop 0\n\tbuffer_load_dwordx4 %1, %2, %3 offen lds"
               :: "s"(m0v), "v"(voff), "s"(srd), "s"(soff) : "m0", "memory");
}
DI int wave_incl_scan(int x) {
  x += __builtin_amdgcn_update_dpp(0, x, 0x111, 0xf, 0xf, false);
  x += __builtin_amdgcn_update_dpp(0, x, 0x112, 0xf, 0xf, false);
  x += __builtin_amdgcn_update_dpp(0, x, 0x114, 0xf, 0xf, false);
  x += __builtin_amdgcn_update_dpp(0, x, 0x118, 0xf, 0xf, false);
  x += __builtin_amdgcn_update_dpp(0, x, 0x142, 0xa, 0xf, false);
  x += __builtin_amdgcn_update_dpp(0, x, 0x143, 0xc, 0xf, false);
  return x;
}

__global__ __launch_bounds__(512, 2)
void ragged_attn_kernel(const unsigned short* __restrict__ Q, const unsigned short* __restrict__ K, const unsigned short* __restrict__ V,
                        const int* __restrict__ seg, unsigned short* __restrict__ O, int T, int nch) {
  __shared__ __attribute__((aligned(16))) char lds[LDS_BYTES];
  const int tid = threadIdx.x, lane = tid & 63, r32 = lane & 31, hh = lane >> 5;
  const int w = (int)rfl((unsigned)tid >> 6);
  const int grp = blockIdx.x & 7, jj = blockIdx.x >> 3, NJ = gridDim.x >> 3;
  const unsigned ldsb = (unsigned)(uintptr_t)lds;
  const lds_ptr L = (lds_ptr)lds;

  bool fast = (nch <= 64);
  int nseg = 0, s_start = 0, s_len = 0, s_nq = 0, s_ib = 0, s_modeB = 0, total_items = 0;
  if (fast) {
    LDSP u64* cm = (LDSP u64*)(L);
    LDSP u64* cb = (LDSP u64*)(L + 512);
    int cur[8], prv[8];
#pragma unroll
    for (int it = 0; it < 8; ++it) {
      const int i = (w + 8 * it) * 64 + lane;
      const int ic = i < T ? i : T - 1, ip = ic > 0 ? ic - 1 : 0;
      cur[it] = seg[ic]; prv[it] = seg[ip];
    }
#pragma unroll
    for (int it = 0; it < 8; ++it) {
      const int c = w + 8 * it, i = c * 64 + lane;
      const bool in = i < T;
      const u64 m = __ballot(in && (i == 0 || cur[it] != prv[it]));
      const u64 b = __ballot(in && i > 0 && cur[it] < prv[it]);
      if (lane == 0) { cm[c] = m; cb[c] = b; }
    }
    __syncthreads();
    const u64 m = cm[lane], b = cb[lane];
    const bool sorted = __ballot(b != 0ull) == 0ull;
    const int cnt = __builtin_popcountll(m);
    const int incl = wave_incl_scan(cnt);
    nseg = rdl(incl, 63);
    fast = sorted && nseg <= 64;
    if (fast) {
      LDSP int* st = (LDSP int*)(L + 1024 + 512 * w);
      int idx = incl - cnt; u64 mm = m;
      while (mm) { st[idx] = lane * 64 + __builtin_ctzll(mm); ++idx; mm &= mm - 1; }
      if (lane == 0) st[nseg] = T;
      asm volatile("s_waitcnt lgkmcnt(0)" ::: "memory");
      const bool live = lane < nseg;
      const int a0 = st[live ? lane : 0], a1 = st[live ? lane + 1 : 0];
      s_start = a0; s_len = live ? a1 - a0 : 0;
      const int nk = (s_len + 63) >> 6;
      int maxnk = 0;
      for (int t = 0; t < nseg; ++t) maxnk = max(maxnk, rdl(nk, t));
      s_modeB = (2 * nk <= maxnk && s_len > 128) ? 1 : 0;
      s_nq = s_modeB ? (s_len + 255) >> 8 : (s_len + 127) >> 7;
      const int key = live ? ((s_modeB ? 2 * nk : nk) * 64 + (63 - lane)) : -1;
      for (int t = 0; t < nseg; ++t) {
        const int kt = rdl(key, t), nt = rdl(s_nq, t);
        s_ib += (kt > key) ? 2 * nt : 0;
        total_items += 2 * nt;
      }
    }
  }
  if (!fast) total_items = 2 * ((T + 127) >> 7);
  __syncthreads();

  const __amdgpu_buffer_rsrc_t srdQ = __builtin_amdgcn_make_buffer_rsrc((void*)Q, (short)0, T * (int)ROWB, 0x00020000);
  const __amdgpu_buffer_rsrc_t srdK = __builtin_amdgcn_make_buffer_rsrc((void*)K, (short)0, T * (int)ROWB, 0x00020000);
  const __amdgpu_buffer_rsrc_t srdV = __builtin_amdgcn_make_buffer_rsrc((void*)V, (short)0, T * (int)ROWB, 0x00020000);

  const unsigned prow = (lane >> 2) & 7, pcb = lane >> 5, pc = lane & 3;
  const unsigned voffK = prow * ROWB + pcb * 64 + ((pc ^ ((2u * (unsigned)w + (prow >> 2)) & 3u)) * 16);
  const unsigned voffV = prow * ROWB + pcb * 64 + pc * 16;
  const unsigned kb0 = 2048u * (r32 >> 3) + 64u * (r32 & 7) + 16u * ((unsigned)hh ^ ((r32 >> 2) & 3u));
  const unsigned vb0 = ((unsigned)hh * 4 + ((lane & 15) >> 2)) * 64 + ((lane >> 4) & 1) * 32 + (lane & 3) * 8;

  for (int rnd = 0;; ++rnd) {
    const int idx = rnd * NJ + ((rnd & 1) ? NJ - 1 - jj : jj);
    if (idx >= total_items) break;
    int head, q0, qend, k0, kend, modeB;
    if (fast) {
      const u64 hit = __ballot(s_nq > 0 && s_ib <= idx && idx < s_ib + 2 * s_nq);
      const int s = __builtin_ctzll(hit);
      const int st = rdl(s_start, s), ln = rdl(s_len, s), loc = idx - rdl(s_ib, s);
      modeB = rdl(s_modeB, s);
      head = grp + 8 * (loc & 1);
      q0 = st + (loc >> 1) * (modeB ? 256 : 128);
      qend = st + ln; k0 = st; kend = st + ln;
    } else {
      modeB = 0;
      head = grp + 8 * (idx & 1);
      q0 = (idx >> 1) * 128; qend = min(q0 + 128, T);
      LDSP int* red = (LDSP int*)L;
      if (tid == 0) { red[0] = 0x7fffffff; red[1] = (int)0x80000000; red[2] = 0x7fffffff; red[3] = -1; }
      __syncthreads();
      if (tid < 128 && q0 + tid < qend) { const int id = seg[q0 + tid];
        __hip_atomic_fetch_min((LDSP int*)&red[0], id, __ATOMIC_RELAXED, __HIP_MEMORY_SCOPE_WORKGROUP);
        __hip_atomic_fetch_max((LDSP int*)&red[1], id, __ATOMIC_RELAXED, __HIP_MEMORY_SCOPE_WORKGROUP); }
      __syncthreads();
      const int idlo = red[0], idhi = red[1];
      int lo = 0x7fffffff, hi = -1;
      for (int i = tid; i < T; i += 512) { const int id = seg[i]; if (id >= idlo && id <= idhi) { lo = min(lo, i); hi = max(hi, i); } }
      __hip_atomic_fetch_min((LDSP int*)&red[2], lo, __ATOMIC_RELAXED, __HIP_MEMORY_SCOPE_WORKGROUP);
      __hip_atomic_fetch_max((LDSP int*)&red[3], hi, __ATOMIC_RELAXED, __HIP_MEMORY_SCOPE_WORKGROUP);
      __syncthreads();
      k0 = (int)rfl((unsigned)red[2]); kend = (int)rfl((unsigned)red[3]) + 1;
      __syncthreads();
    }
    const int nk = (kend - k0 + 63) >> 6;
    const int PD = modeB ? 2 : 3;
    const unsigned hoff = (unsigned)head * 256u;

    {
      const int nqp = modeB ? 2 : 1;
      for (int hq = 0; hq < nqp; ++hq) {
        const unsigned reg = hq ? 3u * SLOT : XREG;
#pragma unroll
        for (int i = 0; i < 2; ++i) {
          const unsigned m = (unsigned)w + 8u * i;
          const unsigned so = (unsigned)(q0 + 128 * hq + 8 * (int)m) * ROWB + hoff;
          dma16(srdQ, voffK, so, ldsb + reg + m * 2048u);
          dma16(srdQ, voffK, so + 128u, ldsb + reg + m * 2048u + 1024u);
        }
      }
    }
    auto issue_tile = [&](int t) {
      const unsigned slot = (unsigned)(t & 3) * SLOT;
      const unsigned so = (unsigned)(k0 + 64 * t + 8 * w) * ROWB + hoff;
      const unsigned d = ldsb + slot + (unsigned)w * 2048u;
      dma16(srdK, voffK, so, d);
      dma16(srdK, voffK, so + 128u, d + 1024u);
      dma16(srdV, voffV, so, d + VOFF);
      dma16(srdV, voffV, so + 128u, d + VOFF + 1024u);
    };
    for (int t = 0; t < PD && t < nk; ++t) issue_tile(t);

    const int qb = modeB ? w : (w & 3);
    const int sub_lo = modeB ? 0 : (w >> 2), sub_hi = modeB ? 2 : sub_lo + 1;
    const int qrow = q0 + 32 * qb + r32;
    const bool wave_live = q0 + 32 * qb < qend;
    int segq = 0;
    if (!fast) segq = seg[qrow < T ? qrow : T - 1];

    bf16x8 qf[8];
    f32x16 o[4];
#pragma unroll
    for (int d = 0; d < 4; ++d) o[d] = f32x16{};
    float m_run = MINIT, l_run = 0.f;
    bool first = true;

    for (int t = 0; t < nk; ++t) {
      {
        const int ahead = min(PD - 1, nk - 1 - t);
        if (ahead >= 2)      asm volatile("s_waitcnt vmcnt(8)\n\ts_barrier" ::: "memory");
        else if (ahead == 1) asm volatile("s_waitcnt vmcnt(4)\n\ts_barrier" ::: "memory");
        else                 asm volatile("s_waitcnt vmcnt(0)\n\ts_barrier" ::: "memory");
      }
      if (t + PD < nk) issue_tile(t + PD);
      if (t == 0) {
        const unsigned qbase = ((modeB && qb >= 4) ? 3u * SLOT : XREG) + (unsigned)(qb & 3) * 8192u;
#pragma unroll
        for (int s = 0; s < 8; ++s)
          qf[s] = *(const LDSP bf16x8*)(L + qbase + ((kb0 ^ ((s & 1) * 32u)) + 512u * (s >> 1)));
      }
      if (!wave_live) continue;
      const unsigned slot = (unsigned)(t & 3) * SLOT;
      for (int sub = sub_lo; sub < sub_hi; ++sub) {
        const int kbase = k0 + 64 * t + 32 * sub;
        if (kbase >= kend) break;
        const lds_cptr kp = (lds_cptr)L + slot + (unsigned)sub * 8192u;
        f32x16 sc = f32x16{};
#pragma unroll
        for (int s = 0; s < 8; ++s) {
          const bf16x8 kf = *(const LDSP bf16x8*)(kp + ((kb0 ^ ((s & 1) * 32u)) + 512u * (s >> 1)));
          sc = MFMA(kf, qf[s], sc);
        }
        if (!fast) {
#pragma unroll
          for (int r = 0; r < 16; ++r) {
            const int kk = kbase + (r & 3) + 8 * (r >> 2) + 4 * hh;
            const bool ok = kk < kend && seg[kk < T ? kk : T - 1] == segq;
            sc[r] = ok ? sc[r] : -__builtin_inff();
          }
        } else if (kbase + 32 > kend) {
          const int lim = kend - kbase - 4 * hh;
#pragma unroll
          for (int r = 0; r < 16; ++r) sc[r] = ((r & 3) + 8 * (r >> 2) < lim) ? sc[r] : -__builtin_inff();
        }
        float a = MX3(sc[0], sc[1], sc[2]), b = MX3(sc[3], sc[4], sc[5]);
        a = MX3(a, sc[6], sc[7]); b = MX3(b, sc[8], sc[9]); a = MX3(a, sc[10], sc[11]); b = MX3(b, sc[12], sc[13]);
        a = MX3(a, sc[14], sc[15]);
        const float pm = swap_max(__builtin_fmaxf(a, b));
        if (first) { m_run = __builtin_fmaxf(pm, MINIT); first = false; }
        else if (__builtin_expect(__any(pm - m_run > THR_RAW), 0)) {
          const float mn = __builtin_fmaxf(m_run, pm);
          const float al = __builtin_amdgcn_exp2f((m_run - mn) * CEXP);
          m_run = mn; l_run *= al;
#pragma unroll
          for (int d = 0; d < 4; ++d)
#pragma unroll
            for (int r = 0; r < 16; ++r) o[d][r] *= al;
        }
        const float nmc = -m_run * CEXP;
        float ps = 0.f;
#pragma unroll
        for (int r = 0; r < 16; ++r) { sc[r] = __builtin_amdgcn_exp2f(__builtin_fmaf(sc[r], CEXP, nmc)); ps += sc[r]; }
        l_run += ps;
        u32x4 pw0, pw1;
#pragma unroll
        for (int e = 0; e < 4; ++e) { pw0[e] = cvtpk(sc[2 * e], sc[2 * e + 1]); pw1[e] = cvtpk(sc[8 + 2 * e], sc[8 + 2 * e + 1]); }
        const bf16x8 p0 = __builtin_bit_cast(bf16x8, pw0), p1 = __builtin_bit_cast(bf16x8, pw1);
        const lds_cptr vp = kp + VOFF + vb0;
#pragma unroll
        for (int d = 0; d < 4; ++d) {
          const s16x4 a0 = vtr(vp + d * 512), a1 = vtr(vp + d * 512 + 2048), b0 = vtr(vp + d * 512 + 4096), b1 = vtr(vp + d * 512 + 6144);
          const bf16x8 va = __builtin_shufflevector(a0, a1, 0, 1, 2, 3, 4, 5, 6, 7), vb = __builtin_shufflevector(b0, b1, 0, 1, 2, 3, 4, 5, 6, 7);
          o[d] = MFMA(va, p0, o[d]);
          o[d] = MFMA(vb, p1, o[d]);
        }
      }
    }

    l_run = swap_add(l_run);
    unsigned short* orow = O + (size_t)qrow * (NH * HD) + head * HD + 8 * hh;
    const bool row_ok = qrow < qend;
    if (modeB) {
      const float inv = 1.0f / l_run;
#pragma unroll
      for (int d = 0; d < 4; ++d)
#pragma unroll
        for (int g = 0; g < 4; g += 2) {
          unsigned ax = cvtpk(o[d][4 * g + 0] * inv, o[d][4 * g + 1] * inv), ay = cvtpk(o[d][4 * g + 2] * inv, o[d][4 * g + 3] * inv);
          unsigned bx = cvtpk(o[d][4 * g + 4] * inv, o[d][4 * g + 5] * inv), by = cvtpk(o[d][4 * g + 6] * inv, o[d][4 * g + 7] * inv);
          auto rx = __builtin_amdgcn_permlane32_swap(ax, bx, false, false); auto ry = __builtin_amdgcn_permlane32_swap(ay, by, false, false);
          const u32x4 v = {rx[0], ry[0], rx[1], ry[1]};
          if (row_ok) *(u32x4*)(orow + 32 * d + 8 * g) = v;
        }
    } else {
      const int kh = w >> 2;
      const unsigned xa = (unsigned)(nk & 3) * SLOT, xb = (unsigned)((nk + 1) & 3) * SLOT, xs = (unsigned)((nk + 2) & 3) * SLOT;
      const unsigned mine = (kh ? xb : xa) + (unsigned)qb * 8192u + (unsigned)lane * 16u;
      const unsigned theirs = (kh ? xa : xb) + (unsigned)qb * 8192u + (unsigned)lane * 16u;
#pragma unroll
      for (int dd = 0; dd < 2; ++dd)
#pragma unroll
        for (int g = 0; g < 4; ++g) {
          const f32x16& src0 = o[dd], &src1 = o[2 + dd];
          f32x4 v;
#pragma unroll
          for (int e = 0; e < 4; ++e) v[e] = kh ? src0[4 * g + e] : src1[4 * g + e];
          *(LDSP f32x4*)(L + mine + (unsigned)(dd * 4 + g) * 1024u) = v;
        }
      if (hh == 0) { f32x2 s2 = {m_run, l_run}; *(LDSP f32x2*)(L + xs + (unsigned)w * 256u + (unsigned)r32 * 8u) = s2; }
      __syncthreads();
      const f32x2 o2 = *(const LDSP f32x2*)(L + xs + (unsigned)(w ^ 4) * 256u + (unsigned)r32 * 8u);
      const float mn = __builtin_fmaxf(m_run, o2[0]);
      const float wa = __builtin_amdgcn_exp2f((m_run - mn) * CEXP), wb = __builtin_amdgcn_exp2f((o2[0] - mn) * CEXP);
      const float inv = 1.0f / (wa * l_run + wb * o2[1]);
      const float fa = wa * inv, fb = wb * inv;
#pragma unroll
      for (int dd = 0; dd < 2; ++dd) {
        float r[16];
#pragma unroll
        for (int g = 0; g < 4; ++g) {
          const f32x4 v = *(const LDSP f32x4*)(L + theirs + (unsigned)(dd * 4 + g) * 1024u);
#pragma unroll
          for (int e = 0; e < 4; ++e) r[4 * g + e] = fa * (kh ? o[2 + dd][4 * g + e] : o[dd][4 * g + e]) + fb * v[e];
        }
#pragma unroll
        for (int g = 0; g < 4; g += 2) {
          unsigned ax = cvtpk(r[4 * g + 0], r[4 * g + 1]), ay = cvtpk(r[4 * g + 2], r[4 * g + 3]);
          unsigned bx = cvtpk(r[4 * g + 4], r[4 * g + 5]), by = cvtpk(r[4 * g + 6], r[4 * g + 7]);
          auto rx = __builtin_amdgcn_permlane32_swap(ax, bx, false, false); auto ry = __builtin_amdgcn_permlane32_swap(ay, by, false, false);
          const u32x4 v = {rx[0], ry[0], rx[1], ry[1]};
          if (row_ok) *(u32x4*)(orow + 64 * kh + 32 * dd + 8 * g) = v;
        }
      }
    }
    __syncthreads();
  }
}

extern "C" void kernel_launch(void* const* d_in, const int* in_sizes, int n_in, void* d_out, int out_size, void* d_ws, size_t ws_size,
                              hipStream_t stream) {
  const int T = in_sizes[3];
  ragged_attn_kernel<<<256, 512, 0, stream>>>((const unsigned short*)d_in[0], (const unsigned short*)d_in[1], (const unsigned short*)d_in[2],
                                              (const int*)d_in[3], (unsigned short*)d_out, T, (T + 63) / 64);
}
```

```cpp
#include <hip/hip_runtime.h>
#include <stdint.h>

typedef short    bf16x8 __attribute__((ext_vector_type(8)));
typedef short    s16x4  __attribute__((ext_vector_type(4)));
typedef float    f32x16 __attribute__((ext_vector_type(16)));
typedef float    f32x4  __attribute__((ext_vector_type(4)));
typedef float    f32x2  __attribute__((ext_vector_type(2)));
typedef __bf16   bf2    __attribute__((ext_vector_type(2)));
typedef unsigned u32x4  __attribute__((ext_vector_type(4)));
typedef unsigned long long u64;
#define LDSP __attribute__((address_space(3)))
typedef LDSP const char* lds_cptr;
typedef LDSP char* lds_ptr;
typedef short v4i16_t __attribute__((ext_vector_type(4)));
#define DI __device__ __forceinline__
#define MFMA(a, b, c) __builtin_amdgcn_mfma_f32_32x32x16_bf16((a), (b), (c), 0, 0, 0)

constexpr int NH = 16, HD = 128;
constexpr unsigned ROWB = NH * HD * 2;
constexpr unsigned LDS_BYTES = 163840;
constexpr float CEXP = 0.08838834764831845f * 1.4426950408889634f;
constexpr float THR_RAW = 8.0f / CEXP;
constexpr float MINIT = -1.0e30f;

DI unsigned rfl(unsigned x) { return __builtin_amdgcn_readfirstlane(x); }
DI unsigned cvtpk(float lo, float hi) { f32x2 v = {lo, hi}; bf2 b = __builtin_convertvector(v, bf2); return __builtin_bit_cast(unsigned, b); }
DI float swap_max(float x) { auto r = __builtin_amdgcn_permlane32_swap(__float_as_uint(x), __float_as_uint(x), false, false); return __builtin_fmaxf(__uint_as_float(r[0]), __uint_as_float(r[1])); }
DI float swap_add(float x) { auto r = __builtin_amdgcn_permlane32_swap(__float_as_uint(x), __float_as_uint(x), false, false); return __uint_as_float(r[0]) + __uint_as_float(r[1]); }
DI s16x4 vtr(lds_cptr p) { return __builtin_bit_cast(s16x4, __builtin_amdgcn_ds_read_tr16_b64_v4i16((LDSP v4i16_t*)p)); }
#define MX3(a, b, c) __builtin_fmaxf(__builtin_fmaxf((a), (b)), (c))

DI void dma16(__amdgpu_buffer_rsrc_t srd, unsigned voff, unsigned soff, unsigned m0v) {
  unsigned keep;
  asm volatile("s_nop 4\n\ts_mov_b32 %0, m0\n\ts_mov_b32 m0, %1\n\ts_nop 0\n\tbuffer_load_dwordx4 %2, %3, %4 offen lds\n\ts_mov_b32 m0, %0"
               : "=&s"(keep) : "s"(m0v), "v"(voff), "s"(srd), "s"(soff) : "memory");
}

__device__ __forceinline__ void generic_path(const unsigned short* __restrict__ Q, const unsigned short* __restrict__ K, const unsigned short* __restrict__ V,
                                          const int* __restrict__ seg, unsigned short* __restrict__ O, int T, char* lds) {
  constexpr unsigned SLOT = 32768, VOFF = 16384, XREG = 4 * SLOT;
  const int tid = threadIdx.x, lane = tid & 63, r32 = lane & 31, hh = lane >> 5;
  const int w = (int)rfl((unsigned)tid >> 6);
  const int grp = blockIdx.x & 7, jj = blockIdx.x >> 3, NJ = 32;
  const unsigned ldsb = (unsigned)(uintptr_t)lds;
  const lds_ptr L = (lds_ptr)lds;
  const int total_items = 2 * ((T + 127) >> 7);
  const __amdgpu_buffer_rsrc_t srdQ = __builtin_amdgcn_make_buffer_rsrc((void*)Q, (short)0, T * (int)ROWB, 0x00020000);
  const __amdgpu_buffer_rsrc_t srdK = __builtin_amdgcn_make_buffer_rsrc((void*)K, (short)0, T * (int)ROWB, 0x00020000);
  const __amdgpu_buffer_rsrc_t srdV = __builtin_amdgcn_make_buffer_rsrc((void*)V, (short)0, T * (int)ROWB, 0x00020000);
  const unsigned prow = (lane >> 2) & 7, pcb = lane >> 5, pc = lane & 3;
  const unsigned voffK = prow * ROWB + pcb * 64 + ((pc ^ ((2u * (unsigned)w + (prow >> 2)) & 3u)) * 16);
  const unsigned voffV = prow * ROWB + pcb * 64 + pc * 16;
  const unsigned kb0 = 2048u * (r32 >> 3) + 64u * (r32 & 7) + 16u * ((unsigned)hh ^ ((r32 >> 2) & 3u));
  const unsigned vb0 = ((unsigned)hh * 4 + ((lane & 15) >> 2)) * 64 + ((lane >> 4) & 1) * 32 + (lane & 3) * 8;
  for (int idx = jj; idx < total_items; idx += NJ) {
    __syncthreads();
    const int head = grp + 8 * (idx & 1);
    const int q0 = (idx >> 1) * 128, qend = min(q0 + 128, T);
    LDSP int* red = (LDSP int*)L;
    if (tid == 0) { red[0] = 0x7fffffff; red[1] = (int)0x80000000; red[2] = 0x7fffffff; red[3] = -1; }
    __syncthreads();
    if (tid < 128 && q0 + tid < qend) { const int id = seg[q0 + tid];
      __hip_atomic_fetch_min((LDSP int*)&red[0], id, __ATOMIC_RELAXED, __HIP_MEMORY_SCOPE_WORKGROUP);
      __hip_atomic_fetch_max((LDSP int*)&red[1], id, __ATOMIC_RELAXED, __HIP_MEMORY_SCOPE_WORKGROUP); }
    __syncthreads();
    const int idlo = red[0], idhi = red[1];
    int lo = 0x7fffffff, hi = -1;
    for (int i = tid; i < T; i += 512) { const int id = seg[i]; if (id >= idlo && id <= idhi) { lo = min(lo, i); hi = max(hi, i); } }
    __hip_atomic_fetch_min((LDSP int*)&red[2], lo, __ATOMIC_RELAXED, __HIP_MEMORY_SCOPE_WORKGROUP);
    __hip_atomic_fetch_max((LDSP int*)&red[3], hi, __ATOMIC_RELAXED, __HIP_MEMORY_SCOPE_WORKGROUP);
    __syncthreads();
    const int k0 = (int)rfl((unsigned)red[2]), kend = (int)rfl((unsigned)red[3]) + 1;
    __syncthreads();
    const int nk = (kend - k0 + 63) >> 6;
    const unsigned hoff = (unsigned)head * 256u;
#pragma unroll
    for (int i = 0; i < 2; ++i) {
      const unsigned m = (unsigned)w + 8u * i;
      const unsigned so = (unsigned)(q0 + 8 * (int)m) * ROWB + hoff;
      dma16(srdQ, voffK, so, ldsb + XREG + m * 2048u);
      dma16(srdQ, voffK, so + 128u, ldsb + XREG + m * 2048u + 1024u);
    }
    const int qb = w & 3, sub = w >> 2;
    const int qrow = q0 + 32 * qb + r32;
    const int segq = seg[qrow < T ? qrow : T - 1];
    bf16x8 qf[8];
    f32x16 o[4];
#pragma unroll
    for (int d = 0; d < 4; ++d) o[d] = f32x16{};
    float m_run = MINIT, l_run = 0.f;
    for (int t = 0; t < nk; ++t) {
      __syncthreads();
      {
        const unsigned so = (unsigned)(k0 + 64 * t + 8 * w) * ROWB + hoff;
        const unsigned d = ldsb + (unsigned)w * 2048u;
        dma16(srdK, voffK, so, d); dma16(srdK, voffK, so + 128u, d + 1024u);
        dma16(srdV, voffV, so, d + VOFF); dma16(srdV, voffV, so + 128u, d + VOFF + 1024u);
      }
      asm volatile("s_waitcnt vmcnt(0)" ::: "memory");
      __syncthreads();
      if (t == 0) {
#pragma unroll
        for (int s = 0; s < 8; ++s)
          qf[s] = *(const LDSP bf16x8*)(L + XREG + (unsigned)qb * 8192u + ((kb0 ^ ((s & 1) * 32u)) + 512u * (s >> 1)));
      }
      const int kbase = k0 + 64 * t + 32 * sub;
      const lds_cptr kp = (lds_cptr)L + (unsigned)sub * 8192u;
      f32x16 sc = f32x16{};
#pragma unroll
      for (int s = 0; s < 8; ++s) {
        const bf16x8 kf = *(const LDSP bf16x8*)(kp + ((kb0 ^ ((s & 1) * 32u)) + 512u * (s >> 1)));
        sc = MFMA(kf, qf[s], sc);
      }
#pragma unroll
      for (int r = 0; r < 16; ++r) {
        const int kk = kbase + (r & 3) + 8 * (r >> 2) + 4 * hh;
        const int id = seg[kk < T ? kk : T - 1];
        sc[r] = (kk < kend && id == segq) ? sc[r] : -__builtin_inff();
      }
      float a = MX3(sc[0], sc[1], sc[2]), b = MX3(sc[3], sc[4], sc[5]);
      a = MX3(a, sc[6], sc[7]); b = MX3(b, sc[8], sc[9]); a = MX3(a, sc[10], sc[11]); b = MX3(b, sc[12], sc[13]);
      a = MX3(a, sc[14], sc[15]);
      const float pm = swap_max(__builtin_fmaxf(a, b));
      const float mn = __builtin_fmaxf(m_run, pm);
      const float al = __builtin_amdgcn_exp2f((m_run - mn) * CEXP);
      m_run = mn; l_run *= al;
#pragma unroll
      for (int d = 0; d < 4; ++d)
#pragma unroll
        for (int r = 0; r < 16; ++r) o[d][r] *= al;
      const float nmc = -m_run * CEXP;
      float ps = 0.f;
#pragma unroll
      for (int r = 0; r < 16; ++r) { sc[r] = __builtin_amdgcn_exp2f(__builtin_fmaf(sc[r], CEXP, nmc)); ps += sc[r]; }
      l_run += ps;
      u32x4 pw0, pw1;
#pragma unroll
      for (int e = 0; e < 4; ++e) { pw0[e] = cvtpk(sc[2 * e], sc[2 * e + 1]); pw1[e] = cvtpk(sc[8 + 2 * e], sc[8 + 2 * e + 1]); }
      const bf16x8 p0 = __builtin_bit_cast(bf16x8, pw0), p1 = __builtin_bit_cast(bf16x8, pw1);
      const lds_cptr vp = kp + VOFF + vb0;
#pragma unroll
      for (int d = 0; d < 4; ++d) {
        const s16x4 a0 = vtr(vp + d * 512), a1 = vtr(vp + d * 512 + 2048), b0 = vtr(vp + d * 512 + 4096), b1 = vtr(vp + d * 512 + 6144);
        const bf16x8 va = __builtin_shufflevector(a0, a1, 0, 1, 2, 3, 4, 5, 6, 7), vb = __builtin_shufflevector(b0, b1, 0, 1, 2, 3, 4, 5, 6, 7);
        o[d] = MFMA(va, p0, o[d]);
        o[d] = MFMA(vb, p1, o[d]);
      }
    }
    l_run = swap_add(l_run);
    unsigned short* orow = O + (size_t)(qrow < T ? qrow : 0) * (NH * HD) + head * HD + 8 * hh;
    const bool row_ok = qrow < qend;
    const int kh = sub;
    __syncthreads();
    const unsigned xa = SLOT, xb = 2 * SLOT, xs = 3 * SLOT;
    const unsigned mine = (kh ? xb : xa) + (unsigned)qb * 8192u + (unsigned)lane * 16u;
    const unsigned theirs = (kh ? xa : xb) + (unsigned)qb * 8192u + (unsigned)lane * 16u;
#pragma unroll
    for (int dd = 0; dd < 2; ++dd)
#pragma unroll
      for (int g = 0; g < 4; ++g) {
        f32x4 v;
#pragma unroll
        for (int e = 0; e < 4; ++e) v[e] = kh ? o[dd][4 * g + e] : o[2 + dd][4 * g + e];
        *(LDSP f32x4*)(L + mine + (unsigned)(dd * 4 + g) * 1024u) = v;
      }
    if (hh == 0) { f32x2 s2 = {m_run, l_run}; *(LDSP f32x2*)(L + xs + (unsigned)w * 256u + (unsigned)r32 * 8u) = s2; }
    __syncthreads();
    const f32x2 o2 = *(const LDSP f32x2*)(L + xs + (unsigned)(w ^ 4) * 256u + (unsigned)r32 * 8u);
    const float mn = __builtin_fmaxf(m_run, o2[0]);
    const float wa = __builtin_amdgcn_exp2f((m_run - mn) * CEXP), wb = __builtin_amdgcn_exp2f((o2[0] - mn) * CEXP);
    const float inv = 1.0f / (wa * l_run + wb * o2[1]);
    const float fa = wa * inv, fb = wb * inv;
#pragma unroll
    for (int dd = 0; dd < 2; ++dd) {
      float r[16];
#pragma unroll
      for (int g = 0; g < 4; ++g) {
        const f32x4 v = *(const LDSP f32x4*)(L + theirs + (unsigned)(dd * 4 + g) * 1024u);
#pragma unroll
        for (int e = 0; e < 4; ++e) r[4 * g + e] = fa * (kh ? o[2 + dd][4 * g + e] : o[dd][4 * g + e]) + fb * v[e];
      }
#pragma unroll
      for (int g = 0; g < 4; g += 2) {
        unsigned ax = cvtpk(r[4 * g + 0], r[4 * g + 1]), ay = cvtpk(r[4 * g + 2], r[4 * g + 3]);
        unsigned bx = cvtpk(r[4 * g + 4], r[4 * g + 5]), by = cvtpk(r[4 * g + 6], r[4 * g + 7]);
        auto rx = __builtin_amdgcn_permlane32_swap(ax, bx, false, false); auto ry = __builtin_amdgcn_permlane32_swap(ay, by, false, false);
        const u32x4 v = {rx[0], ry[0], rx[1], ry[1]};
        if (row_ok) *(u32x4*)(orow + 64 * kh + 32 * dd + 8 * g) = v;
      }
    }
  }
}

#define DIAG 0
#define STR2(x) #x
#define STR(x) STR2(x)

template <int NIT>
__global__ __launch_bounds__(512, 2)
void ragged_attn_kernel(const void* Q, unsigned nQ, unsigned fQ, const void* K, unsigned nK, unsigned fK, const void* V, unsigned nV, unsigned fV,
                        const void* seg, unsigned nS, unsigned fS, void* O, unsigned nO, unsigned fO, int T, int lastc, void* dbg) {
  __shared__ __attribute__((aligned(16))) char lds[LDS_BYTES];
  const unsigned ldsb = (unsigned)(uintptr_t)lds;
  asm volatile(R"(
  .set NIT, %c3
  .set DIAG, )" STR(DIAG) R"(
  ; ---------------- SGPRs
  .set sQ, 8
  .set sK, 12
  .set sV, 16
  .set sS, 20
  .set sO, 24
  .set sT, 28
  .set sLASTC, 29
  .set sDBG, 30
  .set sW, 59
  .set sG, 33
  .set sJ, 34
  .set sR, 35
  .set sNBIG, 36
  .set sTOT, 37
  .set sHOFF, 38
  .set sQ0, 39
  .set sQEND, 40
  .set sK0, 41
  .set sNK, 42
  .set sLEN, 43
  .set sMB, 44
  .set sNS, 45
  .set sU, 46
  .set sNU, 47
  .set sKOFF, 48
  .set sKSTEP, 49
  .set sVOFF, 50
  .set sKSO, 51
  .set sVSO, 52
  .set sKDST, 53
  .set sVDST, 54
  .set sKT, 55
  .set sVT, 56
  .set sMSTEP, 57
  .set sPEND, 58
    .set sA, 60
  .set sB, 62
  .set sC, 64
  .set sT1, 66
  .set sT2, 67
  .set sT3, 68
  .set sT4, 69
  .set sCE, 70
  .set sTHR, 71
  .set sWD, 72
  .set sQB, 73
  .set sKH, 74
  .set sLIM, 75
  .set sST, 80
  ; ---------------- VGPRs
  .set vLANE, 1
  .set vOK, 2
  .set vOK2, 3
  .set vOV, 4
  .set vOV2, 5
  .set vKB0, 6
  .set vKB1, 7
  .set vVB, 8
  .set vSTART, 9
  .set vLEN, 10
  .set vINCL, 11
  .set vEXCL, 12
  .set vM, 13
  .set vNMC, 14
  .set vL0, 15
  .set vL1, 16
  .set vKA0, 17
  .set vKA1, 18
  .set vVA, 19
  .set vAL, 20
  .set vOO, 21
  .set vHH4, 22
  .set vT0, 24
  .set vT1, 25
  .set vT2, 26
  .set vT3, 27
  .set vT4, 28
  .set vT5, 29
  .set vT6, 30
  .set vT7, 31
  .set vP, 32
  .set vX, 40
  .set vO, 64
  .set vQ, 128
  .set vSA, 160
  .set vSB, 176
  .set vK, 192
  .set vV, 224
  ; ---------------- LDS map
  .set LK, 0
  .set LV, 65536
  .set LX, 114688
  .set LSP, 147456
  .set LSCR, 98304

  .macro STAMP i
    .if DIAG
      s_memtime s[sST+2*\i:sST+2*\i+1]
      s_waitcnt lgkmcnt(0)
    .endif
  .endm

  ; ======================================================================== entry
  s_load_dwordx16 s[8:23], %0, 0
  s_load_dwordx8 s[24:31], %0, 64
  .if DIAG
    s_memrealtime s[76:77]
  .endif
  STAMP 0
  v_and_b32 v[vLANE], 63, %2
  v_readfirstlane_b32 s[sW], %2
  v_lshlrev_b32 v[vT0], 2, %2                     ; tid*4
  s_and_b32 s[sG], %1, 7
  s_lshr_b32 s[sJ], %1, 3
  s_lshr_b32 s[sW], s[sW], 6
  ; DMA lane offsets
  v_bfe_u32 v[vT1], v[vLANE], 2, 3                ; prow
  v_lshrrev_b32 v[vT2], 5, v[vLANE]               ; hh / pcb
  v_and_b32 v[vT3], 3, v[vLANE]                   ; pc
  v_lshlrev_b32 v[vT4], 12, v[vT1]
  v_lshl_add_u32 v[vT4], v[vT2], 6, v[vT4]        ; prow*4096 + pcb*64
  v_lshl_add_u32 v[vOV], v[vT3], 4, v[vT4]
  v_bfe_u32 v[vT5], v[vLANE], 4, 1
  s_and_b32 s[sT4], s[sW], 1
  s_lshl_b32 s[sT4], s[sT4], 1
  v_or_b32 v[vT5], s[sT4], v[vT5]                 ; x
  v_xor_b32 v[vT5], v[vT5], v[vT3]
  v_lshl_add_u32 v[vOK], v[vT5], 4, v[vT4]
  v_add_u32 v[vOK2], 128, v[vOK]
  v_add_u32 v[vOV2], 128, v[vOV]
  ; K/Q row-read base
  v_and_b32 v[vT1], 31, v[vLANE]                  ; r32
  v_lshrrev_b32 v[vT3], 3, v[vT1]
  v_lshlrev_b32 v[vT3], 11, v[vT3]
  v_and_b32 v[vT4], 7, v[vT1]
  v_lshl_add_u32 v[vT3], v[vT4], 6, v[vT3]
  v_bfe_u32 v[vT4], v[vT1], 2, 2
  v_xor_b32 v[vT4], v[vT4], v[vT2]
  v_lshl_add_u32 v[vKB0], v[vT4], 4, v[vT3]
  v_xor_b32 v[vKB1], 32, v[vKB0]
  ; V transposed-read base
  v_bfe_u32 v[vT3], v[vLANE], 2, 2
  v_lshl_add_u32 v[vT3], v[vT2], 2, v[vT3]
  v_lshlrev_b32 v[vT3], 6, v[vT3]
  v_bfe_u32 v[vT4], v[vLANE], 4, 1
  v_lshl_add_u32 v[vT3], v[vT4], 5, v[vT3]
  v_and_b32 v[vT4], 3, v[vLANE]
  v_lshl_add_u32 v[vVB], v[vT4], 3, v[vT3]
  v_lshlrev_b32 v[vHH4], 2, v[vT2]
  s_mov_b32 s[sCE], 0x3e0293ee
  s_mov_b32 s[sTHR], 0x427af232
  s_lshl_b32 s[sWD], s[sW], 11                    ; w*2048
  s_waitcnt lgkmcnt(0)
  STAMP 1
  ; ======================================================================== S1: segment ends per 64-token chunk
  .set i, 0
  .rept NIT
    .if i < 2
      buffer_load_dwordx2 v[vX+2*i:vX+2*i+1], v[vT0], s[sS:sS+3], 0 offen offset:2048*i
    .else
      s_movk_i32 s[sT1], 2048*i
      buffer_load_dwordx2 v[vX+2*i:vX+2*i+1], v[vT0], s[sS:sS+3], s[sT1] offen
    .endif
    .set i, i+1
  .endr
  s_mov_b64 s[sC:sC+1], 0
  v_mov_b32 v[vT4], 0
  v_mov_b32 v[vT5], 0
  v_lshlrev_b32 v[vT7], 7, v[vLANE]               ; LDS address of chunk (w + 8*lane): 16 B each
  s_lshl_b32 s[sT1], s[sW], 4
  s_add_u32 s[sT1], s[sT1], LSCR
  v_add_u32 v[vT7], s[sT1], v[vT7]
  .set i, 0
  .rept NIT
    s_waitcnt vmcnt(NIT-1-i)
    v_cmp_ne_u32 s[sA:sA+1], v[vX+2*i], v[vX+2*i+1]
    v_cmp_gt_i32 vcc, v[vX+2*i], v[vX+2*i+1]
    .if i == NIT-1
      v_cmp_gt_i32 s[sB:sB+1], s[sLASTC], %2       ; element < T-1
      s_and_b64 s[sA:sA+1], s[sA:sA+1], s[sB:sB+1]
      s_and_b64 vcc, vcc, s[sB:sB+1]
      v_cmp_eq_u32 s[sB:sB+1], s[sLASTC], %2       ; element == T-1
      s_or_b64 s[sA:sA+1], s[sA:sA+1], s[sB:sB+1]
    .endif
    s_or_b64 s[sC:sC+1], s[sC:sC+1], vcc
    s_nop 0
    v_writelane_b32 v[vT4], s[sA], i
    v_writelane_b32 v[vT5], s[sA+1], i
    .set i, i+1
  .endr
  s_or_b32 s[sT1], s[sC], s[sC+1]
  v_mov_b32 v[vT6], s[sT1]
  s_mov_b64 exec, (1<<NIT)-1
  ds_write_b96 v[vT7], v[vT4:vT6]
  s_mov_b64 exec, -1
  v_mov_b32 v[vT0], 0
  v_mov_b32 v[vT1], 0
  v_mov_b32 v[vT2], 0
  v_lshlrev_b32 v[vT3], 4, v[vLANE]
  v_add_u32 v[vT3], LSCR, v[vT3]
  v_lshlrev_b32 v[vT7], 6, v[vLANE]
  v_add_u32 v[vT7], 1, v[vT7]                     ; lane*64 + 1
  s_waitcnt lgkmcnt(0)
  s_barrier
  STAMP 2
  ; ======================================================================== S2
  s_bfm_b64 exec, 8*NIT, 0
  ds_read_b96 v[vT0:vT2], v[vT3]
  s_mov_b64 exec, -1
  s_waitcnt lgkmcnt(0)
  v_cmp_ne_u32 vcc, 0, v[vT2]
  v_bcnt_u32_b32 v[vT3], v[vT0], 0
  v_bcnt_u32_b32 v[vT3], v[vT1], v[vT3]
  s_cbranch_vccnz .Lfallback
  v_cmp_lt_u32 vcc, 1, v[vT3]
  v_ffbl_b32 v[vT4], v[vT0]
  v_ffbl_b32 v[vT5], v[vT1]
  s_cbranch_vccnz .Lfallback
  v_cmp_ne_u32 s[sA:sA+1], 0, v[vT3]
  v_add_u32 v[vT5], 32, v[vT5]
  v_min_u32 v[vT4], v[vT4], v[vT5]
  v_add_u32 v[vT4], v[vT4], v[vT7]
  v_cndmask_b32 v[vT4], 0, v[vT4], s[sA:sA+1]     ; end (0 if none)
  s_nop 1
  v_mov_b32_dpp v[vSTART], v[vT4] wave_shr:1 row_mask:0xf bank_mask:0xf bound_ctrl:0
  s_nop 1
  v_max_u32_dpp v[vSTART], v[vSTART], v[vSTART] row_shr:1 row_mask:0xf bank_mask:0xf bound_ctrl:0
  s_nop 1
  v_max_u32_dpp v[vSTART], v[vSTART], v[vSTART] row_shr:2 row_mask:0xf bank_mask:0xf bound_ctrl:0
  s_nop 1
  v_max_u32_dpp v[vSTART], v[vSTART], v[vSTART] row_shr:4 row_mask:0xf bank_mask:0xf bound_ctrl:0
  s_nop 1
  v_max_u32_dpp v[vSTART], v[vSTART], v[vSTART] row_shr:8 row_mask:0xf bank_mask:0xf bound_ctrl:0
  s_nop 1
  v_max_u32_dpp v[vSTART], v[vSTART], v[vSTART] row_bcast:15 row_mask:0xa bank_mask:0xf
  s_nop 1
  v_max_u32_dpp v[vSTART], v[vSTART], v[vSTART] row_bcast:31 row_mask:0xc bank_mask:0xf
  s_movk_i32 s[sT1], 128
  s_movk_i32 s[sT2], 256
  v_sub_u32 v[vLEN], v[vT4], v[vSTART]
  v_cndmask_b32 v[vLEN], 0, v[vLEN], s[sA:sA+1]   ; len (0 if none)
  v_add_u32 v[vT5], 127, v[vLEN]
  v_lshrrev_b32 v[vT5], 7, v[vT5]                 ; nq (mode A)
  v_cmp_lt_u32 vcc, s[sT1], v[vLEN]               ; len > 128
  v_cmp_ge_u32 s[sB:sB+1], s[sT2], v[vLEN]        ; len <= 256
  s_and_b64 s[sB:sB+1], s[sB:sB+1], vcc           ; mode B
  v_lshlrev_b32 v[vT5], 1, v[vT5]
  s_nop 0
  v_cndmask_b32 v[vT5], v[vT5], 2, s[sB:sB+1]     ; items (2 heads)
  v_lshlrev_b32 v[vT6], 16, v[vT5]
  v_cndmask_b32 v[vT5], v[vT6], v[vT5], vcc       ; small -> high half
  v_mov_b32 v[vINCL], v[vT5]
  s_nop 1
  v_add_u32_dpp v[vINCL], v[vINCL], v[vINCL] row_shr:1 row_mask:0xf bank_mask:0xf bound_ctrl:0
  s_nop 1
  v_add_u32_dpp v[vINCL], v[vINCL], v[vINCL] row_shr:2 row_mask:0xf bank_mask:0xf bound_ctrl:0
  s_nop 1
  v_add_u32_dpp v[vINCL], v[vINCL], v[vINCL] row_shr:4 row_mask:0xf bank_mask:0xf bound_ctrl:0
  s_nop 1
  v_add_u32_dpp v[vINCL], v[vINCL], v[vINCL] row_shr:8 row_mask:0xf bank_mask:0xf bound_ctrl:0
  s_nop 1
  v_add_u32_dpp v[vINCL], v[vINCL], v[vINCL] row_bcast:15 row_mask:0xa bank_mask:0xf
  s_nop 1
  v_add_u32_dpp v[vINCL], v[vINCL], v[vINCL] row_bcast:31 row_mask:0xc bank_mask:0xf
  s_mov_b32 s[sR], 0
  v_sub_u32 v[vEXCL], v[vINCL], v[vT5]
  v_readlane_b32 s[sT1], v[vINCL], 63
  s_and_b32 s[sNBIG], s[sT1], 0xffff
  s_lshr_b32 s[sTOT], s[sT1], 16
  s_add_u32 s[sTOT], s[sTOT], s[sNBIG]
  STAMP 3

.Litem:
  ; idx = r*32 + (r odd ? 31-j : j)
  s_lshl_b32 s[sT1], s[sR], 5
  s_sub_u32 s[sT2], 31, s[sJ]
  s_bitcmp1_b32 s[sR], 0
  s_cselect_b32 s[sT2], s[sT2], s[sJ]
  s_add_u32 s[sT1], s[sT1], s[sT2]                ; idx
  s_cmp_ge_u32 s[sT1], s[sTOT]
  s_cbranch_scc1 .Ldone
  s_sub_u32 s[sT2], s[sT1], s[sNBIG]
  s_cmp_lt_u32 s[sT1], s[sNBIG]
  s_cselect_b32 s[sT1], s[sT1], s[sT2]            ; index inside its class
  s_cselect_b32 s[sT2], 0, 16                     ; field shift
  v_bfe_u32 v[vT0], v[vINCL], s[sT2], 16
  v_bfe_u32 v[vT1], v[vEXCL], s[sT2], 16
  v_cmp_lt_u32 s[sA:sA+1], s[sT1], v[vT0]
  v_cmp_ge_u32 vcc, s[sT1], v[vT1]
  s_and_b64 s[sA:sA+1], s[sA:sA+1], vcc
  s_ff1_i32_b64 s[sT2], s[sA:sA+1]
  v_readlane_b32 s[sK0], v[vSTART], s[sT2]
  v_readlane_b32 s[sLEN], v[vLEN], s[sT2]
  v_readlane_b32 s[sT3], v[vT1], s[sT2]
  s_sub_u32 s[sT1], s[sT1], s[sT3]                ; local item
  s_and_b32 s[sT2], s[sT1], 1
  s_lshl_b32 s[sT2], s[sT2], 3
  s_add_u32 s[sT2], s[sT2], s[sG]
  s_lshl_b32 s[sHOFF], s[sT2], 8                  ; head*256 bytes
  s_lshr_b32 s[sT1], s[sT1], 1
  s_lshl_b32 s[sT1], s[sT1], 7
  s_add_u32 s[sQ0], s[sK0], s[sT1]
  s_add_u32 s[sQEND], s[sK0], s[sLEN]
  s_add_u32 s[sNK], s[sLEN], 63
  s_lshr_b32 s[sNK], s[sNK], 6
  s_sub_u32 s[sT1], s[sLEN], 129
  s_cmp_lt_u32 s[sT1], 128
  s_cselect_b32 s[sMB], 1, 0                      ; mode B: 128 < len <= 256
  ; ---- Q DMA
  s_lshl_b32 s[sT1], s[sW], 3
  s_add_u32 s[sT2], s[sQ0], s[sT1]
  s_lshl_b32 s[sT2], s[sT2], 12
  s_add_u32 s[sT2], s[sT2], s[sHOFF]              ; (q0 + 8w)*4096 + hoff
  s_add_u32 m0, s[sWD], LX
  s_add_u32 s[sT3], s[sT2], 64*4096
  buffer_load_dwordx4 v[vOK], s[sQ:sQ+3], s[sT2] offen lds
  s_add_u32 m0, s[sWD], LX+1024
  s_add_u32 s[sT4], s[sT2], 128*4096
  buffer_load_dwordx4 v[vOK2], s[sQ:sQ+3], s[sT2] offen lds
  s_add_u32 m0, s[sWD], LX+16384
  s_add_u32 s[sT1], s[sK0], s[sT1]
  buffer_load_dwordx4 v[vOK], s[sQ:sQ+3], s[sT3] offen lds
  s_add_u32 m0, s[sWD], LX+16384+1024
  s_lshl_b32 s[sT1], s[sT1], 12
  buffer_load_dwordx4 v[vOK2], s[sQ:sQ+3], s[sT3] offen lds
  s_add_u32 s[sKSO], s[sT1], s[sHOFF]             ; (k0 + 8w)*4096 + hoff
  s_mov_b32 s[sVSO], s[sKSO]
  s_cmp_eq_u32 s[sMB], 0
  s_cbranch_scc1 .Lq_done
  s_add_u32 m0, s[sWD], LK+49152
  s_add_u32 s[sT3], s[sT4], 64*4096
  buffer_load_dwordx4 v[vOK], s[sQ:sQ+3], s[sT4] offen lds
  s_add_u32 m0, s[sWD], LK+49152+1024
  s_nop 0
  buffer_load_dwordx4 v[vOK2], s[sQ:sQ+3], s[sT4] offen lds
  s_add_u32 m0, s[sWD], LSP
  s_nop 0
  buffer_load_dwordx4 v[vOK], s[sQ:sQ+3], s[sT3] offen lds
  s_add_u32 m0, s[sWD], LSP+1024
  s_nop 0
  buffer_load_dwordx4 v[vOK2], s[sQ:sQ+3], s[sT3] offen lds
.Lq_done:

  ; K tile issue: uses sKT (tile index), sKSO, sKDST ; V likewise
  .macro ISSUE_K
    s_cmp_ge_u32 s[sKT], s[sNK]
    s_cbranch_scc1 .Lik\@
    s_add_u32 m0, s[sKDST], s[sWD]
    s_add_u32 s[sKDST], s[sKDST], 16384
    buffer_load_dwordx4 v[vOK], s[sK:sK+3], s[sKSO] offen lds
    s_add_u32 m0, m0, 1024
    s_and_b32 s[sKDST], s[sKDST], 0xffff
    buffer_load_dwordx4 v[vOK2], s[sK:sK+3], s[sKSO] offen lds
    s_add_u32 s[sKSO], s[sKSO], 64*4096
  .Lik\@:
    s_add_u32 s[sKT], s[sKT], 1
  .endm
  .macro ISSUE_V
    s_cmp_ge_u32 s[sVT], s[sNK]
    s_cbranch_scc1 .Liv\@
    s_add_u32 m0, s[sVDST], s[sWD]
    s_add_u32 s[sVDST], s[sVDST], 16384
    buffer_load_dwordx4 v[vOV], s[sV:sV+3], s[sVSO] offen lds
    s_add_u32 m0, m0, 1024
    s_cmp_eq_u32 s[sVDST], LV+49152
    buffer_load_dwordx4 v[vOV2], s[sV:sV+3], s[sVSO] offen lds
    s_cselect_b32 s[sVDST], LV, s[sVDST]
    s_add_u32 s[sVSO], s[sVSO], 64*4096
  .Liv\@:
    s_add_u32 s[sVT], s[sVT], 1
  .endm

  s_mov_b32 s[sKT], 0
  s_mov_b32 s[sVT], 0
  s_mov_b32 s[sKDST], LK
  s_mov_b32 s[sVDST], LV
  ISSUE_K
  s_cmp_eq_u32 s[sMB], 1
  s_cbranch_scc1 .Lskipk1
  ISSUE_K
.Lskipk1:
  ISSUE_K
  ISSUE_V
  ISSUE_K
  ISSUE_V
  STAMP 4
  ; ---- per-wave geometry
  s_and_b32 s[sT1], s[sW], 3
  s_lshr_b32 s[sT2], s[sW], 2
  s_cmp_eq_u32 s[sMB], 1
  s_cselect_b32 s[sQB], s[sW], s[sT1]             ; q block
  s_cselect_b32 s[sKH], 0, s[sT2]                 ; key half (mode A)
  s_movk_i32 s[sKSTEP], 0x4000
  s_cselect_b32 s[sKSTEP], 8192, s[sKSTEP]
  ; total sub-tiles nst = ceil(len/32)
  s_add_u32 s[sT1], s[sLEN], 31
  s_lshr_b32 s[sT1], s[sT1], 5                    ; nst
  ; wave steps: mode B: nst ; mode A: (nst + 1 - kh) >> 1
  s_add_u32 s[sT2], s[sT1], 1
  s_sub_u32 s[sT2], s[sT2], s[sKH]
  s_lshr_b32 s[sT2], s[sT2], 1
  s_cmp_eq_u32 s[sMB], 1
  s_cselect_b32 s[sNS], s[sT1], s[sT2]
  s_cselect_b32 s[sNU], s[sT1], s[sNK]            ; step slots
  ; masked step: the globally last sub-tile (index nst-1) if len mod 32 != 0, owned by this wave iff mode B or ((nst-1)&1) == kh
  s_and_b32 s[sLIM], s[sLEN], 31
  s_sub_u32 s[sT2], s[sT1], 1
  s_and_b32 s[sT2], s[sT2], 1
  s_cmp_eq_u32 s[sT2], s[sKH]
  s_cselect_b32 s[sT3], 1, s[sMB]                 ; owner
  s_cmp_lg_u32 s[sLIM], 0
  s_cselect_b32 s[sT3], s[sT3], 0
  s_sub_u32 s[sMSTEP], s[sNS], 1
  s_cmp_eq_u32 s[sT3], 1
  s_cselect_b32 s[sMSTEP], s[sMSTEP], -1
  ; wave live?
  s_lshl_b32 s[sT1], s[sQB], 5
  s_add_u32 s[sT1], s[sT1], s[sQ0]                ; first q row of this wave
  s_cmp_lt_u32 s[sT1], s[sQEND]
  s_cselect_b32 s[sNS], s[sNS], 0
  ; output offset for this lane: row*4096 + hoff + kh*128 + hh*16 ; invalid rows -> out of range
  v_and_b32 v[vT0], 31, v[vLANE]
  v_add_u32 v[vT0], s[sT1], v[vT0]                ; q row
  v_lshlrev_b32 v[vOO], 12, v[vT0]
  s_lshl_b32 s[sT2], s[sKH], 7
  s_add_u32 s[sT2], s[sT2], s[sHOFF]
  v_lshl_add_u32 v[vOO], v[vHH4], 2, v[vOO]
  v_add_u32 v[vOO], s[sT2], v[vOO]
  v_cmp_gt_u32 vcc, s[sQEND], v[vT0]
  v_mov_b32 v[vT1], 0x80000000
  s_nop 0
  v_cndmask_b32 v[vOO], v[vT1], v[vOO], vcc
  ; Q read base
  s_lshl_b32 s[sT1], s[sQB], 13
  s_add_u32 s[sT2], s[sT1], LX                    ; qb < 4
  s_add_u32 s[sT3], s[sT1], LK+49152-4*8192       ; qb 4,5
  s_add_u32 s[sT4], s[sT1], LSP-6*8192            ; qb 6,7
  s_cmp_lt_u32 s[sQB], 6
  s_cselect_b32 s[sT3], s[sT3], s[sT4]
  s_cmp_lt_u32 s[sQB], 4
  s_cselect_b32 s[sT2], s[sT2], s[sT3]
  v_add_u32 v[vT0], s[sT2], v[vKB0]
  v_add_u32 v[vT1], s[sT2], v[vKB1]
  ; K read addresses for step 0
  s_lshl_b32 s[sKOFF], s[sKH], 13
  s_add_u32 s[sVOFF], s[sKOFF], LV
  v_add_u32 v[vKA0], s[sKOFF], v[vKB0]
  v_add_u32 v[vKA1], s[sKOFF], v[vKB1]
  ; O = 0, l = 0
  .set i, 0
  .rept 64
    v_mov_b32 v[vO+i], 0
    .set i, i+1
  .endr
  v_mov_b32 v[vL0], 0
  v_mov_b32 v[vL1], 0
  v_mov_b32 v[vM], 0xf149f2ca
  s_mov_b32 s[sPEND], 0
  s_mov_b32 s[sU], 0
  ; ---- barrier P: Q, K0 (K1) landed
  s_cmp_ge_u32 s[sNK], 4
  s_cselect_b32 s[sT1], 1, s[sMB]
  s_cmp_eq_u32 s[sT1], 1
  s_cbranch_scc1 .Lbp8
  s_waitcnt vmcnt(0)
  s_branch .Lbp
.Lbp8:
  s_waitcnt vmcnt(8)
.Lbp:
  s_barrier
  STAMP 5

  .macro KREAD i
    .if (\i & 1)
      ds_read_b128 v[vK+4*\i:vK+4*\i+3], v[vKA1] offset:512*(\i>>1)
    .else
      ds_read_b128 v[vK+4*\i:vK+4*\i+3], v[vKA0] offset:512*(\i>>1)
    .endif
  .endm
  .macro KADV                                       ; advance K read addresses to the next step
    s_add_u32 s[sKOFF], s[sKOFF], s[sKSTEP]
    s_and_b32 s[sKOFF], s[sKOFF], 0xffff
    v_add_u32 v[vKA0], s[sKOFF], v[vKB0]
    v_add_u32 v[vKA1], s[sKOFF], v[vKB1]
  .endm
  .macro QKM c, i
    .if \i == 0
      v_mfma_f32_32x32x16_bf16 v[\c:\c+15], v[vK:vK+3], v[vQ:vQ+3], 0
    .else
      v_mfma_f32_32x32x16_bf16 v[\c:\c+15], v[vK+4*\i:vK+4*\i+3], v[vQ+4*\i:vQ+4*\i+3], v[\c:\c+15]
    .endif
  .endm
  .macro VREAD j                                    ; V fragment j: d = j&3, ks = j>>2
    ds_read_b64_tr_b16 v[vV+4*\j:vV+4*\j+1], v[vVA] offset:512*(\j&3)+4096*(\j>>2)
    ds_read_b64_tr_b16 v[vV+4*\j+2:vV+4*\j+3], v[vVA] offset:512*(\j&3)+4096*(\j>>2)+2048
  .endm
  .macro PVM j
    v_mfma_f32_32x32x16_bf16 v[vO+16*(\j&3):vO+16*(\j&3)+15], v[vV+4*\j:vV+4*\j+3], v[vP+4*(\j>>2):vP+4*(\j>>2)+3], v[vO+16*(\j&3):vO+16*(\j&3)+15]
  .endm
  .macro MASK c                                     ; keys >= lim of this sub-tile -> -inf
    v_sub_u32 v[vT2], s[sLIM], v[vHH4]
    v_mov_b32 v[vT3], 0xff800000
    .set r, 0
    .rept 16
      v_cmp_lt_i32 vcc, (r&3)+8*(r>>2), v[vT2]
      s_nop 1
      v_cndmask_b32 v[\c+r], v[vT3], v[\c+r], vcc
      .set r, r+1
    .endr
  .endm
  .macro ROWMAX1 c
    v_max3_f32 v[vT2], v[\c+0], v[\c+1], v[\c+2]
    v_max3_f32 v[vT3], v[\c+3], v[\c+4], v[\c+5]
    v_max3_f32 v[vT2], v[vT2], v[\c+6], v[\c+7]
    v_max3_f32 v[vT3], v[vT3], v[\c+8], v[\c+9]
    v_max3_f32 v[vT2], v[vT2], v[\c+10], v[\c+11]
    v_max3_f32 v[vT3], v[vT3], v[\c+12], v[\c+13]
    v_max3_f32 v[vT2], v[vT2], v[\c+14], v[\c+15]
    v_max_f32 v[vT2], v[vT2], v[vT3]
    v_mov_b32 v[vT3], v[vT2]
  .endm
  .macro ROWMAX2                                    ; needs 2 wait states after ROWMAX1
    v_permlane32_swap_b32 v[vT2], v[vT3]
    s_nop 1
    v_max_f32 v[vT2], v[vT2], v[vT3]                ; vT2 = row max
  .endm
  .macro EXPE c, r
    v_fma_f32 v[\c+\r], v[\c+\r], s[sCE], v[vNMC]
    v_exp_f32 v[\c+\r], v[\c+\r]
  .endm
  .macro SUMPK p, i                                 ; row sums + pack of elements 2i, 2i+1
    v_add_f32 v[vL0], v[vL0], v[\p+2*\i]
    v_add_f32 v[vL1], v[vL1], v[\p+2*\i+1]
    v_cvt_pk_bf16_f32 v[vP+\i], v[\p+2*\i], v[\p+2*\i+1]
  .endm
  .macro VADDR                                      ; V read address for the current step, then advance
    v_add_u32 v[vVA], s[sVOFF], v[vVB]
    s_add_u32 s[sVOFF], s[sVOFF], s[sKSTEP]
    s_sub_u32 s[sT1], s[sVOFF], 49152
    s_cmp_ge_u32 s[sVOFF], LV+49152
    s_cselect_b32 s[sVOFF], s[sT1], s[sVOFF]
  .endm
  .macro RESCALE
    s_nop 7
    s_nop 7
    .set i, 0
    .rept 64
      v_mul_f32 v[vO+i], v[vAL], v[vO+i]
      .set i, i+1
    .endr
    v_mul_f32 v[vL0], v[vAL], v[vL0]
    v_mul_f32 v[vL1], v[vAL], v[vL1]
    s_mov_b32 s[sPEND], 0
  .endm

  ; ---- prologue compute: step 0
  s_cmp_eq_u32 s[sNS], 0
  s_cbranch_scc1 .Lloop0
  .set i, 0
  .rept 8
    .if (i & 1)
      ds_read_b128 v[vQ+4*i:vQ+4*i+3], v[vT1] offset:512*(i>>1)
    .else
      ds_read_b128 v[vQ+4*i:vQ+4*i+3], v[vT0] offset:512*(i>>1)
    .endif
    KREAD i
    .set i, i+1
  .endr
  KADV
  s_waitcnt lgkmcnt(0)
  .set i, 0
  .rept 8
    QKM vSA, i
    KREAD i
    .set i, i+1
  .endr
  KADV
  s_nop 7
  s_nop 3
  s_cmp_lg_u32 s[sMSTEP], 0
  s_cbranch_scc1 .Lnomask0
  MASK vSA
.Lnomask0:
  ROWMAX1 vSA
  s_nop 1
  ROWMAX2
  v_max_f32 v[vM], v[vM], v[vT2]
  v_mul_f32 v[vNMC], 0xbe0293ee, v[vM]
  .set r, 0
  .rept 16
    EXPE vSA, r
    .set r, r+1
  .endr

  ; ---- tile boundary: wait, barrier, issue
  .macro BARDMA
    s_sub_u32 s[sT1], s[sKT], 1
    s_cmp_lt_u32 s[sT1], s[sNK]
    s_cbranch_scc1 .Lw4\@
    s_sub_u32 s[sT1], s[sVT], 1
    s_cmp_lt_u32 s[sT1], s[sNK]
    s_cbranch_scc1 .Lw2\@
    s_waitcnt vmcnt(0) lgkmcnt(0)
    s_branch .Lwd\@
  .Lw2\@:
    s_waitcnt vmcnt(2) lgkmcnt(0)
    s_branch .Lwd\@
  .Lw4\@:
    s_waitcnt vmcnt(4) lgkmcnt(0)
  .Lwd\@:
    s_barrier
    ISSUE_K
    ISSUE_V
  .endm

  ; ---- one step slot: cur = S buffer receiving step u+1, prev = p of step u
  .macro STEP cur, prev
    s_add_u32 s[sT2], s[sU], 1
    s_cmp_ge_u32 s[sU], s[sNS]
    s_cbranch_scc1 .Lsend\@
    VADDR
    s_cmp_ge_u32 s[sT2], s[sNS]
    s_cbranch_scc1 .Llast\@
    ; ---------------- phase A
    .set g, 0
    .rept 8
      QKM \cur, g
      VREAD g
      SUMPK \prev, g
      .set g, g+1
    .endr
    s_waitcnt lgkmcnt(4)
    PVM 0
    KREAD 0
    KREAD 1
    s_nop 2
    PVM 1
    s_cmp_lg_u32 s[sMSTEP], s[sT2]
    s_cbranch_scc1 .Lnm\@
    MASK \cur
  .Lnm\@:
    ROWMAX1 \cur
    PVM 2
    KREAD 2
    ROWMAX2
    v_sub_f32 v[vT3], v[vT2], v[vM]
    v_cmp_lt_f32 vcc, s[sTHR], v[vT3]
    s_cbranch_vccz .Lkeep\@
    v_max_f32 v[vT3], v[vM], v[vT2]
    v_sub_f32 v[vAL], v[vM], v[vT3]
    v_mul_f32 v[vAL], s[sCE], v[vAL]
    v_exp_f32 v[vAL], v[vAL]
    v_mov_b32 v[vM], v[vT3]
    v_mul_f32 v[vNMC], 0xbe0293ee, v[vM]
    s_mov_b32 s[sPEND], 1
  .Lkeep\@:
    PVM 3
    KREAD 3
    EXPE \cur, 0
    EXPE \cur, 1
    EXPE \cur, 2
    PVM 4
    KREAD 4
    EXPE \cur, 3
    EXPE \cur, 4
    EXPE \cur, 5
    PVM 5
    KREAD 5
    EXPE \cur, 6
    EXPE \cur, 7
    EXPE \cur, 8
    s_waitcnt lgkmcnt(6)
    PVM 6
    KREAD 6
    EXPE \cur, 9
    EXPE \cur, 10
    EXPE \cur, 11
    PVM 7
    KREAD 7
    EXPE \cur, 12
    EXPE \cur, 13
    EXPE \cur, 14
    EXPE \cur, 15
    KADV
    s_cmp_eq_u32 s[sPEND], 0
    s_cbranch_scc1 .Lsend\@
    RESCALE
    s_branch .Lsend\@
  .Llast\@:
    .set g, 0
    .rept 8
      VREAD g
      SUMPK \prev, g
      .set g, g+1
    .endr
    s_waitcnt lgkmcnt(0)
    .set g, 0
    .rept 8
      PVM g
      .set g, g+1
    .endr
  .Lsend\@:
    s_waitcnt lgkmcnt(0)
    s_add_u32 s[sU], s[sU], 1
  .endm

.Lloop0:
.Lloop:
  BARDMA
  .if DIAG
  s_cmp_lg_u32 s[sU], 0
  s_cbranch_scc1 .Lnst6
  STAMP 6
.Lnst6:
  .endif
  STEP vSB, vSA
  s_cmp_ge_u32 s[sU], s[sNU]
  s_cbranch_scc1 .Lepi
  s_cmp_eq_u32 s[sMB], 1
  s_cbranch_scc1 .Lnobar
  BARDMA
.Lnobar:
  STEP vSA, vSB
  s_cmp_lt_u32 s[sU], s[sNU]
  s_cbranch_scc1 .Lloop

  ; ======================================================================== epilogue
.Lepi:
  STAMP 7
  v_add_f32 v[vL0], v[vL0], v[vL1]
  v_mov_b32 v[vL1], v[vL0]
  s_nop 1
  v_permlane32_swap_b32 v[vL0], v[vL1]
  s_nop 1
  v_add_f32 v[vL0], v[vL0], v[vL1]                ; row sum
  s_cmp_eq_u32 s[sMB], 1
  s_cbranch_scc1 .LepiB
  ; ---- mode A: merge the two key halves.  exchange area: K ring, wave w at w*8192 ; stats in X region
  s_nop 7
  v_lshlrev_b32 v[vT0], 4, v[vLANE]
  s_lshl_b32 s[sT1], s[sW], 13
  s_xor_b32 s[sT2], s[sT1], 4*8192
  v_add_u32 v[vT1], s[sT2], v[vT0]                ; partner area
  v_add_u32 v[vT0], s[sT1], v[vT0]                ; own area
  v_and_b32 v[vT2], 31, v[vLANE]
  v_lshlrev_b32 v[vT2], 3, v[vT2]
  v_add_u32 v[vT2], LX, v[vT2]
  s_lshl_b32 s[sT1], s[sW], 8
  s_xor_b32 s[sT2], s[sT1], 4*256
  v_add_u32 v[vT3], s[sT2], v[vT2]
  v_add_u32 v[vT2], s[sT1], v[vT2]
  v_mov_b32 v[vT4], v[vM]
  v_mov_b32 v[vT5], v[vL0]
  ds_write_b64 v[vT2], v[vT4:vT5]
  s_cmp_eq_u32 s[sKH], 0
  s_cbranch_scc1 .Lsend_hi
  ; kh = 1: send d 0..63 (o[0], o[1]); keep o[2], o[3]
  .set i, 0
  .rept 8
    ds_write_b128 v[vT0], v[vO+4*i:vO+4*i+3] offset:1024*i
    .set i, i+1
  .endr
  ; move the kept half down so that both cases continue with vO..vO+31
  .set i, 0
  .rept 32
    v_mov_b32 v[vO+i], v[vO+32+i]
    .set i, i+1
  .endr
  s_branch .Lsent
.Lsend_hi:
  .set i, 0
  .rept 8
    ds_write_b128 v[vT0], v[vO+32+4*i:vO+32+4*i+3] offset:1024*i
    .set i, i+1
  .endr
.Lsent:
  s_waitcnt lgkmcnt(0)
  s_barrier
  ds_read_b64 v[vT4:vT5], v[vT3]
  .set i, 0
  .rept 8
    ds_read_b128 v[vO+32+4*i:vO+32+4*i+3], v[vT1] offset:1024*i
    .set i, i+1
  .endr
  s_waitcnt lgkmcnt(8)
  v_max_f32 v[vT6], v[vM], v[vT4]
  v_sub_f32 v[vT0], v[vM], v[vT6]
  v_sub_f32 v[vT2], v[vT4], v[vT6]
  v_mul_f32 v[vT0], s[sCE], v[vT0]
  v_mul_f32 v[vT2], s[sCE], v[vT2]
  v_exp_f32 v[vT0], v[vT0]
  v_exp_f32 v[vT2], v[vT2]
  s_nop 0
  v_mul_f32 v[vT6], v[vT0], v[vL0]
  v_fma_f32 v[vT6], v[vT2], v[vT5], v[vT6]
  v_rcp_f32 v[vT6], v[vT6]
  s_nop 0
  v_mul_f32 v[vT0], v[vT0], v[vT6]                ; fa
  v_mul_f32 v[vT2], v[vT2], v[vT6]                ; fb
  .set i, 0
  .rept 32
    v_mul_f32 v[vO+i], v[vT0], v[vO+i]
    .set i, i+1
  .endr
  s_waitcnt lgkmcnt(0)
  .set i, 0
  .rept 32
    v_fma_f32 v[vO+i], v[vT2], v[vO+32+i], v[vO+i]
    .set i, i+1
  .endr
  .macro STOREBLK b, off                            ; 16 f32 at vO+b.. -> 2 x dwordx4 at byte offsets off, off+32
    v_cvt_pk_bf16_f32 v[vX+0], v[vO+\b+0], v[vO+\b+1]
    v_cvt_pk_bf16_f32 v[vX+1], v[vO+\b+2], v[vO+\b+3]
    v_cvt_pk_bf16_f32 v[vX+2], v[vO+\b+4], v[vO+\b+5]
    v_cvt_pk_bf16_f32 v[vX+3], v[vO+\b+6], v[vO+\b+7]
    v_cvt_pk_bf16_f32 v[vX+4], v[vO+\b+8], v[vO+\b+9]
    v_cvt_pk_bf16_f32 v[vX+5], v[vO+\b+10], v[vO+\b+11]
    v_cvt_pk_bf16_f32 v[vX+6], v[vO+\b+12], v[vO+\b+13]
    v_cvt_pk_bf16_f32 v[vX+7], v[vO+\b+14], v[vO+\b+15]
    v_permlane32_swap_b32 v[vX+0], v[vX+2]
    v_permlane32_swap_b32 v[vX+1], v[vX+3]
    s_nop 0
    v_permlane32_swap_b32 v[vX+4], v[vX+6]
    v_permlane32_swap_b32 v[vX+5], v[vX+7]
    s_nop 0
    buffer_store_dwordx4 v[vX+0:vX+3], v[vOO], s[sO:sO+3], 0 offen offset:\off
    buffer_store_dwordx4 v[vX+4:vX+7], v[vOO], s[sO:sO+3], 0 offen offset:\off+32
    s_nop 1
  .endm
  STOREBLK 0, 0
  STOREBLK 16, 64
  s_branch .Lnext
.LepiB:
  v_rcp_f32 v[vT0], v[vL0]
  s_nop 7
  .set i, 0
  .rept 64
    v_mul_f32 v[vO+i], v[vT0], v[vO+i]
    .set i, i+1
  .endr
  STOREBLK 0, 0
  STOREBLK 16, 64
  STOREBLK 32, 128
  STOREBLK 48, 192
.Lnext:
  STAMP 8
  s_add_u32 s[sR], s[sR], 1
  s_lshl_b32 s[sT1], s[sR], 5
  s_cmp_ge_u32 s[sT1], s[sTOT]
  s_cbranch_scc1 .Ldone
  s_waitcnt lgkmcnt(0)
  s_barrier
  s_branch .Litem
.Ldone:
  .if DIAG
    s_waitcnt vmcnt(0)
    STAMP 9
    s_memrealtime s[78:79]
    s_waitcnt lgkmcnt(0)
    s_load_dwordx2 s[sDBG:sDBG+1], %0, 88
    s_lshl_b32 s[sT1], %1, 3
    s_add_u32 s[sT1], s[sT1], s[sW]
    s_lshl_b32 s[sT1], s[sT1], 8
    s_waitcnt lgkmcnt(0)
    s_add_u32 s[sDBG], s[sDBG], s[sT1]
    s_addc_u32 s[sDBG+1], s[sDBG+1], 0
    v_mov_b32 v[vT6], 0
    s_mov_b64 exec, 1
    .set i, 0
    .rept 10
      v_mov_b32 v[vT0], s[sST+2*i]
      v_mov_b32 v[vT1], s[sST+2*i+1]
      global_store_dwordx2 v[vT6], v[vT0:vT1], s[sDBG:sDBG+1] offset:8*i
      s_nop 1
      .set i, i+1
    .endr
    .set i, 0
    .rept 2
      v_mov_b32 v[vT0], s[76+2*i]
      v_mov_b32 v[vT1], s[76+2*i+1]
      global_store_dwordx2 v[vT6], v[vT0:vT1], s[sDBG:sDBG+1] offset:80+8*i
      s_nop 1
      .set i, i+1
    .endr
  .endif
  s_endpgm
.Lfallback:
  s_waitcnt vmcnt(0) lgkmcnt(0)
  .purgem STAMP
  .purgem ISSUE_K
  .purgem ISSUE_V
  .purgem KREAD
  .purgem KADV
  .purgem QKM
  .purgem VREAD
  .purgem PVM
  .purgem MASK
  .purgem ROWMAX1
  .purgem ROWMAX2
  .purgem EXPE
  .purgem SUMPK
  .purgem VADDR
  .purgem RESCALE
  .purgem BARDMA
  .purgem STEP
  .purgem STOREBLK
  )" :: "s"(__builtin_amdgcn_kernarg_segment_ptr()), "s"(blockIdx.x), "v"(threadIdx.x), "i"(NIT), "s"(ldsb)
     : "memory", "vcc", "m0",
       "s8","s9","s10","s11","s12","s13","s14","s15","s16","s17","s18","s19","s20","s21","s22","s23","s24","s25","s26","s27","s28","s29","s30","s31",
       "s33","s34","s35","s36","s37","s38","s39","s40","s41","s42","s43","s44","s45","s46","s47","s48","s49","s50","s51","s52","s53","s54","s55",
       "s56","s57","s58","s59","s60","s61","s62","s63","s64","s65","s66","s67","s68","s69","s70","s71","s72","s73","s74","s75","s76","s77","s78","s79",
       "s80","s81","s82","s83","s84","s85","s86","s87","s88","s89","s90","s91","s92","s93","s94","s95","s96","s97","s98","s99",
       "v1","v2","v3","v4","v5","v6","v7","v8","v9","v10","v11","v12","v13","v14","v15","v16","v17","v18","v19","v20","v21","v22","v23","v24","v25","v26","v27","v28","v29","v30","v31",
       "v32","v33","v34","v35","v36","v37","v38","v39","v40","v41","v42","v43","v44","v45","v46","v47","v48","v49","v50","v51","v52","v53","v54","v55","v56","v57","v58","v59","v60","v61","v62","v63",
       "v64","v65","v66","v67","v68","v69","v70","v71","v72","v73","v74","v75","v76","v77","v78","v79","v80","v81","v82","v83","v84","v85","v86","v87","v88","v89","v90","v91","v92","v93","v94","v95",
       "v96","v97","v98","v99","v100","v101","v102","v103","v104","v105","v106","v107","v108","v109","v110","v111","v112","v113","v114","v115","v116","v117","v118","v119","v120","v121","v122","v123","v124","v125","v126","v127",
       "v128","v129","v130","v131","v132","v133","v134","v135","v136","v137","v138","v139","v140","v141","v142","v143","v144","v145","v146","v147","v148","v149","v150","v151","v152","v153","v154","v155","v156","v157","v158","v159",
       "v160","v161","v162","v163","v164","v165","v166","v167","v168","v169","v170","v171","v172","v173","v174","v175","v176","v177","v178","v179","v180","v181","v182","v183","v184","v185","v186","v187","v188","v189","v190","v191",
       "v192","v193","v194","v195","v196","v197","v198","v199","v200","v201","v202","v203","v204","v205","v206","v207","v208","v209","v210","v211","v212","v213","v214","v215","v216","v217","v218","v219","v220","v221","v222","v223",
       "v224","v225","v226","v227","v228","v229","v230","v231","v232","v233","v234","v235","v236","v237","v238","v239","v240","v241","v242","v243","v244","v245","v246","v247","v248","v249","v250","v251","v252","v253","v254","v255");
  generic_path((const unsigned short*)Q, (const unsigned short*)K, (const unsigned short*)V, (const int*)seg, (unsigned short*)O, T, lds);
}

__global__ __launch_bounds__(512, 2)
void ragged_attn_generic(const void* Q, const void* K, const void* V, const void* seg, void* O, int T, int pad) {
  __shared__ __attribute__((aligned(16))) char lds[LDS_BYTES];
  generic_path((const unsigned short*)Q, (const unsigned short*)K, (const unsigned short*)V, (const int*)seg, (unsigned short*)O, T, lds);
}

extern "C" void kernel_launch(void* const* d_in, const int* in_sizes, int n_in, void* d_out, int out_size, void* d_ws, size_t ws_size,
                              hipStream_t stream) {
  const int T = in_sizes[3];
  const unsigned nb = (unsigned)T * ROWB, F = 0x00020000u;
  if (T > 2048 && T <= 2560) {
    ragged_attn_kernel<5><<<256, 512, 0, stream>>>(d_in[0], nb, F, d_in[1], nb, F, d_in[2], nb, F, d_in[3], (unsigned)T * 4u, F, d_out, nb, F,
                                                   T, T - 1 - 512 * 4, d_ws);
  } else {
    ragged_attn_generic<<<256, 512, 0, stream>>>(d_in[0], d_in[1], d_in[2], d_in[3], d_out, T, 0);
  }
}
```

```cpp
#include <hip/hip_runtime.h>
#include <stdint.h>

typedef short    bf16x8 __attribute__((ext_vector_type(8)));
typedef short    s16x4  __attribute__((ext_vector_type(4)));
typedef float    f32x16 __attribute__((ext_vector_type(16)));
typedef float    f32x4  __attribute__((ext_vector_type(4)));
typedef float    f32x2  __attribute__((ext_vector_type(2)));
typedef __bf16   bf2    __attribute__((ext_vector_type(2)));
typedef unsigned u32x4  __attribute__((ext_vector_type(4)));
typedef unsigned long long u64;
#define LDSP __attribute__((address_space(3)))
typedef LDSP const char* lds_cptr;
typedef LDSP char* lds_ptr;
typedef short v4i16_t __attribute__((ext_vector_type(4)));
#define DI __device__ __forceinline__
#define MFMA(a, b, c) __builtin_amdgcn_mfma_f32_32x32x16_bf16((a), (b), (c), 0, 0, 0)

constexpr int NH = 16, HD = 128;
constexpr unsigned ROWB = NH * HD * 2;
constexpr unsigned LDS_BYTES = 163840;
constexpr float CEXP = 0.08838834764831845f * 1.4426950408889634f;
constexpr float THR_RAW = 8.0f / CEXP;
constexpr float MINIT = -1.0e30f;

DI unsigned rfl(unsigned x) { return __builtin_amdgcn_readfirstlane(x); }
DI unsigned cvtpk(float lo, float hi) { f32x2 v = {lo, hi}; bf2 b = __builtin_convertvector(v, bf2); return __builtin_bit_cast(unsigned, b); }
DI float swap_max(float x) { auto r = __builtin_amdgcn_permlane32_swap(__float_as_uint(x), __float_as_uint(x), false, false); return __builtin_fmaxf(__uint_as_float(r[0]), __uint_as_float(r[1])); }
DI float swap_add(float x) { auto r = __builtin_amdgcn_permlane32_swap(__float_as_uint(x), __float_as_uint(x), false, false); return __uint_as_float(r[0]) + __uint_as_float(r[1]); }
DI s16x4 vtr(lds_cptr p) { return __builtin_bit_cast(s16x4, __builtin_amdgcn_ds_read_tr16_b64_v4i16((LDSP v4i16_t*)p)); }
#define MX3(a, b, c) __builtin_fmaxf(__builtin_fmaxf((a), (b)), (c))

DI void dma16(__amdgpu_buffer_rsrc_t srd, unsigned voff, unsigned soff, unsigned m0v) {
  unsigned keep;
  asm volatile("s_nop 4\n\ts_mov_b32 %0, m0\n\ts_mov_b32 m0, %1\n\ts_nop 0\n\tbuffer_load_dwordx4 %2, %3, %4 offen lds\n\ts_mov_b32 m0, %0"
               : "=&s"(keep) : "s"(m0v), "v"(voff), "s"(srd), "s"(soff) : "memory");
}

__device__ __forceinline__ void generic_path(const unsigned short* __restrict__ Q, const unsigned short* __restrict__ K, const unsigned short* __restrict__ V,
                                          const int* __restrict__ seg, unsigned short* __restrict__ O, int T, char* lds) {
  constexpr unsigned SLOT = 32768, VOFF = 16384, XREG = 4 * SLOT;
  const int tid = threadIdx.x, lane = tid & 63, r32 = lane & 31, hh = lane >> 5;
  const int w = (int)rfl((unsigned)tid >> 6);
  const int grp = blockIdx.x & 7, jj = blockIdx.x >> 3, NJ = 32;
  const unsigned ldsb = (unsigned)(uintptr_t)lds;
  const lds_ptr L = (lds_ptr)lds;
  const int total_items = 2 * ((T + 127) >> 7);
  const __amdgpu_buffer_rsrc_t srdQ = __builtin_amdgcn_make_buffer_rsrc((void*)Q, (short)0, T * (int)ROWB, 0x00020000);
  const __amdgpu_buffer_rsrc_t srdK = __builtin_amdgcn_make_buffer_rsrc((void*)K, (short)0, T * (int)ROWB, 0x00020000);
  const __amdgpu_buffer_rsrc_t srdV = __builtin_amdgcn_make_buffer_rsrc((void*)V, (short)0, T * (int)ROWB, 0x00020000);
  const unsigned prow = (lane >> 2) & 7, pcb = lane >> 5, pc = lane & 3;
  const unsigned voffK = prow * ROWB + pcb * 64 + ((pc ^ ((2u * (unsigned)w + (prow >> 2)) & 3u)) * 16);
  const unsigned voffV = prow * ROWB + pcb * 64 + pc * 16;
  const unsigned kb0 = 2048u * (r32 >> 3) + 64u * (r32 & 7) + 16u * ((unsigned)hh ^ ((r32 >> 2) & 3u));
  const unsigned vb0 = ((unsigned)hh * 4 + ((lane & 15) >> 2)) * 64 + ((lane >> 4) & 1) * 32 + (lane & 3) * 8;
  for (int idx = jj; idx < total_items; idx += NJ) {
    __syncthreads();
    const int head = grp + 8 * (idx & 1);
    const int q0 = (idx >> 1) * 128, qend = min(q0 + 128, T);
    LDSP int* red = (LDSP int*)L;
    if (tid == 0) { red[0] = 0x7fffffff; red[1] = (int)0x80000000; red[2] = 0x7fffffff; red[3] = -1; }
    __syncthreads();
    if (tid < 128 && q0 + tid < qend) { const int id = seg[q0 + tid];
      __hip_atomic_fetch_min((LDSP int*)&red[0], id, __ATOMIC_RELAXED, __HIP_MEMORY_SCOPE_WORKGROUP);
      __hip_atomic_fetch_max((LDSP int*)&red[1], id, __ATOMIC_RELAXED, __HIP_MEMORY_SCOPE_WORKGROUP); }
    __syncthreads();
    const int idlo = red[0], idhi = red[1];
    int lo = 0x7fffffff, hi = -1;
    for (int i = tid; i < T; i += 512) { const int id = seg[i]; if (id >= idlo && id <= idhi) { lo = min(lo, i); hi = max(hi, i); } }
    __hip_atomic_fetch_min((LDSP int*)&red[2], lo, __ATOMIC_RELAXED, __HIP_MEMORY_SCOPE_WORKGROUP);
    __hip_atomic_fetch_max((LDSP int*)&red[3], hi, __ATOMIC_RELAXED, __HIP_MEMORY_SCOPE_WORKGROUP);
    __syncthreads();
    const int k0 = (int)rfl((unsigned)red[2]), kend = (int)rfl((unsigned)red[3]) + 1;
    __syncthreads();
    const int nk = (kend - k0 + 63) >> 6;
    const unsigned hoff = (unsigned)head * 256u;
#pragma unroll
    for (int i = 0; i < 2; ++i) {
      const unsigned m = (unsigned)w + 8u * i;
      const unsigned so = (unsigned)(q0 + 8 * (int)m) * ROWB + hoff;
      dma16(srdQ, voffK, so, ldsb + XREG + m * 2048u);
      dma16(srdQ, voffK, so + 128u, ldsb + XREG + m * 2048u + 1024u);
    }
    const int qb = w & 3, sub = w >> 2;
    const int qrow = q0 + 32 * qb + r32;
    const int segq = seg[qrow < T ? qrow : T - 1];
    bf16x8 qf[8];
    f32x16 o[4];
#pragma unroll
    for (int d = 0; d < 4; ++d) o[d] = f32x16{};
    float m_run = MINIT, l_run = 0.f;
    for (int t = 0; t < nk; ++t) {
      __syncthreads();
      {
        const unsigned so = (unsigned)(k0 + 64 * t + 8 * w) * ROWB + hoff;
        const unsigned d = ldsb + (unsigned)w * 2048u;
        dma16(srdK, voffK, so, d); dma16(srdK, voffK, so + 128u, d + 1024u);
        dma16(srdV, voffV, so, d + VOFF); dma16(srdV, voffV, so + 128u, d + VOFF + 1024u);
      }
      asm volatile("s_waitcnt vmcnt(0)" ::: "memory");
      __syncthreads();
      if (t == 0) {
#pragma unroll
        for (int s = 0; s < 8; ++s)
          qf[s] = *(const LDSP bf16x8*)(L + XREG + (unsigned)qb * 8192u + ((kb0 ^ ((s & 1) * 32u)) + 512u * (s >> 1)));
      }
      const int kbase = k0 + 64 * t + 32 * sub;
      const lds_cptr kp = (lds_cptr)L + (unsigned)sub * 8192u;
      f32x16 sc = f32x16{};
#pragma unroll
      for (int s = 0; s < 8; ++s) {
        const bf16x8 kf = *(const LDSP bf16x8*)(kp + ((kb0 ^ ((s & 1) * 32u)) + 512u * (s >> 1)));
        sc = MFMA(kf, qf[s], sc);
      }
#pragma unroll
      for (int r = 0; r < 16; ++r) {
        const int kk = kbase + (r & 3) + 8 * (r >> 2) + 4 * hh;
        const int id = seg[kk < T ? kk : T - 1];
        sc[r] = (kk < kend && id == segq) ? sc[r] : -__builtin_inff();
      }
      float a = MX3(sc[0], sc[1], sc[2]), b = MX3(sc[3], sc[4], sc[5]);
      a = MX3(a, sc[6], sc[7]); b = MX3(b, sc[8], sc[9]); a = MX3(a, sc[10], sc[11]); b = MX3(b, sc[12], sc[13]);
      a = MX3(a, sc[14], sc[15]);
      const float pm = swap_max(__builtin_fmaxf(a, b));
      const float mn = __builtin_fmaxf(m_run, pm);
      const float al = __builtin_amdgcn_exp2f((m_run - mn) * CEXP);
      m_run = mn; l_run *= al;
#pragma unroll
      for (int d = 0; d < 4; ++d)
#pragma unroll
        for (int r = 0; r < 16; ++r) o[d][r] *= al;
      const float nmc = -m_run * CEXP;
      float ps = 0.f;
#pragma unroll
      for (int r = 0; r < 16; ++r) { sc[r] = __builtin_amdgcn_exp2f(__builtin_fmaf(sc[r], CEXP, nmc)); ps += sc[r]; }
      l_run += ps;
      u32x4 pw0, pw1;
#pragma unroll
      for (int e = 0; e < 4; ++e) { pw0[e] = cvtpk(sc[2 * e], sc[2 * e + 1]); pw1[e] = cvtpk(sc[8 + 2 * e], sc[8 + 2 * e + 1]); }
      const bf16x8 p0 = __builtin_bit_cast(bf16x8, pw0), p1 = __builtin_bit_cast(bf16x8, pw1);
      const lds_cptr vp = kp + VOFF + vb0;
#pragma unroll
      for (int d = 0; d < 4; ++d) {
        const s16x4 a0 = vtr(vp + d * 512), a1 = vtr(vp + d * 512 + 2048), b0 = vtr(vp + d * 512 + 4096), b1 = vtr(vp + d * 512 + 6144);
        const bf16x8 va = __builtin_shufflevector(a0, a1, 0, 1, 2, 3, 4, 5, 6, 7), vb = __builtin_shufflevector(b0, b1, 0, 1, 2, 3, 4, 5, 6, 7);
        o[d] = MFMA(va, p0, o[d]);
        o[d] = MFMA(vb, p1, o[d]);
      }
    }
    l_run = swap_add(l_run);
    unsigned short* orow = O + (size_t)(qrow < T ? qrow : 0) * (NH * HD) + head * HD + 8 * hh;
    const bool row_ok = qrow < qend;
    const int kh = sub;
    __syncthreads();
    const unsigned xa = SLOT, xb = 2 * SLOT, xs = 3 * SLOT;
    const unsigned mine = (kh ? xb : xa) + (unsigned)qb * 8192u + (unsigned)lane * 16u;
    const unsigned theirs = (kh ? xa : xb) + (unsigned)qb * 8192u + (unsigned)lane * 16u;
#pragma unroll
    for (int dd = 0; dd < 2; ++dd)
#pragma unroll
      for (int g = 0; g < 4; ++g) {
        f32x4 v;
#pragma unroll
        for (int e = 0; e < 4; ++e) v[e] = kh ? o[dd][4 * g + e] : o[2 + dd][4 * g + e];
        *(LDSP f32x4*)(L + mine + (unsigned)(dd * 4 + g) * 1024u) = v;
      }
    if (hh == 0) { f32x2 s2 = {m_run, l_run}; *(LDSP f32x2*)(L + xs + (unsigned)w * 256u + (unsigned)r32 * 8u) = s2; }
    __syncthreads();
    const f32x2 o2 = *(const LDSP f32x2*)(L + xs + (unsigned)(w ^ 4) * 256u + (unsigned)r32 * 8u);
    const float mn = __builtin_fmaxf(m_run, o2[0]);
    const float wa = __builtin_amdgcn_exp2f((m_run - mn) * CEXP), wb = __builtin_amdgcn_exp2f((o2[0] - mn) * CEXP);
    const float inv = 1.0f / (wa * l_run + wb * o2[1]);
    const float fa = wa * inv, fb = wb * inv;
#pragma unroll
    for (int dd = 0; dd < 2; ++dd) {
      float r[16];
#pragma unroll
      for (int g = 0; g < 4; ++g) {
        const f32x4 v = *(const LDSP f32x4*)(L + theirs + (unsigned)(dd * 4 + g) * 1024u);
#pragma unroll
        for (int e = 0; e < 4; ++e) r[4 * g + e] = fa * (kh ? o[2 + dd][4 * g + e] : o[dd][4 * g + e]) + fb * v[e];
      }
#pragma unroll
      for (int g = 0; g < 4; g += 2) {
        unsigned ax = cvtpk(r[4 * g + 0], r[4 * g + 1]), ay = cvtpk(r[4 * g + 2], r[4 * g + 3]);
        unsigned bx = cvtpk(r[4 * g + 4], r[4 * g + 5]), by = cvtpk(r[4 * g + 6], r[4 * g + 7]);
        auto rx = __builtin_amdgcn_permlane32_swap(ax, bx, false, false); auto ry = __builtin_amdgcn_permlane32_swap(ay, by, false, false);
        const u32x4 v = {rx[0], ry[0], rx[1], ry[1]};
        if (row_ok) *(u32x4*)(orow + 64 * kh + 32 * dd + 8 * g) = v;
      }
    }
  }
}

#define DIAG 0
#define PFQ 1
#define PFKV 0
#define ABL 0
#define PFAT 1
#define DBGW 0
#define TOUCH 0
#define PF1 3
#define PF2 5
#define STR2(x) #x
#define STR(x) STR2(x)

template <int NIT>
__global__ __launch_bounds__(512, 2)
void ragged_attn_kernel(const void* Q, unsigned nQ, unsigned fQ, const void* K, unsigned nK, unsigned fK, const void* V, unsigned nV, unsigned fV,
                        const void* seg, unsigned nS, unsigned fS, void* O, unsigned nO, unsigned fO, int T, int lastc, int pfb, int pad, void* dbg) {
  __shared__ __attribute__((aligned(16))) char lds[LDS_BYTES];
  asm volatile(R"(
  .set NIT, %c3
  .set DIAG, )" STR(DIAG) R"(
  .set PFQ, )" STR(PFQ) R"(
  .set PFKV, )" STR(PFKV) R"(
  .set ABL, )" STR(ABL) R"(
  .set PFAT, )" STR(PFAT) R"(
  .set DBGW, )" STR(DBGW) R"(
  .set TOUCH, )" STR(TOUCH) R"(
  .set PF1, )" STR(PF1) R"(
  .set PF2, )" STR(PF2) R"(
  ; ---------------- SGPRs
  .set sQ, 8
  .set sK, 12
  .set sV, 16
  .set sS, 20
  .set sO, 24
  .set sT, 28
  .set sLASTC, 29
  .set sPFB, 30
  .set sF, 30
  .set sTM16, 31
  .set sG, 34
  .set sR, 35
  .set sNBIG, 36
  .set sTOT, 37
  .set sHOFF, 38
  .set sQ0, 39
  .set sQEND, 40
  .set sK0, 41
  .set sNK, 42
  .set sLEN, 43
  .set sMB, 44
  .set sNS, 45
  .set sU, 46
  .set sNU, 47
  .set sKOFF, 48
  .set sKSTEP, 49
  .set sVOFF, 50
  .set sKSO, 51
  .set sVSO, 52
  .set sKDST, 53
  .set sVDST, 54
  .set sMAXSO, 55
  .set sND, 56
  .set sMSTEP, 57
  .set sPEND, 58
  .set sW, 59
  .set sA, 60
  .set sB, 62
  .set sC, 64
  .set sT1, 66
  .set sT2, 67
  .set sT3, 68
  .set sT4, 69
  .set sCE, 70
  .set sTHR, 71
  .set sWD, 72
  .set sQB, 73
  .set sKH, 74
  .set sLIM, 75
  .set sJ, 76
  .set sQR, 77
  .set sRT, 78
  .set sSAFE, 64
  .set sNF, 65
  .set sNDF, 32+1
  .set sST, 80
  ; ---------------- VGPRs
  .set vLANE, 1
  .set vOK, 2
  .set vOK2, 3
  .set vOV, 4
  .set vOV2, 5
  .set vKB0, 6
  .set vKB1, 7
  .set vVB, 8
  .set vSTART, 9
  .set vLEN, 10
  .set vINCL, 11
  .set vEXCL, 12
  .set vM, 13
  .set vNMC, 14
  .set vL0, 15
  .set vL1, 16
  .set vKA0, 17
  .set vKA1, 18
  .set vVA, 19
  .set vAL, 20
  .set vSO, 21
  .set vHH4, 22
  .set vSW, 23
  .set vT0, 24
  .set vT1, 25
  .set vT2, 26
  .set vT3, 27
  .set vT4, 28
  .set vT5, 29
  .set vT6, 30
  .set vT7, 31
  .set vP, 32
  .set vX, 40
  .set vSR, 56
  .set vO, 64
  .set vQ, 128
  .set vSA, 160
  .set vSB, 176
  .set vK, 192
  .set vV, 224
  ; ---------------- LDS map
  .set LK, 0
  .set LV, 65536
  .set LX, 131072
  .set LQ2, 49152
  .set LQ3, 114688
  .set LSCR, 98304

  .macro STAMP i
    .if DIAG
      s_memtime s[sST+2*\i:sST+2*\i+1]
      s_waitcnt lgkmcnt(0)
    .endif
  .endm

  ; ======================================================================== entry
  s_load_dwordx16 s[8:23], %0, 0
  s_load_dwordx8 s[24:31], %0, 64
  .if DIAG
    s_memrealtime s[sRT:sRT+1]
  .endif
  STAMP 0
  v_and_b32 v[vLANE], 63, %2
  v_readfirstlane_b32 s[sW], %2
  v_lshlrev_b32 v[vT0], 2, %2                     ; tid*4
  s_and_b32 s[sG], %1, 7
  s_lshr_b32 s[sJ], %1, 3
  s_lshr_b32 s[sW], s[sW], 6
  ; prefetch offset: (tid>>5)*4096 + ((tid>>4)&1)*2048 + ((tid>>3)&1)*128 + (tid&7)*16 + g*256
  v_lshrrev_b32 v[vT6], 5, %2
  v_lshlrev_b32 v[vT6], 12, v[vT6]
  v_bfe_u32 v[vT7], %2, 4, 1
  v_lshl_add_u32 v[vT6], v[vT7], 11, v[vT6]
  v_bfe_u32 v[vT7], %2, 3, 1
  v_lshl_add_u32 v[vT6], v[vT7], 7, v[vT6]
  v_and_b32 v[vT7], 7, %2
  v_lshl_add_u32 v[vT6], v[vT7], 4, v[vT6]
  s_lshl_b32 s[sT3], s[sG], 8
  v_add_u32 v[vT6], s[sT3], v[vT6]
  ; DMA lane offsets
  v_bfe_u32 v[vT1], v[vLANE], 2, 3                ; prow
  v_lshrrev_b32 v[vT2], 5, v[vLANE]               ; hh / pcb
  v_and_b32 v[vT3], 3, v[vLANE]                   ; pc
  v_lshlrev_b32 v[vT4], 12, v[vT1]
  v_lshl_add_u32 v[vT4], v[vT2], 6, v[vT4]        ; prow*4096 + pcb*64
  v_lshl_add_u32 v[vOV], v[vT3], 4, v[vT4]
  v_bfe_u32 v[vT5], v[vLANE], 4, 1
  s_and_b32 s[sT4], s[sW], 1
  s_lshl_b32 s[sT4], s[sT4], 1
  v_or_b32 v[vT5], s[sT4], v[vT5]                 ; x
  v_xor_b32 v[vT5], v[vT5], v[vT3]
  v_lshl_add_u32 v[vOK], v[vT5], 4, v[vT4]
  s_waitcnt lgkmcnt(0)
  STAMP 1
  ; ======================================================================== S1: segment ends per 64-token chunk
  s_lshl_b32 s[sT2], s[sT], 2
  s_sub_u32 s[sT2], s[sT2], 8                     ; (T-2)*4
  v_add_u32 v[vT7], 2048*(NIT-1), v[vT0]
  v_min_u32 v[vT7], s[sT2], v[vT7]                ; last chunk: clamped (no out-of-range lanes)
  .set i, 0
  .rept NIT
    .if i == NIT-1
      buffer_load_dwordx2 v[vX+2*i:vX+2*i+1], v[vT7], s[sS:sS+3], 0 offen
    .elseif i < 2
      buffer_load_dwordx2 v[vX+2*i:vX+2*i+1], v[vT0], s[sS:sS+3], 0 offen offset:2048*i
    .else
      s_movk_i32 s[sT1], 2048*i
      buffer_load_dwordx2 v[vX+2*i:vX+2*i+1], v[vT0], s[sS:sS+3], s[sT1] offen
    .endif
    .set i, i+1
  .endr
  s_sub_u32 s[sTM16], s[sT], 16
  s_lshl_b32 s[sTM16], s[sTM16], 12               ; (T-16)*4096
  .macro PREFETCH lo, hi                            ; Q prefetch instructions lo..hi-1 (16 rows each)
    .set n, \lo
    .rept \hi-\lo
      s_mul_i32 s[sT3], s[sJ], s[sPFB]
      s_add_u32 s[sT3], s[sT3], n*16*4096
      s_min_u32 s[sT3], s[sT3], s[sTM16]
      buffer_load_dwordx4 v[vV+4*n:vV+4*n+3], v[vV+28], s[sQ:sQ+3], s[sT3] offen
      .set n, n+1
    .endr
  .endm
  v_mov_b32 v[vV+28], v[vT6]
  ; remaining lane constants (in the shadow of the loads)
  v_add_u32 v[vOK2], 128, v[vOK]
  v_add_u32 v[vOV2], 128, v[vOV]
  v_and_b32 v[vT1], 31, v[vLANE]                  ; r32
  v_lshrrev_b32 v[vT3], 3, v[vT1]
  v_lshlrev_b32 v[vT3], 11, v[vT3]
  v_and_b32 v[vT4], 7, v[vT1]
  v_lshl_add_u32 v[vT3], v[vT4], 6, v[vT3]
  v_bfe_u32 v[vT4], v[vT1], 2, 2
  v_xor_b32 v[vT4], v[vT4], v[vT2]
  v_lshl_add_u32 v[vKB0], v[vT4], 4, v[vT3]
  v_xor_b32 v[vKB1], 32, v[vKB0]
  v_bfe_u32 v[vT3], v[vLANE], 2, 2
  v_lshl_add_u32 v[vT3], v[vT2], 2, v[vT3]
  v_lshlrev_b32 v[vT3], 6, v[vT3]
  v_bfe_u32 v[vT4], v[vLANE], 4, 1
  v_lshl_add_u32 v[vT3], v[vT4], 5, v[vT3]
  v_and_b32 v[vT4], 3, v[vLANE]
  v_lshl_add_u32 v[vVB], v[vT4], 3, v[vT3]
  v_add_u32 v[vVB], LV, v[vVB]
  v_lshlrev_b32 v[vHH4], 2, v[vT2]
  ; output staging: write address  r32*128 + ((2*? + hh) ^ (r32&7))*16 handled per block: base = r32*128, xor term below
  v_and_b32 v[vT3], 7, v[vT1]
  v_xor_b32 v[vT3], v[vT3], v[vT2]                ; (r32&7) ^ hh
  v_lshlrev_b32 v[vT3], 4, v[vT3]
  v_lshl_add_u32 v[vSW], v[vT1], 7, v[vT3]        ; write addr for chunk pair 0 (chunk = hh); chunk pair p: xor with p*32
  ; read address: row = lane>>3, chunk = (lane&7) ^ (row&7) ; rows + 8i -> + 1024 i
  v_lshrrev_b32 v[vT3], 3, v[vLANE]
  v_and_b32 v[vT4], 7, v[vLANE]
  v_xor_b32 v[vT5], v[vT4], v[vT3]
  v_lshlrev_b32 v[vT5], 4, v[vT5]
  v_lshl_add_u32 v[vSR], v[vT3], 7, v[vT5]
  ; global store lane offset: (lane>>3)*4096 + (lane&7)*16
  v_lshlrev_b32 v[vT4], 4, v[vT4]
  v_lshl_add_u32 v[vSO], v[vT3], 12, v[vT4]
  s_mov_b32 s[sCE], 0x3e0293ee
  s_mov_b32 s[sTHR], 0x427af232
  s_lshl_b32 s[sWD], s[sW], 11                    ; w*2048
  s_mov_b64 s[sC:sC+1], 0
  v_mov_b32 v[vT4], 0
  v_mov_b32 v[vT5], 0
  v_lshlrev_b32 v[vT7], 7, v[vLANE]               ; LDS address of chunk (w + 8*lane): 16 B each
  s_lshl_b32 s[sT1], s[sW], 4
  s_add_u32 s[sT1], s[sT1], LSCR
  v_add_u32 v[vT7], s[sT1], v[vT7]
  PREFETCH 0, PF1
  .set i, 0
  .rept NIT
    s_waitcnt vmcnt(NIT-1-i+PF1)
    v_cmp_ne_u32 s[sA:sA+1], v[vX+2*i], v[vX+2*i+1]
    v_cmp_gt_i32 vcc, v[vX+2*i], v[vX+2*i+1]
    .if i == NIT-1
      v_cmp_gt_i32 s[sB:sB+1], s[sLASTC], %2       ; element < T-1
      s_and_b64 s[sA:sA+1], s[sA:sA+1], s[sB:sB+1]
      s_and_b64 vcc, vcc, s[sB:sB+1]
      v_cmp_eq_u32 s[sB:sB+1], s[sLASTC], %2       ; element == T-1
      s_or_b64 s[sA:sA+1], s[sA:sA+1], s[sB:sB+1]
    .endif
    s_or_b64 s[sC:sC+1], s[sC:sC+1], vcc
    s_nop 0
    v_writelane_b32 v[vT4], s[sA], i
    v_writelane_b32 v[vT5], s[sA+1], i
    .set i, i+1
  .endr
  s_or_b32 s[sT1], s[sC], s[sC+1]
  v_mov_b32 v[vT6], s[sT1]
  s_mov_b64 exec, (1<<NIT)-1
  ds_write_b96 v[vT7], v[vT4:vT6]
  s_mov_b64 exec, -1
  v_mov_b32 v[vT0], 0
  v_mov_b32 v[vT1], 0
  v_mov_b32 v[vT2], 0
  v_lshlrev_b32 v[vT3], 4, v[vLANE]
  v_add_u32 v[vT3], LSCR, v[vT3]
  v_lshlrev_b32 v[vT7], 6, v[vLANE]
  v_add_u32 v[vT7], 1, v[vT7]                     ; lane*64 + 1
  PREFETCH PF1, PF2
  s_waitcnt lgkmcnt(0)
  s_barrier
  STAMP 2
  ; ======================================================================== S2
  s_bfm_b64 exec, 8*NIT, 0
  ds_read_b96 v[vT0:vT2], v[vT3]
  s_mov_b64 exec, -1
  s_movk_i32 s[sT1], 128
  s_movk_i32 s[sT2], 256
  s_mov_b32 s[sR], 0
  s_waitcnt lgkmcnt(0)
  v_cmp_ne_u32 vcc, 0, v[vT2]
  v_bcnt_u32_b32 v[vT3], v[vT0], 0
  v_bcnt_u32_b32 v[vT3], v[vT1], v[vT3]
  s_cbranch_vccnz .Lfallback
  v_cmp_lt_u32 vcc, 1, v[vT3]
  v_ffbl_b32 v[vT4], v[vT0]
  v_ffbl_b32 v[vT5], v[vT1]
  s_cbranch_vccnz .Lfallback
  v_cmp_ne_u32 s[sA:sA+1], 0, v[vT3]
  v_add_u32 v[vT5], 32, v[vT5]
  v_min_u32 v[vT4], v[vT4], v[vT5]
  v_add_u32 v[vT4], v[vT4], v[vT7]
  v_cndmask_b32 v[vT4], 0, v[vT4], s[sA:sA+1]     ; end (0 if none)
  s_nop 1
  v_mov_b32_dpp v[vSTART], v[vT4] wave_shr:1 row_mask:0xf bank_mask:0xf bound_ctrl:0
  s_nop 1
  v_max_u32_dpp v[vSTART], v[vSTART], v[vSTART] row_shr:1 row_mask:0xf bank_mask:0xf bound_ctrl:0
  s_nop 1
  v_max_u32_dpp v[vSTART], v[vSTART], v[vSTART] row_shr:2 row_mask:0xf bank_mask:0xf bound_ctrl:0
  s_nop 1
  v_max_u32_dpp v[vSTART], v[vSTART], v[vSTART] row_shr:4 row_mask:0xf bank_mask:0xf bound_ctrl:0
  s_nop 1
  v_max_u32_dpp v[vSTART], v[vSTART], v[vSTART] row_shr:8 row_mask:0xf bank_mask:0xf bound_ctrl:0
  s_nop 1
  v_max_u32_dpp v[vSTART], v[vSTART], v[vSTART] row_bcast:15 row_mask:0xa bank_mask:0xf
  s_nop 1
  v_max_u32_dpp v[vSTART], v[vSTART], v[vSTART] row_bcast:31 row_mask:0xc bank_mask:0xf
  s_nop 0
  v_sub_u32 v[vLEN], v[vT4], v[vSTART]
  v_cndmask_b32 v[vLEN], 0, v[vLEN], s[sA:sA+1]   ; len (0 if none)
  v_add_u32 v[vT5], 127, v[vLEN]
  v_lshrrev_b32 v[vT5], 7, v[vT5]                 ; nq (mode A)
  v_cmp_lt_u32 vcc, s[sT1], v[vLEN]               ; len > 128
  v_cmp_ge_u32 s[sB:sB+1], s[sT2], v[vLEN]        ; len <= 256
  s_and_b64 s[sB:sB+1], s[sB:sB+1], vcc           ; mode B
  v_lshlrev_b32 v[vT5], 1, v[vT5]
  s_nop 0
  v_cndmask_b32 v[vT5], v[vT5], 2, s[sB:sB+1]     ; items (2 heads)
  v_lshlrev_b32 v[vT6], 16, v[vT5]
  v_cndmask_b32 v[vINCL], v[vT6], v[vT5], vcc     ; small -> high half
  v_mov_b32 v[vEXCL], v[vINCL]
  s_nop 0
  v_add_u32_dpp v[vINCL], v[vINCL], v[vINCL] row_shr:1 row_mask:0xf bank_mask:0xf bound_ctrl:0
  s_nop 1
  v_add_u32_dpp v[vINCL], v[vINCL], v[vINCL] row_shr:2 row_mask:0xf bank_mask:0xf bound_ctrl:0
  s_nop 1
  v_add_u32_dpp v[vINCL], v[vINCL], v[vINCL] row_shr:4 row_mask:0xf bank_mask:0xf bound_ctrl:0
  s_nop 1
  v_add_u32_dpp v[vINCL], v[vINCL], v[vINCL] row_shr:8 row_mask:0xf bank_mask:0xf bound_ctrl:0
  s_nop 1
  v_add_u32_dpp v[vINCL], v[vINCL], v[vINCL] row_bcast:15 row_mask:0xa bank_mask:0xf
  s_nop 1
  v_add_u32_dpp v[vINCL], v[vINCL], v[vINCL] row_bcast:31 row_mask:0xc bank_mask:0xf
  s_nop 0
  v_sub_u32 v[vEXCL], v[vINCL], v[vEXCL]
  v_readlane_b32 s[sT1], v[vINCL], 63
  s_and_b32 s[sNBIG], s[sT1], 0xffff
  s_lshr_b32 s[sTOT], s[sT1], 16
  s_add_u32 s[sTOT], s[sTOT], s[sNBIG]
  .if DBGW & 8
  s_mov_b32 s[sSAFE], 1
  .else
  s_mov_b32 s[sSAFE], 0
  .endif
  STAMP 3

  ; ---- DMA pieces.  sKDST / sVDST: ring offset + w*2048 of the next tile; rows beyond the segment end are out of range (zero fill, no traffic)
  .macro DK1
    s_mov_b32 m0, s[sKDST]
    s_add_u32 s[sT1], s[sKDST], 16384
    buffer_load_dwordx4 v[vOK], s[sK:sK+3], s[sKSO] offen lds
    .if DBGW & 4
    s_nop 3
    .endif
  .endm
  .macro DK2
    s_add_u32 m0, s[sKDST], 1024
    s_and_b32 s[sKDST], s[sT1], 0xffff
    buffer_load_dwordx4 v[vOK2], s[sK:sK+3], s[sKSO] offen lds
    .if DBGW & 4
    s_nop 3
    .endif
    s_add_u32 s[sKSO], s[sKSO], 64*4096
    s_min_u32 s[sKSO], s[sKSO], s[sMAXSO]
  .endm
  .macro DV1
    s_add_u32 m0, s[sVDST], LV
    s_add_u32 s[sT3], s[sVDST], 16384
    buffer_load_dwordx4 v[vOV], s[sV:sV+3], s[sVSO] offen lds
    .if DBGW & 4
    s_nop 3
    .endif
  .endm
  .macro DV2
    s_add_u32 m0, s[sVDST], LV+1024
    s_and_b32 s[sVDST], s[sT3], 0xffff
    buffer_load_dwordx4 v[vOV2], s[sV:sV+3], s[sVSO] offen lds
    .if DBGW & 4
    s_nop 3
    .endif
    s_add_u32 s[sVSO], s[sVSO], 64*4096
    s_min_u32 s[sVSO], s[sVSO], s[sMAXSO]
  .endm
  .macro DALL
    DK1
    DK2
    DV1
    DV2
  .endm
  .macro BAR4 modea=0, bit=2                        ; previous group had 4 pieces: everything older has landed
    .if \modea
      s_cmp_eq_u32 s[sMB], 1
      s_cbranch_scc1 .Lnob\@
    .endif
    .if DBGW & \bit
    s_waitcnt vmcnt(0) lgkmcnt(0)
    .else
    s_waitcnt vmcnt(4) lgkmcnt(0)
    .endif
    s_barrier
    .if \modea
    s_branch .Lbd\@
  .Lnob\@:
    s_waitcnt lgkmcnt(0)
  .Lbd\@:
    .endif
  .endm
  .macro BARN modea=0                               ; nothing was issued behind the needed pieces
    .if \modea
      s_cmp_eq_u32 s[sMB], 1
      s_cbranch_scc1 .Lnob\@
    .endif
    s_waitcnt vmcnt(0) lgkmcnt(0)
    s_barrier
    .if \modea
    s_branch .Lbd\@
  .Lnob\@:
    s_waitcnt lgkmcnt(0)
  .Lbd\@:
    .endif
  .endm
  .macro BAR0 modea=0
    .if \modea
      s_cmp_eq_u32 s[sMB], 1
      s_cbranch_scc1 .Lnob\@
    .endif
    s_waitcnt vmcnt(0) lgkmcnt(0)
    s_barrier
    .if \modea
  .Lnob\@:
    .endif
  .endm

.Litem:
  ; idx = r*32 + (r odd ? 31-j : j)
  s_lshl_b32 s[sT1], s[sR], 5
  s_sub_u32 s[sT2], 31, s[sJ]
  s_bitcmp1_b32 s[sR], 0
  s_cselect_b32 s[sT2], s[sT2], s[sJ]
  s_add_u32 s[sT1], s[sT1], s[sT2]                ; idx
  s_cmp_ge_u32 s[sT1], s[sTOT]
  s_cbranch_scc1 .Ldone
  s_sub_u32 s[sT2], s[sT1], s[sNBIG]
  s_cmp_lt_u32 s[sT1], s[sNBIG]
  s_cselect_b32 s[sT1], s[sT1], s[sT2]            ; index inside its class
  s_cselect_b32 s[sT2], 0, 16                     ; field shift
  v_bfe_u32 v[vT0], v[vINCL], s[sT2], 16
  v_bfe_u32 v[vT1], v[vEXCL], s[sT2], 16
  v_cmp_lt_u32 s[sA:sA+1], s[sT1], v[vT0]
  v_cmp_ge_u32 vcc, s[sT1], v[vT1]
  s_and_b64 s[sA:sA+1], s[sA:sA+1], vcc
  s_ff1_i32_b64 s[sT2], s[sA:sA+1]
  v_readlane_b32 s[sK0], v[vSTART], s[sT2]
  v_readlane_b32 s[sLEN], v[vLEN], s[sT2]
  v_readlane_b32 s[sT3], v[vT1], s[sT2]
  s_sub_u32 s[sT1], s[sT1], s[sT3]                ; local item
  s_and_b32 s[sT2], s[sT1], 1
  s_lshl_b32 s[sT2], s[sT2], 3
  s_add_u32 s[sT2], s[sT2], s[sG]
  s_lshl_b32 s[sHOFF], s[sT2], 8                  ; head*256 bytes
  s_lshr_b32 s[sT1], s[sT1], 1
  s_lshl_b32 s[sT1], s[sT1], 7
  s_add_u32 s[sQ0], s[sK0], s[sT1]
  s_lshl_b32 s[sT1], s[sW], 3
  s_add_u32 s[sT2], s[sQ0], s[sT1]
  s_lshl_b32 s[sT2], s[sT2], 12
  s_add_u32 s[sT2], s[sT2], s[sHOFF]              ; (q0 + 8w)*4096 + hoff
  ; ---- Q DMA
  s_add_u32 m0, s[sWD], LX
  s_add_u32 s[sT3], s[sT2], 64*4096
  buffer_load_dwordx4 v[vOK], s[sQ:sQ+3], s[sT2] offen lds
  s_add_u32 m0, s[sWD], LX+1024
  s_add_u32 s[sT4], s[sT2], 128*4096
  buffer_load_dwordx4 v[vOK2], s[sQ:sQ+3], s[sT2] offen lds
  s_add_u32 m0, s[sWD], LX+16384
  s_add_u32 s[sT1], s[sK0], s[sT1]
  buffer_load_dwordx4 v[vOK], s[sQ:sQ+3], s[sT3] offen lds
  s_add_u32 m0, s[sWD], LX+16384+1024
  s_lshl_b32 s[sT1], s[sT1], 12
  buffer_load_dwordx4 v[vOK2], s[sQ:sQ+3], s[sT3] offen lds
  s_add_u32 s[sKSO], s[sT1], s[sHOFF]             ; (k0 + 8w)*4096 + hoff
  s_add_u32 s[sMAXSO], s[sTM16], 15*4096
  s_add_u32 s[sMAXSO], s[sMAXSO], s[sHOFF]        ; (T-1)*4096 + hoff
  s_min_u32 s[sKSO], s[sKSO], s[sMAXSO]
  s_mov_b32 s[sVSO], s[sKSO]
  s_add_u32 s[sQEND], s[sK0], s[sLEN]
  s_add_u32 s[sNK], s[sLEN], 63
  s_lshr_b32 s[sNK], s[sNK], 6
  s_lshl_b32 s[sO+2], s[sQEND], 12                ; the output descriptor ends at the segment end
  s_sub_u32 s[sT1], s[sLEN], 129
  s_cmp_lt_u32 s[sT1], 128
  s_cselect_b32 s[sMB], 1, 0                      ; mode B: 128 < len <= 256
  s_mov_b32 s[sKDST], s[sWD]
  s_mov_b32 s[sVDST], s[sWD]
  s_cbranch_scc0 .Lq_done
  s_add_u32 m0, s[sWD], LQ2
  s_add_u32 s[sT3], s[sT4], 64*4096
  buffer_load_dwordx4 v[vOK], s[sQ:sQ+3], s[sT4] offen lds
  s_add_u32 m0, s[sWD], LQ2+1024
  s_nop 0
  buffer_load_dwordx4 v[vOK2], s[sQ:sQ+3], s[sT4] offen lds
  s_add_u32 m0, s[sWD], LQ3
  s_nop 0
  buffer_load_dwordx4 v[vOK], s[sQ:sQ+3], s[sT3] offen lds
  s_add_u32 m0, s[sWD], LQ3+1024
  s_nop 0
  buffer_load_dwordx4 v[vOK2], s[sQ:sQ+3], s[sT3] offen lds
  DK1
  DK2
  s_branch .Lk_done
.Lq_done:
  DK1
  DK2
  DK1
  DK2
.Lk_done:
  STAMP 4
  ; ---- per-wave geometry
  s_and_b32 s[sT1], s[sW], 3
  s_lshr_b32 s[sT2], s[sW], 2
  s_cmp_eq_u32 s[sMB], 1
  s_cselect_b32 s[sQB], s[sW], s[sT1]             ; q block
  s_cselect_b32 s[sKH], 0, s[sT2]                 ; key half (mode A)
  s_movk_i32 s[sKSTEP], 0x4000
  s_cselect_b32 s[sKSTEP], 8192, s[sKSTEP]
  s_add_u32 s[sT1], s[sLEN], 31
  s_lshr_b32 s[sT1], s[sT1], 5                    ; nst = ceil(len/32)
  s_add_u32 s[sT2], s[sT1], 1
  s_sub_u32 s[sT2], s[sT2], s[sKH]
  s_lshr_b32 s[sT2], s[sT2], 1
  s_cmp_eq_u32 s[sMB], 1
  s_cselect_b32 s[sNS], s[sT1], s[sT2]            ; steps of this wave
  s_cselect_b32 s[sNU], s[sT1], s[sNK]            ; step slots
  s_and_b32 s[sLIM], s[sLEN], 31
  s_sub_u32 s[sT2], s[sT1], 1
  s_and_b32 s[sT2], s[sT2], 1
  s_cmp_eq_u32 s[sT2], s[sKH]
  s_cselect_b32 s[sT3], 1, s[sMB]                 ; owner of the last sub-tile
  s_cmp_lg_u32 s[sLIM], 0
  s_cselect_b32 s[sT3], s[sT3], 0
  s_sub_u32 s[sMSTEP], s[sNS], 1
  s_cmp_eq_u32 s[sT3], 1
  s_cselect_b32 s[sMSTEP], s[sMSTEP], -1
  s_lshl_b32 s[sT1], s[sQB], 5
  s_add_u32 s[sQR], s[sT1], s[sQ0]                ; first q row of this wave
  s_cmp_lt_u32 s[sQR], s[sQEND]
  s_cselect_b32 s[sNS], s[sNS], 0
  ; FULL slots with DMA: tile iteration t <= nk-3
  s_sub_i32 s[sND], s[sNK], 2
  s_max_i32 s[sND], s[sND], 0
  s_lshl_b32 s[sND], s[sND], s[sMB]
  ; Q read base
  s_lshl_b32 s[sT1], s[sQB], 13
  s_add_u32 s[sT2], s[sT1], LX                    ; qb < 4
  s_add_u32 s[sT3], s[sT1], LQ2-4*8192            ; qb 4,5
  s_add_u32 s[sT4], s[sT1], LQ3-6*8192            ; qb 6,7
  s_cmp_lt_u32 s[sQB], 6
  s_cselect_b32 s[sT3], s[sT3], s[sT4]
  s_cmp_lt_u32 s[sQB], 4
  s_cselect_b32 s[sT2], s[sT2], s[sT3]
  v_add_u32 v[vT0], s[sT2], v[vKB0]
  v_add_u32 v[vT1], s[sT2], v[vKB1]
  s_lshl_b32 s[sKOFF], s[sKH], 13
  s_mov_b32 s[sVOFF], s[sKOFF]
  v_add_u32 v[vKA0], s[sKOFF], v[vKB0]
  v_add_u32 v[vKA1], s[sKOFF], v[vKB1]
  ; group 0
  DALL
  .set i, 0
  .rept 64
    v_mov_b32 v[vO+i], 0
    .set i, i+1
  .endr
  v_mov_b32 v[vL0], 0
  v_mov_b32 v[vL1], 0
  v_mov_b32 v[vM], 0xf149f2ca
  s_mov_b32 s[sPEND], 0
  s_mov_b32 s[sU], 0
  BAR4 0, 1                                         ; barrier P: Q, K0 (K1) landed
  STAMP 5

  .macro KREAD i
    .if (ABL & 8)
    .elseif (\i & 1)
      ds_read_b128 v[vK+4*\i:vK+4*\i+3], v[vKA1] offset:512*(\i>>1)
    .else
      ds_read_b128 v[vK+4*\i:vK+4*\i+3], v[vKA0] offset:512*(\i>>1)
    .endif
  .endm
  .macro KADV                                       ; advance K read addresses to the next step
    s_add_u32 s[sKOFF], s[sKOFF], s[sKSTEP]
    s_and_b32 s[sKOFF], s[sKOFF], 0xffff
    v_add_u32 v[vKA0], s[sKOFF], v[vKB0]
    v_add_u32 v[vKA1], s[sKOFF], v[vKB1]
  .endm
  .macro QKM c, i
    .if \i == 0
      v_mfma_f32_32x32x16_bf16 v[\c:\c+15], v[vK:vK+3], v[vQ:vQ+3], 0
    .else
      v_mfma_f32_32x32x16_bf16 v[\c:\c+15], v[vK+4*\i:vK+4*\i+3], v[vQ+4*\i:vQ+4*\i+3], v[\c:\c+15]
    .endif
  .endm
  .macro VREAD j                                    ; V fragment j: d = j&3, ks = j>>2
    .if (ABL & 4) == 0
    ds_read_b64_tr_b16 v[vV+4*\j:vV+4*\j+1], v[vVA] offset:512*(\j&3)+4096*(\j>>2)
    ds_read_b64_tr_b16 v[vV+4*\j+2:vV+4*\j+3], v[vVA] offset:512*(\j&3)+4096*(\j>>2)+2048
    .endif
  .endm
  .macro PVM j
    v_mfma_f32_32x32x16_bf16 v[vO+16*(\j&3):vO+16*(\j&3)+15], v[vV+4*\j:vV+4*\j+3], v[vP+4*(\j>>2):vP+4*(\j>>2)+3], v[vO+16*(\j&3):vO+16*(\j&3)+15]
  .endm
  .macro MASK c                                     ; keys >= lim of this sub-tile -> -inf
    v_sub_u32 v[vT2], s[sLIM], v[vHH4]
    v_mov_b32 v[vT3], 0xff800000
    .set r, 0
    .rept 16
      v_cmp_lt_i32 vcc, (r&3)+8*(r>>2), v[vT2]
      s_nop 1
      v_cndmask_b32 v[\c+r], v[vT3], v[\c+r], vcc
      .set r, r+1
    .endr
  .endm
  .macro ROWMAX1 c
    v_max3_f32 v[vT2], v[\c+0], v[\c+1], v[\c+2]
    v_max3_f32 v[vT3], v[\c+3], v[\c+4], v[\c+5]
    v_max3_f32 v[vT2], v[vT2], v[\c+6], v[\c+7]
    v_max3_f32 v[vT3], v[vT3], v[\c+8], v[\c+9]
    v_max3_f32 v[vT2], v[vT2], v[\c+10], v[\c+11]
    v_max3_f32 v[vT3], v[vT3], v[\c+12], v[\c+13]
    v_max3_f32 v[vT2], v[vT2], v[\c+14], v[\c+15]
    v_max_f32 v[vT2], v[vT2], v[vT3]
    v_mov_b32 v[vT3], v[vT2]
  .endm
  .macro ROWMAX2                                    ; needs 2 wait states after ROWMAX1
    v_permlane32_swap_b32 v[vT2], v[vT3]
    s_nop 0
    v_max_f32 v[vT2], v[vT2], v[vT3]                ; vT2 = row max
  .endm
  .macro EXPE c, r
    .if (ABL & 2) == 0
    v_fma_f32 v[\c+(\r)], v[\c+(\r)], s[sCE], v[vNMC]
    v_exp_f32 v[\c+(\r)], v[\c+(\r)]
    .endif
  .endm
  .macro SUMPK p, i                                 ; row sums + pack of elements 2i, 2i+1
    .if (ABL & 32) == 0
    v_add_f32 v[vL0], v[vL0], v[\p+2*\i]
    v_add_f32 v[vL1], v[vL1], v[\p+2*\i+1]
    v_cvt_pk_bf16_f32 v[vP+\i], v[\p+2*\i], v[\p+2*\i+1]
    .endif
  .endm
  .macro ADDE p, e                                  ; row sum of element e (two chains)
    .if (ABL & 32) == 0
    .if ((\e) & 1)
      v_add_f32 v[vL1], v[vL1], v[\p+(\e)]
    .else
      v_add_f32 v[vL0], v[vL0], v[\p+(\e)]
    .endif
    .endif
  .endm
  .macro CVTE p, i                                  ; pack of elements 2i, 2i+1
    .if (ABL & 32) == 0
    v_cvt_pk_bf16_f32 v[vP+\i], v[\p+2*\i], v[\p+2*\i+1]
    .endif
  .endm
  .macro VADDR                                      ; V read address for the current step, then advance
    v_add_u32 v[vVA], s[sVOFF], v[vVB]
    s_add_u32 s[sVOFF], s[sVOFF], s[sKSTEP]
    s_and_b32 s[sVOFF], s[sVOFF], 0xffff
  .endm
  .macro RESCALE
    s_nop 7
    s_nop 7
    .set i, 0
    .rept 64
      v_mul_f32 v[vO+i], v[vAL], v[vO+i]
      .set i, i+1
    .endr
    v_mul_f32 v[vL0], v[vAL], v[vL0]
    v_mul_f32 v[vL1], v[vAL], v[vL1]
    s_mov_b32 s[sPEND], 0
  .endm

  ; ---- prologue compute: step 0
  s_cmp_eq_u32 s[sNS], 0
  s_cbranch_scc1 .Lpro_none
  .set i, 0
  .rept 8
    KREAD i
    .if (i & 1)
      ds_read_b128 v[vQ+4*i:vQ+4*i+3], v[vT1] offset:512*(i>>1)
    .else
      ds_read_b128 v[vQ+4*i:vQ+4*i+3], v[vT0] offset:512*(i>>1)
    .endif
    .set i, i+1
  .endr
  KADV
  .set i, 0
  .rept 8
    .if i == 0
      s_waitcnt lgkmcnt(14)
    .elseif i == 1
      s_waitcnt lgkmcnt(13)
    .elseif i == 2
      s_waitcnt lgkmcnt(12)
    .elseif i == 3
      s_waitcnt lgkmcnt(11)
    .elseif i == 4
      s_waitcnt lgkmcnt(10)
    .elseif i == 5
      s_waitcnt lgkmcnt(9)
    .elseif i == 6
      s_waitcnt lgkmcnt(8)
    .else
      s_waitcnt lgkmcnt(7)
    .endif
    QKM vSA, i
    KREAD i
    .if i == 1
      DK1
    .elseif i == 3
      DK2
    .elseif i == 5
      DV1
    .elseif i == 7
      DV2
    .endif
    .set i, i+1
  .endr
  KADV
  s_nop 7
  s_cmp_lg_u32 s[sMSTEP], 0
  s_cbranch_scc1 .Lnomask0
  MASK vSA
.Lnomask0:
  ROWMAX1 vSA
  s_nop 1
  ROWMAX2
  v_max_f32 v[vM], v[vM], v[vT2]
  v_mul_f32 v[vNMC], 0xbe0293ee, v[vM]
  s_sub_u32 s[sNF], s[sNS], 1                     ; FULL slots of this wave
  s_min_u32 s[sNDF], s[sND], s[sNF]               ; ... of which issue DMA
  VADDR
  s_cmp_eq_u32 s[sSAFE], 1
  s_cbranch_scc1 .Lpro_safe
  .set r, 0
  .rept 11
    EXPE vSA, r
    .set r, r+1
  .endr
  .set r, 0
  .rept 4
    ADDE vSA, r
    .set r, r+1
  .endr
  STAMP 6
  s_cmp_lt_u32 s[sU], s[sNDF]
  s_cbranch_scc1 .LloopF
  s_branch .LdispF0
.Lpro_safe:
  .set r, 0
  .rept 16
    EXPE vSA, r
    .set r, r+1
  .endr
  s_cmp_eq_u32 s[sNF], 0
  s_cbranch_scc1 .LlastS0
  s_branch .LloopS
.Lpro_none:
  DALL
  s_branch .Lnone

  ; ======================================================================== SAFE step (online max every step)
  .macro FULLS cur, prev, modea
    s_cmp_lt_u32 s[sU], s[sND]
    s_cselect_b32 s[sF], 1, 0
    s_cbranch_scc0 .Lbn\@
    BAR4 \modea
    s_branch .Lbq\@
  .Lbn\@:
    BARN \modea
  .Lbq\@:
    .set g, 0
    .rept 8
      QKM \cur, g
      VREAD g
      SUMPK \prev, g
      .if g == 1
        DMAK1 \modea, 2
      .elseif g == 5
        DMAK2 \modea, 2
      .endif
      .set g, g+1
    .endr
    s_waitcnt lgkmcnt(4)
    PVM 0
    KREAD 0
    KREAD 1
    s_add_u32 s[sU], s[sU], 1
    s_nop 0
    PVM 1
    s_cmp_lg_u32 s[sMSTEP], s[sU]
    s_cbranch_scc1 .Lnm\@
    MASK \cur
  .Lnm\@:
    ROWMAX1 \cur
    PVM 2
    KREAD 2
    ROWMAX2
    v_sub_f32 v[vT3], v[vT2], v[vM]
    v_cmp_lt_f32 vcc, s[sTHR], v[vT3]
    s_cbranch_vccz .Lkeep\@
    v_max_f32 v[vT3], v[vM], v[vT2]
    v_sub_f32 v[vAL], v[vM], v[vT3]
    v_mul_f32 v[vAL], s[sCE], v[vAL]
    v_exp_f32 v[vAL], v[vAL]
    v_mov_b32 v[vM], v[vT3]
    v_mul_f32 v[vNMC], 0xbe0293ee, v[vM]
    s_mov_b32 s[sPEND], 1
  .Lkeep\@:
    PVM 3
    KREAD 3
    DMAV1 \modea, 2
    EXPE \cur, 0
    EXPE \cur, 1
    EXPE \cur, 2
    PVM 4
    KREAD 4
    EXPE \cur, 3
    EXPE \cur, 4
    EXPE \cur, 5
    PVM 5
    KREAD 5
    EXPE \cur, 6
    EXPE \cur, 7
    EXPE \cur, 8
    s_waitcnt lgkmcnt(6)
    PVM 6
    KREAD 6
    DMAV2 \modea, 2
    EXPE \cur, 9
    EXPE \cur, 10
    EXPE \cur, 11
    PVM 7
    KREAD 7
    EXPE \cur, 12
    EXPE \cur, 13
    EXPE \cur, 14
    EXPE \cur, 15
    KADV
    VADDR
    s_cmp_eq_u32 s[sPEND], 0
    s_cbranch_scc1 .Lsend\@
    RESCALE
  .Lsend\@:
    s_cmp_eq_u32 s[sU], s[sNF]
  .endm
  .macro LASTS prev, modea
    BAR0 \modea
    .set g, 0
    .rept 8
      VREAD g
      SUMPK \prev, g
      .set g, g+1
    .endr
    s_add_u32 s[sU], s[sU], 1
    s_waitcnt lgkmcnt(0)
    .set g, 0
    .rept 8
      PVM g
      .set g, g+1
    .endr
    s_branch .Lnone_tail
  .endm

  ; DMA slots inside a step: odd slots of mode B issue nothing.  dma: 0 none, 1 always, 2 run-time flag sF
  .macro DMAK1 modea, dma=1
    .if \dma
    .if \dma == 2
      s_cmp_eq_u32 s[sF], 0
      s_cbranch_scc1 .Ld\@
    .endif
    .if \modea
      s_cmp_eq_u32 s[sMB], 1
      s_cbranch_scc1 .Ld\@
    .endif
    DK1
  .Ld\@:
    .endif
  .endm
  .macro DMAK2 modea, dma=1
    .if \dma
    .if \dma == 2
      s_cmp_eq_u32 s[sF], 0
      s_cbranch_scc1 .Ld\@
    .endif
    .if \modea
      s_cmp_eq_u32 s[sMB], 1
      s_cbranch_scc1 .Ld\@
    .endif
    DK2
  .Ld\@:
    .endif
  .endm
  .macro DMAV1 modea, dma=1
    .if \dma
    .if \dma == 2
      s_cmp_eq_u32 s[sF], 0
      s_cbranch_scc1 .Ld\@
    .endif
    .if \modea
      s_cmp_eq_u32 s[sMB], 1
      s_cbranch_scc1 .Ld\@
    .endif
    DV1
  .Ld\@:
    .endif
  .endm
  .macro DMAV2 modea, dma=1
    .if \dma
    .if \dma == 2
      s_cmp_eq_u32 s[sF], 0
      s_cbranch_scc1 .Ld\@
    .endif
    .if \modea
      s_cmp_eq_u32 s[sMB], 1
      s_cbranch_scc1 .Ld\@
    .endif
    DV2
  .Ld\@:
    .endif
  .endm

  ; ======================================================================== FAST step (reference = row max of the wave's first sub-tile;
  ; overflow is detected on the row sums at the end of the item and the item is redone with the SAFE steps)
  ; prev enters with exp done for e0..e10 and e0..e3 summed; vVA is set for this step
  .macro FULLF cur, prev, modea, dma
    .if \dma
      BAR4 \modea
    .else
      BARN \modea
    .endif
    ; ---------------- phase A
    QKM \cur, 0
    VREAD 0
    EXPE \prev, 11
    CVTE \prev, 0
    QKM \cur, 1
    VREAD 1
    EXPE \prev, 12
    CVTE \prev, 1
    QKM \cur, 2
    VREAD 2
    DMAK1 \modea, \dma
    EXPE \prev, 13
    CVTE \prev, 2
    QKM \cur, 3
    VREAD 3
    EXPE \prev, 14
    CVTE \prev, 3
    ADDE \prev, 4
    QKM \cur, 4
    VREAD 4
    EXPE \prev, 15
    CVTE \prev, 4
    ADDE \prev, 5
    QKM \cur, 5
    VREAD 5
    CVTE \prev, 5
    CVTE \prev, 6
    ADDE \prev, 6
    ADDE \prev, 7
    ADDE \prev, 8
    QKM \cur, 6
    VREAD 6
    DMAK2 \modea, \dma
    CVTE \prev, 7
    ADDE \prev, 9
    ADDE \prev, 10
    ADDE \prev, 11
    QKM \cur, 7
    VREAD 7
    ADDE \prev, 12
    ADDE \prev, 13
    ADDE \prev, 14
    ADDE \prev, 15
    ; ---------------- phase B
    s_waitcnt lgkmcnt(4)
    PVM 0
    KREAD 0
    s_add_u32 s[sU], s[sU], 1
    DMAV1 \modea, \dma
    s_cmp_lg_u32 s[sMSTEP], s[sU]
    s_cbranch_scc1 .Lnm\@
    MASK \cur
  .Lnm\@:
    EXPE \cur, 0
    PVM 1
    KREAD 1
    EXPE \cur, 1
    EXPE \cur, 2
    PVM 2
    KREAD 2
    KREAD 3
    EXPE \cur, 3
    ADDE \cur, 0
    PVM 3
    KREAD 4
    EXPE \cur, 4
    ADDE \cur, 1
    ADDE \cur, 2
    PVM 4
    KREAD 5
    EXPE \cur, 5
    EXPE \cur, 6
    PVM 5
    KREAD 6
    KREAD 7
    EXPE \cur, 7
    ADDE \cur, 3
    s_waitcnt lgkmcnt(8)
    PVM 6
    DMAV2 \modea, \dma
    EXPE \cur, 8
    EXPE \cur, 9
    KADV
    VADDR
    .if \dma
      s_cmp_lt_u32 s[sU], s[sNDF]
    .else
      s_cmp_lt_u32 s[sU], s[sNF]
    .endif
    PVM 7
    EXPE \cur, 10
  .endm
  .macro LASTF prev, modea
    BAR0 \modea
    .set g, 0
    .rept 8
      VREAD g
      .if g < 5
        EXPE \prev, 11+g
      .endif
      .set g, g+1
    .endr
    s_add_u32 s[sU], s[sU], 1
    .set g, 0
    .rept 8
      CVTE \prev, g
      .set g, g+1
    .endr
    s_waitcnt lgkmcnt(0)
    .set g, 0
    .rept 8
      PVM g
      ADDE \prev, 4+g
      .set g, g+1
    .endr
    ADDE \prev, 12
    ADDE \prev, 13
    ADDE \prev, 14
    ADDE \prev, 15
    s_branch .Lnone_tail
  .endm

.LloopS:
  FULLS vSB, vSA, 0
  s_cbranch_scc1 .LlastS1
  FULLS vSA, vSB, 1
  s_cbranch_scc0 .LloopS
.LlastS0:
  LASTS vSA, 0
.LlastS1:
  LASTS vSB, 1
.LloopF:
  FULLF vSB, vSA, 0, 1
  s_cbranch_scc0 .LdispF1
.LloopF1:
  FULLF vSA, vSB, 1, 1
  s_cbranch_scc1 .LloopF
.LdispF0:
  s_cmp_lt_u32 s[sU], s[sNF]
  s_cbranch_scc0 .LlastF0
.LfullN0:
  FULLF vSB, vSA, 0, 0
  s_cbranch_scc0 .LlastF1
.LfullN1:
  FULLF vSA, vSB, 1, 0
  s_cbranch_scc1 .LfullN0
.LlastF0:
  LASTF vSA, 0
.LdispF1:
  s_cmp_lt_u32 s[sU], s[sNF]
  s_cbranch_scc1 .LfullN1
.LlastF1:
  LASTF vSB, 1

  ; ---- slots in which this wave has nothing to compute
.Lnone:                                             ; wave without rows: keep loading its share of the tiles
  s_cmp_ge_u32 s[sU], s[sNU]
  s_cbranch_scc1 .Lepi
  s_bitcmp1_b32 s[sU], 0
  s_cselect_b32 s[sT1], s[sMB], 0
  s_cmp_eq_u32 s[sT1], 1
  s_cbranch_scc1 .Lnone_skip
  s_cmp_lt_u32 s[sU], s[sND]
  s_cbranch_scc0 .Lnone_fin
  s_waitcnt vmcnt(4) lgkmcnt(0)
  s_barrier
  DALL
  s_branch .Lnone_skip
.Lnone_fin:
  s_waitcnt vmcnt(0) lgkmcnt(0)
  s_barrier
.Lnone_skip:
  s_add_u32 s[sU], s[sU], 1
  s_branch .Lnone
.Lnone_tail:                                        ; after the wave's last step: only barriers remain
  s_cmp_ge_u32 s[sU], s[sNU]
  s_cbranch_scc1 .Lepi
  s_bitcmp1_b32 s[sU], 0
  s_cselect_b32 s[sT1], s[sMB], 0
  s_cmp_eq_u32 s[sT1], 1
  s_cbranch_scc1 .Lnt_skip
  s_barrier
.Lnt_skip:
  s_add_u32 s[sU], s[sU], 1
  s_branch .Lnone_tail

  ; ======================================================================== epilogue
  ; 32 f32 at vO+b.. (d 0..63 of a 64-wide half) -> bf16 -> LDS stage (row-major 128 B rows, 16-B chunks xor row) -> 4 full-line stores
  .macro OUTHALF b, stg                             ; stg: SGPR with the LDS stage base ; sT3 = global offset of row 0 of this half
    .set dd, 0
    .rept 2
      .set g, 0
      .rept 2
        v_cvt_pk_bf16_f32 v[vX+0], v[vO+\b+16*dd+8*g+0], v[vO+\b+16*dd+8*g+1]
        v_cvt_pk_bf16_f32 v[vX+1], v[vO+\b+16*dd+8*g+2], v[vO+\b+16*dd+8*g+3]
        v_cvt_pk_bf16_f32 v[vX+2], v[vO+\b+16*dd+8*g+4], v[vO+\b+16*dd+8*g+5]
        v_cvt_pk_bf16_f32 v[vX+3], v[vO+\b+16*dd+8*g+6], v[vO+\b+16*dd+8*g+7]
        v_xor_b32 v[vT4], (2*dd+g)*32, v[vT5]
        s_nop 0
        v_permlane32_swap_b32 v[vX+0], v[vX+2]
        v_permlane32_swap_b32 v[vX+1], v[vX+3]
        s_nop 0
        ds_write_b128 v[vT4], v[vX+0:vX+3]
        .set g, g+1
      .endr
      .set dd, dd+1
    .endr
    s_waitcnt lgkmcnt(0)
    ds_read_b128 v[vX+0:vX+3], v[vT6]
    ds_read_b128 v[vX+4:vX+7], v[vT6] offset:1024
    ds_read_b128 v[vX+8:vX+11], v[vT6] offset:2048
    ds_read_b128 v[vX+12:vX+15], v[vT6] offset:3072
    s_add_u32 s[sT4], s[sT3], 8*4096
    s_waitcnt lgkmcnt(3)
    buffer_store_dwordx4 v[vX+0:vX+3], v[vSO], s[sO:sO+3], s[sT3] offen
    s_add_u32 s[sT1], s[sT3], 16*4096
    s_waitcnt lgkmcnt(2)
    buffer_store_dwordx4 v[vX+4:vX+7], v[vSO], s[sO:sO+3], s[sT4] offen
    s_add_u32 s[sT2], s[sT3], 24*4096
    s_waitcnt lgkmcnt(1)
    buffer_store_dwordx4 v[vX+8:vX+11], v[vSO], s[sO:sO+3], s[sT1] offen
    s_waitcnt lgkmcnt(0)
    buffer_store_dwordx4 v[vX+12:vX+15], v[vSO], s[sO:sO+3], s[sT2] offen
  .endm
  .macro MERGE kb, rb                               ; kb: kept 32 regs, rb: 32 regs sent / received
    .set i, 0
    .rept 8
      ds_write_b128 v[vT0], v[vO+\rb+4*i:vO+\rb+4*i+3] offset:1024*i
      .set i, i+1
    .endr
    s_waitcnt lgkmcnt(0)
    s_barrier
    ds_read_b64 v[vT4:vT5], v[vT3]
    .set i, 0
    .rept 8
      ds_read_b128 v[vO+\rb+4*i:vO+\rb+4*i+3], v[vT1] offset:1024*i
      .set i, i+1
    .endr
    s_waitcnt lgkmcnt(8)
    v_max_f32 v[vT6], v[vM], v[vT4]
    v_sub_f32 v[vT0], v[vM], v[vT6]
    v_sub_f32 v[vT2], v[vT4], v[vT6]
    v_mul_f32 v[vT0], s[sCE], v[vT0]
    v_mul_f32 v[vT2], s[sCE], v[vT2]
    v_exp_f32 v[vT0], v[vT0]
    v_exp_f32 v[vT2], v[vT2]
    s_nop 0
    v_mul_f32 v[vT6], v[vT0], v[vL0]
    v_fma_f32 v[vT6], v[vT2], v[vT5], v[vT6]
    v_rcp_f32 v[vT6], v[vT6]
    s_nop 0
    v_mul_f32 v[vT0], v[vT0], v[vT6]                ; fa
    v_mul_f32 v[vT2], v[vT2], v[vT6]                ; fb
    .set i, 0
    .rept 32
      v_mul_f32 v[vO+\kb+i], v[vT0], v[vO+\kb+i]
      .set i, i+1
    .endr
    .set i, 0
    .rept 8
      .if i == 0
        s_waitcnt lgkmcnt(7)
      .elseif i == 1
        s_waitcnt lgkmcnt(6)
      .elseif i == 2
        s_waitcnt lgkmcnt(5)
      .elseif i == 3
        s_waitcnt lgkmcnt(4)
      .elseif i == 4
        s_waitcnt lgkmcnt(3)
      .elseif i == 5
        s_waitcnt lgkmcnt(2)
      .elseif i == 6
        s_waitcnt lgkmcnt(1)
      .else
        s_waitcnt lgkmcnt(0)
      .endif
      v_fma_f32 v[vO+\kb+4*i+0], v[vT2], v[vO+\rb+4*i+0], v[vO+\kb+4*i+0]
      v_fma_f32 v[vO+\kb+4*i+1], v[vT2], v[vO+\rb+4*i+1], v[vO+\kb+4*i+1]
      v_fma_f32 v[vO+\kb+4*i+2], v[vT2], v[vO+\rb+4*i+2], v[vO+\kb+4*i+2]
      v_fma_f32 v[vO+\kb+4*i+3], v[vT2], v[vO+\rb+4*i+3], v[vO+\kb+4*i+3]
      .set i, i+1
    .endr
    v_mov_b32 v[vT5], v[vAL]
    v_mov_b32 v[vT6], v[vNMC]
    OUTHALF \kb, 0
  .endm

.Lepi:
  STAMP 7
  v_mov_b32 v[vT7], 0x71800000
  v_add_f32 v[vL0], v[vL0], v[vL1]
  v_mov_b32 v[vL1], v[vL0]
  s_lshl_b32 s[sT3], s[sQR], 12
  s_add_u32 s[sT3], s[sT3], s[sHOFF]
  v_permlane32_swap_b32 v[vL0], v[vL1]
  s_cmp_eq_u32 s[sMB], 1
  v_add_f32 v[vL0], v[vL0], v[vL1]                ; row sum
  s_cbranch_scc1 .LepiB
  ; ---- mode A: exchange area: K ring (wave w at w*8192) ; stats in X region ; output stage in the V ring (w*4096)
  v_lshlrev_b32 v[vT0], 4, v[vLANE]
  s_lshl_b32 s[sT1], s[sW], 13
  s_xor_b32 s[sT2], s[sT1], 4*8192
  v_add_u32 v[vT1], s[sT2], v[vT0]                ; partner area
  v_add_u32 v[vT0], s[sT1], v[vT0]                ; own area
  v_and_b32 v[vT2], 31, v[vLANE]
  v_lshlrev_b32 v[vT2], 3, v[vT2]
  v_add_u32 v[vT2], LX, v[vT2]
  s_lshl_b32 s[sT1], s[sW], 8
  s_xor_b32 s[sT2], s[sT1], 4*256
  v_add_u32 v[vT3], s[sT2], v[vT2]
  v_add_u32 v[vT2], s[sT1], v[vT2]
  v_mov_b32 v[vT4], v[vM]
  v_mov_b32 v[vT5], v[vL0]
  ds_write_b64 v[vT2], v[vT4:vT5]
  s_lshl_b32 s[sT1], s[sKH], 7
  s_add_u32 s[sT3], s[sT3], s[sT1]
  s_lshl_b32 s[sT1], s[sW], 12
  s_add_u32 s[sT1], s[sT1], LV
  v_add_u32 v[vAL], s[sT1], v[vSW]                ; stage write base  (kept in vAL / vNMC across MERGE)
  v_add_u32 v[vNMC], s[sT1], v[vSR]               ; stage read base
  s_cmp_eq_u32 s[sKH], 0
  s_cbranch_scc1 .Lmerge0
  MERGE 32, 0
  s_branch .Lnext
.Lmerge0:
  MERGE 0, 32
  s_branch .Lnext
.LepiB:
  v_rcp_f32 v[vT0], v[vL0]
  s_waitcnt vmcnt(0)
  s_barrier
  s_lshl_b32 s[sT1], s[sW], 13
  v_add_u32 v[vT5], s[sT1], v[vSW]
  v_add_u32 v[vT6], s[sT1], v[vSR]
  s_nop 7
  .set i, 0
  .rept 64
    v_mul_f32 v[vO+i], v[vT0], v[vO+i]
    .set i, i+1
  .endr
  OUTHALF 0, 0
  v_add_u32 v[vT5], 4096, v[vT5]
  v_add_u32 v[vT6], 4096, v[vT6]
  s_add_u32 s[sT3], s[sT3], 128
  s_nop 1
  OUTHALF 32, 0
.Lnext:
  STAMP 8
  ; overflow check of the FAST path: any row sum that is not a normal-sized number -> redo this item with the online max
  v_cmp_nlt_f32 vcc, v[vL0], v[vT7]
  s_lshl_b32 s[sT1], s[sW], 2
  s_add_u32 s[sT1], s[sT1], LX+8192
  s_cmp_lg_u64 vcc, 0
  s_cselect_b32 s[sT2], 1, 0
  v_mov_b32 v[vT0], s[sT1]
  v_mov_b32 v[vT1], s[sT2]
  v_and_b32 v[vT2], 7, v[vLANE]
  v_lshlrev_b32 v[vT2], 2, v[vT2]
  v_add_u32 v[vT2], LX+8192, v[vT2]
  ds_write_b32 v[vT0], v[vT1]
  s_waitcnt lgkmcnt(0)
  s_barrier
  ds_read_b32 v[vT1], v[vT2]
  s_waitcnt lgkmcnt(0)
  v_cmp_ne_u32 vcc, 0, v[vT1]
  s_cmp_eq_u32 s[sSAFE], 1
  s_cbranch_scc1 .Lsafe_done
  s_cbranch_vccz .Lsafe_done
  s_mov_b32 s[sSAFE], 1
  s_barrier
  s_branch .Litem
.Lsafe_done:
  s_mov_b32 s[sSAFE], 0
  s_add_u32 s[sR], s[sR], 1
  s_lshl_b32 s[sT1], s[sR], 5
  s_cmp_ge_u32 s[sT1], s[sTOT]
  s_cbranch_scc1 .Ldone
  s_barrier
  s_branch .Litem
.Ldone:
  .if DIAG
    s_waitcnt vmcnt(0)
    STAMP 9
    s_memrealtime s[sA:sA+1]
    s_load_dwordx2 s[sB:sB+1], %0, 96
    s_lshl_b32 s[sT1], %1, 3
    s_add_u32 s[sT1], s[sT1], s[sW]
    s_lshl_b32 s[sT1], s[sT1], 8
    s_waitcnt lgkmcnt(0)
    s_add_u32 s[sB], s[sB], s[sT1]
    s_addc_u32 s[sB+1], s[sB+1], 0
    v_mov_b32 v[vT6], 0
    s_mov_b64 exec, 1
    .set i, 0
    .rept 10
      v_mov_b32 v[vT0], s[sST+2*i]
      v_mov_b32 v[vT1], s[sST+2*i+1]
      global_store_dwordx2 v[vT6], v[vT0:vT1], s[sB:sB+1] offset:8*i
      s_nop 1
      .set i, i+1
    .endr
    v_mov_b32 v[vT0], s[sRT]
    v_mov_b32 v[vT1], s[sRT+1]
    global_store_dwordx2 v[vT6], v[vT0:vT1], s[sB:sB+1] offset:80
    s_nop 1
    v_mov_b32 v[vT0], s[sA]
    v_mov_b32 v[vT1], s[sA+1]
    global_store_dwordx2 v[vT6], v[vT0:vT1], s[sB:sB+1] offset:88
    s_nop 1
  .endif
  s_endpgm
.Lfallback:
  s_waitcnt vmcnt(0) lgkmcnt(0)
  .purgem STAMP
  .purgem PREFETCH
  .purgem DK1
  .purgem DALL
  .purgem BAR4
  .purgem BAR0
  .purgem BARN
  .purgem FULLS
  .purgem LASTS
  .purgem FULLF
  .purgem LASTF
  .purgem DMAK1
  .purgem DMAK2
  .purgem DMAV1
  .purgem DMAV2
  .purgem DK2
  .purgem DV1
  .purgem DV2
  .purgem KREAD
  .purgem KADV
  .purgem QKM
  .purgem VREAD
  .purgem PVM
  .purgem MASK
  .purgem ROWMAX1
  .purgem ROWMAX2
  .purgem EXPE
  .purgem SUMPK
  .purgem VADDR
  .purgem RESCALE
  .purgem ADDE
  .purgem CVTE
  .purgem OUTHALF
  .purgem MERGE
  )" :: "s"(__builtin_amdgcn_kernarg_segment_ptr()), "s"(blockIdx.x), "v"(threadIdx.x), "i"(NIT)
     : "memory", "vcc", "m0",
       "s8","s9","s10","s11","s12","s13","s14","s15","s16","s17","s18","s19","s20","s21","s22","s23","s24","s25","s26","s27","s28","s29","s30","s31",
       "s33","s34","s35","s36","s37","s38","s39","s40","s41","s42","s43","s44","s45","s46","s47","s48","s49","s50","s51","s52","s53","s54","s55",
       "s56","s57","s58","s59","s60","s61","s62","s63","s64","s65","s66","s67","s68","s69","s70","s71","s72","s73","s74","s75","s76","s77","s78","s79",
       "s80","s81","s82","s83","s84","s85","s86","s87","s88","s89","s90","s91","s92","s93","s94","s95","s96","s97","s98","s99",
       "v1","v2","v3","v4","v5","v6","v7","v8","v9","v10","v11","v12","v13","v14","v15","v16","v17","v18","v19","v20","v21","v22","v23","v24","v25","v26","v27","v28","v29","v30","v31",
       "v32","v33","v34","v35","v36","v37","v38","v39","v40","v41","v42","v43","v44","v45","v46","v47","v48","v49","v50","v51","v52","v53","v54","v55","v56","v57","v58","v59","v60","v61","v62","v63",
       "v64","v65","v66","v67","v68","v69","v70","v71","v72","v73","v74","v75","v76","v77","v78","v79","v80","v81","v82","v83","v84","v85","v86","v87","v88","v89","v90","v91","v92","v93","v94","v95",
       "v96","v97","v98","v99","v100","v101","v102","v103","v104","v105","v106","v107","v108","v109","v110","v111","v112","v113","v114","v115","v116","v117","v118","v119","v120","v121","v122","v123","v124","v125","v126","v127",
       "v128","v129","v130","v131","v132","v133","v134","v135","v136","v137","v138","v139","v140","v141","v142","v143","v144","v145","v146","v147","v148","v149","v150","v151","v152","v153","v154","v155","v156","v157","v158","v159",
       "v160","v161","v162","v163","v164","v165","v166","v167","v168","v169","v170","v171","v172","v173","v174","v175","v176","v177","v178","v179","v180","v181","v182","v183","v184","v185","v186","v187","v188","v189","v190","v191",
       "v192","v193","v194","v195","v196","v197","v198","v199","v200","v201","v202","v203","v204","v205","v206","v207","v208","v209","v210","v211","v212","v213","v214","v215","v216","v217","v218","v219","v220","v221","v222","v223",
       "v224","v225","v226","v227","v228","v229","v230","v231","v232","v233","v234","v235","v236","v237","v238","v239","v240","v241","v242","v243","v244","v245","v246","v247","v248","v249","v250","v251","v252","v253","v254","v255");
  generic_path((const unsigned short*)Q, (const unsigned short*)K, (const unsigned short*)V, (const int*)seg, (unsigned short*)O, T, lds);
}

__global__ __launch_bounds__(512, 2)
void ragged_attn_generic(const void* Q, const void* K, const void* V, const void* seg, void* O, int T, int pad) {
  __shared__ __attribute__((aligned(16))) char lds[LDS_BYTES];
  generic_path((const unsigned short*)Q, (const unsigned short*)K, (const unsigned short*)V, (const int*)seg, (unsigned short*)O, T, lds);
}

extern "C" void kernel_launch(void* const* d_in, const int* in_sizes, int n_in, void* d_out, int out_size, void* d_ws, size_t ws_size,
                              hipStream_t stream) {
  const int T = in_sizes[3];
  const unsigned nb = (unsigned)T * ROWB, F = 0x00020000u;
  if (T > 2048 && T <= 2560) {
    ragged_attn_kernel<5><<<256, 512, 0, stream>>>(d_in[0], nb, F, d_in[1], nb, F, d_in[2], nb, F, d_in[3], (unsigned)T * 4u, F, d_out, nb, F,
                                                   T, T - 1 - 512 * 4, ((T + 31) / 32) * (int)ROWB, 0, d_ws);
  } else {
    ragged_attn_generic<<<256, 512, 0, stream>>>(d_in[0], d_in[1], d_in[2], d_in[3], d_out, T, 0);
  }
}
```

```cpp
#include <hip/hip_runtime.h>
#include <stdint.h>

typedef short    bf16x8 __attribute__((ext_vector_type(8)));
typedef short    s16x4  __attribute__((ext_vector_type(4)));
typedef float    f32x16 __attribute__((ext_vector_type(16)));
typedef float    f32x4  __attribute__((ext_vector_type(4)));
typedef float    f32x2  __attribute__((ext_vector_type(2)));
typedef __bf16   bf2    __attribute__((ext_vector_type(2)));
typedef unsigned u32x4  __attribute__((ext_vector_type(4)));
typedef unsigned long long u64;
#define LDSP __attribute__((address_space(3)))
typedef LDSP const char* lds_cptr;
typedef LDSP char* lds_ptr;
typedef short v4i16_t __attribute__((ext_vector_type(4)));
#define DI __device__ __forceinline__
#define MFMA(a, b, c) __builtin_amdgcn_mfma_f32_32x32x16_bf16((a), (b), (c), 0, 0, 0)

constexpr int NH = 16, HD = 128;
constexpr unsigned ROWB = NH * HD * 2;
constexpr unsigned LDS_BYTES = 163840;
constexpr float CEXP = 0.08838834764831845f * 1.4426950408889634f;
constexpr float THR_RAW = 8.0f / CEXP;
constexpr float MINIT = -1.0e30f;

DI unsigned rfl(unsigned x) { return __builtin_amdgcn_readfirstlane(x); }
DI unsigned cvtpk(float lo, float hi) { f32x2 v = {lo, hi}; bf2 b = __builtin_convertvector(v, bf2); return __builtin_bit_cast(unsigned, b); }
DI float swap_max(float x) { auto r = __builtin_amdgcn_permlane32_swap(__float_as_uint(x), __float_as_uint(x), false, false); return __builtin_fmaxf(__uint_as_float(r[0]), __uint_as_float(r[1])); }
DI float swap_add(float x) { auto r = __builtin_amdgcn_permlane32_swap(__float_as_uint(x), __float_as_uint(x), false, false); return __uint_as_float(r[0]) + __uint_as_float(r[1]); }
DI s16x4 vtr(lds_cptr p) { return __builtin_bit_cast(s16x4, __builtin_amdgcn_ds_read_tr16_b64_v4i16((LDSP v4i16_t*)p)); }
#define MX3(a, b, c) __builtin_fmaxf(__builtin_fmaxf((a), (b)), (c))

DI void dma16(__amdgpu_buffer_rsrc_t srd, unsigned voff, unsigned soff, unsigned m0v) {
  unsigned keep;
  asm volatile("s_nop 4\n\ts_mov_b32 %0, m0\n\ts_mov_b32 m0, %1\n\ts_nop 0\n\tbuffer_load_dwordx4 %2, %3, %4 offen lds\n\ts_mov_b32 m0, %0"
               : "=&s"(keep) : "s"(m0v), "v"(voff), "s"(srd), "s"(soff) : "memory");
}

__device__ __forceinline__ void generic_path(const unsigned short* __restrict__ Q, const unsigned short* __restrict__ K, const unsigned short* __restrict__ V,
                                          const int* __restrict__ seg, unsigned short* __restrict__ O, int T, char* lds) {
  constexpr unsigned SLOT = 32768, VOFF = 16384, XREG = 4 * SLOT;
  const int tid = threadIdx.x, lane = tid & 63, r32 = lane & 31, hh = lane >> 5;
  const int w = (int)rfl((unsigned)tid >> 6);
  const int grp = blockIdx.x & 7, jj = blockIdx.x >> 3, NJ = 32;
  const unsigned ldsb = (unsigned)(uintptr_t)lds;
  const lds_ptr L = (lds_ptr)lds;
  const int total_items = 2 * ((T + 127) >> 7);
  const __amdgpu_buffer_rsrc_t srdQ = __builtin_amdgcn_make_buffer_rsrc((void*)Q, (short)0, T * (int)ROWB, 0x00020000);
  const __amdgpu_buffer_rsrc_t srdK = __builtin_amdgcn_make_buffer_rsrc((void*)K, (short)0, T * (int)ROWB, 0x00020000);
  const __amdgpu_buffer_rsrc_t srdV = __builtin_amdgcn_make_buffer_rsrc((void*)V, (short)0, T * (int)ROWB, 0x00020000);
  const unsigned prow = (lane >> 2) & 7, pcb = lane >> 5, pc = lane & 3;
  const unsigned voffK = prow * ROWB + pcb * 64 + ((pc ^ ((2u * (unsigned)w + (prow >> 2)) & 3u)) * 16);
  const unsigned voffV = prow * ROWB + pcb * 64 + pc * 16;
  const unsigned kb0 = 2048u * (r32 >> 3) + 64u * (r32 & 7) + 16u * ((unsigned)hh ^ ((r32 >> 2) & 3u));
  const unsigned vb0 = ((unsigned)hh * 4 + ((lane & 15) >> 2)) * 64 + ((lane >> 4) & 1) * 32 + (lane & 3) * 8;
  for (int idx = jj; idx < total_items; idx += NJ) {
    __syncthreads();
    const int head = grp + 8 * (idx & 1);
    const int q0 = (idx >> 1) * 128, qend = min(q0 + 128, T);
    LDSP int* red = (LDSP int*)L;
    if (tid == 0) { red[0] = 0x7fffffff; red[1] = (int)0x80000000; red[2] = 0x7fffffff; red[3] = -1; }
    __syncthreads();
    if (tid < 128 && q0 + tid < qend) { const int id = seg[q0 + tid];
      __hip_atomic_fetch_min((LDSP int*)&red[0], id, __ATOMIC_RELAXED, __HIP_MEMORY_SCOPE_WORKGROUP);
      __hip_atomic_fetch_max((LDSP int*)&red[1], id, __ATOMIC_RELAXED, __HIP_MEMORY_SCOPE_WORKGROUP); }
    __syncthreads();
    const int idlo = red[0], idhi = red[1];
    int lo = 0x7fffffff, hi = -1;
    for (int i = tid; i < T; i += 512) { const int id = seg[i]; if (id >= idlo && id <= idhi) { lo = min(lo, i); hi = max(hi, i); } }
    __hip_atomic_fetch_min((LDSP int*)&red[2], lo, __ATOMIC_RELAXED, __HIP_MEMORY_SCOPE_WORKGROUP);
    __hip_atomic_fetch_max((LDSP int*)&red[3], hi, __ATOMIC_RELAXED, __HIP_MEMORY_SCOPE_WORKGROUP);
    __syncthreads();
    const int k0 = (int)rfl((unsigned)red[2]), kend = (int)rfl((unsigned)red[3]) + 1;
    __syncthreads();
    const int nk = (kend - k0 + 63) >> 6;
    const unsigned hoff = (unsigned)head * 256u;
#pragma unroll
    for (int i = 0; i < 2; ++i) {
      const unsigned m = (unsigned)w + 8u * i;
      const unsigned so = (unsigned)(q0 + 8 * (int)m) * ROWB + hoff;
      dma16(srdQ, voffK, so, ldsb + XREG + m * 2048u);
      dma16(srdQ, voffK, so + 128u, ldsb + XREG + m * 2048u + 1024u);
    }
    const int qb = w & 3, sub = w >> 2;
    const int qrow = q0 + 32 * qb + r32;
    const int segq = seg[qrow < T ? qrow : T - 1];
    bf16x8 qf[8];
    f32x16 o[4];
#pragma unroll
    for (int d = 0; d < 4; ++d) o[d] = f32x16{};
    float m_run = MINIT, l_run = 0.f;
    for (int t = 0; t < nk; ++t) {
      __syncthreads();
      {
        const unsigned so = (unsigned)(k0 + 64 * t + 8 * w) * ROWB + hoff;
        const unsigned d = ldsb + (unsigned)w * 2048u;
        dma16(srdK, voffK, so, d); dma16(srdK, voffK, so + 128u, d + 1024u);
        dma16(srdV, voffV, so, d + VOFF); dma16(srdV, voffV, so + 128u, d + VOFF + 1024u);
      }
      asm volatile("s_waitcnt vmcnt(0)" ::: "memory");
      __syncthreads();
      if (t == 0) {
#pragma unroll
        for (int s = 0; s < 8; ++s)
          qf[s] = *(const LDSP bf16x8*)(L + XREG + (unsigned)qb * 8192u + ((kb0 ^ ((s & 1) * 32u)) + 512u * (s >> 1)));
      }
      const int kbase = k0 + 64 * t + 32 * sub;
      const lds_cptr kp = (lds_cptr)L + (unsigned)sub * 8192u;
      f32x16 sc = f32x16{};
#pragma unroll
      for (int s = 0; s < 8; ++s) {
        const bf16x8 kf = *(const LDSP bf16x8*)(kp + ((kb0 ^ ((s & 1) * 32u)) + 512u * (s >> 1)));
        sc = MFMA(kf, qf[s], sc);
      }
#pragma unroll
      for (int r = 0; r < 16; ++r) {
        const int kk = kbase + (r & 3) + 8 * (r >> 2) + 4 * hh;
        const int id = seg[kk < T ? kk : T - 1];
        sc[r] = (kk < kend && id == segq) ? sc[r] : -__builtin_inff();
      }
      float a = MX3(sc[0], sc[1], sc[2]), b = MX3(sc[3], sc[4], sc[5]);
      a = MX3(a, sc[6], sc[7]); b = MX3(b, sc[8], sc[9]); a = MX3(a, sc[10], sc[11]); b = MX3(b, sc[12], sc[13]);
      a = MX3(a, sc[14], sc[15]);
      const float pm = swap_max(__builtin_fmaxf(a, b));
      const float mn = __builtin_fmaxf(m_run, pm);
      const float al = __builtin_amdgcn_exp2f((m_run - mn) * CEXP);
      m_run = mn; l_run *= al;
#pragma unroll
      for (int d = 0; d < 4; ++d)
#pragma unroll
        for (int r = 0; r < 16; ++r) o[d][r] *= al;
      const float nmc = -m_run * CEXP;
      float ps = 0.f;
#pragma unroll
      for (int r = 0; r < 16; ++r) { sc[r] = __builtin_amdgcn_exp2f(__builtin_fmaf(sc[r], CEXP, nmc)); ps += sc[r]; }
      l_run += ps;
      u32x4 pw0, pw1;
#pragma unroll
      for (int e = 0; e < 4; ++e) { pw0[e] = cvtpk(sc[2 * e], sc[2 * e + 1]); pw1[e] = cvtpk(sc[8 + 2 * e], sc[8 + 2 * e + 1]); }
      const bf16x8 p0 = __builtin_bit_cast(bf16x8, pw0), p1 = __builtin_bit_cast(bf16x8, pw1);
      const lds_cptr vp = kp + VOFF + vb0;
#pragma unroll
      for (int d = 0; d < 4; ++d) {
        const s16x4 a0 = vtr(vp + d * 512), a1 = vtr(vp + d * 512 + 2048), b0 = vtr(vp + d * 512 + 4096), b1 = vtr(vp + d * 512 + 6144);
        const bf16x8 va = __builtin_shufflevector(a0, a1, 0, 1, 2, 3, 4, 5, 6, 7), vb = __builtin_shufflevector(b0, b1, 0, 1, 2, 3, 4, 5, 6, 7);
        o[d] = MFMA(va, p0, o[d]);
        o[d] = MFMA(vb, p1, o[d]);
      }
    }
    l_run = swap_add(l_run);
    unsigned short* orow = O + (size_t)(qrow < T ? qrow : 0) * (NH * HD) + head * HD + 8 * hh;
    const bool row_ok = qrow < qend;
    const int kh = sub;
    __syncthreads();
    const unsigned xa = SLOT, xb = 2 * SLOT, xs = 3 * SLOT;
    const unsigned mine = (kh ? xb : xa) + (unsigned)qb * 8192u + (unsigned)lane * 16u;
    const unsigned theirs = (kh ? xa : xb) + (unsigned)qb * 8192u + (unsigned)lane * 16u;
#pragma unroll
    for (int dd = 0; dd < 2; ++dd)
#pragma unroll
      for (int g = 0; g < 4; ++g) {
        f32x4 v;
#pragma unroll
        for (int e = 0; e < 4; ++e) v[e] = kh ? o[dd][4 * g + e] : o[2 + dd][4 * g + e];
        *(LDSP f32x4*)(L + mine + (unsigned)(dd * 4 + g) * 1024u) = v;
      }
    if (hh == 0) { f32x2 s2 = {m_run, l_run}; *(LDSP f32x2*)(L + xs + (unsigned)w * 256u + (unsigned)r32 * 8u) = s2; }
    __syncthreads();
    const f32x2 o2 = *(const LDSP f32x2*)(L + xs + (unsigned)(w ^ 4) * 256u + (unsigned)r32 * 8u);
    const float mn = __builtin_fmaxf(m_run, o2[0]);
    const float wa = __builtin_amdgcn_exp2f((m_run - mn) * CEXP), wb = __builtin_amdgcn_exp2f((o2[0] - mn) * CEXP);
    const float inv = 1.0f / (wa * l_run + wb * o2[1]);
    const float fa = wa * inv, fb = wb * inv;
#pragma unroll
    for (int dd = 0; dd < 2; ++dd) {
      float r[16];
#pragma unroll
      for (int g = 0; g < 4; ++g) {
        const f32x4 v = *(const LDSP f32x4*)(L + theirs + (unsigned)(dd * 4 + g) * 1024u);
#pragma unroll
        for (int e = 0; e < 4; ++e) r[4 * g + e] = fa * (kh ? o[2 + dd][4 * g + e] : o[dd][4 * g + e]) + fb * v[e];
      }
#pragma unroll
      for (int g = 0; g < 4; g += 2) {
        unsigned ax = cvtpk(r[4 * g + 0], r[4 * g + 1]), ay = cvtpk(r[4 * g + 2], r[4 * g + 3]);
        unsigned bx = cvtpk(r[4 * g + 4], r[4 * g + 5]), by = cvtpk(r[4 * g + 6], r[4 * g + 7]);
        auto rx = __builtin_amdgcn_permlane32_swap(ax, bx, false, false); auto ry = __builtin_amdgcn_permlane32_swap(ay, by, false, false);
        const u32x4 v = {rx[0], ry[0], rx[1], ry[1]};
        if (row_ok) *(u32x4*)(orow + 64 * kh + 32 * dd + 8 * g) = v;
      }
    }
  }
}

#define DIAG 0
#define PFQ 1
#define PFKV 0
#define ABL 0
#define PFAT 1
#define DBGW 0
#define TOUCH 0
#define PF1 3
#define PF2 5
#define STR2(x) #x
#define STR(x) STR2(x)

template <int NIT>
__global__ __launch_bounds__(512, 2)
void ragged_attn_kernel(const void* Q, unsigned nQ, unsigned fQ, const void* K, unsigned nK, unsigned fK, const void* V, unsigned nV, unsigned fV,
                        const void* seg, unsigned nS, unsigned fS, void* O, unsigned nO, unsigned fO, int T, int lastc, int pfb, int pad, void* dbg) {
  __shared__ __attribute__((aligned(16))) char lds[LDS_BYTES];
  asm volatile(R"(
  .set NIT, %c3
  .set DIAG, )" STR(DIAG) R"(
  .set PFQ, )" STR(PFQ) R"(
  .set PFKV, )" STR(PFKV) R"(
  .set ABL, )" STR(ABL) R"(
  .set PFAT, )" STR(PFAT) R"(
  .set DBGW, )" STR(DBGW) R"(
  .set TOUCH, )" STR(TOUCH) R"(
  .set PF1, )" STR(PF1) R"(
  .set PF2, )" STR(PF2) R"(
  ; ---------------- SGPRs
  .set sQ, 8
  .set sK, 12
  .set sV, 16
  .set sS, 20
  .set sO, 24
  .set sT, 28
  .set sLASTC, 29
  .set sPFB, 30
  .set sF, 30
  .set sTM16, 31
  .set sG, 34
  .set sR, 35
  .set sNBIG, 36
  .set sTOT, 37
  .set sHOFF, 38
  .set sQ0, 39
  .set sQEND, 40
  .set sK0, 41
  .set sNK, 42
  .set sLEN, 43
  .set sMB, 44
  .set sNS, 45
  .set sU, 46
  .set sNU, 47
  .set sKOFF, 48
  .set sKSTEP, 49
  .set sVOFF, 50
  .set sKSO, 51
  .set sVSO, 52
  .set sKDST, 53
  .set sVDST, 54
  .set sMAXSO, 55
  .set sND, 56
  .set sMSTEP, 57
  .set sPEND, 58
  .set sW, 59
  .set sA, 60
  .set sB, 62
  .set sC, 64
  .set sT1, 66
  .set sT2, 67
  .set sT3, 68
  .set sT4, 69
  .set sCE, 70
  .set sTHR, 71
  .set sWD, 72
  .set sQB, 73
  .set sKH, 74
  .set sLIM, 75
  .set sJ, 76
  .set sQR, 77
  .set sRT, 78
  .set sSAFE, 64
  .set sNF, 65
  .set sNDF, 32+1
  .set sST, 80
  ; ---------------- VGPRs
  .set vLANE, 1
  .set vOK, 2
  .set vOK2, 3
  .set vOV, 4
  .set vOV2, 5
  .set vKB0, 6
  .set vKB1, 7
  .set vVB, 8
  .set vSTART, 9
  .set vLEN, 10
  .set vINCL, 11
  .set vEXCL, 12
  .set vM, 13
  .set vNMC, 14
  .set vL0, 15
  .set vL1, 16
  .set vKA0, 17
  .set vKA1, 18
  .set vVA, 19
  .set vAL, 20
  .set vSO, 21
  .set vHH4, 22
  .set vSW, 23
  .set vT0, 24
  .set vT1, 25
  .set vT2, 26
  .set vT3, 27
  .set vT4, 28
  .set vT5, 29
  .set vT6, 30
  .set vT7, 31
  .set vP, 32
  .set vX, 40
  .set vSR, 56
  .set vO, 64
  .set vQ, 128
  .set vSA, 160
  .set vSB, 176
  .set vK, 192
  .set vV, 224
  ; ---------------- LDS map
  .set LK, 0
  .set LV, 65536
  .set LX, 131072
  .set LQ2, 49152
  .set LQ3, 114688
  .set LSCR, 98304

  .macro STAMP i
    .if DIAG
      s_memtime s[sST+2*\i:sST+2*\i+1]
      s_waitcnt lgkmcnt(0)
    .endif
  .endm

  ; ======================================================================== entry
  s_load_dwordx16 s[8:23], %0, 0
  s_load_dwordx8 s[24:31], %0, 64
  .if DIAG
    s_memrealtime s[sRT:sRT+1]
  .endif
  STAMP 0
  v_and_b32 v[vLANE], 63, %2
  v_readfirstlane_b32 s[sW], %2
  v_lshlrev_b32 v[vT0], 2, %2                     ; tid*4
  s_and_b32 s[sG], %1, 7
  s_lshr_b32 s[sJ], %1, 3
  s_lshr_b32 s[sW], s[sW], 6
  ; prefetch offset: (tid>>5)*4096 + ((tid>>4)&1)*2048 + ((tid>>3)&1)*128 + (tid&7)*16 + g*256
  v_lshrrev_b32 v[vT6], 5, %2
  v_lshlrev_b32 v[vT6], 12, v[vT6]
  v_bfe_u32 v[vT7], %2, 4, 1
  v_lshl_add_u32 v[vT6], v[vT7], 11, v[vT6]
  v_bfe_u32 v[vT7], %2, 3, 1
  v_lshl_add_u32 v[vT6], v[vT7], 7, v[vT6]
  v_and_b32 v[vT7], 7, %2
  v_lshl_add_u32 v[vT6], v[vT7], 4, v[vT6]
  s_lshl_b32 s[sT3], s[sG], 8
  v_add_u32 v[vT6], s[sT3], v[vT6]
  ; DMA lane offsets
  v_bfe_u32 v[vT1], v[vLANE], 2, 3                ; prow
  v_lshrrev_b32 v[vT2], 5, v[vLANE]               ; hh / pcb
  v_and_b32 v[vT3], 3, v[vLANE]                   ; pc
  v_lshlrev_b32 v[vT4], 12, v[vT1]
  v_lshl_add_u32 v[vT4], v[vT2], 6, v[vT4]        ; prow*4096 + pcb*64
  v_lshl_add_u32 v[vOV], v[vT3], 4, v[vT4]
  v_bfe_u32 v[vT5], v[vLANE], 4, 1
  s_and_b32 s[sT4], s[sW], 1
  s_lshl_b32 s[sT4], s[sT4], 1
  v_or_b32 v[vT5], s[sT4], v[vT5]                 ; x
  v_xor_b32 v[vT5], v[vT5], v[vT3]
  v_lshl_add_u32 v[vOK], v[vT5], 4, v[vT4]
  s_waitcnt lgkmcnt(0)
  STAMP 1
  ; ======================================================================== S1: segment ends per 64-token chunk
  s_lshl_b32 s[sT2], s[sT], 2
  s_sub_u32 s[sT2], s[sT2], 8                     ; (T-2)*4
  v_add_u32 v[vT7], 2048*(NIT-1), v[vT0]
  v_min_u32 v[vT7], s[sT2], v[vT7]                ; last chunk: clamped (no out-of-range lanes)
  .set i, 0
  .rept NIT
    .if i == NIT-1
      buffer_load_dwordx2 v[vX+2*i:vX+2*i+1], v[vT7], s[sS:sS+3], 0 offen
    .elseif i < 2
      buffer_load_dwordx2 v[vX+2*i:vX+2*i+1], v[vT0], s[sS:sS+3], 0 offen offset:2048*i
    .else
      s_movk_i32 s[sT1], 2048*i
      buffer_load_dwordx2 v[vX+2*i:vX+2*i+1], v[vT0], s[sS:sS+3], s[sT1] offen
    .endif
    .set i, i+1
  .endr
  .if TOUCH
    v_lshlrev_b32 v[vV+29], 20, v[vLANE]            ; 1 MiB stride
    s_sub_u32 s[sT3], s[sK+2], 4
    v_min_u32 v[vV+29], s[sT3], v[vV+29]
    s_mov_b64 exec, 0x3ff
    buffer_load_dword v[vV+20], v[vV+29], s[sK:sK+3], 0 offen
    buffer_load_dword v[vV+21], v[vV+29], s[sV:sV+3], 0 offen
    buffer_load_dword v[vV+22], v[vV+29], s[sQ:sQ+3], 0 offen
    buffer_load_dword v[vV+23], v[vV+29], s[sO:sO+3], 0 offen
    s_mov_b64 exec, -1
  .endif
  s_sub_u32 s[sTM16], s[sT], 16
  s_lshl_b32 s[sTM16], s[sTM16], 12               ; (T-16)*4096
  .macro PREFETCH lo, hi                            ; Q prefetch instructions lo..hi-1 (16 rows each)
    .set n, \lo
    .rept \hi-\lo
      s_mul_i32 s[sT3], s[sJ], s[sPFB]
      s_add_u32 s[sT3], s[sT3], n*16*4096
      s_min_u32 s[sT3], s[sT3], s[sTM16]
      buffer_load_dwordx4 v[vV+4*n:vV+4*n+3], v[vV+28], s[sQ:sQ+3], s[sT3] offen
      .set n, n+1
    .endr
  .endm
  v_mov_b32 v[vV+28], v[vT6]
  ; remaining lane constants (in the shadow of the loads)
  v_add_u32 v[vOK2], 128, v[vOK]
  v_add_u32 v[vOV2], 128, v[vOV]
  v_and_b32 v[vT1], 31, v[vLANE]                  ; r32
  v_lshrrev_b32 v[vT3], 3, v[vT1]
  v_lshlrev_b32 v[vT3], 11, v[vT3]
  v_and_b32 v[vT4], 7, v[vT1]
  v_lshl_add_u32 v[vT3], v[vT4], 6, v[vT3]
  v_bfe_u32 v[vT4], v[vT1], 2, 2
  v_xor_b32 v[vT4], v[vT4], v[vT2]
  v_lshl_add_u32 v[vKB0], v[vT4], 4, v[vT3]
  v_xor_b32 v[vKB1], 32, v[vKB0]
  v_bfe_u32 v[vT3], v[vLANE], 2, 2
  v_lshl_add_u32 v[vT3], v[vT2], 2, v[vT3]
  v_lshlrev_b32 v[vT3], 6, v[vT3]
  v_bfe_u32 v[vT4], v[vLANE], 4, 1
  v_lshl_add_u32 v[vT3], v[vT4], 5, v[vT3]
  v_and_b32 v[vT4], 3, v[vLANE]
  v_lshl_add_u32 v[vVB], v[vT4], 3, v[vT3]
  v_add_u32 v[vVB], LV, v[vVB]
  v_lshlrev_b32 v[vHH4], 2, v[vT2]
  ; output staging: write address  r32*128 + ((2*? + hh) ^ (r32&7))*16 handled per block: base = r32*128, xor term below
  v_and_b32 v[vT3], 7, v[vT1]
  v_xor_b32 v[vT3], v[vT3], v[vT2]                ; (r32&7) ^ hh
  v_lshlrev_b32 v[vT3], 4, v[vT3]
  v_lshl_add_u32 v[vSW], v[vT1], 7, v[vT3]        ; write addr for chunk pair 0 (chunk = hh); chunk pair p: xor with p*32
  ; read address: row = lane>>3, chunk = (lane&7) ^ (row&7) ; rows + 8i -> + 1024 i
  v_lshrrev_b32 v[vT3], 3, v[vLANE]
  v_and_b32 v[vT4], 7, v[vLANE]
  v_xor_b32 v[vT5], v[vT4], v[vT3]
  v_lshlrev_b32 v[vT5], 4, v[vT5]
  v_lshl_add_u32 v[vSR], v[vT3], 7, v[vT5]
  ; global store lane offset: (lane>>3)*4096 + (lane&7)*16
  v_lshlrev_b32 v[vT4], 4, v[vT4]
  v_lshl_add_u32 v[vSO], v[vT3], 12, v[vT4]
  s_mov_b32 s[sCE], 0x3e0293ee
  s_mov_b32 s[sTHR], 0x427af232
  s_lshl_b32 s[sWD], s[sW], 11                    ; w*2048
  s_mov_b64 s[sC:sC+1], 0
  v_mov_b32 v[vT4], 0
  v_mov_b32 v[vT5], 0
  v_lshlrev_b32 v[vT7], 7, v[vLANE]               ; LDS address of chunk (w + 8*lane): 16 B each
  s_lshl_b32 s[sT1], s[sW], 4
  s_add_u32 s[sT1], s[sT1], LSCR
  v_add_u32 v[vT7], s[sT1], v[vT7]
  PREFETCH 0, PF1
  .set i, 0
  .rept NIT
    s_waitcnt vmcnt(NIT-1-i+PF1+4*TOUCH)
    v_cmp_ne_u32 s[sA:sA+1], v[vX+2*i], v[vX+2*i+1]
    v_cmp_gt_i32 vcc, v[vX+2*i], v[vX+2*i+1]
    .if i == NIT-1
      v_cmp_gt_i32 s[sB:sB+1], s[sLASTC], %2       ; element < T-1
      s_and_b64 s[sA:sA+1], s[sA:sA+1], s[sB:sB+1]
      s_and_b64 vcc, vcc, s[sB:sB+1]
      v_cmp_eq_u32 s[sB:sB+1], s[sLASTC], %2       ; element == T-1
      s_or_b64 s[sA:sA+1], s[sA:sA+1], s[sB:sB+1]
    .endif
    s_or_b64 s[sC:sC+1], s[sC:sC+1], vcc
    s_nop 0
    v_writelane_b32 v[vT4], s[sA], i
    v_writelane_b32 v[vT5], s[sA+1], i
    .set i, i+1
  .endr
  s_or_b32 s[sT1], s[sC], s[sC+1]
  v_mov_b32 v[vT6], s[sT1]
  s_mov_b64 exec, (1<<NIT)-1
  ds_write_b96 v[vT7], v[vT4:vT6]
  s_mov_b64 exec, -1
  v_mov_b32 v[vT0], 0
  v_mov_b32 v[vT1], 0
  v_mov_b32 v[vT2], 0
  v_lshlrev_b32 v[vT3], 4, v[vLANE]
  v_add_u32 v[vT3], LSCR, v[vT3]
  v_lshlrev_b32 v[vT7], 6, v[vLANE]
  v_add_u32 v[vT7], 1, v[vT7]                     ; lane*64 + 1
  PREFETCH PF1, PF2
  s_waitcnt lgkmcnt(0)
  s_barrier
  STAMP 2
  ; ======================================================================== S2
  s_bfm_b64 exec, 8*NIT, 0
  ds_read_b96 v[vT0:vT2], v[vT3]
  s_mov_b64 exec, -1
  s_movk_i32 s[sT1], 128
  s_movk_i32 s[sT2], 256
  s_mov_b32 s[sR], 0
  s_waitcnt lgkmcnt(0)
  v_cmp_ne_u32 vcc, 0, v[vT2]
  v_bcnt_u32_b32 v[vT3], v[vT0], 0
  v_bcnt_u32_b32 v[vT3], v[vT1], v[vT3]
  s_cbranch_vccnz .Lfallback
  v_cmp_lt_u32 vcc, 1, v[vT3]
  v_ffbl_b32 v[vT4], v[vT0]
  v_ffbl_b32 v[vT5], v[vT1]
  s_cbranch_vccnz .Lfallback
  v_cmp_ne_u32 s[sA:sA+1], 0, v[vT3]
  v_add_u32 v[vT5], 32, v[vT5]
  v_min_u32 v[vT4], v[vT4], v[vT5]
  v_add_u32 v[vT4], v[vT4], v[vT7]
  v_cndmask_b32 v[vT4], 0, v[vT4], s[sA:sA+1]     ; end (0 if none)
  s_nop 1
  v_mov_b32_dpp v[vSTART], v[vT4] wave_shr:1 row_mask:0xf bank_mask:0xf bound_ctrl:0
  s_nop 1
  v_max_u32_dpp v[vSTART], v[vSTART], v[vSTART] row_shr:1 row_mask:0xf bank_mask:0xf bound_ctrl:0
  s_nop 1
  v_max_u32_dpp v[vSTART], v[vSTART], v[vSTART] row_shr:2 row_mask:0xf bank_mask:0xf bound_ctrl:0
  s_nop 1
  v_max_u32_dpp v[vSTART], v[vSTART], v[vSTART] row_shr:4 row_mask:0xf bank_mask:0xf bound_ctrl:0
  s_nop 1
  v_max_u32_dpp v[vSTART], v[vSTART], v[vSTART] row_shr:8 row_mask:0xf bank_mask:0xf bound_ctrl:0
  s_nop 1
  v_max_u32_dpp v[vSTART], v[vSTART], v[vSTART] row_bcast:15 row_mask:0xa bank_mask:0xf
  s_nop 1
  v_max_u32_dpp v[vSTART], v[vSTART], v[vSTART] row_bcast:31 row_mask:0xc bank_mask:0xf
  s_nop 0
  v_sub_u32 v[vLEN], v[vT4], v[vSTART]
  v_cndmask_b32 v[vLEN], 0, v[vLEN], s[sA:sA+1]   ; len (0 if none)
  v_add_u32 v[vT5], 127, v[vLEN]
  v_lshrrev_b32 v[vT5], 7, v[vT5]                 ; nq (mode A)
  v_cmp_lt_u32 vcc, s[sT1], v[vLEN]               ; len > 128
  v_cmp_ge_u32 s[sB:sB+1], s[sT2], v[vLEN]        ; len <= 256
  s_and_b64 s[sB:sB+1], s[sB:sB+1], vcc           ; mode B
  v_lshlrev_b32 v[vT5], 1, v[vT5]
  s_nop 0
  v_cndmask_b32 v[vT5], v[vT5], 2, s[sB:sB+1]     ; items (2 heads)
  v_lshlrev_b32 v[vT6], 16, v[vT5]
  v_cndmask_b32 v[vINCL], v[vT6], v[vT5], vcc     ; small -> high half
  v_mov_b32 v[vEXCL], v[vINCL]
  s_nop 0
  v_add_u32_dpp v[vINCL], v[vINCL], v[vINCL] row_shr:1 row_mask:0xf bank_mask:0xf bound_ctrl:0
  s_nop 1
  v_add_u32_dpp v[vINCL], v[vINCL], v[vINCL] row_shr:2 row_mask:0xf bank_mask:0xf bound_ctrl:0
  s_nop 1
  v_add_u32_dpp v[vINCL], v[vINCL], v[vINCL] row_shr:4 row_mask:0xf bank_mask:0xf bound_ctrl:0
  s_nop 1
  v_add_u32_dpp v[vINCL], v[vINCL], v[vINCL] row_shr:8 row_mask:0xf bank_mask:0xf bound_ctrl:0
  s_nop 1
  v_add_u32_dpp v[vINCL], v[vINCL], v[vINCL] row_bcast:15 row_mask:0xa bank_mask:0xf
  s_nop 1
  v_add_u32_dpp v[vINCL], v[vINCL], v[vINCL] row_bcast:31 row_mask:0xc bank_mask:0xf
  s_nop 0
  v_sub_u32 v[vEXCL], v[vINCL], v[vEXCL]
  v_readlane_b32 s[sT1], v[vINCL], 63
  s_and_b32 s[sNBIG], s[sT1], 0xffff
  s_lshr_b32 s[sTOT], s[sT1], 16
  s_add_u32 s[sTOT], s[sTOT], s[sNBIG]
  .if DBGW & 8
  s_mov_b32 s[sSAFE], 1
  .else
  s_mov_b32 s[sSAFE], 0
  .endif
  STAMP 3

  ; ---- DMA pieces.  sKDST / sVDST: ring offset + w*2048 of the next tile; rows beyond the segment end are out of range (zero fill, no traffic)
  .macro DK1
    s_mov_b32 m0, s[sKDST]
    s_add_u32 s[sT1], s[sKDST], 16384
    buffer_load_dwordx4 v[vOK], s[sK:sK+3], s[sKSO] offen lds
    .if DBGW & 4
    s_nop 3
    .endif
  .endm
  .macro DK2
    s_add_u32 m0, s[sKDST], 1024
    s_and_b32 s[sKDST], s[sT1], 0xffff
    buffer_load_dwordx4 v[vOK2], s[sK:sK+3], s[sKSO] offen lds
    .if DBGW & 4
    s_nop 3
    .endif
    s_add_u32 s[sKSO], s[sKSO], 64*4096
    s_min_u32 s[sKSO], s[sKSO], s[sMAXSO]
  .endm
  .macro DV1
    s_add_u32 m0, s[sVDST], LV
    s_add_u32 s[sT3], s[sVDST], 16384
    buffer_load_dwordx4 v[vOV], s[sV:sV+3], s[sVSO] offen lds
    .if DBGW & 4
    s_nop 3
    .endif
  .endm
  .macro DV2
    s_add_u32 m0, s[sVDST], LV+1024
    s_and_b32 s[sVDST], s[sT3], 0xffff
    buffer_load_dwordx4 v[vOV2], s[sV:sV+3], s[sVSO] offen lds
    .if DBGW & 4
    s_nop 3
    .endif
    s_add_u32 s[sVSO], s[sVSO], 64*4096
    s_min_u32 s[sVSO], s[sVSO], s[sMAXSO]
  .endm
  .macro DALL
    DK1
    DK2
    DV1
    DV2
  .endm
  .macro BAR4 modea=0, bit=2                        ; previous group had 4 pieces: everything older has landed
    .if \modea
      s_cmp_eq_u32 s[sMB], 1
      s_cbranch_scc1 .Lnob\@
    .endif
    .if DBGW & \bit
    s_waitcnt vmcnt(0) lgkmcnt(0)
    .else
    s_waitcnt vmcnt(4) lgkmcnt(0)
    .endif
    s_barrier
    .if \modea
    s_branch .Lbd\@
  .Lnob\@:
    s_waitcnt lgkmcnt(0)
  .Lbd\@:
    .endif
  .endm
  .macro BARN modea=0                               ; nothing was issued behind the needed pieces
    .if \modea
      s_cmp_eq_u32 s[sMB], 1
      s_cbranch_scc1 .Lnob\@
    .endif
    s_waitcnt vmcnt(0) lgkmcnt(0)
    s_barrier
    .if \modea
    s_branch .Lbd\@
  .Lnob\@:
    s_waitcnt lgkmcnt(0)
  .Lbd\@:
    .endif
  .endm
  .macro BAR0 modea=0
    .if \modea
      s_cmp_eq_u32 s[sMB], 1
      s_cbranch_scc1 .Lnob\@
    .endif
    s_waitcnt vmcnt(0) lgkmcnt(0)
    s_barrier
    .if \modea
  .Lnob\@:
    .endif
  .endm

.Litem:
  ; idx = r*32 + (r odd ? 31-j : j)
  s_lshl_b32 s[sT1], s[sR], 5
  s_sub_u32 s[sT2], 31, s[sJ]
  s_bitcmp1_b32 s[sR], 0
  s_cselect_b32 s[sT2], s[sT2], s[sJ]
  s_add_u32 s[sT1], s[sT1], s[sT2]                ; idx
  s_cmp_ge_u32 s[sT1], s[sTOT]
  s_cbranch_scc1 .Ldone
  s_sub_u32 s[sT2], s[sT1], s[sNBIG]
  s_cmp_lt_u32 s[sT1], s[sNBIG]
  s_cselect_b32 s[sT1], s[sT1], s[sT2]            ; index inside its class
  s_cselect_b32 s[sT2], 0, 16                     ; field shift
  v_bfe_u32 v[vT0], v[vINCL], s[sT2], 16
  v_bfe_u32 v[vT1], v[vEXCL], s[sT2], 16
  v_cmp_lt_u32 s[sA:sA+1], s[sT1], v[vT0]
  v_cmp_ge_u32 vcc, s[sT1], v[vT1]
  s_and_b64 s[sA:sA+1], s[sA:sA+1], vcc
  s_ff1_i32_b64 s[sT2], s[sA:sA+1]
  v_readlane_b32 s[sK0], v[vSTART], s[sT2]
  v_readlane_b32 s[sLEN], v[vLEN], s[sT2]
  v_readlane_b32 s[sT3], v[vT1], s[sT2]
  s_sub_u32 s[sT1], s[sT1], s[sT3]                ; local item
  s_and_b32 s[sT2], s[sT1], 1
  s_lshl_b32 s[sT2], s[sT2], 3
  s_add_u32 s[sT2], s[sT2], s[sG]
  s_lshl_b32 s[sHOFF], s[sT2], 8                  ; head*256 bytes
  s_lshr_b32 s[sT1], s[sT1], 1
  s_lshl_b32 s[sT1], s[sT1], 7
  s_add_u32 s[sQ0], s[sK0], s[sT1]
  s_lshl_b32 s[sT1], s[sW], 3
  s_add_u32 s[sT2], s[sQ0], s[sT1]
  s_lshl_b32 s[sT2], s[sT2], 12
  s_add_u32 s[sT2], s[sT2], s[sHOFF]              ; (q0 + 8w)*4096 + hoff
  ; ---- Q DMA
  s_add_u32 m0, s[sWD], LX
  s_add_u32 s[sT3], s[sT2], 64*4096
  buffer_load_dwordx4 v[vOK], s[sQ:sQ+3], s[sT2] offen lds
  s_add_u32 m0, s[sWD], LX+1024
  s_add_u32 s[sT4], s[sT2], 128*4096
  buffer_load_dwordx4 v[vOK2], s[sQ:sQ+3], s[sT2] offen lds
  s_add_u32 m0, s[sWD], LX+16384
  s_add_u32 s[sT1], s[sK0], s[sT1]
  buffer_load_dwordx4 v[vOK], s[sQ:sQ+3], s[sT3] offen lds
  s_add_u32 m0, s[sWD], LX+16384+1024
  s_lshl_b32 s[sT1], s[sT1], 12
  buffer_load_dwordx4 v[vOK2], s[sQ:sQ+3], s[sT3] offen lds
  s_add_u32 s[sKSO], s[sT1], s[sHOFF]             ; (k0 + 8w)*4096 + hoff
  s_add_u32 s[sMAXSO], s[sTM16], 15*4096
  s_add_u32 s[sMAXSO], s[sMAXSO], s[sHOFF]        ; (T-1)*4096 + hoff
  s_min_u32 s[sKSO], s[sKSO], s[sMAXSO]
  s_mov_b32 s[sVSO], s[sKSO]
  s_add_u32 s[sQEND], s[sK0], s[sLEN]
  s_add_u32 s[sNK], s[sLEN], 63
  s_lshr_b32 s[sNK], s[sNK], 6
  s_lshl_b32 s[sO+2], s[sQEND], 12                ; the output descriptor ends at the segment end
  s_sub_u32 s[sT1], s[sLEN], 129
  s_cmp_lt_u32 s[sT1], 128
  s_cselect_b32 s[sMB], 1, 0                      ; mode B: 128 < len <= 256
  s_mov_b32 s[sKDST], s[sWD]
  s_mov_b32 s[sVDST], s[sWD]
  s_cbranch_scc0 .Lq_done
  s_add_u32 m0, s[sWD], LQ2
  s_add_u32 s[sT3], s[sT4], 64*4096
  buffer_load_dwordx4 v[vOK], s[sQ:sQ+3], s[sT4] offen lds
  s_add_u32 m0, s[sWD], LQ2+1024
  s_nop 0
  buffer_load_dwordx4 v[vOK2], s[sQ:sQ+3], s[sT4] offen lds
  s_add_u32 m0, s[sWD], LQ3
  s_nop 0
  buffer_load_dwordx4 v[vOK], s[sQ:sQ+3], s[sT3] offen lds
  s_add_u32 m0, s[sWD], LQ3+1024
  s_nop 0
  buffer_load_dwordx4 v[vOK2], s[sQ:sQ+3], s[sT3] offen lds
  DK1
  DK2
  s_branch .Lk_done
.Lq_done:
  DK1
  DK2
  DK1
  DK2
.Lk_done:
  STAMP 4
  ; ---- per-wave geometry
  s_and_b32 s[sT1], s[sW], 3
  s_lshr_b32 s[sT2], s[sW], 2
  s_cmp_eq_u32 s[sMB], 1
  s_cselect_b32 s[sQB], s[sW], s[sT1]             ; q block
  s_cselect_b32 s[sKH], 0, s[sT2]                 ; key half (mode A)
  s_movk_i32 s[sKSTEP], 0x4000
  s_cselect_b32 s[sKSTEP], 8192, s[sKSTEP]
  s_add_u32 s[sT1], s[sLEN], 31
  s_lshr_b32 s[sT1], s[sT1], 5                    ; nst = ceil(len/32)
  s_add_u32 s[sT2], s[sT1], 1
  s_sub_u32 s[sT2], s[sT2], s[sKH]
  s_lshr_b32 s[sT2], s[sT2], 1
  s_cmp_eq_u32 s[sMB], 1
  s_cselect_b32 s[sNS], s[sT1], s[sT2]            ; steps of this wave
  s_cselect_b32 s[sNU], s[sT1], s[sNK]            ; step slots
  s_and_b32 s[sLIM], s[sLEN], 31
  s_sub_u32 s[sT2], s[sT1], 1
  s_and_b32 s[sT2], s[sT2], 1
  s_cmp_eq_u32 s[sT2], s[sKH]
  s_cselect_b32 s[sT3], 1, s[sMB]                 ; owner of the last sub-tile
  s_cmp_lg_u32 s[sLIM], 0
  s_cselect_b32 s[sT3], s[sT3], 0
  s_sub_u32 s[sMSTEP], s[sNS], 1
  s_cmp_eq_u32 s[sT3], 1
  s_cselect_b32 s[sMSTEP], s[sMSTEP], -1
  s_lshl_b32 s[sT1], s[sQB], 5
  s_add_u32 s[sQR], s[sT1], s[sQ0]                ; first q row of this wave
  s_cmp_lt_u32 s[sQR], s[sQEND]
  s_cselect_b32 s[sNS], s[sNS], 0
  ; FULL slots with DMA: tile iteration t <= nk-3
  s_sub_i32 s[sND], s[sNK], 2
  s_max_i32 s[sND], s[sND], 0
  s_lshl_b32 s[sND], s[sND], s[sMB]
  ; Q read base
  s_lshl_b32 s[sT1], s[sQB], 13
  s_add_u32 s[sT2], s[sT1], LX                    ; qb < 4
  s_add_u32 s[sT3], s[sT1], LQ2-4*8192            ; qb 4,5
  s_add_u32 s[sT4], s[sT1], LQ3-6*8192            ; qb 6,7
  s_cmp_lt_u32 s[sQB], 6
  s_cselect_b32 s[sT3], s[sT3], s[sT4]
  s_cmp_lt_u32 s[sQB], 4
  s_cselect_b32 s[sT2], s[sT2], s[sT3]
  v_add_u32 v[vT0], s[sT2], v[vKB0]
  v_add_u32 v[vT1], s[sT2], v[vKB1]
  s_lshl_b32 s[sKOFF], s[sKH], 13
  s_mov_b32 s[sVOFF], s[sKOFF]
  v_add_u32 v[vKA0], s[sKOFF], v[vKB0]
  v_add_u32 v[vKA1], s[sKOFF], v[vKB1]
  ; group 0
  DALL
  .set i, 0
  .rept 64
    v_mov_b32 v[vO+i], 0
    .set i, i+1
  .endr
  v_mov_b32 v[vL0], 0
  v_mov_b32 v[vL1], 0
  v_mov_b32 v[vM], 0xf149f2ca
  s_mov_b32 s[sPEND], 0
  s_mov_b32 s[sU], 0
  BAR4 0, 1                                         ; barrier P: Q, K0 (K1) landed
  STAMP 5

  .macro KREAD i
    .if (ABL & 8)
    .elseif (\i & 1)
      ds_read_b128 v[vK+4*\i:vK+4*\i+3], v[vKA1] offset:512*(\i>>1)
    .else
      ds_read_b128 v[vK+4*\i:vK+4*\i+3], v[vKA0] offset:512*(\i>>1)
    .endif
  .endm
  .macro KADV                                       ; advance K read addresses to the next step
    s_add_u32 s[sKOFF], s[sKOFF], s[sKSTEP]
    s_and_b32 s[sKOFF], s[sKOFF], 0xffff
    v_add_u32 v[vKA0], s[sKOFF], v[vKB0]
    v_add_u32 v[vKA1], s[sKOFF], v[vKB1]
  .endm
  .macro QKM c, i
    .if \i == 0
      v_mfma_f32_32x32x16_bf16 v[\c:\c+15], v[vK:vK+3], v[vQ:vQ+3], 0
    .else
      v_mfma_f32_32x32x16_bf16 v[\c:\c+15], v[vK+4*\i:vK+4*\i+3], v[vQ+4*\i:vQ+4*\i+3], v[\c:\c+15]
    .endif
  .endm
  .macro VREAD j                                    ; V fragment j: d = j&3, ks = j>>2
    .if (ABL & 4) == 0
    ds_read_b64_tr_b16 v[vV+4*\j:vV+4*\j+1], v[vVA] offset:512*(\j&3)+4096*(\j>>2)
    ds_read_b64_tr_b16 v[vV+4*\j+2:vV+4*\j+3], v[vVA] offset:512*(\j&3)+4096*(\j>>2)+2048
    .endif
  .endm
  .macro PVM j
    v_mfma_f32_32x32x16_bf16 v[vO+16*(\j&3):vO+16*(\j&3)+15], v[vV+4*\j:vV+4*\j+3], v[vP+4*(\j>>2):vP+4*(\j>>2)+3], v[vO+16*(\j&3):vO+16*(\j&3)+15]
  .endm
  .macro MASK c                                     ; keys >= lim of this sub-tile -> -inf
    v_sub_u32 v[vT2], s[sLIM], v[vHH4]
    v_mov_b32 v[vT3], 0xff800000
    .set r, 0
    .rept 16
      v_cmp_lt_i32 vcc, (r&3)+8*(r>>2), v[vT2]
      s_nop 1
      v_cndmask_b32 v[\c+r], v[vT3], v[\c+r], vcc
      .set r, r+1
    .endr
  .endm
  .macro ROWMAX1 c
    v_max3_f32 v[vT2], v[\c+0], v[\c+1], v[\c+2]
    v_max3_f32 v[vT3], v[\c+3], v[\c+4], v[\c+5]
    v_max3_f32 v[vT2], v[vT2], v[\c+6], v[\c+7]
    v_max3_f32 v[vT3], v[vT3], v[\c+8], v[\c+9]
    v_max3_f32 v[vT2], v[vT2], v[\c+10], v[\c+11]
    v_max3_f32 v[vT3], v[vT3], v[\c+12], v[\c+13]
    v_max3_f32 v[vT2], v[vT2], v[\c+14], v[\c+15]
    v_max_f32 v[vT2], v[vT2], v[vT3]
    v_mov_b32 v[vT3], v[vT2]
  .endm
  .macro ROWMAX2                                    ; needs 2 wait states after ROWMAX1
    v_permlane32_swap_b32 v[vT2], v[vT3]
    s_nop 0
    v_max_f32 v[vT2], v[vT2], v[vT3]                ; vT2 = row max
  .endm
  .macro EXPE c, r
    .if (ABL & 2) == 0
    v_fma_f32 v[\c+(\r)], v[\c+(\r)], s[sCE], v[vNMC]
    v_exp_f32 v[\c+(\r)], v[\c+(\r)]
    .endif
  .endm
  .macro SUMPK p, i                                 ; row sums + pack of elements 2i, 2i+1
    .if (ABL & 32) == 0
    v_add_f32 v[vL0], v[vL0], v[\p+2*\i]
    v_add_f32 v[vL1], v[vL1], v[\p+2*\i+1]
    v_cvt_pk_bf16_f32 v[vP+\i], v[\p+2*\i], v[\p+2*\i+1]
    .endif
  .endm
  .macro ADDE p, e                                  ; row sum of element e (two chains)
    .if (ABL & 32) == 0
    .if ((\e) & 1)
      v_add_f32 v[vL1], v[vL1], v[\p+(\e)]
    .else
      v_add_f32 v[vL0], v[vL0], v[\p+(\e)]
    .endif
    .endif
  .endm
  .macro CVTE p, i                                  ; pack of elements 2i, 2i+1
    .if (ABL & 32) == 0
    v_cvt_pk_bf16_f32 v[vP+\i], v[\p+2*\i], v[\p+2*\i+1]
    .endif
  .endm
  .macro VADDR                                      ; V read address for the current step, then advance
    v_add_u32 v[vVA], s[sVOFF], v[vVB]
    s_add_u32 s[sVOFF], s[sVOFF], s[sKSTEP]
    s_and_b32 s[sVOFF], s[sVOFF], 0xffff
  .endm
  .macro RESCALE
    s_nop 7
    s_nop 7
    .set i, 0
    .rept 64
      v_mul_f32 v[vO+i], v[vAL], v[vO+i]
      .set i, i+1
    .endr
    v_mul_f32 v[vL0], v[vAL], v[vL0]
    v_mul_f32 v[vL1], v[vAL], v[vL1]
    s_mov_b32 s[sPEND], 0
  .endm

  ; ---- prologue compute: step 0
  s_cmp_eq_u32 s[sNS], 0
  s_cbranch_scc1 .Lpro_none
  .set i, 0
  .rept 8
    KREAD i
    .if (i & 1)
      ds_read_b128 v[vQ+4*i:vQ+4*i+3], v[vT1] offset:512*(i>>1)
    .else
      ds_read_b128 v[vQ+4*i:vQ+4*i+3], v[vT0] offset:512*(i>>1)
    .endif
    .set i, i+1
  .endr
  KADV
  .set i, 0
  .rept 8
    .if i == 0
      s_waitcnt lgkmcnt(14)
    .elseif i == 1
      s_waitcnt lgkmcnt(13)
    .elseif i == 2
      s_waitcnt lgkmcnt(12)
    .elseif i == 3
      s_waitcnt lgkmcnt(11)
    .elseif i == 4
      s_waitcnt lgkmcnt(10)
    .elseif i == 5
      s_waitcnt lgkmcnt(9)
    .elseif i == 6
      s_waitcnt lgkmcnt(8)
    .else
      s_waitcnt lgkmcnt(7)
    .endif
    QKM vSA, i
    KREAD i
    .if i == 1
      DK1
    .elseif i == 3
      DK2
    .elseif i == 5
      DV1
    .elseif i == 7
      DV2
    .endif
    .set i, i+1
  .endr
  KADV
  s_nop 7
  s_cmp_lg_u32 s[sMSTEP], 0
  s_cbranch_scc1 .Lnomask0
  MASK vSA
.Lnomask0:
  ROWMAX1 vSA
  s_nop 1
  ROWMAX2
  v_max_f32 v[vM], v[vM], v[vT2]
  v_mul_f32 v[vNMC], 0xbe0293ee, v[vM]
  s_sub_u32 s[sNF], s[sNS], 1                     ; FULL slots of this wave
  s_min_u32 s[sNDF], s[sND], s[sNF]               ; ... of which issue DMA
  VADDR
  s_cmp_eq_u32 s[sSAFE], 1
  s_cbranch_scc1 .Lpro_safe
  .set r, 0
  .rept 11
    EXPE vSA, r
    .set r, r+1
  .endr
  .set r, 0
  .rept 4
    ADDE vSA, r
    .set r, r+1
  .endr
  STAMP 6
  s_cmp_lt_u32 s[sU], s[sNDF]
  s_cbranch_scc1 .LloopF
  s_branch .LdispF0
.Lpro_safe:
  .set r, 0
  .rept 16
    EXPE vSA, r
    .set r, r+1
  .endr
  s_cmp_eq_u32 s[sNF], 0
  s_cbranch_scc1 .LlastS0
  s_branch .LloopS
.Lpro_none:
  DALL
  s_branch .Lnone

  ; ======================================================================== SAFE step (online max every step)
  .macro FULLS cur, prev, modea
    s_cmp_lt_u32 s[sU], s[sND]
    s_cselect_b32 s[sF], 1, 0
    s_cbranch_scc0 .Lbn\@
    BAR4 \modea
    s_branch .Lbq\@
  .Lbn\@:
    BARN \modea
  .Lbq\@:
    .set g, 0
    .rept 8
      QKM \cur, g
      VREAD g
      SUMPK \prev, g
      .if g == 1
        DMAK1 \modea, 2
      .elseif g == 5
        DMAK2 \modea, 2
      .endif
      .set g, g+1
    .endr
    s_waitcnt lgkmcnt(4)
    PVM 0
    KREAD 0
    KREAD 1
    s_add_u32 s[sU], s[sU], 1
    s_nop 0
    PVM 1
    s_cmp_lg_u32 s[sMSTEP], s[sU]
    s_cbranch_scc1 .Lnm\@
    MASK \cur
  .Lnm\@:
    ROWMAX1 \cur
    PVM 2
    KREAD 2
    ROWMAX2
    v_sub_f32 v[vT3], v[vT2], v[vM]
    v_cmp_lt_f32 vcc, s[sTHR], v[vT3]
    s_cbranch_vccz .Lkeep\@
    v_max_f32 v[vT3], v[vM], v[vT2]
    v_sub_f32 v[vAL], v[vM], v[vT3]
    v_mul_f32 v[vAL], s[sCE], v[vAL]
    v_exp_f32 v[vAL], v[vAL]
    v_mov_b32 v[vM], v[vT3]
    v_mul_f32 v[vNMC], 0xbe0293ee, v[vM]
    s_mov_b32 s[sPEND], 1
  .Lkeep\@:
    PVM 3
    KREAD 3
    DMAV1 \modea, 2
    EXPE \cur, 0
    EXPE \cur, 1
    EXPE \cur, 2
    PVM 4
    KREAD 4
    EXPE \cur, 3
    EXPE \cur, 4
    EXPE \cur, 5
    PVM 5
    KREAD 5
    EXPE \cur, 6
    EXPE \cur, 7
    EXPE \cur, 8
    s_waitcnt lgkmcnt(6)
    PVM 6
    KREAD 6
    DMAV2 \modea, 2
    EXPE \cur, 9
    EXPE \cur, 10
    EXPE \cur, 11
    PVM 7
    KREAD 7
    EXPE \cur, 12
    EXPE \cur, 13
    EXPE \cur, 14
    EXPE \cur, 15
    KADV
    VADDR
    s_cmp_eq_u32 s[sPEND], 0
    s_cbranch_scc1 .Lsend\@
    RESCALE
  .Lsend\@:
    s_cmp_eq_u32 s[sU], s[sNF]
  .endm
  .macro LASTS prev, modea
    BAR0 \modea
    .set g, 0
    .rept 8
      VREAD g
      SUMPK \prev, g
      .set g, g+1
    .endr
    s_add_u32 s[sU], s[sU], 1
    s_waitcnt lgkmcnt(0)
    .set g, 0
    .rept 8
      PVM g
      .set g, g+1
    .endr
    s_branch .Lnone_tail
  .endm

  ; DMA slots inside a step: odd slots of mode B issue nothing.  dma: 0 none, 1 always, 2 run-time flag sF
  .macro DMAK1 modea, dma=1
    .if \dma
    .if \dma == 2
      s_cmp_eq_u32 s[sF], 0
      s_cbranch_scc1 .Ld\@
    .endif
    .if \modea
      s_cmp_eq_u32 s[sMB], 1
      s_cbranch_scc1 .Ld\@
    .endif
    DK1
  .Ld\@:
    .endif
  .endm
  .macro DMAK2 modea, dma=1
    .if \dma
    .if \dma == 2
      s_cmp_eq_u32 s[sF], 0
      s_cbranch_scc1 .Ld\@
    .endif
    .if \modea
      s_cmp_eq_u32 s[sMB], 1
      s_cbranch_scc1 .Ld\@
    .endif
    DK2
  .Ld\@:
    .endif
  .endm
  .macro DMAV1 modea, dma=1
    .if \dma
    .if \dma == 2
      s_cmp_eq_u32 s[sF], 0
      s_cbranch_scc1 .Ld\@
    .endif
    .if \modea
      s_cmp_eq_u32 s[sMB], 1
      s_cbranch_scc1 .Ld\@
    .endif
    DV1
  .Ld\@:
    .endif
  .endm
  .macro DMAV2 modea, dma=1
    .if \dma
    .if \dma == 2
      s_cmp_eq_u32 s[sF], 0
      s_cbranch_scc1 .Ld\@
    .endif
    .if \modea
      s_cmp_eq_u32 s[sMB], 1
      s_cbranch_scc1 .Ld\@
    .endif
    DV2
  .Ld\@:
    .endif
  .endm

  ; ======================================================================== FAST step (reference = row max of the wave's first sub-tile;
  ; overflow is detected on the row sums at the end of the item and the item is redone with the SAFE steps)
  ; prev enters with exp done for e0..e10 and e0..e3 summed; vVA is set for this step
  .macro FULLF cur, prev, modea, dma
    .if \dma
      BAR4 \modea
    .else
      BARN \modea
    .endif
    ; ---------------- phase A
    QKM \cur, 0
    VREAD 0
    EXPE \prev, 11
    CVTE \prev, 0
    QKM \cur, 1
    VREAD 1
    EXPE \prev, 12
    CVTE \prev, 1
    QKM \cur, 2
    VREAD 2
    DMAK1 \modea, \dma
    EXPE \prev, 13
    CVTE \prev, 2
    QKM \cur, 3
    VREAD 3
    EXPE \prev, 14
    CVTE \prev, 3
    ADDE \prev, 4
    QKM \cur, 4
    VREAD 4
    EXPE \prev, 15
    CVTE \prev, 4
    ADDE \prev, 5
    QKM \cur, 5
    VREAD 5
    CVTE \prev, 5
    CVTE \prev, 6
    ADDE \prev, 6
    ADDE \prev, 7
    ADDE \prev, 8
    QKM \cur, 6
    VREAD 6
    DMAK2 \modea, \dma
    CVTE \prev, 7
    ADDE \prev, 9
    ADDE \prev, 10
    ADDE \prev, 11
    QKM \cur, 7
    VREAD 7
    ADDE \prev, 12
    ADDE \prev, 13
    ADDE \prev, 14
    ADDE \prev, 15
    ; ---------------- phase B
    s_waitcnt lgkmcnt(4)
    PVM 0
    KREAD 0
    s_add_u32 s[sU], s[sU], 1
    DMAV1 \modea, \dma
    s_cmp_lg_u32 s[sMSTEP], s[sU]
    s_cbranch_scc1 .Lnm\@
    MASK \cur
  .Lnm\@:
    EXPE \cur, 0
    PVM 1
    KREAD 1
    EXPE \cur, 1
    EXPE \cur, 2
    PVM 2
    KREAD 2
    KREAD 3
    EXPE \cur, 3
    ADDE \cur, 0
    PVM 3
    KREAD 4
    EXPE \cur, 4
    ADDE \cur, 1
    ADDE \cur, 2
    PVM 4
    KREAD 5
    EXPE \cur, 5
    EXPE \cur, 6
    PVM 5
    KREAD 6
    KREAD 7
    EXPE \cur, 7
    ADDE \cur, 3
    s_waitcnt lgkmcnt(8)
    PVM 6
    DMAV2 \modea, \dma
    EXPE \cur, 8
    EXPE \cur, 9
    KADV
    VADDR
    .if \dma
      s_cmp_lt_u32 s[sU], s[sNDF]
    .else
      s_cmp_lt_u32 s[sU], s[sNF]
    .endif
    PVM 7
    EXPE \cur, 10
  .endm
  .macro LASTF prev, modea
    BAR0 \modea
    .set g, 0
    .rept 8
      VREAD g
      .if g < 5
        EXPE \prev, 11+g
      .endif
      .set g, g+1
    .endr
    s_add_u32 s[sU], s[sU], 1
    .set g, 0
    .rept 8
      CVTE \prev, g
      .set g, g+1
    .endr
    s_waitcnt lgkmcnt(0)
    .set g, 0
    .rept 8
      PVM g
      ADDE \prev, 4+g
      .set g, g+1
    .endr
    ADDE \prev, 12
    ADDE \prev, 13
    ADDE \prev, 14
    ADDE \prev, 15
    s_branch .Lnone_tail
  .endm

.LloopS:
  FULLS vSB, vSA, 0
  s_cbranch_scc1 .LlastS1
  FULLS vSA, vSB, 1
  s_cbranch_scc0 .LloopS
.LlastS0:
  LASTS vSA, 0
.LlastS1:
  LASTS vSB, 1
.LloopF:
  FULLF vSB, vSA, 0, 1
  s_cbranch_scc0 .LdispF1
.LloopF1:
  FULLF vSA, vSB, 1, 1
  s_cbranch_scc1 .LloopF
.LdispF0:
  s_cmp_lt_u32 s[sU], s[sNF]
  s_cbranch_scc0 .LlastF0
.LfullN0:
  FULLF vSB, vSA, 0, 0
  s_cbranch_scc0 .LlastF1
.LfullN1:
  FULLF vSA, vSB, 1, 0
  s_cbranch_scc1 .LfullN0
.LlastF0:
  LASTF vSA, 0
.LdispF1:
  s_cmp_lt_u32 s[sU], s[sNF]
  s_cbranch_scc1 .LfullN1
.LlastF1:
  LASTF vSB, 1

  ; ---- slots in which this wave has nothing to compute
.Lnone:                                             ; wave without rows: keep loading its share of the tiles
  s_cmp_ge_u32 s[sU], s[sNU]
  s_cbranch_scc1 .Lepi
  s_bitcmp1_b32 s[sU], 0
  s_cselect_b32 s[sT1], s[sMB], 0
  s_cmp_eq_u32 s[sT1], 1
  s_cbranch_scc1 .Lnone_skip
  s_cmp_lt_u32 s[sU], s[sND]
  s_cbranch_scc0 .Lnone_fin
  s_waitcnt vmcnt(4) lgkmcnt(0)
  s_barrier
  DALL
  s_branch .Lnone_skip
.Lnone_fin:
  s_waitcnt vmcnt(0) lgkmcnt(0)
  s_barrier
.Lnone_skip:
  s_add_u32 s[sU], s[sU], 1
  s_branch .Lnone
.Lnone_tail:                                        ; after the wave's last step: only barriers remain
  s_cmp_ge_u32 s[sU], s[sNU]
  s_cbranch_scc1 .Lepi
  s_bitcmp1_b32 s[sU], 0
  s_cselect_b32 s[sT1], s[sMB], 0
  s_cmp_eq_u32 s[sT1], 1
  s_cbranch_scc1 .Lnt_skip
  s_barrier
.Lnt_skip:
  s_add_u32 s[sU], s[sU], 1
  s_branch .Lnone_tail

  ; ======================================================================== epilogue
  ; 32 f32 at vO+b.. (d 0..63 of a 64-wide half) -> bf16 -> LDS stage (row-major 128 B rows, 16-B chunks xor row) -> 4 full-line stores
  .macro OUTHALF b, stg                             ; stg: SGPR with the LDS stage base ; sT3 = global offset of row 0 of this half
    .set dd, 0
    .rept 2
      .set g, 0
      .rept 2
        v_cvt_pk_bf16_f32 v[vX+0], v[vO+\b+16*dd+8*g+0], v[vO+\b+16*dd+8*g+1]
        v_cvt_pk_bf16_f32 v[vX+1], v[vO+\b+16*dd+8*g+2], v[vO+\b+16*dd+8*g+3]
        v_cvt_pk_bf16_f32 v[vX+2], v[vO+\b+16*dd+8*g+4], v[vO+\b+16*dd+8*g+5]
        v_cvt_pk_bf16_f32 v[vX+3], v[vO+\b+16*dd+8*g+6], v[vO+\b+16*dd+8*g+7]
        v_xor_b32 v[vT4], (2*dd+g)*32, v[vT5]
        s_nop 0
        v_permlane32_swap_b32 v[vX+0], v[vX+2]
        v_permlane32_swap_b32 v[vX+1], v[vX+3]
        s_nop 0
        ds_write_b128 v[vT4], v[vX+0:vX+3]
        .set g, g+1
      .endr
      .set dd, dd+1
    .endr
    s_waitcnt lgkmcnt(0)
    ds_read_b128 v[vX+0:vX+3], v[vT6]
    ds_read_b128 v[vX+4:vX+7], v[vT6] offset:1024
    ds_read_b128 v[vX+8:vX+11], v[vT6] offset:2048
    ds_read_b128 v[vX+12:vX+15], v[vT6] offset:3072
    s_add_u32 s[sT4], s[sT3], 8*4096
    s_waitcnt lgkmcnt(3)
    buffer_store_dwordx4 v[vX+0:vX+3], v[vSO], s[sO:sO+3], s[sT3] offen
    s_add_u32 s[sT1], s[sT3], 16*4096
    s_waitcnt lgkmcnt(2)
    buffer_store_dwordx4 v[vX+4:vX+7], v[vSO], s[sO:sO+3], s[sT4] offen
    s_add_u32 s[sT2], s[sT3], 24*4096
    s_waitcnt lgkmcnt(1)
    buffer_store_dwordx4 v[vX+8:vX+11], v[vSO], s[sO:sO+3], s[sT1] offen
    s_waitcnt lgkmcnt(0)
    buffer_store_dwordx4 v[vX+12:vX+15], v[vSO], s[sO:sO+3], s[sT2] offen
  .endm
  .macro MERGE kb, rb                               ; kb: kept 32 regs, rb: 32 regs sent / received
    .set i, 0
    .rept 8
      ds_write_b128 v[vT0], v[vO+\rb+4*i:vO+\rb+4*i+3] offset:1024*i
      .set i, i+1
    .endr
    s_waitcnt lgkmcnt(0)
    s_barrier
    ds_read_b64 v[vT4:vT5], v[vT3]
    .set i, 0
    .rept 8
      ds_read_b128 v[vO+\rb+4*i:vO+\rb+4*i+3], v[vT1] offset:1024*i
      .set i, i+1
    .endr
    s_waitcnt lgkmcnt(8)
    v_max_f32 v[vT6], v[vM], v[vT4]
    v_sub_f32 v[vT0], v[vM], v[vT6]
    v_sub_f32 v[vT2], v[vT4], v[vT6]
    v_mul_f32 v[vT0], s[sCE], v[vT0]
    v_mul_f32 v[vT2], s[sCE], v[vT2]
    v_exp_f32 v[vT0], v[vT0]
    v_exp_f32 v[vT2], v[vT2]
    s_nop 0
    v_mul_f32 v[vT6], v[vT0], v[vL0]
    v_fma_f32 v[vT6], v[vT2], v[vT5], v[vT6]
    v_rcp_f32 v[vT6], v[vT6]
    s_nop 0
    v_mul_f32 v[vT0], v[vT0], v[vT6]                ; fa
    v_mul_f32 v[vT2], v[vT2], v[vT6]                ; fb
    .set i, 0
    .rept 32
      v_mul_f32 v[vO+\kb+i], v[vT0], v[vO+\kb+i]
      .set i, i+1
    .endr
    .set i, 0
    .rept 8
      .if i == 0
        s_waitcnt lgkmcnt(7)
      .elseif i == 1
        s_waitcnt lgkmcnt(6)
      .elseif i == 2
        s_waitcnt lgkmcnt(5)
      .elseif i == 3
        s_waitcnt lgkmcnt(4)
      .elseif i == 4
        s_waitcnt lgkmcnt(3)
      .elseif i == 5
        s_waitcnt lgkmcnt(2)
      .elseif i == 6
        s_waitcnt lgkmcnt(1)
      .else
        s_waitcnt lgkmcnt(0)
      .endif
      v_fma_f32 v[vO+\kb+4*i+0], v[vT2], v[vO+\rb+4*i+0], v[vO+\kb+4*i+0]
      v_fma_f32 v[vO+\kb+4*i+1], v[vT2], v[vO+\rb+4*i+1], v[vO+\kb+4*i+1]
      v_fma_f32 v[vO+\kb+4*i+2], v[vT2], v[vO+\rb+4*i+2], v[vO+\kb+4*i+2]
      v_fma_f32 v[vO+\kb+4*i+3], v[vT2], v[vO+\rb+4*i+3], v[vO+\kb+4*i+3]
      .set i, i+1
    .endr
    v_mov_b32 v[vT5], v[vAL]
    v_mov_b32 v[vT6], v[vNMC]
    OUTHALF \kb, 0
  .endm

.Lepi:
  STAMP 7
  v_mov_b32 v[vT7], 0x71800000
  v_add_f32 v[vL0], v[vL0], v[vL1]
  v_mov_b32 v[vL1], v[vL0]
  s_lshl_b32 s[sT3], s[sQR], 12
  s_add_u32 s[sT3], s[sT3], s[sHOFF]
  v_permlane32_swap_b32 v[vL0], v[vL1]
  s_cmp_eq_u32 s[sMB], 1
  v_add_f32 v[vL0], v[vL0], v[vL1]                ; row sum
  s_cbranch_scc1 .LepiB
  ; ---- mode A: exchange area: K ring (wave w at w*8192) ; stats in X region ; output stage in the V ring (w*4096)
  v_lshlrev_b32 v[vT0], 4, v[vLANE]
  s_lshl_b32 s[sT1], s[sW], 13
  s_xor_b32 s[sT2], s[sT1], 4*8192
  v_add_u32 v[vT1], s[sT2], v[vT0]                ; partner area
  v_add_u32 v[vT0], s[sT1], v[vT0]                ; own area
  v_and_b32 v[vT2], 31, v[vLANE]
  v_lshlrev_b32 v[vT2], 3, v[vT2]
  v_add_u32 v[vT2], LX, v[vT2]
  s_lshl_b32 s[sT1], s[sW], 8
  s_xor_b32 s[sT2], s[sT1], 4*256
  v_add_u32 v[vT3], s[sT2], v[vT2]
  v_add_u32 v[vT2], s[sT1], v[vT2]
  v_mov_b32 v[vT4], v[vM]
  v_mov_b32 v[vT5], v[vL0]
  ds_write_b64 v[vT2], v[vT4:vT5]
  s_lshl_b32 s[sT1], s[sKH], 7
  s_add_u32 s[sT3], s[sT3], s[sT1]
  s_lshl_b32 s[sT1], s[sW], 12
  s_add_u32 s[sT1], s[sT1], LV
  v_add_u32 v[vAL], s[sT1], v[vSW]                ; stage write base  (kept in vAL / vNMC across MERGE)
  v_add_u32 v[vNMC], s[sT1], v[vSR]               ; stage read base
  s_cmp_eq_u32 s[sKH], 0
  s_cbranch_scc1 .Lmerge0
  MERGE 32, 0
  s_branch .Lnext
.Lmerge0:
  MERGE 0, 32
  s_branch .Lnext
.LepiB:
  v_rcp_f32 v[vT0], v[vL0]
  s_waitcnt vmcnt(0)
  s_barrier
  s_lshl_b32 s[sT1], s[sW], 13
  v_add_u32 v[vT5], s[sT1], v[vSW]
  v_add_u32 v[vT6], s[sT1], v[vSR]
  s_nop 7
  .set i, 0
  .rept 64
    v_mul_f32 v[vO+i], v[vT0], v[vO+i]
    .set i, i+1
  .endr
  OUTHALF 0, 0
  v_add_u32 v[vT5], 4096, v[vT5]
  v_add_u32 v[vT6], 4096, v[vT6]
  s_add_u32 s[sT3], s[sT3], 128
  s_nop 1
  OUTHALF 32, 0
.Lnext:
  STAMP 8
  ; overflow check of the FAST path: any row sum that is not a normal-sized number -> redo this item with the online max
  v_cmp_nlt_f32 vcc, v[vL0], v[vT7]
  s_lshl_b32 s[sT1], s[sW], 2
  s_add_u32 s[sT1], s[sT1], LX+8192
  s_cmp_lg_u64 vcc, 0
  s_cselect_b32 s[sT2], 1, 0
  v_mov_b32 v[vT0], s[sT1]
  v_mov_b32 v[vT1], s[sT2]
  v_and_b32 v[vT2], 7, v[vLANE]
  v_lshlrev_b32 v[vT2], 2, v[vT2]
  v_add_u32 v[vT2], LX+8192, v[vT2]
  ds_write_b32 v[vT0], v[vT1]
  s_waitcnt lgkmcnt(0)
  s_barrier
  ds_read_b32 v[vT1], v[vT2]
  s_waitcnt lgkmcnt(0)
  v_cmp_ne_u32 vcc, 0, v[vT1]
  s_cmp_eq_u32 s[sSAFE], 1
  s_cbranch_scc1 .Lsafe_done
  s_cbranch_vccz .Lsafe_done
  s_mov_b32 s[sSAFE], 1
  s_barrier
  s_branch .Litem
.Lsafe_done:
  s_mov_b32 s[sSAFE], 0
  s_add_u32 s[sR], s[sR], 1
  s_lshl_b32 s[sT1], s[sR], 5
  s_cmp_ge_u32 s[sT1], s[sTOT]
  s_cbranch_scc1 .Ldone
  s_barrier
  s_branch .Litem
.Ldone:
  .if DIAG
    s_waitcnt vmcnt(0)
    STAMP 9
    s_memrealtime s[sA:sA+1]
    s_load_dwordx2 s[sB:sB+1], %0, 96
    s_lshl_b32 s[sT1], %1, 3
    s_add_u32 s[sT1], s[sT1], s[sW]
    s_lshl_b32 s[sT1], s[sT1], 8
    s_waitcnt lgkmcnt(0)
    s_add_u32 s[sB], s[sB], s[sT1]
    s_addc_u32 s[sB+1], s[sB+1], 0
    v_mov_b32 v[vT6], 0
    s_mov_b64 exec, 1
    .set i, 0
    .rept 10
      v_mov_b32 v[vT0], s[sST+2*i]
      v_mov_b32 v[vT1], s[sST+2*i+1]
      global_store_dwordx2 v[vT6], v[vT0:vT1], s[sB:sB+1] offset:8*i
      s_nop 1
      .set i, i+1
    .endr
    v_mov_b32 v[vT0], s[sRT]
    v_mov_b32 v[vT1], s[sRT+1]
    global_store_dwordx2 v[vT6], v[vT0:vT1], s[sB:sB+1] offset:80
    s_nop 1
    v_mov_b32 v[vT0], s[sA]
    v_mov_b32 v[vT1], s[sA+1]
    global_store_dwordx2 v[vT6], v[vT0:vT1], s[sB:sB+1] offset:88
    s_nop 1
  .endif
  s_endpgm
.Lfallback:
  s_waitcnt vmcnt(0) lgkmcnt(0)
  .purgem STAMP
  .purgem PREFETCH
  .purgem DK1
  .purgem DALL
  .purgem BAR4
  .purgem BAR0
  .purgem BARN
  .purgem FULLS
  .purgem LASTS
  .purgem FULLF
  .purgem LASTF
  .purgem DMAK1
  .purgem DMAK2
  .purgem DMAV1
  .purgem DMAV2
  .purgem DK2
  .purgem DV1
  .purgem DV2
  .purgem KREAD
  .purgem KADV
  .purgem QKM
  .purgem VREAD
  .purgem PVM
  .purgem MASK
  .purgem ROWMAX1
  .purgem ROWMAX2
  .purgem EXPE
  .purgem SUMPK
  .purgem VADDR
  .purgem RESCALE
  .purgem ADDE
  .purgem CVTE
  .purgem OUTHALF
  .purgem MERGE
  )" :: "s"(__builtin_amdgcn_kernarg_segment_ptr()), "s"(blockIdx.x), "v"(threadIdx.x), "i"(NIT)
     : "memory", "vcc", "m0",
       "s8","s9","s10","s11","s12","s13","s14","s15","s16","s17","s18","s19","s20","s21","s22","s23","s24","s25","s26","s27","s28","s29","s30","s31",
       "s33","s34","s35","s36","s37","s38","s39","s40","s41","s42","s43","s44","s45","s46","s47","s48","s49","s50","s51","s52","s53","s54","s55",
       "s56","s57","s58","s59","s60","s61","s62","s63","s64","s65","s66","s67","s68","s69","s70","s71","s72","s73","s74","s75","s76","s77","s78","s79",
       "s80","s81","s82","s83","s84","s85","s86","s87","s88","s89","s90","s91","s92","s93","s94","s95","s96","s97","s98","s99",
       "v1","v2","v3","v4","v5","v6","v7","v8","v9","v10","v11","v12","v13","v14","v15","v16","v17","v18","v19","v20","v21","v22","v23","v24","v25","v26","v27","v28","v29","v30","v31",
       "v32","v33","v34","v35","v36","v37","v38","v39","v40","v41","v42","v43","v44","v45","v46","v47","v48","v49","v50","v51","v52","v53","v54","v55","v56","v57","v58","v59","v60","v61","v62","v63",
       "v64","v65","v66","v67","v68","v69","v70","v71","v72","v73","v74","v75","v76","v77","v78","v79","v80","v81","v82","v83","v84","v85","v86","v87","v88","v89","v90","v91","v92","v93","v94","v95",
       "v96","v97","v98","v99","v100","v101","v102","v103","v104","v105","v106","v107","v108","v109","v110","v111","v112","v113","v114","v115","v116","v117","v118","v119","v120","v121","v122","v123","v124","v125","v126","v127",
       "v128","v129","v130","v131","v132","v133","v134","v135","v136","v137","v138","v139","v140","v141","v142","v143","v144","v145","v146","v147","v148","v149","v150","v151","v152","v153","v154","v155","v156","v157","v158","v159",
       "v160","v161","v162","v163","v164","v165","v166","v167","v168","v169","v170","v171","v172","v173","v174","v175","v176","v177","v178","v179","v180","v181","v182","v183","v184","v185","v186","v187","v188","v189","v190","v191",
       "v192","v193","v194","v195","v196","v197","v198","v199","v200","v201","v202","v203","v204","v205","v206","v207","v208","v209","v210","v211","v212","v213","v214","v215","v216","v217","v218","v219","v220","v221","v222","v223",
       "v224","v225","v226","v227","v228","v229","v230","v231","v232","v233","v234","v235","v236","v237","v238","v239","v240","v241","v242","v243","v244","v245","v246","v247","v248","v249","v250","v251","v252","v253","v254","v255");
  generic_path((const unsigned short*)Q, (const unsigned short*)K, (const unsigned short*)V, (const int*)seg, (unsigned short*)O, T, lds);
}

__global__ __launch_bounds__(512, 2)
void ragged_attn_generic(const void* Q, const void* K, const void* V, const void* seg, void* O, int T, int pad) {
  __shared__ __attribute__((aligned(16))) char lds[LDS_BYTES];
  generic_path((const unsigned short*)Q, (const unsigned short*)K, (const unsigned short*)V, (const int*)seg, (unsigned short*)O, T, lds);
}

extern "C" void kernel_launch(void* const* d_in, const int* in_sizes, int n_in, void* d_out, int out_size, void* d_ws, size_t ws_size,
                              hipStream_t stream) {
  const int T = in_sizes[3];
  const unsigned nb = (unsigned)T * ROWB, F = 0x00020000u;
  if (T > 2048 && T <= 2560) {
    ragged_attn_kernel<5><<<256, 512, 0, stream>>>(d_in[0], nb, F, d_in[1], nb, F, d_in[2], nb, F, d_in[3], (unsigned)T * 4u, F, d_out, nb, F,
                                                   T, T - 1 - 512 * 4, ((T + 31) / 32) * (int)ROWB, 0, d_ws);
  } else {
    ragged_attn_generic<<<256, 512, 0, stream>>>(d_in[0], d_in[1], d_in[2], d_in[3], d_out, T, 0);
  }
}
```

```cpp
#include <hip/hip_runtime.h>
#include <stdint.h>

typedef short    bf16x8 __attribute__((ext_vector_type(8)));
typedef short    s16x4  __attribute__((ext_vector_type(4)));
typedef float    f32x16 __attribute__((ext_vector_type(16)));
typedef float    f32x4  __attribute__((ext_vector_type(4)));
typedef float    f32x2  __attribute__((ext_vector_type(2)));
typedef __bf16   bf2    __attribute__((ext_vector_type(2)));
typedef unsigned u32x4  __attribute__((ext_vector_type(4)));
typedef unsigned long long u64;
#define LDSP __attribute__((address_space(3)))
typedef LDSP const char* lds_cptr;
typedef LDSP char* lds_ptr;
typedef short v4i16_t __attribute__((ext_vector_type(4)));
#define DI __device__ __forceinline__
#define MFMA(a, b, c) __builtin_amdgcn_mfma_f32_32x32x16_bf16((a), (b), (c), 0, 0, 0)

constexpr int NH = 16, HD = 128;
constexpr unsigned ROWB = NH * HD * 2;
constexpr unsigned LDS_BYTES = 163840;
constexpr float CEXP = 0.08838834764831845f * 1.4426950408889634f;
constexpr float THR_RAW = 8.0f / CEXP;
constexpr float MINIT = -1.0e30f;

DI unsigned rfl(unsigned x) { return __builtin_amdgcn_readfirstlane(x); }
DI unsigned cvtpk(float lo, float hi) { f32x2 v = {lo, hi}; bf2 b = __builtin_convertvector(v, bf2); return __builtin_bit_cast(unsigned, b); }
DI float swap_max(float x) { auto r = __builtin_amdgcn_permlane32_swap(__float_as_uint(x), __float_as_uint(x), false, false); return __builtin_fmaxf(__uint_as_float(r[0]), __uint_as_float(r[1])); }
DI float swap_add(float x) { auto r = __builtin_amdgcn_permlane32_swap(__float_as_uint(x), __float_as_uint(x), false, false); return __uint_as_float(r[0]) + __uint_as_float(r[1]); }
DI s16x4 vtr(lds_cptr p) { return __builtin_bit_cast(s16x4, __builtin_amdgcn_ds_read_tr16_b64_v4i16((LDSP v4i16_t*)p)); }
#define MX3(a, b, c) __builtin_fmaxf(__builtin_fmaxf((a), (b)), (c))

DI void dma16(__amdgpu_buffer_rsrc_t srd, unsigned voff, unsigned soff, unsigned m0v) {
  unsigned keep;
  asm volatile("s_nop 4\n\ts_mov_b32 %0, m0\n\ts_mov_b32 m0, %1\n\ts_nop 0\n\tbuffer_load_dwordx4 %2, %3, %4 offen lds\n\ts_mov_b32 m0, %0"
               : "=&s"(keep) : "s"(m0v), "v"(voff), "s"(srd), "s"(soff) : "memory");
}

__device__ __forceinline__ void generic_path(const unsigned short* __restrict__ Q, const unsigned short* __restrict__ K, const unsigned short* __restrict__ V,
                                          const int* __restrict__ seg, unsigned short* __restrict__ O, int T, char* lds) {
  constexpr unsigned SLOT = 32768, VOFF = 16384, XREG = 4 * SLOT;
  const int tid = threadIdx.x, lane = tid & 63, r32 = lane & 31, hh = lane >> 5;
  const int w = (int)rfl((unsigned)tid >> 6);
  const int grp = blockIdx.x & 7, jj = blockIdx.x >> 3, NJ = 32;
  const unsigned ldsb = (unsigned)(uintptr_t)lds;
  const lds_ptr L = (lds_ptr)lds;
  const int total_items = 2 * ((T + 127) >> 7);
  const __amdgpu_buffer_rsrc_t srdQ = __builtin_amdgcn_make_buffer_rsrc((void*)Q, (short)0, T * (int)ROWB, 0x00020000);
  const __amdgpu_buffer_rsrc_t srdK = __builtin_amdgcn_make_buffer_rsrc((void*)K, (short)0, T * (int)ROWB, 0x00020000);
  const __amdgpu_buffer_rsrc_t srdV = __builtin_amdgcn_make_buffer_rsrc((void*)V, (short)0, T * (int)ROWB, 0x00020000);
  const unsigned prow = (lane >> 2) & 7, pcb = lane >> 5, pc = lane & 3;
  const unsigned voffK = prow * ROWB + pcb * 64 + ((pc ^ ((2u * (unsigned)w + (prow >> 2)) & 3u)) * 16);
  const unsigned voffV = prow * ROWB + pcb * 64 + pc * 16;
  const unsigned kb0 = 2048u * (r32 >> 3) + 64u * (r32 & 7) + 16u * ((unsigned)hh ^ ((r32 >> 2) & 3u));
  const unsigned vb0 = ((unsigned)hh * 4 + ((lane & 15) >> 2)) * 64 + ((lane >> 4) & 1) * 32 + (lane & 3) * 8;
  for (int idx = jj; idx < total_items; idx += NJ) {
    __syncthreads();
    const int head = grp + 8 * (idx & 1);
    const int q0 = (idx >> 1) * 128, qend = min(q0 + 128, T);
    LDSP int* red = (LDSP int*)L;
    if (tid == 0) { red[0] = 0x7fffffff; red[1] = (int)0x80000000; red[2] = 0x7fffffff; red[3] = -1; }
    __syncthreads();
    if (tid < 128 && q0 + tid < qend) { const int id = seg[q0 + tid];
      __hip_atomic_fetch_min((LDSP int*)&red[0], id, __ATOMIC_RELAXED, __HIP_MEMORY_SCOPE_WORKGROUP);
      __hip_atomic_fetch_max((LDSP int*)&red[1], id, __ATOMIC_RELAXED, __HIP_MEMORY_SCOPE_WORKGROUP); }
    __syncthreads();
    const int idlo = red[0], idhi = red[1];
    int lo = 0x7fffffff, hi = -1;
    for (int i = tid; i < T; i += 512) { const int id = seg[i]; if (id >= idlo && id <= idhi) { lo = min(lo, i); hi = max(hi, i); } }
    __hip_atomic_fetch_min((LDSP int*)&red[2], lo, __ATOMIC_RELAXED, __HIP_MEMORY_SCOPE_WORKGROUP);
    __hip_atomic_fetch_max((LDSP int*)&red[3], hi, __ATOMIC_RELAXED, __HIP_MEMORY_SCOPE_WORKGROUP);
    __syncthreads();
    const int k0 = (int)rfl((unsigned)red[2]), kend = (int)rfl((unsigned)red[3]) + 1;
    __syncthreads();
    const int nk = (kend - k0 + 63) >> 6;
    const unsigned hoff = (unsigned)head * 256u;
#pragma unroll
    for (int i = 0; i < 2; ++i) {
      const unsigned m = (unsigned)w + 8u * i;
      const unsigned so = (unsigned)(q0 + 8 * (int)m) * ROWB + hoff;
      dma16(srdQ, voffK, so, ldsb + XREG + m * 2048u);
      dma16(srdQ, voffK, so + 128u, ldsb + XREG + m * 2048u + 1024u);
    }
    const int qb = w & 3, sub = w >> 2;
    const int qrow = q0 + 32 * qb + r32;
    const int segq = seg[qrow < T ? qrow : T - 1];
    bf16x8 qf[8];
    f32x16 o[4];
#pragma unroll
    for (int d = 0; d < 4; ++d) o[d] = f32x16{};
    float m_run = MINIT, l_run = 0.f;
    for (int t = 0; t < nk; ++t) {
      __syncthreads();
      {
        const unsigned so = (unsigned)(k0 + 64 * t + 8 * w) * ROWB + hoff;
        const unsigned d = ldsb + (unsigned)w * 2048u;
        dma16(srdK, voffK, so, d); dma16(srdK, voffK, so + 128u, d + 1024u);
        dma16(srdV, voffV, so, d + VOFF); dma16(srdV, voffV, so + 128u, d + VOFF + 1024u);
      }
      asm volatile("s_waitcnt vmcnt(0)" ::: "memory");
      __syncthreads();
      if (t == 0) {
#pragma unroll
        for (int s = 0; s < 8; ++s)
          qf[s] = *(const LDSP bf16x8*)(L + XREG + (unsigned)qb * 8192u + ((kb0 ^ ((s & 1) * 32u)) + 512u * (s >> 1)));
      }
      const int kbase = k0 + 64 * t + 32 * sub;
      const lds_cptr kp = (lds_cptr)L + (unsigned)sub * 8192u;
      f32x16 sc = f32x16{};
#pragma unroll
      for (int s = 0; s < 8; ++s) {
        const bf16x8 kf = *(const LDSP bf16x8*)(kp + ((kb0 ^ ((s & 1) * 32u)) + 512u * (s >> 1)));
        sc = MFMA(kf, qf[s], sc);
      }
#pragma unroll
      for (int r = 0; r < 16; ++r) {
        const int kk = kbase + (r & 3) + 8 * (r >> 2) + 4 * hh;
        const int id = seg[kk < T ? kk : T - 1];
        sc[r] = (kk < kend && id == segq) ? sc[r] : -__builtin_inff();
      }
      float a = MX3(sc[0], sc[1], sc[2]), b = MX3(sc[3], sc[4], sc[5]);
      a = MX3(a, sc[6], sc[7]); b = MX3(b, sc[8], sc[9]); a = MX3(a, sc[10], sc[11]); b = MX3(b, sc[12], sc[13]);
      a = MX3(a, sc[14], sc[15]);
      const float pm = swap_max(__builtin_fmaxf(a, b));
      const float mn = __builtin_fmaxf(m_run, pm);
      const float al = __builtin_amdgcn_exp2f((m_run - mn) * CEXP);
      m_run = mn; l_run *= al;
#pragma unroll
      for (int d = 0; d < 4; ++d)
#pragma unroll
        for (int r = 0; r < 16; ++r) o[d][r] *= al;
      const float nmc = -m_run * CEXP;
      float ps = 0.f;
#pragma unroll
      for (int r = 0; r < 16; ++r) { sc[r] = __builtin_amdgcn_exp2f(__builtin_fmaf(sc[r], CEXP, nmc)); ps += sc[r]; }
      l_run += ps;
      u32x4 pw0, pw1;
#pragma unroll
      for (int e = 0; e < 4; ++e) { pw0[e] = cvtpk(sc[2 * e], sc[2 * e + 1]); pw1[e] = cvtpk(sc[8 + 2 * e], sc[8 + 2 * e + 1]); }
      const bf16x8 p0 = __builtin_bit_cast(bf16x8, pw0), p1 = __builtin_bit_cast(bf16x8, pw1);
      const lds_cptr vp = kp + VOFF + vb0;
#pragma unroll
      for (int d = 0; d < 4; ++d) {
        const s16x4 a0 = vtr(vp + d * 512), a1 = vtr(vp + d * 512 + 2048), b0 = vtr(vp + d * 512 + 4096), b1 = vtr(vp + d * 512 + 6144);
        const bf16x8 va = __builtin_shufflevector(a0, a1, 0, 1, 2, 3, 4, 5, 6, 7), vb = __builtin_shufflevector(b0, b1, 0, 1, 2, 3, 4, 5, 6, 7);
        o[d] = MFMA(va, p0, o[d]);
        o[d] = MFMA(vb, p1, o[d]);
      }
    }
    l_run = swap_add(l_run);
    unsigned short* orow = O + (size_t)(qrow < T ? qrow : 0) * (NH * HD) + head * HD + 8 * hh;
    const bool row_ok = qrow < qend;
    const int kh = sub;
    __syncthreads();
    const unsigned xa = SLOT, xb = 2 * SLOT, xs = 3 * SLOT;
    const unsigned mine = (kh ? xb : xa) + (unsigned)qb * 8192u + (unsigned)lane * 16u;
    const unsigned theirs = (kh ? xa : xb) + (unsigned)qb * 8192u + (unsigned)lane * 16u;
#pragma unroll
    for (int dd = 0; dd < 2; ++dd)
#pragma unroll
      for (int g = 0; g < 4; ++g) {
        f32x4 v;
#pragma unroll
        for (int e = 0; e < 4; ++e) v[e] = kh ? o[dd][4 * g + e] : o[2 + dd][4 * g + e];
        *(LDSP f32x4*)(L + mine + (unsigned)(dd * 4 + g) * 1024u) = v;
      }
    if (hh == 0) { f32x2 s2 = {m_run, l_run}; *(LDSP f32x2*)(L + xs + (unsigned)w * 256u + (unsigned)r32 * 8u) = s2; }
    __syncthreads();
    const f32x2 o2 = *(const LDSP f32x2*)(L + xs + (unsigned)(w ^ 4) * 256u + (unsigned)r32 * 8u);
    const float mn = __builtin_fmaxf(m_run, o2[0]);
    const float wa = __builtin_amdgcn_exp2f((m_run - mn) * CEXP), wb = __builtin_amdgcn_exp2f((o2[0] - mn) * CEXP);
    const float inv = 1.0f / (wa * l_run + wb * o2[1]);
    const float fa = wa * inv, fb = wb * inv;
#pragma unroll
    for (int dd = 0; dd < 2; ++dd) {
      float r[16];
#pragma unroll
      for (int g = 0; g < 4; ++g) {
        const f32x4 v = *(const LDSP f32x4*)(L + theirs + (unsigned)(dd * 4 + g) * 1024u);
#pragma unroll
        for (int e = 0; e < 4; ++e) r[4 * g + e] = fa * (kh ? o[2 + dd][4 * g + e] : o[dd][4 * g + e]) + fb * v[e];
      }
#pragma unroll
      for (int g = 0; g < 4; g += 2) {
        unsigned ax = cvtpk(r[4 * g + 0], r[4 * g + 1]), ay = cvtpk(r[4 * g + 2], r[4 * g + 3]);
        unsigned bx = cvtpk(r[4 * g + 4], r[4 * g + 5]), by = cvtpk(r[4 * g + 6], r[4 * g + 7]);
        auto rx = __builtin_amdgcn_permlane32_swap(ax, bx, false, false); auto ry = __builtin_amdgcn_permlane32_swap(ay, by, false, false);
        const u32x4 v = {rx[0], ry[0], rx[1], ry[1]};
        if (row_ok) *(u32x4*)(orow + 64 * kh + 32 * dd + 8 * g) = v;
      }
    }
  }
}

#define DIAG 0
#define PFQ 1
#define PFKV 0
#define ABL 0
#define PFAT 1
#define DBGW 0
#define TOUCH 0
#define PF1 3
#define PF2 5
#define STR2(x) #x
#define STR(x) STR2(x)

template <int NIT>
__global__ __launch_bounds__(512, 2)
void ragged_attn_kernel(const void* Q, unsigned nQ, unsigned fQ, const void* K, unsigned nK, unsigned fK, const void* V, unsigned nV, unsigned fV,
                        const void* seg, unsigned nS, unsigned fS, void* O, unsigned nO, unsigned fO, int T, int lastc, int pfb, int pad, void* dbg) {
  __shared__ __attribute__((aligned(16))) char lds[LDS_BYTES];
  asm volatile(R"(
  .set NIT, %c3
  .set DIAG, )" STR(DIAG) R"(
  .set PFQ, )" STR(PFQ) R"(
  .set PFKV, )" STR(PFKV) R"(
  .set ABL, )" STR(ABL) R"(
  .set PFAT, )" STR(PFAT) R"(
  .set DBGW, )" STR(DBGW) R"(
  .set TOUCH, )" STR(TOUCH) R"(
  .set PF1, )" STR(PF1) R"(
  .set PF2, )" STR(PF2) R"(
  ; ---------------- SGPRs
  .set sQ, 8
  .set sK, 12
  .set sV, 16
  .set sS, 20
  .set sO, 24
  .set sT, 28
  .set sLASTC, 29
  .set sPFB, 30
  .set sF, 30
  .set sTM16, 31
  .set sG, 34
  .set sR, 35
  .set sNBIG, 36
  .set sTOT, 37
  .set sHOFF, 38
  .set sQ0, 39
  .set sQEND, 40
  .set sK0, 41
  .set sNK, 42
  .set sLEN, 43
  .set sMB, 44
  .set sNS, 45
  .set sU, 46
  .set sNU, 47
  .set sKOFF, 48
  .set sKSTEP, 49
  .set sVOFF, 50
  .set sKSO, 51
  .set sVSO, 52
  .set sKDST, 53
  .set sVDST, 54
  .set sMAXSO, 55
  .set sND, 56
  .set sMSTEP, 57
  .set sPEND, 58
  .set sW, 59
  .set sA, 60
  .set sB, 62
  .set sC, 64
  .set sT1, 66
  .set sT2, 67
  .set sT3, 68
  .set sT4, 69
  .set sCE, 70
  .set sTHR, 71
  .set sWD, 72
  .set sQB, 73
  .set sKH, 74
  .set sLIM, 75
  .set sJ, 76
  .set sQR, 77
  .set sRT, 78
  .set sSAFE, 64
  .set sNF, 65
  .set sNDF, 32+1
  .set sST, 80
  ; ---------------- VGPRs
  .set vLANE, 1
  .set vOK, 2
  .set vOK2, 3
  .set vOV, 4
  .set vOV2, 5
  .set vKB0, 6
  .set vKB1, 7
  .set vVB, 8
  .set vSTART, 9
  .set vLEN, 10
  .set vINCL, 11
  .set vEXCL, 12
  .set vM, 13
  .set vNMC, 14
  .set vL0, 15
  .set vL1, 16
  .set vKA0, 17
  .set vKA1, 18
  .set vVA, 19
  .set vAL, 20
  .set vSO, 21
  .set vHH4, 22
  .set vSW, 23
  .set vT0, 24
  .set vT1, 25
  .set vT2, 26
  .set vT3, 27
  .set vT4, 28
  .set vT5, 29
  .set vT6, 30
  .set vT7, 31
  .set vP, 32
  .set vX, 40
  .set vSR, 56
  .set vO, 64
  .set vQ, 128
  .set vSA, 160
  .set vSB, 176
  .set vK, 192
  .set vV, 224
  ; ---------------- LDS map
  .set LK, 0
  .set LV, 65536
  .set LX, 131072
  .set LQ2, 49152
  .set LQ3, 114688
  .set LSCR, 98304

  .macro STAMP i
    .if DIAG
      s_memtime s[sST+2*\i:sST+2*\i+1]
      s_waitcnt lgkmcnt(0)
    .endif
  .endm

  ; ======================================================================== entry
  s_load_dwordx16 s[8:23], %0, 0
  s_load_dwordx8 s[24:31], %0, 64
  .if DIAG
    s_memrealtime s[sRT:sRT+1]
  .endif
  STAMP 0
  v_and_b32 v[vLANE], 63, %2
  v_readfirstlane_b32 s[sW], %2
  v_lshlrev_b32 v[vT0], 2, %2                     ; tid*4
  s_and_b32 s[sG], %1, 7
  s_lshr_b32 s[sJ], %1, 3
  s_lshr_b32 s[sW], s[sW], 6
  ; prefetch offset: (tid>>5)*4096 + ((tid>>4)&1)*2048 + ((tid>>3)&1)*128 + (tid&7)*16 + g*256
  v_lshrrev_b32 v[vT6], 5, %2
  v_lshlrev_b32 v[vT6], 12, v[vT6]
  v_bfe_u32 v[vT7], %2, 4, 1
  v_lshl_add_u32 v[vT6], v[vT7], 11, v[vT6]
  v_bfe_u32 v[vT7], %2, 3, 1
  v_lshl_add_u32 v[vT6], v[vT7], 7, v[vT6]
  v_and_b32 v[vT7], 7, %2
  v_lshl_add_u32 v[vT6], v[vT7], 4, v[vT6]
  s_lshl_b32 s[sT3], s[sG], 8
  v_add_u32 v[vT6], s[sT3], v[vT6]
  ; DMA lane offsets
  v_bfe_u32 v[vT1], v[vLANE], 2, 3                ; prow
  v_lshrrev_b32 v[vT2], 5, v[vLANE]               ; hh / pcb
  v_and_b32 v[vT3], 3, v[vLANE]                   ; pc
  v_lshlrev_b32 v[vT4], 12, v[vT1]
  v_lshl_add_u32 v[vT4], v[vT2], 6, v[vT4]        ; prow*4096 + pcb*64
  v_lshl_add_u32 v[vOV], v[vT3], 4, v[vT4]
  v_bfe_u32 v[vT5], v[vLANE], 4, 1
  s_and_b32 s[sT4], s[sW], 1
  s_lshl_b32 s[sT4], s[sT4], 1
  v_or_b32 v[vT5], s[sT4], v[vT5]                 ; x
  v_xor_b32 v[vT5], v[vT5], v[vT3]
  v_lshl_add_u32 v[vOK], v[vT5], 4, v[vT4]
  s_waitcnt lgkmcnt(0)
  STAMP 1
  ; ======================================================================== S1: segment ends per 64-token chunk
  s_lshl_b32 s[sT2], s[sT], 2
  s_sub_u32 s[sT2], s[sT2], 8                     ; (T-2)*4
  v_add_u32 v[vT7], 2048*(NIT-1), v[vT0]
  v_min_u32 v[vT7], s[sT2], v[vT7]                ; last chunk: clamped (no out-of-range lanes)
  .set i, 0
  .rept NIT
    .if i == NIT-1
      buffer_load_dwordx2 v[vX+2*i:vX+2*i+1], v[vT7], s[sS:sS+3], 0 offen
    .elseif i < 2
      buffer_load_dwordx2 v[vX+2*i:vX+2*i+1], v[vT0], s[sS:sS+3], 0 offen offset:2048*i
    .else
      s_movk_i32 s[sT1], 2048*i
      buffer_load_dwordx2 v[vX+2*i:vX+2*i+1], v[vT0], s[sS:sS+3], s[sT1] offen
    .endif
    .set i, i+1
  .endr
  .if TOUCH
    v_lshlrev_b32 v[vV+29], 20, v[vLANE]            ; 1 MiB stride
    s_sub_u32 s[sT3], s[sK+2], 4
    v_min_u32 v[vV+29], s[sT3], v[vV+29]
    s_mov_b64 exec, 0x3ff
    buffer_load_dword v[vV+20], v[vV+29], s[sK:sK+3], 0 offen
    buffer_load_dword v[vV+21], v[vV+29], s[sV:sV+3], 0 offen
    buffer_load_dword v[vV+22], v[vV+29], s[sQ:sQ+3], 0 offen
    buffer_load_dword v[vV+23], v[vV+29], s[sO:sO+3], 0 offen
    s_mov_b64 exec, -1
  .endif
  s_sub_u32 s[sTM16], s[sT], 16
  s_lshl_b32 s[sTM16], s[sTM16], 12               ; (T-16)*4096
  .macro PREFETCH lo, hi                            ; Q prefetch instructions lo..hi-1 (16 rows each)
    .set n, \lo
    .rept \hi-\lo
      s_mul_i32 s[sT3], s[sJ], s[sPFB]
      s_add_u32 s[sT3], s[sT3], n*16*4096
      s_min_u32 s[sT3], s[sT3], s[sTM16]
      buffer_load_dwordx4 v[vV+4*n:vV+4*n+3], v[vV+28], s[sQ:sQ+3], s[sT3] offen
      .set n, n+1
    .endr
  .endm
  v_mov_b32 v[vV+28], v[vT6]
  ; remaining lane constants (in the shadow of the loads)
  v_add_u32 v[vOK2], 128, v[vOK]
  v_add_u32 v[vOV2], 128, v[vOV]
  v_and_b32 v[vT1], 31, v[vLANE]                  ; r32
  v_lshrrev_b32 v[vT3], 3, v[vT1]
  v_lshlrev_b32 v[vT3], 11, v[vT3]
  v_and_b32 v[vT4], 7, v[vT1]
  v_lshl_add_u32 v[vT3], v[vT4], 6, v[vT3]
  v_bfe_u32 v[vT4], v[vT1], 2, 2
  v_xor_b32 v[vT4], v[vT4], v[vT2]
  v_lshl_add_u32 v[vKB0], v[vT4], 4, v[vT3]
  v_xor_b32 v[vKB1], 32, v[vKB0]
  v_bfe_u32 v[vT3], v[vLANE], 2, 2
  v_lshl_add_u32 v[vT3], v[vT2], 2, v[vT3]
  v_lshlrev_b32 v[vT3], 6, v[vT3]
  v_bfe_u32 v[vT4], v[vLANE], 4, 1
  v_lshl_add_u32 v[vT3], v[vT4], 5, v[vT3]
  v_and_b32 v[vT4], 3, v[vLANE]
  v_lshl_add_u32 v[vVB], v[vT4], 3, v[vT3]
  v_add_u32 v[vVB], LV, v[vVB]
  v_lshlrev_b32 v[vHH4], 2, v[vT2]
  ; output staging: write address  r32*128 + ((2*? + hh) ^ (r32&7))*16 handled per block: base = r32*128, xor term below
  v_and_b32 v[vT3], 7, v[vT1]
  v_xor_b32 v[vT3], v[vT3], v[vT2]                ; (r32&7) ^ hh
  v_lshlrev_b32 v[vT3], 4, v[vT3]
  v_lshl_add_u32 v[vSW], v[vT1], 7, v[vT3]        ; write addr for chunk pair 0 (chunk = hh); chunk pair p: xor with p*32
  ; read address: row = lane>>3, chunk = (lane&7) ^ (row&7) ; rows + 8i -> + 1024 i
  v_lshrrev_b32 v[vT3], 3, v[vLANE]
  v_and_b32 v[vT4], 7, v[vLANE]
  v_xor_b32 v[vT5], v[vT4], v[vT3]
  v_lshlrev_b32 v[vT5], 4, v[vT5]
  v_lshl_add_u32 v[vSR], v[vT3], 7, v[vT5]
  ; global store lane offset: (lane>>3)*4096 + (lane&7)*16
  v_lshlrev_b32 v[vT4], 4, v[vT4]
  v_lshl_add_u32 v[vSO], v[vT3], 12, v[vT4]
  s_mov_b32 s[sCE], 0x3e0293ee
  s_mov_b32 s[sTHR], 0x427af232
  s_lshl_b32 s[sWD], s[sW], 11                    ; w*2048
  s_mov_b64 s[sC:sC+1], 0
  v_mov_b32 v[vT4], 0
  v_mov_b32 v[vT5], 0
  v_lshlrev_b32 v[vT7], 7, v[vLANE]               ; LDS address of chunk (w + 8*lane): 16 B each
  s_lshl_b32 s[sT1], s[sW], 4
  s_add_u32 s[sT1], s[sT1], LSCR
  v_add_u32 v[vT7], s[sT1], v[vT7]
  PREFETCH 0, PF1
  .set i, 0
  .rept NIT
    s_waitcnt vmcnt(NIT-1-i+PF1+4*TOUCH)
    v_cmp_ne_u32 s[sA:sA+1], v[vX+2*i], v[vX+2*i+1]
    v_cmp_gt_i32 vcc, v[vX+2*i], v[vX+2*i+1]
    .if i == NIT-1
      v_cmp_gt_i32 s[sB:sB+1], s[sLASTC], %2       ; element < T-1
      s_and_b64 s[sA:sA+1], s[sA:sA+1], s[sB:sB+1]
      s_and_b64 vcc, vcc, s[sB:sB+1]
      v_cmp_eq_u32 s[sB:sB+1], s[sLASTC], %2       ; element == T-1
      s_or_b64 s[sA:sA+1], s[sA:sA+1], s[sB:sB+1]
    .endif
    s_or_b64 s[sC:sC+1], s[sC:sC+1], vcc
    s_nop 0
    v_writelane_b32 v[vT4], s[sA], i
    v_writelane_b32 v[vT5], s[sA+1], i
    .set i, i+1
  .endr
  s_or_b32 s[sT1], s[sC], s[sC+1]
  v_mov_b32 v[vT6], s[sT1]
  s_mov_b64 exec, (1<<NIT)-1
  ds_write_b96 v[vT7], v[vT4:vT6]
  s_mov_b64 exec, -1
  v_mov_b32 v[vT0], 0
  v_mov_b32 v[vT1], 0
  v_mov_b32 v[vT2], 0
  v_lshlrev_b32 v[vT3], 4, v[vLANE]
  v_add_u32 v[vT3], LSCR, v[vT3]
  v_lshlrev_b32 v[vT7], 6, v[vLANE]
  v_add_u32 v[vT7], 1, v[vT7]                     ; lane*64 + 1
  PREFETCH PF1, PF2
  s_waitcnt lgkmcnt(0)
  s_barrier
  STAMP 2
  ; ======================================================================== S2
  s_bfm_b64 exec, 8*NIT, 0
  ds_read_b96 v[vT0:vT2], v[vT3]
  s_mov_b64 exec, -1
  s_movk_i32 s[sT1], 128
  s_movk_i32 s[sT2], 256
  s_mov_b32 s[sR], 0
  s_waitcnt lgkmcnt(0)
  v_cmp_ne_u32 vcc, 0, v[vT2]
  v_bcnt_u32_b32 v[vT3], v[vT0], 0
  v_bcnt_u32_b32 v[vT3], v[vT1], v[vT3]
  s_cbranch_vccnz .Lfallback
  v_cmp_lt_u32 vcc, 1, v[vT3]
  v_ffbl_b32 v[vT4], v[vT0]
  v_ffbl_b32 v[vT5], v[vT1]
  s_cbranch_vccnz .Lfallback
  v_cmp_ne_u32 s[sA:sA+1], 0, v[vT3]
  v_add_u32 v[vT5], 32, v[vT5]
  v_min_u32 v[vT4], v[vT4], v[vT5]
  v_add_u32 v[vT4], v[vT4], v[vT7]
  v_cndmask_b32 v[vT4], 0, v[vT4], s[sA:sA+1]     ; end (0 if none)
  s_nop 1
  v_mov_b32_dpp v[vSTART], v[vT4] wave_shr:1 row_mask:0xf bank_mask:0xf bound_ctrl:0
  s_nop 1
  v_max_u32_dpp v[vSTART], v[vSTART], v[vSTART] row_shr:1 row_mask:0xf bank_mask:0xf bound_ctrl:0
  s_nop 1
  v_max_u32_dpp v[vSTART], v[vSTART], v[vSTART] row_shr:2 row_mask:0xf bank_mask:0xf bound_ctrl:0
  s_nop 1
  v_max_u32_dpp v[vSTART], v[vSTART], v[vSTART] row_shr:4 row_mask:0xf bank_mask:0xf bound_ctrl:0
  s_nop 1
  v_max_u32_dpp v[vSTART], v[vSTART], v[vSTART] row_shr:8 row_mask:0xf bank_mask:0xf bound_ctrl:0
  s_nop 1
  v_max_u32_dpp v[vSTART], v[vSTART], v[vSTART] row_bcast:15 row_mask:0xa bank_mask:0xf
  s_nop 1
  v_max_u32_dpp v[vSTART], v[vSTART], v[vSTART] row_bcast:31 row_mask:0xc bank_mask:0xf
  s_nop 0
  v_sub_u32 v[vLEN], v[vT4], v[vSTART]
  v_cndmask_b32 v[vLEN], 0, v[vLEN], s[sA:sA+1]   ; len (0 if none)
  v_add_u32 v[vT5], 127, v[vLEN]
  v_lshrrev_b32 v[vT5], 7, v[vT5]                 ; nq (mode A)
  v_cmp_lt_u32 vcc, s[sT1], v[vLEN]               ; len > 128
  v_cmp_ge_u32 s[sB:sB+1], s[sT2], v[vLEN]        ; len <= 256
  s_and_b64 s[sB:sB+1], s[sB:sB+1], vcc           ; mode B
  v_lshlrev_b32 v[vT5], 1, v[vT5]
  s_nop 0
  v_cndmask_b32 v[vT5], v[vT5], 2, s[sB:sB+1]     ; items (2 heads)
  v_lshlrev_b32 v[vT6], 16, v[vT5]
  v_cndmask_b32 v[vINCL], v[vT6], v[vT5], vcc     ; small -> high half
  v_mov_b32 v[vEXCL], v[vINCL]
  s_nop 0
  v_add_u32_dpp v[vINCL], v[vINCL], v[vINCL] row_shr:1 row_mask:0xf bank_mask:0xf bound_ctrl:0
  s_nop 1
  v_add_u32_dpp v[vINCL], v[vINCL], v[vINCL] row_shr:2 row_mask:0xf bank_mask:0xf bound_ctrl:0
  s_nop 1
  v_add_u32_dpp v[vINCL], v[vINCL], v[vINCL] row_shr:4 row_mask:0xf bank_mask:0xf bound_ctrl:0
  s_nop 1
  v_add_u32_dpp v[vINCL], v[vINCL], v[vINCL] row_shr:8 row_mask:0xf bank_mask:0xf bound_ctrl:0
  s_nop 1
  v_add_u32_dpp v[vINCL], v[vINCL], v[vINCL] row_bcast:15 row_mask:0xa bank_mask:0xf
  s_nop 1
  v_add_u32_dpp v[vINCL], v[vINCL], v[vINCL] row_bcast:31 row_mask:0xc bank_mask:0xf
  s_nop 0
  v_sub_u32 v[vEXCL], v[vINCL], v[vEXCL]
  v_readlane_b32 s[sT1], v[vINCL], 63
  s_and_b32 s[sNBIG], s[sT1], 0xffff
  s_lshr_b32 s[sTOT], s[sT1], 16
  s_add_u32 s[sTOT], s[sTOT], s[sNBIG]
  .if DBGW & 8
  s_mov_b32 s[sSAFE], 1
  .else
  s_mov_b32 s[sSAFE], 0
  .endif
  STAMP 3

  ; ---- DMA pieces.  sKDST / sVDST: ring offset + w*2048 of the next tile; rows beyond the segment end are out of range (zero fill, no traffic)
  .macro DK1
    s_mov_b32 m0, s[sKDST]
    s_add_u32 s[sT1], s[sKDST], 16384
    buffer_load_dwordx4 v[vOK], s[sK:sK+3], s[sKSO] offen lds
    .if DBGW & 4
    s_nop 3
    .endif
  .endm
  .macro DK2
    s_add_u32 m0, s[sKDST], 1024
    s_and_b32 s[sKDST], s[sT1], 0xffff
    buffer_load_dwordx4 v[vOK2], s[sK:sK+3], s[sKSO] offen lds
    .if DBGW & 4
    s_nop 3
    .endif
    s_add_u32 s[sKSO], s[sKSO], 64*4096
    s_min_u32 s[sKSO], s[sKSO], s[sMAXSO]
  .endm
  .macro DV1
    s_add_u32 m0, s[sVDST], LV
    s_add_u32 s[sT3], s[sVDST], 16384
    buffer_load_dwordx4 v[vOV], s[sV:sV+3], s[sVSO] offen lds
    .if DBGW & 4
    s_nop 3
    .endif
  .endm
  .macro DV2
    s_add_u32 m0, s[sVDST], LV+1024
    s_and_b32 s[sVDST], s[sT3], 0xffff
    buffer_load_dwordx4 v[vOV2], s[sV:sV+3], s[sVSO] offen lds
    .if DBGW & 4
    s_nop 3
    .endif
    s_add_u32 s[sVSO], s[sVSO], 64*4096
    s_min_u32 s[sVSO], s[sVSO], s[sMAXSO]
  .endm
  .macro DALL
    DK1
    DK2
    DV1
    DV2
  .endm
  .macro BAR4 modea=0, bit=2                        ; previous group had 4 pieces: everything older has landed
    .if \modea
      s_cmp_eq_u32 s[sMB], 1
      s_cbranch_scc1 .Lnob\@
    .endif
    .if DBGW & \bit
    s_waitcnt vmcnt(0) lgkmcnt(0)
    .else
    s_waitcnt vmcnt(4) lgkmcnt(0)
    .endif
    s_barrier
    .if \modea
    s_branch .Lbd\@
  .Lnob\@:
    s_waitcnt lgkmcnt(0)
  .Lbd\@:
    .endif
  .endm
  .macro BARN modea=0                               ; nothing was issued behind the needed pieces
    .if \modea
      s_cmp_eq_u32 s[sMB], 1
      s_cbranch_scc1 .Lnob\@
    .endif
    s_waitcnt vmcnt(0) lgkmcnt(0)
    s_barrier
    .if \modea
    s_branch .Lbd\@
  .Lnob\@:
    s_waitcnt lgkmcnt(0)
  .Lbd\@:
    .endif
  .endm
  .macro BAR0 modea=0
    .if \modea
      s_cmp_eq_u32 s[sMB], 1
      s_cbranch_scc1 .Lnob\@
    .endif
    s_waitcnt vmcnt(0) lgkmcnt(0)
    s_barrier
    .if \modea
  .Lnob\@:
    .endif
  .endm

.Litem:
  ; idx = r*32 + (r odd ? 31-j : j)
  s_lshl_b32 s[sT1], s[sR], 5
  s_sub_u32 s[sT2], 31, s[sJ]
  s_bitcmp1_b32 s[sR], 0
  s_cselect_b32 s[sT2], s[sT2], s[sJ]
  s_add_u32 s[sT1], s[sT1], s[sT2]                ; idx
  s_cmp_ge_u32 s[sT1], s[sTOT]
  s_cbranch_scc1 .Ldone
  s_sub_u32 s[sT2], s[sT1], s[sNBIG]
  s_cmp_lt_u32 s[sT1], s[sNBIG]
  s_cselect_b32 s[sT1], s[sT1], s[sT2]            ; index inside its class
  s_cselect_b32 s[sT2], 0, 16                     ; field shift
  v_bfe_u32 v[vT0], v[vINCL], s[sT2], 16
  v_bfe_u32 v[vT1], v[vEXCL], s[sT2], 16
  v_cmp_lt_u32 s[sA:sA+1], s[sT1], v[vT0]
  v_cmp_ge_u32 vcc, s[sT1], v[vT1]
  s_and_b64 s[sA:sA+1], s[sA:sA+1], vcc
  s_ff1_i32_b64 s[sT2], s[sA:sA+1]
  v_readlane_b32 s[sK0], v[vSTART], s[sT2]
  v_readlane_b32 s[sLEN], v[vLEN], s[sT2]
  v_readlane_b32 s[sT3], v[vT1], s[sT2]
  s_sub_u32 s[sT1], s[sT1], s[sT3]                ; local item
  s_and_b32 s[sT2], s[sT1], 1
  s_lshl_b32 s[sT2], s[sT2], 3
  s_add_u32 s[sT2], s[sT2], s[sG]
  s_lshl_b32 s[sHOFF], s[sT2], 8                  ; head*256 bytes
  s_lshr_b32 s[sT1], s[sT1], 1
  s_lshl_b32 s[sT1], s[sT1], 7
  s_add_u32 s[sQ0], s[sK0], s[sT1]
  s_lshl_b32 s[sT1], s[sW], 3
  s_add_u32 s[sT2], s[sQ0], s[sT1]
  s_lshl_b32 s[sT2], s[sT2], 12
  s_add_u32 s[sT2], s[sT2], s[sHOFF]              ; (q0 + 8w)*4096 + hoff
  ; ---- Q DMA
  s_add_u32 m0, s[sWD], LX
  s_add_u32 s[sT3], s[sT2], 64*4096
  buffer_load_dwordx4 v[vOK], s[sQ:sQ+3], s[sT2] offen lds
  s_add_u32 m0, s[sWD], LX+1024
  s_add_u32 s[sT4], s[sT2], 128*4096
  buffer_load_dwordx4 v[vOK2], s[sQ:sQ+3], s[sT2] offen lds
  s_add_u32 m0, s[sWD], LX+16384
  s_add_u32 s[sT1], s[sK0], s[sT1]
  buffer_load_dwordx4 v[vOK], s[sQ:sQ+3], s[sT3] offen lds
  s_add_u32 m0, s[sWD], LX+16384+1024
  s_lshl_b32 s[sT1], s[sT1], 12
  buffer_load_dwordx4 v[vOK2], s[sQ:sQ+3], s[sT3] offen lds
  s_add_u32 s[sKSO], s[sT1], s[sHOFF]             ; (k0 + 8w)*4096 + hoff
  s_add_u32 s[sMAXSO], s[sTM16], 15*4096
  s_add_u32 s[sMAXSO], s[sMAXSO], s[sHOFF]        ; (T-1)*4096 + hoff
  s_min_u32 s[sKSO], s[sKSO], s[sMAXSO]
  s_mov_b32 s[sVSO], s[sKSO]
  s_add_u32 s[sQEND], s[sK0], s[sLEN]
  s_add_u32 s[sNK], s[sLEN], 63
  s_lshr_b32 s[sNK], s[sNK], 6
  s_lshl_b32 s[sO+2], s[sQEND], 12                ; the output descriptor ends at the segment end
  s_sub_u32 s[sT1], s[sLEN], 129
  s_cmp_lt_u32 s[sT1], 128
  s_cselect_b32 s[sMB], 1, 0                      ; mode B: 128 < len <= 256
  s_mov_b32 s[sKDST], s[sWD]
  s_mov_b32 s[sVDST], s[sWD]
  s_cbranch_scc0 .Lq_done
  s_add_u32 m0, s[sWD], LQ2
  s_add_u32 s[sT3], s[sT4], 64*4096
  buffer_load_dwordx4 v[vOK], s[sQ:sQ+3], s[sT4] offen lds
  s_add_u32 m0, s[sWD], LQ2+1024
  s_nop 0
  buffer_load_dwordx4 v[vOK2], s[sQ:sQ+3], s[sT4] offen lds
  s_add_u32 m0, s[sWD], LQ3
  s_nop 0
  buffer_load_dwordx4 v[vOK], s[sQ:sQ+3], s[sT3] offen lds
  s_add_u32 m0, s[sWD], LQ3+1024
  s_nop 0
  buffer_load_dwordx4 v[vOK2], s[sQ:sQ+3], s[sT3] offen lds
  DK1
  DK2
  s_branch .Lk_done
.Lq_done:
  DK1
  DK2
  DK1
  DK2
.Lk_done:
  STAMP 4
  ; ---- per-wave geometry
  s_and_b32 s[sT1], s[sW], 3
  s_lshr_b32 s[sT2], s[sW], 2
  s_cmp_eq_u32 s[sMB], 1
  s_cselect_b32 s[sQB], s[sW], s[sT1]             ; q block
  s_cselect_b32 s[sKH], 0, s[sT2]                 ; key half (mode A)
  s_movk_i32 s[sKSTEP], 0x4000
  s_cselect_b32 s[sKSTEP], 8192, s[sKSTEP]
  s_add_u32 s[sT1], s[sLEN], 31
  s_lshr_b32 s[sT1], s[sT1], 5                    ; nst = ceil(len/32)
  s_add_u32 s[sT2], s[sT1], 1
  s_sub_u32 s[sT2], s[sT2], s[sKH]
  s_lshr_b32 s[sT2], s[sT2], 1
  s_cmp_eq_u32 s[sMB], 1
  s_cselect_b32 s[sNS], s[sT1], s[sT2]            ; steps of this wave
  s_cselect_b32 s[sNU], s[sT1], s[sNK]            ; step slots
  s_and_b32 s[sLIM], s[sLEN], 31
  s_sub_u32 s[sT2], s[sT1], 1
  s_and_b32 s[sT2], s[sT2], 1
  s_cmp_eq_u32 s[sT2], s[sKH]
  s_cselect_b32 s[sT3], 1, s[sMB]                 ; owner of the last sub-tile
  s_cmp_lg_u32 s[sLIM], 0
  s_cselect_b32 s[sT3], s[sT3], 0
  s_sub_u32 s[sMSTEP], s[sNS], 1
  s_cmp_eq_u32 s[sT3], 1
  s_cselect_b32 s[sMSTEP], s[sMSTEP], -1
  s_lshl_b32 s[sT1], s[sQB], 5
  s_add_u32 s[sQR], s[sT1], s[sQ0]                ; first q row of this wave
  s_cmp_lt_u32 s[sQR], s[sQEND]
  s_cselect_b32 s[sNS], s[sNS], 0
  ; FULL slots with DMA: tile iteration t <= nk-3
  s_sub_i32 s[sND], s[sNK], 2
  s_max_i32 s[sND], s[sND], 0
  s_lshl_b32 s[sND], s[sND], s[sMB]
  ; Q read base
  s_lshl_b32 s[sT1], s[sQB], 13
  s_add_u32 s[sT2], s[sT1], LX                    ; qb < 4
  s_add_u32 s[sT3], s[sT1], LQ2-4*8192            ; qb 4,5
  s_add_u32 s[sT4], s[sT1], LQ3-6*8192            ; qb 6,7
  s_cmp_lt_u32 s[sQB], 6
  s_cselect_b32 s[sT3], s[sT3], s[sT4]
  s_cmp_lt_u32 s[sQB], 4
  s_cselect_b32 s[sT2], s[sT2], s[sT3]
  v_add_u32 v[vT0], s[sT2], v[vKB0]
  v_add_u32 v[vT1], s[sT2], v[vKB1]
  s_lshl_b32 s[sKOFF], s[sKH], 13
  s_mov_b32 s[sVOFF], s[sKOFF]
  v_add_u32 v[vKA0], s[sKOFF], v[vKB0]
  v_add_u32 v[vKA1], s[sKOFF], v[vKB1]
  ; group 0
  DALL
  .set i, 0
  .rept 64
    v_mov_b32 v[vO+i], 0
    .set i, i+1
  .endr
  v_mov_b32 v[vL0], 0
  v_mov_b32 v[vL1], 0
  v_mov_b32 v[vM], 0xf149f2ca
  s_mov_b32 s[sPEND], 0
  s_mov_b32 s[sU], 0
  BAR4 0, 1                                         ; barrier P: Q, K0 (K1) landed
  STAMP 5

  .macro KREAD i
    .if (ABL & 8)
    .elseif (\i & 1)
      ds_read_b128 v[vK+4*\i:vK+4*\i+3], v[vKA1] offset:512*(\i>>1)
    .else
      ds_read_b128 v[vK+4*\i:vK+4*\i+3], v[vKA0] offset:512*(\i>>1)
    .endif
  .endm
  .macro KADV                                       ; advance K read addresses to the next step
    s_add_u32 s[sKOFF], s[sKOFF], s[sKSTEP]
    s_and_b32 s[sKOFF], s[sKOFF], 0xffff
    v_add_u32 v[vKA0], s[sKOFF], v[vKB0]
    v_add_u32 v[vKA1], s[sKOFF], v[vKB1]
  .endm
  .macro QKM c, i
    .if \i == 0
      v_mfma_f32_32x32x16_bf16 v[\c:\c+15], v[vK:vK+3], v[vQ:vQ+3], 0
    .else
      v_mfma_f32_32x32x16_bf16 v[\c:\c+15], v[vK+4*\i:vK+4*\i+3], v[vQ+4*\i:vQ+4*\i+3], v[\c:\c+15]
    .endif
  .endm
  .macro VREAD j                                    ; V fragment j: d = j&3, ks = j>>2
    .if (ABL & 4) == 0
    ds_read_b64_tr_b16 v[vV+4*\j:vV+4*\j+1], v[vVA] offset:512*(\j&3)+4096*(\j>>2)
    ds_read_b64_tr_b16 v[vV+4*\j+2:vV+4*\j+3], v[vVA] offset:512*(\j&3)+4096*(\j>>2)+2048
    .endif
  .endm
  .macro PVM j
    v_mfma_f32_32x32x16_bf16 v[vO+16*(\j&3):vO+16*(\j&3)+15], v[vV+4*\j:vV+4*\j+3], v[vP+4*(\j>>2):vP+4*(\j>>2)+3], v[vO+16*(\j&3):vO+16*(\j&3)+15]
  .endm
  .macro MASK c                                     ; keys >= lim of this sub-tile -> -inf
    v_sub_u32 v[vT2], s[sLIM], v[vHH4]
    v_mov_b32 v[vT3], 0xff800000
    .set r, 0
    .rept 16
      v_cmp_lt_i32 vcc, (r&3)+8*(r>>2), v[vT2]
      s_nop 1
      v_cndmask_b32 v[\c+r], v[vT3], v[\c+r], vcc
      .set r, r+1
    .endr
  .endm
  .macro ROWMAX1 c
    v_max3_f32 v[vT2], v[\c+0], v[\c+1], v[\c+2]
    v_max3_f32 v[vT3], v[\c+3], v[\c+4], v[\c+5]
    v_max3_f32 v[vT2], v[vT2], v[\c+6], v[\c+7]
    v_max3_f32 v[vT3], v[vT3], v[\c+8], v[\c+9]
    v_max3_f32 v[vT2], v[vT2], v[\c+10], v[\c+11]
    v_max3_f32 v[vT3], v[vT3], v[\c+12], v[\c+13]
    v_max3_f32 v[vT2], v[vT2], v[\c+14], v[\c+15]
    v_max_f32 v[vT2], v[vT2], v[vT3]
    v_mov_b32 v[vT3], v[vT2]
  .endm
  .macro ROWMAX2                                    ; needs 2 wait states after ROWMAX1
    v_permlane32_swap_b32 v[vT2], v[vT3]
    s_nop 0
    v_max_f32 v[vT2], v[vT2], v[vT3]                ; vT2 = row max
  .endm
  .macro EXPE c, r
    .if (ABL & 2) == 0
    v_fma_f32 v[\c+(\r)], v[\c+(\r)], s[sCE], v[vNMC]
    v_exp_f32 v[\c+(\r)], v[\c+(\r)]
    .endif
  .endm
  .macro SUMPK p, i                                 ; row sums + pack of elements 2i, 2i+1
    .if (ABL & 32) == 0
    v_add_f32 v[vL0], v[vL0], v[\p+2*\i]
    v_add_f32 v[vL1], v[vL1], v[\p+2*\i+1]
    v_cvt_pk_bf16_f32 v[vP+\i], v[\p+2*\i], v[\p+2*\i+1]
    .endif
  .endm
  .macro ADDE p, e                                  ; row sum of element e (two chains)
    .if (ABL & 32) == 0
    .if ((\e) & 1)
      v_add_f32 v[vL1], v[vL1], v[\p+(\e)]
    .else
      v_add_f32 v[vL0], v[vL0], v[\p+(\e)]
    .endif
    .endif
  .endm
  .macro CVTE p, i                                  ; pack of elements 2i, 2i+1
    .if (ABL & 32) == 0
    v_cvt_pk_bf16_f32 v[vP+\i], v[\p+2*\i], v[\p+2*\i+1]
    .endif
  .endm
  .macro VADDR                                      ; V read address for the current step, then advance
    v_add_u32 v[vVA], s[sVOFF], v[vVB]
    s_add_u32 s[sVOFF], s[sVOFF], s[sKSTEP]
    s_and_b32 s[sVOFF], s[sVOFF], 0xffff
  .endm
  .macro RESCALE
    s_nop 7
    s_nop 7
    .set i, 0
    .rept 64
      v_mul_f32 v[vO+i], v[vAL], v[vO+i]
      .set i, i+1
    .endr
    v_mul_f32 v[vL0], v[vAL], v[vL0]
    v_mul_f32 v[vL1], v[vAL], v[vL1]
    s_mov_b32 s[sPEND], 0
  .endm

  ; ---- prologue compute: step 0
  s_cmp_eq_u32 s[sNS], 0
  s_cbranch_scc1 .Lpro_none
  .set i, 0
  .rept 8
    KREAD i
    .if (i & 1)
      ds_read_b128 v[vQ+4*i:vQ+4*i+3], v[vT1] offset:512*(i>>1)
    .else
      ds_read_b128 v[vQ+4*i:vQ+4*i+3], v[vT0] offset:512*(i>>1)
    .endif
    .set i, i+1
  .endr
  KADV
  .set i, 0
  .rept 8
    .if i == 0
      s_waitcnt lgkmcnt(14)
    .elseif i == 1
      s_waitcnt lgkmcnt(13)
    .elseif i == 2
      s_waitcnt lgkmcnt(12)
    .elseif i == 3
      s_waitcnt lgkmcnt(11)
    .elseif i == 4
      s_waitcnt lgkmcnt(10)
    .elseif i == 5
      s_waitcnt lgkmcnt(9)
    .elseif i == 6
      s_waitcnt lgkmcnt(8)
    .else
      s_waitcnt lgkmcnt(7)
    .endif
    QKM vSA, i
    KREAD i
    .if i == 1
      DK1
    .elseif i == 3
      DK2
    .elseif i == 5
      DV1
    .elseif i == 7
      DV2
    .endif
    .set i, i+1
  .endr
  KADV
  s_nop 7
  s_cmp_lg_u32 s[sMSTEP], 0
  s_cbranch_scc1 .Lnomask0
  MASK vSA
.Lnomask0:
  ROWMAX1 vSA
  s_nop 1
  ROWMAX2
  v_max_f32 v[vM], v[vM], v[vT2]
  v_mul_f32 v[vNMC], 0xbe0293ee, v[vM]
  s_sub_u32 s[sNF], s[sNS], 1                     ; FULL slots of this wave
  s_min_u32 s[sNDF], s[sND], s[sNF]               ; ... of which issue DMA
  VADDR
  s_cmp_eq_u32 s[sSAFE], 1
  s_cbranch_scc1 .Lpro_safe
  .set r, 0
  .rept 11
    EXPE vSA, r
    .set r, r+1
  .endr
  .set r, 0
  .rept 4
    ADDE vSA, r
    .set r, r+1
  .endr
  STAMP 6
  s_cmp_lt_u32 s[sU], s[sNDF]
  s_cbranch_scc1 .LloopF
  s_branch .LdispF0
.Lpro_safe:
  .set r, 0
  .rept 16
    EXPE vSA, r
    .set r, r+1
  .endr
  s_cmp_eq_u32 s[sNF], 0
  s_cbranch_scc1 .LlastS0
  s_branch .LloopS
.Lpro_none:
  DALL
  s_branch .Lnone

  ; ======================================================================== SAFE step (online max every step)
  .macro FULLS cur, prev, modea
    s_cmp_lt_u32 s[sU], s[sND]
    s_cselect_b32 s[sF], 1, 0
    s_cbranch_scc0 .Lbn\@
    BAR4 \modea
    s_branch .Lbq\@
  .Lbn\@:
    BARN \modea
  .Lbq\@:
    .set g, 0
    .rept 8
      QKM \cur, g
      VREAD g
      SUMPK \prev, g
      .if g == 1
        DMAK1 \modea, 2
      .elseif g == 5
        DMAK2 \modea, 2
      .endif
      .set g, g+1
    .endr
    s_waitcnt lgkmcnt(4)
    PVM 0
    KREAD 0
    KREAD 1
    s_add_u32 s[sU], s[sU], 1
    s_nop 0
    PVM 1
    s_cmp_lg_u32 s[sMSTEP], s[sU]
    s_cbranch_scc1 .Lnm\@
    MASK \cur
  .Lnm\@:
    ROWMAX1 \cur
    PVM 2
    KREAD 2
    ROWMAX2
    v_sub_f32 v[vT3], v[vT2], v[vM]
    v_cmp_lt_f32 vcc, s[sTHR], v[vT3]
    s_cbranch_vccz .Lkeep\@
    v_max_f32 v[vT3], v[vM], v[vT2]
    v_sub_f32 v[vAL], v[vM], v[vT3]
    v_mul_f32 v[vAL], s[sCE], v[vAL]
    v_exp_f32 v[vAL], v[vAL]
    v_mov_b32 v[vM], v[vT3]
    v_mul_f32 v[vNMC], 0xbe0293ee, v[vM]
    s_mov_b32 s[sPEND], 1
  .Lkeep\@:
    PVM 3
    KREAD 3
    DMAV1 \modea, 2
    EXPE \cur, 0
    EXPE \cur, 1
    EXPE \cur, 2
    PVM 4
    KREAD 4
    EXPE \cur, 3
    EXPE \cur, 4
    EXPE \cur, 5
    PVM 5
    KREAD 5
    EXPE \cur, 6
    EXPE \cur, 7
    EXPE \cur, 8
    s_waitcnt lgkmcnt(6)
    PVM 6
    KREAD 6
    DMAV2 \modea, 2
    EXPE \cur, 9
    EXPE \cur, 10
    EXPE \cur, 11
    PVM 7
    KREAD 7
    EXPE \cur, 12
    EXPE \cur, 13
    EXPE \cur, 14
    EXPE \cur, 15
    KADV
    VADDR
    s_cmp_eq_u32 s[sPEND], 0
    s_cbranch_scc1 .Lsend\@
    RESCALE
  .Lsend\@:
    s_cmp_eq_u32 s[sU], s[sNF]
  .endm
  .macro LASTS prev, modea
    BAR0 \modea
    .set g, 0
    .rept 8
      VREAD g
      SUMPK \prev, g
      .set g, g+1
    .endr
    s_add_u32 s[sU], s[sU], 1
    s_waitcnt lgkmcnt(0)
    .set g, 0
    .rept 8
      PVM g
      .set g, g+1
    .endr
    s_branch .Lnone_tail
  .endm

  ; DMA slots inside a step: odd slots of mode B issue nothing.  dma: 0 none, 1 always, 2 run-time flag sF
  .macro DMAK1 modea, dma=1
    .if \dma
    .if \dma == 2
      s_cmp_eq_u32 s[sF], 0
      s_cbranch_scc1 .Ld\@
    .endif
    .if \modea
      s_cmp_eq_u32 s[sMB], 1
      s_cbranch_scc1 .Ld\@
    .endif
    DK1
  .Ld\@:
    .endif
  .endm
  .macro DMAK2 modea, dma=1
    .if \dma
    .if \dma == 2
      s_cmp_eq_u32 s[sF], 0
      s_cbranch_scc1 .Ld\@
    .endif
    .if \modea
      s_cmp_eq_u32 s[sMB], 1
      s_cbranch_scc1 .Ld\@
    .endif
    DK2
  .Ld\@:
    .endif
  .endm
  .macro DMAV1 modea, dma=1
    .if \dma
    .if \dma == 2
      s_cmp_eq_u32 s[sF], 0
      s_cbranch_scc1 .Ld\@
    .endif
    .if \modea
      s_cmp_eq_u32 s[sMB], 1
      s_cbranch_scc1 .Ld\@
    .endif
    DV1
  .Ld\@:
    .endif
  .endm
  .macro DMAV2 modea, dma=1
    .if \dma
    .if \dma == 2
      s_cmp_eq_u32 s[sF], 0
      s_cbranch_scc1 .Ld\@
    .endif
    .if \modea
      s_cmp_eq_u32 s[sMB], 1
      s_cbranch_scc1 .Ld\@
    .endif
    DV2
  .Ld\@:
    .endif
  .endm

  ; ======================================================================== FAST step (reference = row max of the wave's first sub-tile;
  ; overflow is detected on the row sums at the end of the item and the item is redone with the SAFE steps)
  ; prev enters with exp done for e0..e10 and e0..e3 summed; vVA is set for this step
  .macro FULLF cur, prev, modea, dma
    .if \dma
      BAR4 \modea
    .else
      BARN \modea
    .endif
    ; ---------------- phase A
    QKM \cur, 0
    VREAD 0
    EXPE \prev, 11
    CVTE \prev, 0
    QKM \cur, 1
    VREAD 1
    EXPE \prev, 12
    CVTE \prev, 1
    QKM \cur, 2
    VREAD 2
    DMAK1 \modea, \dma
    EXPE \prev, 13
    CVTE \prev, 2
    QKM \cur, 3
    VREAD 3
    EXPE \prev, 14
    CVTE \prev, 3
    ADDE \prev, 4
    QKM \cur, 4
    VREAD 4
    EXPE \prev, 15
    CVTE \prev, 4
    ADDE \prev, 5
    QKM \cur, 5
    VREAD 5
    CVTE \prev, 5
    CVTE \prev, 6
    ADDE \prev, 6
    ADDE \prev, 7
    ADDE \prev, 8
    QKM \cur, 6
    VREAD 6
    DMAK2 \modea, \dma
    CVTE \prev, 7
    ADDE \prev, 9
    ADDE \prev, 10
    ADDE \prev, 11
    QKM \cur, 7
    VREAD 7
    ADDE \prev, 12
    ADDE \prev, 13
    ADDE \prev, 14
    ADDE \prev, 15
    ; ---------------- phase B
    s_waitcnt lgkmcnt(4)
    PVM 0
    KREAD 0
    s_add_u32 s[sU], s[sU], 1
    DMAV1 \modea, \dma
    s_cmp_lg_u32 s[sMSTEP], s[sU]
    s_cbranch_scc1 .Lnm\@
    MASK \cur
  .Lnm\@:
    EXPE \cur, 0
    PVM 1
    KREAD 1
    EXPE \cur, 1
    EXPE \cur, 2
    PVM 2
    KREAD 2
    KREAD 3
    EXPE \cur, 3
    ADDE \cur, 0
    PVM 3
    KREAD 4
    EXPE \cur, 4
    ADDE \cur, 1
    ADDE \cur, 2
    PVM 4
    KREAD 5
    EXPE \cur, 5
    EXPE \cur, 6
    PVM 5
    KREAD 6
    KREAD 7
    EXPE \cur, 7
    ADDE \cur, 3
    s_waitcnt lgkmcnt(8)
    PVM 6
    DMAV2 \modea, \dma
    EXPE \cur, 8
    EXPE \cur, 9
    KADV
    VADDR
    .if \dma
      s_cmp_lt_u32 s[sU], s[sNDF]
    .else
      s_cmp_lt_u32 s[sU], s[sNF]
    .endif
    PVM 7
    EXPE \cur, 10
  .endm
  .macro LASTF prev, modea
    BAR0 \modea
    .set g, 0
    .rept 8
      VREAD g
      .if g < 5
        EXPE \prev, 11+g
      .endif
      .set g, g+1
    .endr
    s_add_u32 s[sU], s[sU], 1
    .set g, 0
    .rept 8
      CVTE \prev, g
      .set g, g+1
    .endr
    s_waitcnt lgkmcnt(0)
    .set g, 0
    .rept 8
      PVM g
      ADDE \prev, 4+g
      .set g, g+1
    .endr
    ADDE \prev, 12
    ADDE \prev, 13
    ADDE \prev, 14
    ADDE \prev, 15
    s_branch .Lnone_tail
  .endm

.LloopS:
  FULLS vSB, vSA, 0
  s_cbranch_scc1 .LlastS1
  FULLS vSA, vSB, 1
  s_cbranch_scc0 .LloopS
.LlastS0:
  LASTS vSA, 0
.LlastS1:
  LASTS vSB, 1
.LloopF:
  FULLF vSB, vSA, 0, 1
  s_cbranch_scc0 .LdispF1
.LloopF1:
  FULLF vSA, vSB, 1, 1
  s_cbranch_scc1 .LloopF
.LdispF0:
  s_cmp_lt_u32 s[sU], s[sNF]
  s_cbranch_scc0 .LlastF0
.LfullN0:
  FULLF vSB, vSA, 0, 0
  s_cbranch_scc0 .LlastF1
.LfullN1:
  FULLF vSA, vSB, 1, 0
  s_cbranch_scc1 .LfullN0
.LlastF0:
  LASTF vSA, 0
.LdispF1:
  s_cmp_lt_u32 s[sU], s[sNF]
  s_cbranch_scc1 .LfullN1
.LlastF1:
  LASTF vSB, 1

  ; ---- slots in which this wave has nothing to compute
.Lnone:                                             ; wave without rows: keep loading its share of the tiles
  s_cmp_ge_u32 s[sU], s[sNU]
  s_cbranch_scc1 .Lepi
  s_bitcmp1_b32 s[sU], 0
  s_cselect_b32 s[sT1], s[sMB], 0
  s_cmp_eq_u32 s[sT1], 1
  s_cbranch_scc1 .Lnone_skip
  s_cmp_lt_u32 s[sU], s[sND]
  s_cbranch_scc0 .Lnone_fin
  s_waitcnt vmcnt(4) lgkmcnt(0)
  s_barrier
  DALL
  s_branch .Lnone_skip
.Lnone_fin:
  s_waitcnt vmcnt(0) lgkmcnt(0)
  s_barrier
.Lnone_skip:
  s_add_u32 s[sU], s[sU], 1
  s_branch .Lnone
.Lnone_tail:                                        ; after the wave's last step: only barriers remain
  s_cmp_ge_u32 s[sU], s[sNU]
  s_cbranch_scc1 .Lepi
  s_bitcmp1_b32 s[sU], 0
  s_cselect_b32 s[sT1], s[sMB], 0
  s_cmp_eq_u32 s[sT1], 1
  s_cbranch_scc1 .Lnt_skip
  s_barrier
.Lnt_skip:
  s_add_u32 s[sU], s[sU], 1
  s_branch .Lnone_tail

  ; ======================================================================== epilogue
  ; 32 f32 at vO+b.. (d 0..63 of a 64-wide half) -> bf16 -> LDS stage (row-major 128 B rows, 16-B chunks xor row) -> 4 full-line stores
  .macro OUTHALF b, stg                             ; stg: SGPR with the LDS stage base ; sT3 = global offset of row 0 of this half
    .set dd, 0
    .rept 2
      .set g, 0
      .rept 2
        v_cvt_pk_bf16_f32 v[vX+0], v[vO+\b+16*dd+8*g+0], v[vO+\b+16*dd+8*g+1]
        v_cvt_pk_bf16_f32 v[vX+1], v[vO+\b+16*dd+8*g+2], v[vO+\b+16*dd+8*g+3]
        v_cvt_pk_bf16_f32 v[vX+2], v[vO+\b+16*dd+8*g+4], v[vO+\b+16*dd+8*g+5]
        v_cvt_pk_bf16_f32 v[vX+3], v[vO+\b+16*dd+8*g+6], v[vO+\b+16*dd+8*g+7]
        v_xor_b32 v[vT4], (2*dd+g)*32, v[vT5]
        s_nop 0
        v_permlane32_swap_b32 v[vX+0], v[vX+2]
        v_permlane32_swap_b32 v[vX+1], v[vX+3]
        s_nop 0
        ds_write_b128 v[vT4], v[vX+0:vX+3]
        .set g, g+1
      .endr
      .set dd, dd+1
    .endr
    s_waitcnt lgkmcnt(0)
    ds_read_b128 v[vX+0:vX+3], v[vT6]
    ds_read_b128 v[vX+4:vX+7], v[vT6] offset:1024
    ds_read_b128 v[vX+8:vX+11], v[vT6] offset:2048
    ds_read_b128 v[vX+12:vX+15], v[vT6] offset:3072
    s_add_u32 s[sT4], s[sT3], 8*4096
    s_waitcnt lgkmcnt(3)
    buffer_store_dwordx4 v[vX+0:vX+3], v[vSO], s[sO:sO+3], s[sT3] offen sc1
    s_add_u32 s[sT1], s[sT3], 16*4096
    s_waitcnt lgkmcnt(2)
    buffer_store_dwordx4 v[vX+4:vX+7], v[vSO], s[sO:sO+3], s[sT4] offen sc1
    s_add_u32 s[sT2], s[sT3], 24*4096
    s_waitcnt lgkmcnt(1)
    buffer_store_dwordx4 v[vX+8:vX+11], v[vSO], s[sO:sO+3], s[sT1] offen sc1
    s_waitcnt lgkmcnt(0)
    buffer_store_dwordx4 v[vX+12:vX+15], v[vSO], s[sO:sO+3], s[sT2] offen sc1
  .endm
  .macro MERGE kb, rb                               ; kb: kept 32 regs, rb: 32 regs sent / received
    .set i, 0
    .rept 8
      ds_write_b128 v[vT0], v[vO+\rb+4*i:vO+\rb+4*i+3] offset:1024*i
      .set i, i+1
    .endr
    s_waitcnt lgkmcnt(0)
    s_barrier
    ds_read_b64 v[vT4:vT5], v[vT3]
    .set i, 0
    .rept 8
      ds_read_b128 v[vO+\rb+4*i:vO+\rb+4*i+3], v[vT1] offset:1024*i
      .set i, i+1
    .endr
    s_waitcnt lgkmcnt(8)
    v_max_f32 v[vT6], v[vM], v[vT4]
    v_sub_f32 v[vT0], v[vM], v[vT6]
    v_sub_f32 v[vT2], v[vT4], v[vT6]
    v_mul_f32 v[vT0], s[sCE], v[vT0]
    v_mul_f32 v[vT2], s[sCE], v[vT2]
    v_exp_f32 v[vT0], v[vT0]
    v_exp_f32 v[vT2], v[vT2]
    s_nop 0
    v_mul_f32 v[vT6], v[vT0], v[vL0]
    v_fma_f32 v[vT6], v[vT2], v[vT5], v[vT6]
    v_rcp_f32 v[vT6], v[vT6]
    s_nop 0
    v_mul_f32 v[vT0], v[vT0], v[vT6]                ; fa
    v_mul_f32 v[vT2], v[vT2], v[vT6]                ; fb
    .set i, 0
    .rept 32
      v_mul_f32 v[vO+\kb+i], v[vT0], v[vO+\kb+i]
      .set i, i+1
    .endr
    .set i, 0
    .rept 8
      .if i == 0
        s_waitcnt lgkmcnt(7)
      .elseif i == 1
        s_waitcnt lgkmcnt(6)
      .elseif i == 2
        s_waitcnt lgkmcnt(5)
      .elseif i == 3
        s_waitcnt lgkmcnt(4)
      .elseif i == 4
        s_waitcnt lgkmcnt(3)
      .elseif i == 5
        s_waitcnt lgkmcnt(2)
      .elseif i == 6
        s_waitcnt lgkmcnt(1)
      .else
        s_waitcnt lgkmcnt(0)
      .endif
      v_fma_f32 v[vO+\kb+4*i+0], v[vT2], v[vO+\rb+4*i+0], v[vO+\kb+4*i+0]
      v_fma_f32 v[vO+\kb+4*i+1], v[vT2], v[vO+\rb+4*i+1], v[vO+\kb+4*i+1]
      v_fma_f32 v[vO+\kb+4*i+2], v[vT2], v[vO+\rb+4*i+2], v[vO+\kb+4*i+2]
      v_fma_f32 v[vO+\kb+4*i+3], v[vT2], v[vO+\rb+4*i+3], v[vO+\kb+4*i+3]
      .set i, i+1
    .endr
    v_mov_b32 v[vT5], v[vAL]
    v_mov_b32 v[vT6], v[vNMC]
    OUTHALF \kb, 0
  .endm

.Lepi:
  STAMP 7
  v_mov_b32 v[vT7], 0x71800000
  v_add_f32 v[vL0], v[vL0], v[vL1]
  v_mov_b32 v[vL1], v[vL0]
  s_lshl_b32 s[sT3], s[sQR], 12
  s_add_u32 s[sT3], s[sT3], s[sHOFF]
  v_permlane32_swap_b32 v[vL0], v[vL1]
  s_cmp_eq_u32 s[sMB], 1
  v_add_f32 v[vL0], v[vL0], v[vL1]                ; row sum
  s_cbranch_scc1 .LepiB
  ; ---- mode A: exchange area: K ring (wave w at w*8192) ; stats in X region ; output stage in the V ring (w*4096)
  v_lshlrev_b32 v[vT0], 4, v[vLANE]
  s_lshl_b32 s[sT1], s[sW], 13
  s_xor_b32 s[sT2], s[sT1], 4*8192
  v_add_u32 v[vT1], s[sT2], v[vT0]                ; partner area
  v_add_u32 v[vT0], s[sT1], v[vT0]                ; own area
  v_and_b32 v[vT2], 31, v[vLANE]
  v_lshlrev_b32 v[vT2], 3, v[vT2]
  v_add_u32 v[vT2], LX, v[vT2]
  s_lshl_b32 s[sT1], s[sW], 8
  s_xor_b32 s[sT2], s[sT1], 4*256
  v_add_u32 v[vT3], s[sT2], v[vT2]
  v_add_u32 v[vT2], s[sT1], v[vT2]
  v_mov_b32 v[vT4], v[vM]
  v_mov_b32 v[vT5], v[vL0]
  ds_write_b64 v[vT2], v[vT4:vT5]
  s_lshl_b32 s[sT1], s[sKH], 7
  s_add_u32 s[sT3], s[sT3], s[sT1]
  s_lshl_b32 s[sT1], s[sW], 12
  s_add_u32 s[sT1], s[sT1], LV
  v_add_u32 v[vAL], s[sT1], v[vSW]                ; stage write base  (kept in vAL / vNMC across MERGE)
  v_add_u32 v[vNMC], s[sT1], v[vSR]               ; stage read base
  s_cmp_eq_u32 s[sKH], 0
  s_cbranch_scc1 .Lmerge0
  MERGE 32, 0
  s_branch .Lnext
.Lmerge0:
  MERGE 0, 32
  s_branch .Lnext
.LepiB:
  v_rcp_f32 v[vT0], v[vL0]
  s_waitcnt vmcnt(0)
  s_barrier
  s_lshl_b32 s[sT1], s[sW], 13
  v_add_u32 v[vT5], s[sT1], v[vSW]
  v_add_u32 v[vT6], s[sT1], v[vSR]
  s_nop 7
  .set i, 0
  .rept 64
    v_mul_f32 v[vO+i], v[vT0], v[vO+i]
    .set i, i+1
  .endr
  OUTHALF 0, 0
  v_add_u32 v[vT5], 4096, v[vT5]
  v_add_u32 v[vT6], 4096, v[vT6]
  s_add_u32 s[sT3], s[sT3], 128
  s_nop 1
  OUTHALF 32, 0
.Lnext:
  STAMP 8
  ; overflow check of the FAST path: any row sum that is not a normal-sized number -> redo this item with the online max
  v_cmp_nlt_f32 vcc, v[vL0], v[vT7]
  s_lshl_b32 s[sT1], s[sW], 2
  s_add_u32 s[sT1], s[sT1], LX+8192
  s_cmp_lg_u64 vcc, 0
  s_cselect_b32 s[sT2], 1, 0
  v_mov_b32 v[vT0], s[sT1]
  v_mov_b32 v[vT1], s[sT2]
  v_and_b32 v[vT2], 7, v[vLANE]
  v_lshlrev_b32 v[vT2], 2, v[vT2]
  v_add_u32 v[vT2], LX+8192, v[vT2]
  ds_write_b32 v[vT0], v[vT1]
  s_waitcnt lgkmcnt(0)
  s_barrier
  ds_read_b32 v[vT1], v[vT2]
  s_waitcnt lgkmcnt(0)
  v_cmp_ne_u32 vcc, 0, v[vT1]
  s_cmp_eq_u32 s[sSAFE], 1
  s_cbranch_scc1 .Lsafe_done
  s_cbranch_vccz .Lsafe_done
  s_mov_b32 s[sSAFE], 1
  s_barrier
  s_branch .Litem
.Lsafe_done:
  s_mov_b32 s[sSAFE], 0
  s_add_u32 s[sR], s[sR], 1
  s_lshl_b32 s[sT1], s[sR], 5
  s_cmp_ge_u32 s[sT1], s[sTOT]
  s_cbranch_scc1 .Ldone
  s_barrier
  s_branch .Litem
.Ldone:
  .if DIAG
    s_waitcnt vmcnt(0)
    STAMP 9
    s_memrealtime s[sA:sA+1]
    s_load_dwordx2 s[sB:sB+1], %0, 96
    s_lshl_b32 s[sT1], %1, 3
    s_add_u32 s[sT1], s[sT1], s[sW]
    s_lshl_b32 s[sT1], s[sT1], 8
    s_waitcnt lgkmcnt(0)
    s_add_u32 s[sB], s[sB], s[sT1]
    s_addc_u32 s[sB+1], s[sB+1], 0
    v_mov_b32 v[vT6], 0
    s_mov_b64 exec, 1
    .set i, 0
    .rept 10
      v_mov_b32 v[vT0], s[sST+2*i]
      v_mov_b32 v[vT1], s[sST+2*i+1]
      global_store_dwordx2 v[vT6], v[vT0:vT1], s[sB:sB+1] offset:8*i
      s_nop 1
      .set i, i+1
    .endr
    v_mov_b32 v[vT0], s[sRT]
    v_mov_b32 v[vT1], s[sRT+1]
    global_store_dwordx2 v[vT6], v[vT0:vT1], s[sB:sB+1] offset:80
    s_nop 1
    v_mov_b32 v[vT0], s[sA]
    v_mov_b32 v[vT1], s[sA+1]
    global_store_dwordx2 v[vT6], v[vT0:vT1], s[sB:sB+1] offset:88
    s_nop 1
  .endif
  s_endpgm
.Lfallback:
  s_waitcnt vmcnt(0) lgkmcnt(0)
  .purgem STAMP
  .purgem PREFETCH
  .purgem DK1
  .purgem DALL
  .purgem BAR4
  .purgem BAR0
  .purgem BARN
  .purgem FULLS
  .purgem LASTS
  .purgem FULLF
  .purgem LASTF
  .purgem DMAK1
  .purgem DMAK2
  .purgem DMAV1
  .purgem DMAV2
  .purgem DK2
  .purgem DV1
  .purgem DV2
  .purgem KREAD
  .purgem KADV
  .purgem QKM
  .purgem VREAD
  .purgem PVM
  .purgem MASK
  .purgem ROWMAX1
  .purgem ROWMAX2
  .purgem EXPE
  .purgem SUMPK
  .purgem VADDR
  .purgem RESCALE
  .purgem ADDE
  .purgem CVTE
  .purgem OUTHALF
  .purgem MERGE
  )" :: "s"(__builtin_amdgcn_kernarg_segment_ptr()), "s"(blockIdx.x), "v"(threadIdx.x), "i"(NIT)
     : "memory", "vcc", "m0",
       "s8","s9","s10","s11","s12","s13","s14","s15","s16","s17","s18","s19","s20","s21","s22","s23","s24","s25","s26","s27","s28","s29","s30","s31",
       "s33","s34","s35","s36","s37","s38","s39","s40","s41","s42","s43","s44","s45","s46","s47","s48","s49","s50","s51","s52","s53","s54","s55",
       "s56","s57","s58","s59","s60","s61","s62","s63","s64","s65","s66","s67","s68","s69","s70","s71","s72","s73","s74","s75","s76","s77","s78","s79",
       "s80","s81","s82","s83","s84","s85","s86","s87","s88","s89","s90","s91","s92","s93","s94","s95","s96","s97","s98","s99",
       "v1","v2","v3","v4","v5","v6","v7","v8","v9","v10","v11","v12","v13","v14","v15","v16","v17","v18","v19","v20","v21","v22","v23","v24","v25","v26","v27","v28","v29","v30","v31",
       "v32","v33","v34","v35","v36","v37","v38","v39","v40","v41","v42","v43","v44","v45","v46","v47","v48","v49","v50","v51","v52","v53","v54","v55","v56","v57","v58","v59","v60","v61","v62","v63",
       "v64","v65","v66","v67","v68","v69","v70","v71","v72","v73","v74","v75","v76","v77","v78","v79","v80","v81","v82","v83","v84","v85","v86","v87","v88","v89","v90","v91","v92","v93","v94","v95",
       "v96","v97","v98","v99","v100","v101","v102","v103","v104","v105","v106","v107","v108","v109","v110","v111","v112","v113","v114","v115","v116","v117","v118","v119","v120","v121","v122","v123","v124","v125","v126","v127",
       "v128","v129","v130","v131","v132","v133","v134","v135","v136","v137","v138","v139","v140","v141","v142","v143","v144","v145","v146","v147","v148","v149","v150","v151","v152","v153","v154","v155","v156","v157","v158","v159",
       "v160","v161","v162","v163","v164","v165","v166","v167","v168","v169","v170","v171","v172","v173","v174","v175","v176","v177","v178","v179","v180","v181","v182","v183","v184","v185","v186","v187","v188","v189","v190","v191",
       "v192","v193","v194","v195","v196","v197","v198","v199","v200","v201","v202","v203","v204","v205","v206","v207","v208","v209","v210","v211","v212","v213","v214","v215","v216","v217","v218","v219","v220","v221","v222","v223",
       "v224","v225","v226","v227","v228","v229","v230","v231","v232","v233","v234","v235","v236","v237","v238","v239","v240","v241","v242","v243","v244","v245","v246","v247","v248","v249","v250","v251","v252","v253","v254","v255");
  generic_path((const unsigned short*)Q, (const unsigned short*)K, (const unsigned short*)V, (const int*)seg, (unsigned short*)O, T, lds);
}

__global__ __launch_bounds__(512, 2)
void ragged_attn_generic(const void* Q, const void* K, const void* V, const void* seg, void* O, int T, int pad) {
  __shared__ __attribute__((aligned(16))) char lds[LDS_BYTES];
  generic_path((const unsigned short*)Q, (const unsigned short*)K, (const unsigned short*)V, (const int*)seg, (unsigned short*)O, T, lds);
}

extern "C" void kernel_launch(void* const* d_in, const int* in_sizes, int n_in, void* d_out, int out_size, void* d_ws, size_t ws_size,
                              hipStream_t stream) {
  const int T = in_sizes[3];
  const unsigned nb = (unsigned)T * ROWB, F = 0x00020000u;
  if (T > 2048 && T <= 2560) {
    ragged_attn_kernel<5><<<256, 512, 0, stream>>>(d_in[0], nb, F, d_in[1], nb, F, d_in[2], nb, F, d_in[3], (unsigned)T * 4u, F, d_out, nb, F,
                                                   T, T - 1 - 512 * 4, ((T + 31) / 32) * (int)ROWB, 0, d_ws);
  } else {
    ragged_attn_generic<<<256, 512, 0, stream>>>(d_in[0], d_in[1], d_in[2], d_in[3], d_out, T, 0);
  }
}
```

```cpp
#include <hip/hip_runtime.h>
#include <stdint.h>

typedef short    bf16x8 __attribute__((ext_vector_type(8)));
typedef short    s16x4  __attribute__((ext_vector_type(4)));
typedef float    f32x16 __attribute__((ext_vector_type(16)));
typedef float    f32x4  __attribute__((ext_vector_type(4)));
typedef float    f32x2  __attribute__((ext_vector_type(2)));
typedef __bf16   bf2    __attribute__((ext_vector_type(2)));
typedef unsigned u32x4  __attribute__((ext_vector_type(4)));
typedef unsigned long long u64;
#define LDSP __attribute__((address_space(3)))
typedef LDSP const char* lds_cptr;
typedef LDSP char* lds_ptr;
typedef short v4i16_t __attribute__((ext_vector_type(4)));
#define DI __device__ __forceinline__
#define MFMA(a, b, c) __builtin_amdgcn_mfma_f32_32x32x16_bf16((a), (b), (c), 0, 0, 0)

constexpr int NH = 16, HD = 128;
constexpr unsigned ROWB = NH * HD * 2;
constexpr unsigned LDS_BYTES = 163840;
constexpr float CEXP = 0.08838834764831845f * 1.4426950408889634f;
constexpr float THR_RAW = 8.0f / CEXP;
constexpr float MINIT = -1.0e30f;

DI unsigned rfl(unsigned x) { return __builtin_amdgcn_readfirstlane(x); }
DI unsigned cvtpk(float lo, float hi) { f32x2 v = {lo, hi}; bf2 b = __builtin_convertvector(v, bf2); return __builtin_bit_cast(unsigned, b); }
DI float swap_max(float x) { auto r = __builtin_amdgcn_permlane32_swap(__float_as_uint(x), __float_as_uint(x), false, false); return __builtin_fmaxf(__uint_as_float(r[0]), __uint_as_float(r[1])); }
DI float swap_add(float x) { auto r = __builtin_amdgcn_permlane32_swap(__float_as_uint(x), __float_as_uint(x), false, false); return __uint_as_float(r[0]) + __uint_as_float(r[1]); }
DI s16x4 vtr(lds_cptr p) { return __builtin_bit_cast(s16x4, __builtin_amdgcn_ds_read_tr16_b64_v4i16((LDSP v4i16_t*)p)); }
#define MX3(a, b, c) __builtin_fmaxf(__builtin_fmaxf((a), (b)), (c))

DI void dma16(__amdgpu_buffer_rsrc_t srd, unsigned voff, unsigned soff, unsigned m0v) {
  unsigned keep;
  asm volatile("s_nop 4\n\ts_mov_b32 %0, m0\n\ts_mov_b32 m0, %1\n\ts_nop 0\n\tbuffer_load_dwordx4 %2, %3, %4 offen lds\n\ts_mov_b32 m0, %0"
               : "=&s"(keep) : "s"(m0v), "v"(voff), "s"(srd), "s"(soff) : "memory");
}

__device__ __forceinline__ void generic_path(const unsigned short* __restrict__ Q, const unsigned short* __restrict__ K, const unsigned short* __restrict__ V,
                                          const int* __restrict__ seg, unsigned short* __restrict__ O, int T, char* lds) {
  constexpr unsigned SLOT = 32768, VOFF = 16384, XREG = 4 * SLOT;
  const int tid = threadIdx.x, lane = tid & 63, r32 = lane & 31, hh = lane >> 5;
  const int w = (int)rfl((unsigned)tid >> 6);
  const int grp = blockIdx.x & 7, jj = blockIdx.x >> 3, NJ = 32;
  const unsigned ldsb = (unsigned)(uintptr_t)lds;
  const lds_ptr L = (lds_ptr)lds;
  const int total_items = 2 * ((T + 127) >> 7);
  const __amdgpu_buffer_rsrc_t srdQ = __builtin_amdgcn_make_buffer_rsrc((void*)Q, (short)0, T * (int)ROWB, 0x00020000);
  const __amdgpu_buffer_rsrc_t srdK = __builtin_amdgcn_make_buffer_rsrc((void*)K, (short)0, T * (int)ROWB, 0x00020000);
  const __amdgpu_buffer_rsrc_t srdV = __builtin_amdgcn_make_buffer_rsrc((void*)V, (short)0, T * (int)ROWB, 0x00020000);
  const unsigned prow = (lane >> 2) & 7, pcb = lane >> 5, pc = lane & 3;
  const unsigned voffK = prow * ROWB + pcb * 64 + ((pc ^ ((2u * (unsigned)w + (prow >> 2)) & 3u)) * 16);
  const unsigned voffV = prow * ROWB + pcb * 64 + pc * 16;
  const unsigned kb0 = 2048u * (r32 >> 3) + 64u * (r32 & 7) + 16u * ((unsigned)hh ^ ((r32 >> 2) & 3u));
  const unsigned vb0 = ((unsigned)hh * 4 + ((lane & 15) >> 2)) * 64 + ((lane >> 4) & 1) * 32 + (lane & 3) * 8;
  for (int idx = jj; idx < total_items; idx += NJ) {
    __syncthreads();
    const int head = grp + 8 * (idx & 1);
    const int q0 = (idx >> 1) * 128, qend = min(q0 + 128, T);
    LDSP int* red = (LDSP int*)L;
    if (tid == 0) { red[0] = 0x7fffffff; red[1] = (int)0x80000000; red[2] = 0x7fffffff; red[3] = -1; }
    __syncthreads();
    if (tid < 128 && q0 + tid < qend) { const int id = seg[q0 + tid];
      __hip_atomic_fetch_min((LDSP int*)&red[0], id, __ATOMIC_RELAXED, __HIP_MEMORY_SCOPE_WORKGROUP);
      __hip_atomic_fetch_max((LDSP int*)&red[1], id, __ATOMIC_RELAXED, __HIP_MEMORY_SCOPE_WORKGROUP); }
    __syncthreads();
    const int idlo = red[0], idhi = red[1];
    int lo = 0x7fffffff, hi = -1;
    for (int i = tid; i < T; i += 512) { const int id = seg[i]; if (id >= idlo && id <= idhi) { lo = min(lo, i); hi = max(hi, i); } }
    __hip_atomic_fetch_min((LDSP int*)&red[2], lo, __ATOMIC_RELAXED, __HIP_MEMORY_SCOPE_WORKGROUP);
    __hip_atomic_fetch_max((LDSP int*)&red[3], hi, __ATOMIC_RELAXED, __HIP_MEMORY_SCOPE_WORKGROUP);
    __syncthreads();
    const int k0 = (int)rfl((unsigned)red[2]), kend = (int)rfl((unsigned)red[3]) + 1;
    __syncthreads();
    const int nk = (kend - k0 + 63) >> 6;
    const unsigned hoff = (unsigned)head * 256u;
#pragma unroll
    for (int i = 0; i < 2; ++i) {
      const unsigned m = (unsigned)w + 8u * i;
      const unsigned so = (unsigned)(q0 + 8 * (int)m) * ROWB + hoff;
      dma16(srdQ, voffK, so, ldsb + XREG + m * 2048u);
      dma16(srdQ, voffK, so + 128u, ldsb + XREG + m * 2048u + 1024u);
    }
    const int qb = w & 3, sub = w >> 2;
    const int qrow = q0 + 32 * qb + r32;
    const int segq = seg[qrow < T ? qrow : T - 1];
    bf16x8 qf[8];
    f32x16 o[4];
#pragma unroll
    for (int d = 0; d < 4; ++d) o[d] = f32x16{};
    float m_run = MINIT, l_run = 0.f;
    for (int t = 0; t < nk; ++t) {
      __syncthreads();
      {
        const unsigned so = (unsigned)(k0 + 64 * t + 8 * w) * ROWB + hoff;
        const unsigned d = ldsb + (unsigned)w * 2048u;
        dma16(srdK, voffK, so, d); dma16(srdK, voffK, so + 128u, d + 1024u);
        dma16(srdV, voffV, so, d + VOFF); dma16(srdV, voffV, so + 128u, d + VOFF + 1024u);
      }
      asm volatile("s_waitcnt vmcnt(0)" ::: "memory");
      __syncthreads();
      if (t == 0) {
#pragma unroll
        for (int s = 0; s < 8; ++s)
          qf[s] = *(const LDSP bf16x8*)(L + XREG + (unsigned)qb * 8192u + ((kb0 ^ ((s & 1) * 32u)) + 512u * (s >> 1)));
      }
      const int kbase = k0 + 64 * t + 32 * sub;
      const lds_cptr kp = (lds_cptr)L + (unsigned)sub * 8192u;
      f32x16 sc = f32x16{};
#pragma unroll
      for (int s = 0; s < 8; ++s) {
        const bf16x8 kf = *(const LDSP bf16x8*)(kp + ((kb0 ^ ((s & 1) * 32u)) + 512u * (s >> 1)));
        sc = MFMA(kf, qf[s], sc);
      }
#pragma unroll
      for (int r = 0; r < 16; ++r) {
        const int kk = kbase + (r & 3) + 8 * (r >> 2) + 4 * hh;
        const int id = seg[kk < T ? kk : T - 1];
        sc[r] = (kk < kend && id == segq) ? sc[r] : -__builtin_inff();
      }
      float a = MX3(sc[0], sc[1], sc[2]), b = MX3(sc[3], sc[4], sc[5]);
      a = MX3(a, sc[6], sc[7]); b = MX3(b, sc[8], sc[9]); a = MX3(a, sc[10], sc[11]); b = MX3(b, sc[12], sc[13]);
      a = MX3(a, sc[14], sc[15]);
      const float pm = swap_max(__builtin_fmaxf(a, b));
      const float mn = __builtin_fmaxf(m_run, pm);
      const float al = __builtin_amdgcn_exp2f((m_run - mn) * CEXP);
      m_run = mn; l_run *= al;
#pragma unroll
      for (int d = 0; d < 4; ++d)
#pragma unroll
        for (int r = 0; r < 16; ++r) o[d][r] *= al;
      const float nmc = -m_run * CEXP;
      float ps = 0.f;
#pragma unroll
      for (int r = 0; r < 16; ++r) { sc[r] = __builtin_amdgcn_exp2f(__builtin_fmaf(sc[r], CEXP, nmc)); ps += sc[r]; }
      l_run += ps;
      u32x4 pw0, pw1;
#pragma unroll
      for (int e = 0; e < 4; ++e) { pw0[e] = cvtpk(sc[2 * e], sc[2 * e + 1]); pw1[e] = cvtpk(sc[8 + 2 * e], sc[8 + 2 * e + 1]); }
      const bf16x8 p0 = __builtin_bit_cast(bf16x8, pw0), p1 = __builtin_bit_cast(bf16x8, pw1);
      const lds_cptr vp = kp + VOFF + vb0;
#pragma unroll
      for (int d = 0; d < 4; ++d) {
        const s16x4 a0 = vtr(vp + d * 512), a1 = vtr(vp + d * 512 + 2048), b0 = vtr(vp + d * 512 + 4096), b1 = vtr(vp + d * 512 + 6144);
        const bf16x8 va = __builtin_shufflevector(a0, a1, 0, 1, 2, 3, 4, 5, 6, 7), vb = __builtin_shufflevector(b0, b1, 0, 1, 2, 3, 4, 5, 6, 7);
        o[d] = MFMA(va, p0, o[d]);
        o[d] = MFMA(vb, p1, o[d]);
      }
    }
    l_run = swap_add(l_run);
    unsigned short* orow = O + (size_t)(qrow < T ? qrow : 0) * (NH * HD) + head * HD + 8 * hh;
    const bool row_ok = qrow < qend;
    const int kh = sub;
    __syncthreads();
    const unsigned xa = SLOT, xb = 2 * SLOT, xs = 3 * SLOT;
    const unsigned mine = (kh ? xb : xa) + (unsigned)qb * 8192u + (unsigned)lane * 16u;
    const unsigned theirs = (kh ? xa : xb) + (unsigned)qb * 8192u + (unsigned)lane * 16u;
#pragma unroll
    for (int dd = 0; dd < 2; ++dd)
#pragma unroll
      for (int g = 0; g < 4; ++g) {
        f32x4 v;
#pragma unroll
        for (int e = 0; e < 4; ++e) v[e] = kh ? o[dd][4 * g + e] : o[2 + dd][4 * g + e];
        *(LDSP f32x4*)(L + mine + (unsigned)(dd * 4 + g) * 1024u) = v;
      }
    if (hh == 0) { f32x2 s2 = {m_run, l_run}; *(LDSP f32x2*)(L + xs + (unsigned)w * 256u + (unsigned)r32 * 8u) = s2; }
    __syncthreads();
    const f32x2 o2 = *(const LDSP f32x2*)(L + xs + (unsigned)(w ^ 4) * 256u + (unsigned)r32 * 8u);
    const float mn = __builtin_fmaxf(m_run, o2[0]);
    const float wa = __builtin_amdgcn_exp2f((m_run - mn) * CEXP), wb = __builtin_amdgcn_exp2f((o2[0] - mn) * CEXP);
    const float inv = 1.0f / (wa * l_run + wb * o2[1]);
    const float fa = wa * inv, fb = wb * inv;
#pragma unroll
    for (int dd = 0; dd < 2; ++dd) {
      float r[16];
#pragma unroll
      for (int g = 0; g < 4; ++g) {
        const f32x4 v = *(const LDSP f32x4*)(L + theirs + (unsigned)(dd * 4 + g) * 1024u);
#pragma unroll
        for (int e = 0; e < 4; ++e) r[4 * g + e] = fa * (kh ? o[2 + dd][4 * g + e] : o[dd][4 * g + e]) + fb * v[e];
      }
#pragma unroll
      for (int g = 0; g < 4; g += 2) {
        unsigned ax = cvtpk(r[4 * g + 0], r[4 * g + 1]), ay = cvtpk(r[4 * g + 2], r[4 * g + 3]);
        unsigned bx = cvtpk(r[4 * g + 4], r[4 * g + 5]), by = cvtpk(r[4 * g + 6], r[4 * g + 7]);
        auto rx = __builtin_amdgcn_permlane32_swap(ax, bx, false, false); auto ry = __builtin_amdgcn_permlane32_swap(ay, by, false, false);
        const u32x4 v = {rx[0], ry[0], rx[1], ry[1]};
        if (row_ok) *(u32x4*)(orow + 64 * kh + 32 * dd + 8 * g) = v;
      }
    }
  }
}

#define DIAG 0
#define PFQ 1
#define PFKV 0
#define ABL 0
#define PFAT 1
#define DBGW 0
#define TOUCH 0
#define PF1 3
#define PF2 5
#define WQ 1
#define PRIO 0
#define STR2(x) #x
#define STR(x) STR2(x)

template <int NIT>
__global__ __launch_bounds__(512, 2)
void ragged_attn_kernel(const void* Q, unsigned nQ, unsigned fQ, const void* K, unsigned nK, unsigned fK, const void* V, unsigned nV, unsigned fV,
                        const void* seg, unsigned nS, unsigned fS, void* O, unsigned nO, unsigned fO, int T, int lastc, int pfb, int pad, void* dbg) {
  __shared__ __attribute__((aligned(16))) char lds[LDS_BYTES];
  asm volatile(R"(
  .set NIT, %c3
  .set DIAG, )" STR(DIAG) R"(
  .set PFQ, )" STR(PFQ) R"(
  .set PFKV, )" STR(PFKV) R"(
  .set ABL, )" STR(ABL) R"(
  .set PFAT, )" STR(PFAT) R"(
  .set DBGW, )" STR(DBGW) R"(
  .set TOUCH, )" STR(TOUCH) R"(
  .set PRIO, )" STR(PRIO) R"(
  .set WQ, )" STR(WQ) R"(
  .set PF1, )" STR(PF1) R"(
  .set PF2, )" STR(PF2) R"(
  ; ---------------- SGPRs
  .set sQ, 8
  .set sK, 12
  .set sV, 16
  .set sS, 20
  .set sO, 24
  .set sT, 28
  .set sLASTC, 29
  .set sPFB, 30
  .set sF, 30
  .set sTM16, 31
  .set sG, 34
  .set sR, 35
  .set sNBIG, 36
  .set sTOT, 37
  .set sHOFF, 38
  .set sQ0, 39
  .set sQEND, 40
  .set sK0, 41
  .set sNK, 42
  .set sLEN, 43
  .set sMB, 44
  .set sNS, 45
  .set sU, 46
  .set sNU, 47
  .set sKOFF, 48
  .set sKSTEP, 49
  .set sVOFF, 50
  .set sKSO, 51
  .set sVSO, 52
  .set sKDST, 53
  .set sVDST, 54
  .set sMAXSO, 55
  .set sND, 56
  .set sMSTEP, 57
  .set sPEND, 58
  .set sW, 59
  .set sA, 60
  .set sB, 62
  .set sC, 64
  .set sT1, 66
  .set sT2, 67
  .set sT3, 68
  .set sT4, 69
  .set sCE, 70
  .set sTHR, 71
  .set sWD, 72
  .set sQB, 73
  .set sKH, 74
  .set sLIM, 75
  .set sJ, 76
  .set sQR, 77
  .set sRT, 78
  .set sLIVE, 30
  .set sSAFE, 64
  .set sNF, 65
  .set sNDF, 32+1
  .set sST, 80
  ; ---------------- VGPRs
  .set vLANE, 1
  .set vOK, 2
  .set vOK2, 3
  .set vOV, 4
  .set vOV2, 5
  .set vKB0, 6
  .set vKB1, 7
  .set vVB, 8
  .set vSTART, 9
  .set vLEN, 10
  .set vINCL, 11
  .set vEXCL, 12
  .set vM, 13
  .set vNMC, 14
  .set vL0, 15
  .set vL1, 16
  .set vKA0, 17
  .set vKA1, 18
  .set vVA, 19
  .set vAL, 20
  .set vSO, 21
  .set vHH4, 22
  .set vSW, 23
  .set vT0, 24
  .set vT1, 25
  .set vT2, 26
  .set vT3, 27
  .set vT4, 28
  .set vT5, 29
  .set vT6, 30
  .set vT7, 31
  .set vP, 32
  .set vX, 40
  .set vSR, 56
  .set vLS, 40
  .set vONE, 60
  .set vXO, 44
  .set vXP, 45
  .set vSTA, 46
  .set vSTP, 47
  .set vSGW, 48
  .set vSGR, 49
  .set vFA, 50
  .set vFB, 52
  .set vO, 64
  .set vQ, 128
  .set vSA, 160
  .set vSB, 176
  .set vK, 192
  .set vV, 224
  ; ---------------- LDS map
  .set LK, 0
  .set LV, 65536
  .set LX, 131072
  .set LQ2, 49152
  .set LQ3, 114688
  .set LSCR, 98304

  .macro STAMP i
    .if DIAG && ((DIAG == 1) || (\i == 0) || (\i >= 5))
      s_memtime s[sST+2*\i:sST+2*\i+1]
      s_waitcnt lgkmcnt(0)
    .endif
  .endm
  .macro STAMPE i
    .if DIAG == 2
      s_memtime s[sST+2*\i:sST+2*\i+1]
      s_waitcnt lgkmcnt(0)
    .endif
  .endm

  ; ======================================================================== entry
  s_load_dwordx16 s[8:23], %0, 0
  s_load_dwordx8 s[24:31], %0, 64
  .if DIAG
    s_memrealtime s[sRT:sRT+1]
  .endif
  STAMP 0
  v_and_b32 v[vLANE], 63, %2
  v_readfirstlane_b32 s[sW], %2
  v_lshlrev_b32 v[vT0], 2, %2                     ; tid*4
  s_and_b32 s[sG], %1, 7
  s_lshr_b32 s[sJ], %1, 3
  s_lshr_b32 s[sW], s[sW], 6
  ; prefetch offset: (tid>>5)*4096 + ((tid>>4)&1)*2048 + ((tid>>3)&1)*128 + (tid&7)*16 + g*256
  v_lshrrev_b32 v[vT6], 5, %2
  v_lshlrev_b32 v[vT6], 12, v[vT6]
  v_bfe_u32 v[vT7], %2, 4, 1
  v_lshl_add_u32 v[vT6], v[vT7], 11, v[vT6]
  v_bfe_u32 v[vT7], %2, 3, 1
  v_lshl_add_u32 v[vT6], v[vT7], 7, v[vT6]
  v_and_b32 v[vT7], 7, %2
  v_lshl_add_u32 v[vT6], v[vT7], 4, v[vT6]
  s_lshl_b32 s[sT3], s[sG], 8
  v_add_u32 v[vT6], s[sT3], v[vT6]
  ; DMA lane offsets
  v_bfe_u32 v[vT1], v[vLANE], 2, 3                ; prow
  v_lshrrev_b32 v[vT2], 5, v[vLANE]               ; hh / pcb
  v_and_b32 v[vT3], 3, v[vLANE]                   ; pc
  v_lshlrev_b32 v[vT4], 12, v[vT1]
  v_lshl_add_u32 v[vT4], v[vT2], 6, v[vT4]        ; prow*4096 + pcb*64
  v_lshl_add_u32 v[vOV], v[vT3], 4, v[vT4]
  v_bfe_u32 v[vT5], v[vLANE], 4, 1
  s_and_b32 s[sT4], s[sW], 1
  s_lshl_b32 s[sT4], s[sT4], 1
  v_or_b32 v[vT5], s[sT4], v[vT5]                 ; x
  v_xor_b32 v[vT5], v[vT5], v[vT3]
  v_lshl_add_u32 v[vOK], v[vT5], 4, v[vT4]
  s_waitcnt lgkmcnt(0)
  STAMP 1
  ; ======================================================================== S1: segment ends per 64-token chunk
  s_lshl_b32 s[sT2], s[sT], 2
  s_sub_u32 s[sT2], s[sT2], 8                     ; (T-2)*4
  v_add_u32 v[vT7], 2048*(NIT-1), v[vT0]
  v_min_u32 v[vT7], s[sT2], v[vT7]                ; last chunk: clamped (no out-of-range lanes)
  .set i, 0
  .rept NIT
    .if i == NIT-1
      buffer_load_dwordx2 v[vX+2*i:vX+2*i+1], v[vT7], s[sS:sS+3], 0 offen
    .elseif i < 2
      buffer_load_dwordx2 v[vX+2*i:vX+2*i+1], v[vT0], s[sS:sS+3], 0 offen offset:2048*i
    .else
      s_movk_i32 s[sT1], 2048*i
      buffer_load_dwordx2 v[vX+2*i:vX+2*i+1], v[vT0], s[sS:sS+3], s[sT1] offen
    .endif
    .set i, i+1
  .endr
  .if TOUCH
    v_lshlrev_b32 v[vV+29], 20, v[vLANE]            ; 1 MiB stride
    s_sub_u32 s[sT3], s[sK+2], 4
    v_min_u32 v[vV+29], s[sT3], v[vV+29]
    s_mov_b64 exec, 0x3ff
    buffer_load_dword v[vV+20], v[vV+29], s[sK:sK+3], 0 offen
    buffer_load_dword v[vV+21], v[vV+29], s[sV:sV+3], 0 offen
    buffer_load_dword v[vV+22], v[vV+29], s[sQ:sQ+3], 0 offen
    buffer_load_dword v[vV+23], v[vV+29], s[sO:sO+3], 0 offen
    s_mov_b64 exec, -1
  .endif
  s_sub_u32 s[sTM16], s[sT], 16
  s_lshl_b32 s[sTM16], s[sTM16], 12               ; (T-16)*4096
  .macro PREFETCH lo, hi                            ; Q prefetch instructions lo..hi-1 (16 rows each)
    .set n, \lo
    .rept \hi-\lo
      s_mul_i32 s[sT3], s[sJ], s[sPFB]
      s_add_u32 s[sT3], s[sT3], n*16*4096
      s_min_u32 s[sT3], s[sT3], s[sTM16]
      buffer_load_dwordx4 v[vV+4*n:vV+4*n+3], v[vV+28], s[sQ:sQ+3], s[sT3] offen
      .set n, n+1
    .endr
  .endm
  v_mov_b32 v[vV+28], v[vT6]
  ; remaining lane constants (in the shadow of the loads)
  v_add_u32 v[vOK2], 128, v[vOK]
  v_add_u32 v[vOV2], 128, v[vOV]
  v_and_b32 v[vT1], 31, v[vLANE]                  ; r32
  v_lshrrev_b32 v[vT3], 3, v[vT1]
  v_lshlrev_b32 v[vT3], 11, v[vT3]
  v_and_b32 v[vT4], 7, v[vT1]
  v_lshl_add_u32 v[vT3], v[vT4], 6, v[vT3]
  v_bfe_u32 v[vT4], v[vT1], 2, 2
  v_xor_b32 v[vT4], v[vT4], v[vT2]
  v_lshl_add_u32 v[vKB0], v[vT4], 4, v[vT3]
  v_xor_b32 v[vKB1], 32, v[vKB0]
  v_bfe_u32 v[vT3], v[vLANE], 2, 2
  v_lshl_add_u32 v[vT3], v[vT2], 2, v[vT3]
  v_lshlrev_b32 v[vT3], 6, v[vT3]
  v_bfe_u32 v[vT4], v[vLANE], 4, 1
  v_lshl_add_u32 v[vT3], v[vT4], 5, v[vT3]
  v_and_b32 v[vT4], 3, v[vLANE]
  v_lshl_add_u32 v[vVB], v[vT4], 3, v[vT3]
  v_add_u32 v[vVB], LV, v[vVB]
  v_lshlrev_b32 v[vHH4], 2, v[vT2]
  ; output staging: write address  r32*128 + ((2*? + hh) ^ (r32&7))*16 handled per block: base = r32*128, xor term below
  v_and_b32 v[vT3], 7, v[vT1]
  v_xor_b32 v[vT3], v[vT3], v[vT2]                ; (r32&7) ^ hh
  v_lshlrev_b32 v[vT3], 4, v[vT3]
  v_lshl_add_u32 v[vSW], v[vT1], 7, v[vT3]        ; write addr for chunk pair 0 (chunk = hh); chunk pair p: xor with p*32
  ; read address: row = lane>>3, chunk = (lane&7) ^ (row&7) ; rows + 8i -> + 1024 i
  v_lshrrev_b32 v[vT3], 3, v[vLANE]
  v_and_b32 v[vT4], 7, v[vLANE]
  v_xor_b32 v[vT5], v[vT4], v[vT3]
  v_lshlrev_b32 v[vT5], 4, v[vT5]
  v_lshl_add_u32 v[vSR], v[vT3], 7, v[vT5]
  ; global store lane offset: (lane>>3)*4096 + (lane&7)*16
  v_lshlrev_b32 v[vT4], 4, v[vT4]
  v_lshl_add_u32 v[vSO], v[vT3], 12, v[vT4]
  ; row-sum MFMA A operand: row (lane&15) 0 takes k-groups 0,2 ; row 1 takes k-groups 1,3
  v_and_b32 v[vT3], 15, v[vLANE]
  v_bfe_u32 v[vT4], v[vLANE], 4, 1
  v_cmp_eq_u32 vcc, v[vT3], v[vT4]
  v_mov_b32 v[vT5], 0x3f803f80
  s_nop 0
  v_cndmask_b32 v[vONE], 0, v[vT5], vcc
  v_cndmask_b32 v[vONE+1], 0, v[vT5], vcc
  v_cndmask_b32 v[vONE+2], 0, v[vT5], vcc
  v_cndmask_b32 v[vONE+3], 0, v[vT5], vcc
  s_mov_b32 s[sCE], 0x3e0293ee
  s_mov_b32 s[sTHR], 0x427af232
  s_lshl_b32 s[sWD], s[sW], 11                    ; w*2048
  s_mov_b64 s[sC:sC+1], 0
  v_mov_b32 v[vT4], 0
  v_mov_b32 v[vT5], 0
  v_lshlrev_b32 v[vT7], 7, v[vLANE]               ; LDS address of chunk (w + 8*lane): 16 B each
  s_lshl_b32 s[sT1], s[sW], 4
  s_add_u32 s[sT1], s[sT1], LSCR
  v_add_u32 v[vT7], s[sT1], v[vT7]
  PREFETCH 0, PF1
  .set i, 0
  .rept NIT
    s_waitcnt vmcnt(NIT-1-i+PF1+4*TOUCH)
    v_cmp_ne_u32 s[sA:sA+1], v[vX+2*i], v[vX+2*i+1]
    v_cmp_gt_i32 vcc, v[vX+2*i], v[vX+2*i+1]
    .if i == NIT-1
      v_cmp_gt_i32 s[sB:sB+1], s[sLASTC], %2       ; element < T-1
      s_and_b64 s[sA:sA+1], s[sA:sA+1], s[sB:sB+1]
      s_and_b64 vcc, vcc, s[sB:sB+1]
      v_cmp_eq_u32 s[sB:sB+1], s[sLASTC], %2       ; element == T-1
      s_or_b64 s[sA:sA+1], s[sA:sA+1], s[sB:sB+1]
    .endif
    s_or_b64 s[sC:sC+1], s[sC:sC+1], vcc
    s_nop 0
    v_writelane_b32 v[vT4], s[sA], i
    v_writelane_b32 v[vT5], s[sA+1], i
    .set i, i+1
  .endr
  s_or_b32 s[sT1], s[sC], s[sC+1]
  v_mov_b32 v[vT6], s[sT1]
  s_mov_b64 exec, (1<<NIT)-1
  ds_write_b96 v[vT7], v[vT4:vT6]
  s_mov_b64 exec, -1
  v_mov_b32 v[vT0], 0
  v_mov_b32 v[vT1], 0
  v_mov_b32 v[vT2], 0
  v_lshlrev_b32 v[vT3], 4, v[vLANE]
  v_add_u32 v[vT3], LSCR, v[vT3]
  v_lshlrev_b32 v[vT7], 6, v[vLANE]
  v_add_u32 v[vT7], 1, v[vT7]                     ; lane*64 + 1
  PREFETCH PF1, PF2
  s_waitcnt lgkmcnt(0)
  s_barrier
  STAMP 2
  ; ======================================================================== S2
  s_bfm_b64 exec, 8*NIT, 0
  ds_read_b96 v[vT0:vT2], v[vT3]
  s_mov_b64 exec, -1
  s_movk_i32 s[sT1], 128
  s_movk_i32 s[sT2], 256
  s_mov_b32 s[sR], 0
  s_waitcnt lgkmcnt(0)
  v_cmp_ne_u32 vcc, 0, v[vT2]
  v_bcnt_u32_b32 v[vT3], v[vT0], 0
  v_bcnt_u32_b32 v[vT3], v[vT1], v[vT3]
  s_cbranch_vccnz .Lfallback
  v_cmp_lt_u32 vcc, 1, v[vT3]
  v_ffbl_b32 v[vT4], v[vT0]
  v_ffbl_b32 v[vT5], v[vT1]
  s_cbranch_vccnz .Lfallback
  v_cmp_ne_u32 s[sA:sA+1], 0, v[vT3]
  v_add_u32 v[vT5], 32, v[vT5]
  v_min_u32 v[vT4], v[vT4], v[vT5]
  v_add_u32 v[vT4], v[vT4], v[vT7]
  v_cndmask_b32 v[vT4], 0, v[vT4], s[sA:sA+1]     ; end (0 if none)
  s_nop 1
  v_mov_b32_dpp v[vSTART], v[vT4] wave_shr:1 row_mask:0xf bank_mask:0xf bound_ctrl:0
  s_nop 1
  v_max_u32_dpp v[vSTART], v[vSTART], v[vSTART] row_shr:1 row_mask:0xf bank_mask:0xf bound_ctrl:0
  s_nop 1
  v_max_u32_dpp v[vSTART], v[vSTART], v[vSTART] row_shr:2 row_mask:0xf bank_mask:0xf bound_ctrl:0
  s_nop 1
  v_max_u32_dpp v[vSTART], v[vSTART], v[vSTART] row_shr:4 row_mask:0xf bank_mask:0xf bound_ctrl:0
  s_nop 1
  v_max_u32_dpp v[vSTART], v[vSTART], v[vSTART] row_shr:8 row_mask:0xf bank_mask:0xf bound_ctrl:0
  s_nop 1
  v_max_u32_dpp v[vSTART], v[vSTART], v[vSTART] row_bcast:15 row_mask:0xa bank_mask:0xf
  s_nop 1
  v_max_u32_dpp v[vSTART], v[vSTART], v[vSTART] row_bcast:31 row_mask:0xc bank_mask:0xf
  s_nop 0
  v_sub_u32 v[vLEN], v[vT4], v[vSTART]
  v_cndmask_b32 v[vLEN], 0, v[vLEN], s[sA:sA+1]   ; len (0 if none)
  v_add_u32 v[vT5], 127, v[vLEN]
  v_lshrrev_b32 v[vT5], 7, v[vT5]                 ; nq (mode A)
  v_cmp_lt_u32 vcc, s[sT1], v[vLEN]               ; len > 128
  v_cmp_ge_u32 s[sB:sB+1], s[sT2], v[vLEN]        ; len <= 256
  s_and_b64 s[sB:sB+1], s[sB:sB+1], vcc           ; mode B
  v_lshlrev_b32 v[vT5], 1, v[vT5]
  s_nop 0
  v_cndmask_b32 v[vT5], v[vT5], 2, s[sB:sB+1]     ; items (2 heads)
  v_lshlrev_b32 v[vT6], 16, v[vT5]
  v_cndmask_b32 v[vINCL], v[vT6], v[vT5], vcc     ; small -> high half
  v_mov_b32 v[vEXCL], v[vINCL]
  s_nop 0
  v_add_u32_dpp v[vINCL], v[vINCL], v[vINCL] row_shr:1 row_mask:0xf bank_mask:0xf bound_ctrl:0
  s_nop 1
  v_add_u32_dpp v[vINCL], v[vINCL], v[vINCL] row_shr:2 row_mask:0xf bank_mask:0xf bound_ctrl:0
  s_nop 1
  v_add_u32_dpp v[vINCL], v[vINCL], v[vINCL] row_shr:4 row_mask:0xf bank_mask:0xf bound_ctrl:0
  s_nop 1
  v_add_u32_dpp v[vINCL], v[vINCL], v[vINCL] row_shr:8 row_mask:0xf bank_mask:0xf bound_ctrl:0
  s_nop 1
  v_add_u32_dpp v[vINCL], v[vINCL], v[vINCL] row_bcast:15 row_mask:0xa bank_mask:0xf
  s_nop 1
  v_add_u32_dpp v[vINCL], v[vINCL], v[vINCL] row_bcast:31 row_mask:0xc bank_mask:0xf
  s_nop 0
  v_sub_u32 v[vEXCL], v[vINCL], v[vEXCL]
  v_readlane_b32 s[sT1], v[vINCL], 63
  s_and_b32 s[sNBIG], s[sT1], 0xffff
  s_lshr_b32 s[sTOT], s[sT1], 16
  s_add_u32 s[sTOT], s[sTOT], s[sNBIG]
  .if DBGW & 8
  s_mov_b32 s[sSAFE], 1
  .else
  s_mov_b32 s[sSAFE], 0
  .endif
  STAMP 3

  .if PRIO == 1
    s_cmp_lt_u32 s[sW], 4
    s_cbranch_scc1 .Lnoprio
    s_setprio 1
.Lnoprio:
  .elseif PRIO == 2
    s_cmp_ge_u32 s[sW], 4
    s_cbranch_scc1 .Lnoprio
    s_setprio 1
.Lnoprio:
  .endif
  ; ---- DMA pieces.  sKDST / sVDST: ring offset + w*2048 of the next tile; rows beyond the segment end are out of range (zero fill, no traffic)
  .macro DK1
    s_mov_b32 m0, s[sKDST]
    s_add_u32 s[sT1], s[sKDST], 16384
    buffer_load_dwordx4 v[vOK], s[sK:sK+3], s[sKSO] offen lds
    .if DBGW & 4
    s_nop 3
    .endif
  .endm
  .macro DK2
    s_add_u32 m0, s[sKDST], 1024
    s_and_b32 s[sKDST], s[sT1], 0xffff
    buffer_load_dwordx4 v[vOK2], s[sK:sK+3], s[sKSO] offen lds
    .if DBGW & 4
    s_nop 3
    .endif
    s_add_u32 s[sKSO], s[sKSO], 64*4096
    s_min_u32 s[sKSO], s[sKSO], s[sMAXSO]
  .endm
  .macro DV1
    s_add_u32 m0, s[sVDST], LV
    s_add_u32 s[sT3], s[sVDST], 16384
    buffer_load_dwordx4 v[vOV], s[sV:sV+3], s[sVSO] offen lds
    .if DBGW & 4
    s_nop 3
    .endif
  .endm
  .macro DV2
    s_add_u32 m0, s[sVDST], LV+1024
    s_and_b32 s[sVDST], s[sT3], 0xffff
    buffer_load_dwordx4 v[vOV2], s[sV:sV+3], s[sVSO] offen lds
    .if DBGW & 4
    s_nop 3
    .endif
    s_add_u32 s[sVSO], s[sVSO], 64*4096
    s_min_u32 s[sVSO], s[sVSO], s[sMAXSO]
  .endm
  .macro DALL
    DK1
    DK2
    DV1
    DV2
  .endm
  .macro BAR4 modea=0, bit=2                        ; previous group had 4 pieces: everything older has landed
    .if \modea
      s_cmp_eq_u32 s[sMB], 1
      s_cbranch_scc1 .Lnob\@
    .endif
    .if DBGW & \bit
    s_waitcnt vmcnt(0) lgkmcnt(0)
    .else
    s_waitcnt vmcnt(4) lgkmcnt(0)
    .endif
    s_barrier
    .if \modea
    s_branch .Lbd\@
  .Lnob\@:
    s_waitcnt lgkmcnt(0)
  .Lbd\@:
    .endif
  .endm
  .macro BARN modea=0                               ; nothing was issued behind the needed pieces
    .if \modea
      s_cmp_eq_u32 s[sMB], 1
      s_cbranch_scc1 .Lnob\@
    .endif
    s_waitcnt vmcnt(0) lgkmcnt(0)
    s_barrier
    .if \modea
    s_branch .Lbd\@
  .Lnob\@:
    s_waitcnt lgkmcnt(0)
  .Lbd\@:
    .endif
  .endm
  .macro BAR0 modea=0
    .if \modea
      s_cmp_eq_u32 s[sMB], 1
      s_cbranch_scc1 .Lnob\@
    .endif
    s_waitcnt vmcnt(0) lgkmcnt(0)
    s_barrier
    .if \modea
  .Lnob\@:
    .endif
  .endm

.Litem:
  ; idx = r*32 + (r odd ? 31-j : j)
  s_lshl_b32 s[sT1], s[sR], 5
  s_sub_u32 s[sT2], 31, s[sJ]
  s_bitcmp1_b32 s[sR], 0
  s_cselect_b32 s[sT2], s[sT2], s[sJ]
  s_add_u32 s[sT1], s[sT1], s[sT2]                ; idx
  s_cmp_ge_u32 s[sT1], s[sTOT]
  s_cbranch_scc1 .Ldone
  s_sub_u32 s[sT2], s[sT1], s[sNBIG]
  s_cmp_lt_u32 s[sT1], s[sNBIG]
  s_cselect_b32 s[sT1], s[sT1], s[sT2]            ; index inside its class
  s_cselect_b32 s[sT2], 0, 16                     ; field shift
  v_bfe_u32 v[vT0], v[vINCL], s[sT2], 16
  v_bfe_u32 v[vT1], v[vEXCL], s[sT2], 16
  v_cmp_lt_u32 s[sA:sA+1], s[sT1], v[vT0]
  v_cmp_ge_u32 vcc, s[sT1], v[vT1]
  s_and_b64 s[sA:sA+1], s[sA:sA+1], vcc
  s_ff1_i32_b64 s[sT2], s[sA:sA+1]
  v_readlane_b32 s[sK0], v[vSTART], s[sT2]
  v_readlane_b32 s[sLEN], v[vLEN], s[sT2]
  v_readlane_b32 s[sT3], v[vT1], s[sT2]
  s_sub_u32 s[sT1], s[sT1], s[sT3]                ; local item
  s_and_b32 s[sT2], s[sT1], 1
  s_lshl_b32 s[sT2], s[sT2], 3
  s_add_u32 s[sT2], s[sT2], s[sG]
  s_lshl_b32 s[sHOFF], s[sT2], 8                  ; head*256 bytes
  s_lshr_b32 s[sT1], s[sT1], 1
  s_lshl_b32 s[sT1], s[sT1], 7
  s_add_u32 s[sQ0], s[sK0], s[sT1]
  s_lshl_b32 s[sT1], s[sW], 3
  s_add_u32 s[sT2], s[sQ0], s[sT1]
  s_lshl_b32 s[sT2], s[sT2], 12
  s_add_u32 s[sT2], s[sT2], s[sHOFF]              ; (q0 + 8w)*4096 + hoff
  ; ---- Q DMA
  s_add_u32 m0, s[sWD], LX
  s_add_u32 s[sT3], s[sT2], 64*4096
  buffer_load_dwordx4 v[vOK], s[sQ:sQ+3], s[sT2] offen lds
  s_add_u32 m0, s[sWD], LX+1024
  s_add_u32 s[sT4], s[sT2], 128*4096
  buffer_load_dwordx4 v[vOK2], s[sQ:sQ+3], s[sT2] offen lds
  s_add_u32 m0, s[sWD], LX+16384
  s_add_u32 s[sT1], s[sK0], s[sT1]
  buffer_load_dwordx4 v[vOK], s[sQ:sQ+3], s[sT3] offen lds
  s_add_u32 m0, s[sWD], LX+16384+1024
  s_lshl_b32 s[sT1], s[sT1], 12
  buffer_load_dwordx4 v[vOK2], s[sQ:sQ+3], s[sT3] offen lds
  s_add_u32 s[sKSO], s[sT1], s[sHOFF]             ; (k0 + 8w)*4096 + hoff
  s_add_u32 s[sMAXSO], s[sTM16], 15*4096
  s_add_u32 s[sMAXSO], s[sMAXSO], s[sHOFF]        ; (T-1)*4096 + hoff
  s_min_u32 s[sKSO], s[sKSO], s[sMAXSO]
  s_mov_b32 s[sVSO], s[sKSO]
  s_add_u32 s[sQEND], s[sK0], s[sLEN]
  s_add_u32 s[sNK], s[sLEN], 63
  s_lshr_b32 s[sNK], s[sNK], 6
  s_lshl_b32 s[sO+2], s[sQEND], 12                ; the output descriptor ends at the segment end
  s_sub_u32 s[sT1], s[sLEN], 129
  s_cmp_lt_u32 s[sT1], 128
  s_cselect_b32 s[sMB], 1, 0                      ; mode B: 128 < len <= 256
  s_mov_b32 s[sKDST], s[sWD]
  s_mov_b32 s[sVDST], s[sWD]
  s_cbranch_scc0 .Lq_done
  s_add_u32 m0, s[sWD], LQ2
  s_add_u32 s[sT3], s[sT4], 64*4096
  buffer_load_dwordx4 v[vOK], s[sQ:sQ+3], s[sT4] offen lds
  s_add_u32 m0, s[sWD], LQ2+1024
  s_nop 0
  buffer_load_dwordx4 v[vOK2], s[sQ:sQ+3], s[sT4] offen lds
  s_add_u32 m0, s[sWD], LQ3
  s_nop 0
  buffer_load_dwordx4 v[vOK], s[sQ:sQ+3], s[sT3] offen lds
  s_add_u32 m0, s[sWD], LQ3+1024
  s_nop 0
  buffer_load_dwordx4 v[vOK2], s[sQ:sQ+3], s[sT3] offen lds
.Lq_done:
  DK1
  DK2
  STAMP 4
  ; ---- per-wave geometry
  s_and_b32 s[sT1], s[sW], 3
  s_lshr_b32 s[sT2], s[sW], 2
  s_cmp_eq_u32 s[sMB], 1
  s_cselect_b32 s[sQB], s[sW], s[sT1]             ; q block
  s_cselect_b32 s[sKH], 0, s[sT2]                 ; key half (mode A)
  s_movk_i32 s[sKSTEP], 0x4000
  s_cselect_b32 s[sKSTEP], 8192, s[sKSTEP]
  s_add_u32 s[sT1], s[sLEN], 31
  s_lshr_b32 s[sT1], s[sT1], 5                    ; nst = ceil(len/32)
  s_add_u32 s[sT2], s[sT1], 1
  s_sub_u32 s[sT2], s[sT2], s[sKH]
  s_lshr_b32 s[sT2], s[sT2], 1
  s_cmp_eq_u32 s[sMB], 1
  s_cselect_b32 s[sNS], s[sT1], s[sT2]            ; steps of this wave
  s_cselect_b32 s[sNU], s[sT1], s[sNK]            ; step slots
  s_and_b32 s[sLIM], s[sLEN], 31
  s_sub_u32 s[sT2], s[sT1], 1
  s_and_b32 s[sT2], s[sT2], 1
  s_cmp_eq_u32 s[sT2], s[sKH]
  s_cselect_b32 s[sT3], 1, s[sMB]                 ; owner of the last sub-tile
  s_cmp_lg_u32 s[sLIM], 0
  s_cselect_b32 s[sT3], s[sT3], 0
  s_sub_u32 s[sMSTEP], s[sNS], 1
  s_cmp_eq_u32 s[sT3], 1
  s_cselect_b32 s[sMSTEP], s[sMSTEP], -1
  s_lshl_b32 s[sT1], s[sQB], 5
  s_add_u32 s[sQR], s[sT1], s[sQ0]                ; first q row of this wave
  s_cmp_lt_u32 s[sQR], s[sQEND]
  s_cselect_b32 s[sNS], s[sNS], 0
  s_cselect_b32 s[sLIVE], 1, 0
  ; FULL slots with DMA: tile iteration t <= nk-3
  s_sub_i32 s[sND], s[sNK], 2
  s_max_i32 s[sND], s[sND], 0
  s_lshl_b32 s[sND], s[sND], s[sMB]
  ; Q read base
  s_lshl_b32 s[sT1], s[sQB], 13
  s_add_u32 s[sT2], s[sT1], LX                    ; qb < 4
  s_add_u32 s[sT3], s[sT1], LQ2-4*8192            ; qb 4,5
  s_add_u32 s[sT4], s[sT1], LQ3-6*8192            ; qb 6,7
  s_cmp_lt_u32 s[sQB], 6
  s_cselect_b32 s[sT3], s[sT3], s[sT4]
  s_cmp_lt_u32 s[sQB], 4
  s_cselect_b32 s[sT2], s[sT2], s[sT3]
  v_add_u32 v[vT0], s[sT2], v[vKB0]
  v_add_u32 v[vT1], s[sT2], v[vKB1]
  s_lshl_b32 s[sKOFF], s[sKH], 13
  s_mov_b32 s[sVOFF], s[sKOFF]
  v_add_u32 v[vKA0], s[sKOFF], v[vKB0]
  v_add_u32 v[vKA1], s[sKOFF], v[vKB1]
  ; epilogue addresses
  v_lshlrev_b32 v[vT2], 4, v[vLANE]
  s_lshl_b32 s[sT1], s[sW], 13
  s_xor_b32 s[sT2], s[sT1], 4*8192
  v_add_u32 v[vXP], s[sT2], v[vT2]                ; partner's exchange area
  v_add_u32 v[vXO], s[sT1], v[vT2]                ; own exchange area
  v_and_b32 v[vT2], 31, v[vLANE]
  v_lshlrev_b32 v[vT2], 3, v[vT2]
  v_add_u32 v[vT2], LX, v[vT2]
  s_lshl_b32 s[sT1], s[sW], 8
  s_xor_b32 s[sT2], s[sT1], 4*256
  v_add_u32 v[vSTP], s[sT2], v[vT2]
  v_add_u32 v[vSTA], s[sT1], v[vT2]
  s_lshl_b32 s[sT1], s[sW], 12                    ; stage: mode A in the V ring, mode B in the X region... (4 KiB per wave)
  s_add_u32 s[sT2], s[sT1], LV
  s_lshl_b32 s[sT3], s[sW], 13
  s_cmp_eq_u32 s[sMB], 1
  s_cselect_b32 s[sT1], s[sT3], s[sT2]
  v_add_u32 v[vSGW], s[sT1], v[vSW]
  v_add_u32 v[vSGR], s[sT1], v[vSR]
  .set i, 0
  .rept 64
    v_mov_b32 v[vO+i], 0
    .set i, i+1
  .endr
  v_mov_b32 v[vL0], 0
  v_mov_b32 v[vL1], 0
  v_mov_b32 v[vLS], 0
  v_mov_b32 v[vLS+1], 0
  v_mov_b32 v[vLS+2], 0
  v_mov_b32 v[vLS+3], 0
  v_mov_b32 v[vM], 0xf149f2ca
  s_mov_b32 s[sPEND], 0
  s_mov_b32 s[sU], 0
  ; mid group: mode A: K1, K2, V0 ; mode B: K1, V0
  .if WQ
    s_waitcnt vmcnt(2)
  .endif
  DK1
  DK2
  s_cmp_eq_u32 s[sMB], 1
  s_cbranch_scc1 .Lmid_b
  DK1
  DK2
  DV1
  DV2
  s_waitcnt vmcnt(6)
  s_branch .Lmid_d
.Lmid_b:
  DV1
  DV2
  s_waitcnt vmcnt(4)
.Lmid_d:
  s_barrier                                         ; barrier P: Q, K0 landed
  STAMP 5

  .macro KREAD i
    .if (ABL & 8)
    .elseif (\i & 1)
      ds_read_b128 v[vK+4*\i:vK+4*\i+3], v[vKA1] offset:512*(\i>>1)
    .else
      ds_read_b128 v[vK+4*\i:vK+4*\i+3], v[vKA0] offset:512*(\i>>1)
    .endif
  .endm
  .macro KADV                                       ; advance K read addresses to the next step
    s_add_u32 s[sKOFF], s[sKOFF], s[sKSTEP]
    s_and_b32 s[sKOFF], s[sKOFF], 0xffff
    v_add_u32 v[vKA0], s[sKOFF], v[vKB0]
    v_add_u32 v[vKA1], s[sKOFF], v[vKB1]
  .endm
  .macro QKM c, i
    .if \i == 0
      v_mfma_f32_32x32x16_bf16 v[\c:\c+15], v[vK:vK+3], v[vQ:vQ+3], 0
    .else
      v_mfma_f32_32x32x16_bf16 v[\c:\c+15], v[vK+4*\i:vK+4*\i+3], v[vQ+4*\i:vQ+4*\i+3], v[\c:\c+15]
    .endif
  .endm
  .macro VREAD j                                    ; V fragment j: d = j&3, ks = j>>2
    .if (ABL & 4) == 0
    ds_read_b64_tr_b16 v[vV+4*\j:vV+4*\j+1], v[vVA] offset:512*(\j&3)+4096*(\j>>2)
    ds_read_b64_tr_b16 v[vV+4*\j+2:vV+4*\j+3], v[vVA] offset:512*(\j&3)+4096*(\j>>2)+2048
    .endif
  .endm
  .macro PVM j
    v_mfma_f32_32x32x16_bf16 v[vO+16*(\j&3):vO+16*(\j&3)+15], v[vV+4*\j:vV+4*\j+3], v[vP+4*(\j>>2):vP+4*(\j>>2)+3], v[vO+16*(\j&3):vO+16*(\j&3)+15]
  .endm
  .macro LSUM ks
    .if (ABL & 64) == 0
    v_mfma_f32_16x16x32_bf16 v[vLS:vLS+3], v[vONE:vONE+3], v[vP+4*\ks:vP+4*\ks+3], v[vLS:vLS+3]
    .endif
  .endm
  .macro FMAE c, r
    .if (ABL & 2) == 0
    v_fma_f32 v[\c+(\r)], v[\c+(\r)], s[sCE], v[vNMC]
    .endif
  .endm
  .macro XPE c, r
    .if (ABL & 2) == 0
    v_exp_f32 v[\c+(\r)], v[\c+(\r)]
    .endif
  .endm
  .macro MASK c                                     ; keys >= lim of this sub-tile -> -inf
    v_sub_u32 v[vT2], s[sLIM], v[vHH4]
    v_mov_b32 v[vT3], 0xff800000
    .set r, 0
    .rept 16
      v_cmp_lt_i32 vcc, (r&3)+8*(r>>2), v[vT2]
      s_nop 1
      v_cndmask_b32 v[\c+r], v[vT3], v[\c+r], vcc
      .set r, r+1
    .endr
  .endm
  .macro ROWMAX1 c
    v_max3_f32 v[vT2], v[\c+0], v[\c+1], v[\c+2]
    v_max3_f32 v[vT3], v[\c+3], v[\c+4], v[\c+5]
    v_max3_f32 v[vT2], v[vT2], v[\c+6], v[\c+7]
    v_max3_f32 v[vT3], v[vT3], v[\c+8], v[\c+9]
    v_max3_f32 v[vT2], v[vT2], v[\c+10], v[\c+11]
    v_max3_f32 v[vT3], v[vT3], v[\c+12], v[\c+13]
    v_max3_f32 v[vT2], v[vT2], v[\c+14], v[\c+15]
    v_max_f32 v[vT2], v[vT2], v[vT3]
    v_mov_b32 v[vT3], v[vT2]
  .endm
  .macro ROWMAX2                                    ; needs 2 wait states after ROWMAX1
    v_permlane32_swap_b32 v[vT2], v[vT3]
    s_nop 0
    v_max_f32 v[vT2], v[vT2], v[vT3]                ; vT2 = row max
  .endm
  .macro EXPE c, r
    .if (ABL & 2) == 0
    v_fma_f32 v[\c+(\r)], v[\c+(\r)], s[sCE], v[vNMC]
    v_exp_f32 v[\c+(\r)], v[\c+(\r)]
    .endif
  .endm
  .macro SUMPK p, i                                 ; row sums + pack of elements 2i, 2i+1
    .if (ABL & 32) == 0
    v_add_f32 v[vL0], v[vL0], v[\p+2*\i]
    v_add_f32 v[vL1], v[vL1], v[\p+2*\i+1]
    v_cvt_pk_bf16_f32 v[vP+\i], v[\p+2*\i], v[\p+2*\i+1]
    .endif
  .endm
  .macro ADDE p, e                                  ; row sum of element e (two chains)
    .if (ABL & 32) == 0
    .if ((\e) & 1)
      v_add_f32 v[vL1], v[vL1], v[\p+(\e)]
    .else
      v_add_f32 v[vL0], v[vL0], v[\p+(\e)]
    .endif
    .endif
  .endm
  .macro CVTE p, i                                  ; pack of elements 2i, 2i+1
    .if (ABL & 32) == 0
    v_cvt_pk_bf16_f32 v[vP+\i], v[\p+2*\i], v[\p+2*\i+1]
    .endif
  .endm
  .macro VADDR                                      ; V read address for the current step, then advance
    v_add_u32 v[vVA], s[sVOFF], v[vVB]
    s_add_u32 s[sVOFF], s[sVOFF], s[sKSTEP]
    s_and_b32 s[sVOFF], s[sVOFF], 0xffff
  .endm
  .macro RESCALE
    s_nop 7
    s_nop 7
    .set i, 0
    .rept 64
      v_mul_f32 v[vO+i], v[vAL], v[vO+i]
      .set i, i+1
    .endr
    v_mul_f32 v[vL0], v[vAL], v[vL0]
    v_mul_f32 v[vL1], v[vAL], v[vL1]
    s_mov_b32 s[sPEND], 0
  .endm

  ; ---- prologue compute: step 0
  s_cmp_eq_u32 s[sNS], 0
  s_cbranch_scc1 .Lpro_none
  .set i, 0
  .rept 8
    KREAD i
    .if (i & 1)
      ds_read_b128 v[vQ+4*i:vQ+4*i+3], v[vT1] offset:512*(i>>1)
    .else
      ds_read_b128 v[vQ+4*i:vQ+4*i+3], v[vT0] offset:512*(i>>1)
    .endif
    .set i, i+1
  .endr
  KADV
  .macro QK0 rd                                     ; rd: read the next step's K fragments behind each MFMA (mode B: same tile)
    .set i, 0
    .rept 8
      .if \rd
        .if i == 0
          s_waitcnt lgkmcnt(14)
        .elseif i == 1
          s_waitcnt lgkmcnt(13)
        .elseif i == 2
          s_waitcnt lgkmcnt(12)
        .elseif i == 3
          s_waitcnt lgkmcnt(11)
        .elseif i == 4
          s_waitcnt lgkmcnt(10)
        .elseif i == 5
          s_waitcnt lgkmcnt(9)
        .elseif i == 6
          s_waitcnt lgkmcnt(8)
        .else
          s_waitcnt lgkmcnt(7)
        .endif
      .else
        .if i == 0
          s_waitcnt lgkmcnt(14)
        .elseif i == 1
          s_waitcnt lgkmcnt(12)
        .elseif i == 2
          s_waitcnt lgkmcnt(10)
        .elseif i == 3
          s_waitcnt lgkmcnt(8)
        .elseif i == 4
          s_waitcnt lgkmcnt(6)
        .elseif i == 5
          s_waitcnt lgkmcnt(4)
        .elseif i == 6
          s_waitcnt lgkmcnt(2)
        .else
          s_waitcnt lgkmcnt(0)
        .endif
      .endif
      QKM vSA, i
      .if \rd
        KREAD i
      .endif
      .if i == 1
        DK1
      .elseif i == 3
        DK2
      .elseif i == 5
        DV1
      .elseif i == 7
        DV2
      .endif
      .set i, i+1
    .endr
  .endm
  s_cmp_eq_u32 s[sMB], 1
  s_cbranch_scc1 .Lqk0_b
  QK0 0
  s_branch .Lqk0_d
.Lqk0_b:
  QK0 1
  KADV
.Lqk0_d:
  s_nop 7
  s_cmp_lg_u32 s[sMSTEP], 0
  s_cbranch_scc1 .Lnomask0
  MASK vSA
.Lnomask0:
  ROWMAX1 vSA
  s_nop 1
  ROWMAX2
  v_max_f32 v[vM], v[vM], v[vT2]
  v_mul_f32 v[vNMC], 0xbe0293ee, v[vM]
  s_sub_u32 s[sNF], s[sNS], 1                     ; FULL slots of this wave
  s_min_u32 s[sNDF], s[sND], s[sNF]               ; ... of which issue DMA
  VADDR
  .macro P2                                         ; mode A: K1 landed -> its fragments
    s_cmp_eq_u32 s[sMB], 1
    s_cbranch_scc1 .Lp2\@
    s_waitcnt vmcnt(8)
    s_barrier
    .set i, 0
    .rept 8
      KREAD i
      .set i, i+1
    .endr
    KADV
  .Lp2\@:
  .endm
  s_cmp_eq_u32 s[sSAFE], 1
  s_cbranch_scc1 .Lpro_safe
  .set r, 0
  .rept 6
    EXPE vSA, r
    .set r, r+1
  .endr
  P2
  .set r, 6
  .rept 5
    EXPE vSA, r
    .set r, r+1
  .endr
  STAMP 6
  s_cmp_lt_u32 s[sU], s[sNDF]
  s_cbranch_scc1 .LloopF
  s_branch .LdispF0
.Lpro_safe:
  P2
  .set r, 0
  .rept 16
    EXPE vSA, r
    .set r, r+1
  .endr
  s_cmp_eq_u32 s[sNF], 0
  s_cbranch_scc1 .LlastS0
  s_branch .LloopS
.Lpro_none:
  DALL
  s_cmp_eq_u32 s[sMB], 1
  s_cbranch_scc1 .Lnone
  s_waitcnt vmcnt(8)
  s_barrier
  s_branch .Lnone

  ; ======================================================================== SAFE step (online max every step)
  .macro FULLS cur, prev, modea
    s_cmp_lt_u32 s[sU], s[sND]
    s_cselect_b32 s[sF], 1, 0
    s_cbranch_scc0 .Lbn\@
    BAR4 \modea
    s_branch .Lbq\@
  .Lbn\@:
    BARN \modea
  .Lbq\@:
    .set g, 0
    .rept 8
      QKM \cur, g
      VREAD g
      SUMPK \prev, g
      .if g == 1
        DMAK1 \modea, 2
      .elseif g == 5
        DMAK2 \modea, 2
      .endif
      .set g, g+1
    .endr
    s_waitcnt lgkmcnt(4)
    PVM 0
    KREAD 0
    KREAD 1
    s_add_u32 s[sU], s[sU], 1
    s_nop 0
    PVM 1
    s_cmp_lg_u32 s[sMSTEP], s[sU]
    s_cbranch_scc1 .Lnm\@
    MASK \cur
  .Lnm\@:
    ROWMAX1 \cur
    PVM 2
    KREAD 2
    ROWMAX2
    v_sub_f32 v[vT3], v[vT2], v[vM]
    v_cmp_lt_f32 vcc, s[sTHR], v[vT3]
    s_cbranch_vccz .Lkeep\@
    v_max_f32 v[vT3], v[vM], v[vT2]
    v_sub_f32 v[vAL], v[vM], v[vT3]
    v_mul_f32 v[vAL], s[sCE], v[vAL]
    v_exp_f32 v[vAL], v[vAL]
    v_mov_b32 v[vM], v[vT3]
    v_mul_f32 v[vNMC], 0xbe0293ee, v[vM]
    s_mov_b32 s[sPEND], 1
  .Lkeep\@:
    PVM 3
    KREAD 3
    DMAV1 \modea, 2
    EXPE \cur, 0
    EXPE \cur, 1
    EXPE \cur, 2
    PVM 4
    KREAD 4
    EXPE \cur, 3
    EXPE \cur, 4
    EXPE \cur, 5
    PVM 5
    KREAD 5
    EXPE \cur, 6
    EXPE \cur, 7
    EXPE \cur, 8
    s_waitcnt lgkmcnt(6)
    PVM 6
    KREAD 6
    DMAV2 \modea, 2
    EXPE \cur, 9
    EXPE \cur, 10
    EXPE \cur, 11
    PVM 7
    KREAD 7
    EXPE \cur, 12
    EXPE \cur, 13
    EXPE \cur, 14
    EXPE \cur, 15
    KADV
    VADDR
    s_cmp_eq_u32 s[sPEND], 0
    s_cbranch_scc1 .Lsend\@
    RESCALE
  .Lsend\@:
    s_cmp_eq_u32 s[sU], s[sNF]
  .endm
  .macro LASTS prev, modea
    BAR0 \modea
    .set g, 0
    .rept 8
      VREAD g
      SUMPK \prev, g
      .set g, g+1
    .endr
    s_add_u32 s[sU], s[sU], 1
    s_waitcnt lgkmcnt(0)
    .set g, 0
    .rept 8
      PVM g
      .set g, g+1
    .endr
    s_branch .Lnone_tail
  .endm

  ; DMA slots inside a step: odd slots of mode B issue nothing.  dma: 0 none, 1 always, 2 run-time flag sF
  .macro DMAK1 modea, dma=1
    .if (\dma) && ((ABL & 1) == 0)
    .if \dma == 2
      s_cmp_eq_u32 s[sF], 0
      s_cbranch_scc1 .Ld\@
    .endif
    .if \modea
      s_cmp_eq_u32 s[sMB], 1
      s_cbranch_scc1 .Ld\@
    .endif
    DK1
  .Ld\@:
    .endif
  .endm
  .macro DMAK2 modea, dma=1
    .if (\dma) && ((ABL & 1) == 0)
    .if \dma == 2
      s_cmp_eq_u32 s[sF], 0
      s_cbranch_scc1 .Ld\@
    .endif
    .if \modea
      s_cmp_eq_u32 s[sMB], 1
      s_cbranch_scc1 .Ld\@
    .endif
    DK2
  .Ld\@:
    .endif
  .endm
  .macro DMAV1 modea, dma=1
    .if (\dma) && ((ABL & 1) == 0)
    .if \dma == 2
      s_cmp_eq_u32 s[sF], 0
      s_cbranch_scc1 .Ld\@
    .endif
    .if \modea
      s_cmp_eq_u32 s[sMB], 1
      s_cbranch_scc1 .Ld\@
    .endif
    DV1
  .Ld\@:
    .endif
  .endm
  .macro DMAV2 modea, dma=1
    .if (\dma) && ((ABL & 1) == 0)
    .if \dma == 2
      s_cmp_eq_u32 s[sF], 0
      s_cbranch_scc1 .Ld\@
    .endif
    .if \modea
      s_cmp_eq_u32 s[sMB], 1
      s_cbranch_scc1 .Ld\@
    .endif
    DV2
  .Ld\@:
    .endif
  .endm

  ; ======================================================================== FAST step (reference = row max of the wave's first sub-tile;
  ; overflow is detected on the row sums at the end of the item and the item is redone with the SAFE steps)
  ; prev enters with exp done for e0..e10 and e0..e3 summed; vVA is set for this step
  .macro FULLF cur, prev, modea, dma
    .if \dma
      BAR4 \modea
    .else
      BARN \modea
    .endif
    ; ---------------- phase A
    QKM \cur, 0
    VREAD 0
    EXPE \prev, 11
    QKM \cur, 1
    VREAD 1
    EXPE \prev, 12
    CVTE \prev, 0
    QKM \cur, 2
    VREAD 2
    DMAK1 \modea, \dma
    EXPE \prev, 13
    CVTE \prev, 1
    QKM \cur, 3
    VREAD 3
    EXPE \prev, 14
    CVTE \prev, 2
    QKM \cur, 4
    VREAD 4
    EXPE \prev, 15
    CVTE \prev, 3
    QKM \cur, 5
    VREAD 5
    CVTE \prev, 4
    CVTE \prev, 5
    QKM \cur, 6
    VREAD 6
    DMAK2 \modea, \dma
    QKM \cur, 7
    VREAD 7
    CVTE \prev, 6
    CVTE \prev, 7
    s_add_u32 s[sU], s[sU], 1
    ; ---------------- phase B
    s_waitcnt lgkmcnt(4)
    PVM 0
    KREAD 0
    KREAD 1
    DMAV1 \modea, \dma
    s_cmp_lg_u32 s[sMSTEP], s[sU]
    s_nop 0
    s_cbranch_scc1 .Lnm\@
    MASK \cur
  .Lnm\@:
    EXPE \cur, 0
    PVM 1
    EXPE \cur, 1
    LSUM 0
    EXPE \cur, 2
    PVM 2
    KREAD 2
    KREAD 3
    EXPE \cur, 3
    PVM 3
    KREAD 4
    EXPE \cur, 4
    FMAE \cur, 5
    PVM 4
    KREAD 5
    XPE \cur, 5
    EXPE \cur, 6
    PVM 5
    KREAD 6
    KREAD 7
    EXPE \cur, 7
    LSUM 1
    EXPE \cur, 8
    s_waitcnt lgkmcnt(8)
    PVM 6
    DMAV2 \modea, \dma
    EXPE \cur, 9
    KADV
    VADDR
    .if \dma
      s_cmp_lt_u32 s[sU], s[sNDF]
    .else
      s_cmp_lt_u32 s[sU], s[sNF]
    .endif
    PVM 7
    EXPE \cur, 10
  .endm
  .macro PVJ d, ks
    v_mfma_f32_32x32x16_bf16 v[vO+16*\d:vO+16*\d+15], v[vV+4*(\d+4*\ks):vV+4*(\d+4*\ks)+3], v[vP+4*\ks:vP+4*\ks+3], v[vO+16*\d:vO+16*\d+15]
  .endm
  .macro SENDB d                                    ; d-block d (16 regs) -> own exchange area
    .set i, 0
    .rept 4
      ds_write_b128 v[vXO], v[vO+16*\d+4*i:vO+16*\d+4*i+3] offset:4096*(\d&1)+1024*i
      .set i, i+1
    .endr
  .endm
  .macro LASTV da, db, dc, dd                       ; last P.V: the blocks to send (da, db) first
    PVJ \da, 0
    PVJ \db, 0
    PVJ \da, 1
    PVJ \db, 1
    PVJ \dc, 0
    s_nop 7
    s_nop 1
    SENDB \da
    PVJ \dd, 0
    SENDB \db
    PVJ \dc, 1
    PVJ \dd, 1
    s_branch .Lepi_sent
  .endm
  .macro LASTF prev, modea
    BAR0 \modea
    .set g, 0
    .rept 8
      VREAD g
      .if g < 5
        EXPE \prev, 11+g
      .endif
      .set g, g+1
    .endr
    s_add_u32 s[sU], s[sU], 1
    .set g, 0
    .rept 8
      CVTE \prev, g
      .set g, g+1
    .endr
    ; the send-early form needs the final barrier behind it (mode A, last slot)
    s_cmp_eq_u32 s[sU], s[sNU]
    s_cselect_b32 s[sT1], 1, 0
    s_andn2_b32 s[sT1], s[sT1], s[sMB]
    s_cmp_eq_u32 s[sT1], 1
    s_waitcnt lgkmcnt(0)
    LSUM 0
    LSUM 1
    s_cbranch_scc0 .Lplain\@
    s_cmp_eq_u32 s[sKH], 0
    s_cbranch_scc0 .Lhi\@
    LASTV 2, 3, 0, 1
  .Lhi\@:
    LASTV 0, 1, 2, 3
  .Lplain\@:
    .set g, 0
    .rept 8
      PVM g
      .set g, g+1
    .endr
    s_branch .Lnone_tail
  .endm

.LloopS:
  FULLS vSB, vSA, 0
  s_cbranch_scc1 .LlastS1
  FULLS vSA, vSB, 1
  s_cbranch_scc0 .LloopS
.LlastS0:
  LASTS vSA, 0
.LlastS1:
  LASTS vSB, 1
.LloopF:
  FULLF vSB, vSA, 0, 1
  s_cbranch_scc0 .LdispF1
.LloopF1:
  FULLF vSA, vSB, 1, 1
  s_cbranch_scc1 .LloopF
.LdispF0:
  s_cmp_lt_u32 s[sU], s[sNF]
  s_cbranch_scc0 .LlastF0
.LfullN0:
  FULLF vSB, vSA, 0, 0
  s_cbranch_scc0 .LlastF1
.LfullN1:
  FULLF vSA, vSB, 1, 0
  s_cbranch_scc1 .LfullN0
.LlastF0:
  LASTF vSA, 0
.LdispF1:
  s_cmp_lt_u32 s[sU], s[sNF]
  s_cbranch_scc1 .LfullN1
.LlastF1:
  LASTF vSB, 1

  ; ---- slots in which this wave has nothing to compute
.Lnone:                                             ; wave without rows: keep loading its share of the tiles
  s_cmp_ge_u32 s[sU], s[sNU]
  s_cbranch_scc1 .Lepi
  s_bitcmp1_b32 s[sU], 0
  s_cselect_b32 s[sT1], s[sMB], 0
  s_cmp_eq_u32 s[sT1], 1
  s_cbranch_scc1 .Lnone_skip
  s_cmp_lt_u32 s[sU], s[sND]
  s_cbranch_scc0 .Lnone_fin
  s_waitcnt vmcnt(4) lgkmcnt(0)
  s_barrier
  DALL
  s_branch .Lnone_skip
.Lnone_fin:
  s_waitcnt vmcnt(0) lgkmcnt(0)
  s_barrier
.Lnone_skip:
  s_add_u32 s[sU], s[sU], 1
  s_branch .Lnone
.Lnone_tail:                                        ; after the wave's last step: only barriers remain
  s_cmp_ge_u32 s[sU], s[sNU]
  s_cbranch_scc1 .Lepi
  s_bitcmp1_b32 s[sU], 0
  s_cselect_b32 s[sT1], s[sMB], 0
  s_cmp_eq_u32 s[sT1], 1
  s_cbranch_scc1 .Lnt_skip
  s_barrier
.Lnt_skip:
  s_add_u32 s[sU], s[sU], 1
  s_branch .Lnone_tail

  ; ======================================================================== epilogue
  ; 32 f32 at vO+b.. (d 0..63 of a 64-wide half) -> bf16 -> LDS stage (row-major 128 B rows, 16-B chunks xor row) -> 4 full-line stores
  ; t: 16 dead registers (vO+t..) ; vSGW / vSGR: stage addresses ; sT3 = global offset of row 0 of this half
  .macro OUTHALF b, t
    .set q, 0
    .rept 4
      v_cvt_pk_bf16_f32 v[vO+\t+4*q+0], v[vO+\b+8*q+0], v[vO+\b+8*q+1]
      v_cvt_pk_bf16_f32 v[vO+\t+4*q+1], v[vO+\b+8*q+2], v[vO+\b+8*q+3]
      v_cvt_pk_bf16_f32 v[vO+\t+4*q+2], v[vO+\b+8*q+4], v[vO+\b+8*q+5]
      v_cvt_pk_bf16_f32 v[vO+\t+4*q+3], v[vO+\b+8*q+6], v[vO+\b+8*q+7]
      v_xor_b32 v[vT0+q], q*32, v[vSGW]
      .set q, q+1
    .endr
    .set q, 0
    .rept 4
      v_permlane32_swap_b32 v[vO+\t+4*q+0], v[vO+\t+4*q+2]
      v_permlane32_swap_b32 v[vO+\t+4*q+1], v[vO+\t+4*q+3]
      .set q, q+1
    .endr
    .set q, 0
    .rept 4
      ds_write_b128 v[vT0+q], v[vO+\t+4*q:vO+\t+4*q+3]
      .set q, q+1
    .endr
    s_add_u32 s[sT4], s[sT3], 8*4096
    s_add_u32 s[sT1], s[sT3], 16*4096
    s_add_u32 s[sT2], s[sT3], 24*4096
    s_waitcnt lgkmcnt(0)
    STAMPE 4
    ds_read_b128 v[vO+\t+0:vO+\t+3], v[vSGR]
    ds_read_b128 v[vO+\t+4:vO+\t+7], v[vSGR] offset:1024
    ds_read_b128 v[vO+\t+8:vO+\t+11], v[vSGR] offset:2048
    ds_read_b128 v[vO+\t+12:vO+\t+15], v[vSGR] offset:3072
    s_waitcnt lgkmcnt(3)
    buffer_store_dwordx4 v[vO+\t+0:vO+\t+3], v[vSO], s[sO:sO+3], s[sT3] offen
    s_waitcnt lgkmcnt(2)
    buffer_store_dwordx4 v[vO+\t+4:vO+\t+7], v[vSO], s[sO:sO+3], s[sT4] offen
    s_waitcnt lgkmcnt(1)
    buffer_store_dwordx4 v[vO+\t+8:vO+\t+11], v[vSO], s[sO:sO+3], s[sT1] offen
    s_waitcnt lgkmcnt(0)
    buffer_store_dwordx4 v[vO+\t+12:vO+\t+15], v[vSO], s[sO:sO+3], s[sT2] offen
  .endm
  .macro MERGE kb, rb                               ; kb: kept 32 regs, rb: 32 regs receiving the partner's half
    ds_read_b64 v[vT4:vT5], v[vSTP]
    .set i, 0
    .rept 8
      ds_read_b128 v[vO+\rb+4*i:vO+\rb+4*i+3], v[vXP] offset:1024*i
      .set i, i+1
    .endr
    s_waitcnt lgkmcnt(8)
    v_max_f32 v[vT6], v[vM], v[vT4]
    v_sub_f32 v[vFA], v[vM], v[vT6]
    v_sub_f32 v[vFB], v[vT4], v[vT6]
    v_mul_f32 v[vFA], s[sCE], v[vFA]
    v_mul_f32 v[vFB], s[sCE], v[vFB]
    v_exp_f32 v[vFA], v[vFA]
    v_exp_f32 v[vFB], v[vFB]
    s_nop 0
    v_mul_f32 v[vT6], v[vFA], v[vL0]
    v_fma_f32 v[vT6], v[vFB], v[vT5], v[vT6]
    v_rcp_f32 v[vT6], v[vT6]
    s_nop 0
    v_mul_f32 v[vFA], v[vFA], v[vT6]
    v_mul_f32 v[vFB], v[vFB], v[vT6]
    STAMPE 2
    .set i, 0
    .rept 16
      v_pk_mul_f32 v[vO+\kb+2*i:vO+\kb+2*i+1], v[vFA:vFA+1], v[vO+\kb+2*i:vO+\kb+2*i+1] op_sel_hi:[0,1]
      .set i, i+1
    .endr
    .set i, 0
    .rept 8
      .if i == 0
        s_waitcnt lgkmcnt(7)
      .elseif i == 1
        s_waitcnt lgkmcnt(6)
      .elseif i == 2
        s_waitcnt lgkmcnt(5)
      .elseif i == 3
        s_waitcnt lgkmcnt(4)
      .elseif i == 4
        s_waitcnt lgkmcnt(3)
      .elseif i == 5
        s_waitcnt lgkmcnt(2)
      .elseif i == 6
        s_waitcnt lgkmcnt(1)
      .else
        s_waitcnt lgkmcnt(0)
      .endif
      v_pk_fma_f32 v[vO+\kb+4*i:vO+\kb+4*i+1], v[vFB:vFB+1], v[vO+\rb+4*i:vO+\rb+4*i+1], v[vO+\kb+4*i:vO+\kb+4*i+1] op_sel_hi:[0,1,1]
      v_pk_fma_f32 v[vO+\kb+4*i+2:vO+\kb+4*i+3], v[vFB:vFB+1], v[vO+\rb+4*i+2:vO+\rb+4*i+3], v[vO+\kb+4*i+2:vO+\kb+4*i+3] op_sel_hi:[0,1,1]
      .set i, i+1
    .endr
    STAMPE 3
    OUTHALF \kb, \rb
  .endm
  .macro ROWSUM                                     ; complete row sum of this wave's keys -> vL0 (all lanes)
    s_cmp_eq_u32 s[sSAFE], 1
    s_cbranch_scc1 .Lss\@
    v_permlane16_swap_b32 v[vLS], v[vLS+1]
    s_nop 0
    v_mov_b32 v[vL0], v[vLS]
    s_nop 1
    v_permlane32_swap_b32 v[vL0], v[vLS]
    s_branch .Lsd\@
  .Lss\@:
    v_add_f32 v[vL0], v[vL0], v[vL1]
    v_mov_b32 v[vL1], v[vL0]
    s_nop 1
    v_permlane32_swap_b32 v[vL0], v[vL1]
    s_nop 0
    v_add_f32 v[vL0], v[vL0], v[vL1]
  .Lsd\@:
  .endm

.Lepi:                                              ; O complete, nothing sent yet
  s_cmp_eq_u32 s[sMB], 1
  s_cbranch_scc1 .LepiB
  s_nop 7
  s_cmp_eq_u32 s[sKH], 0
  s_cbranch_scc0 .Lsend_lo
  SENDB 2
  SENDB 3
  s_branch .Lepi_sent
.Lsend_lo:
  SENDB 0
  SENDB 1
.Lepi_sent:
  STAMP 7
  v_mov_b32 v[vT7], 0x71800000
  s_lshl_b32 s[sT3], s[sQR], 12
  s_add_u32 s[sT3], s[sT3], s[sHOFF]
  ROWSUM
  v_mov_b32 v[vT4], v[vM]
  v_mov_b32 v[vT5], v[vL0]
  s_lshl_b32 s[sT1], s[sKH], 7
  s_add_u32 s[sT3], s[sT3], s[sT1]
  ds_write_b64 v[vSTA], v[vT4:vT5]
  s_cmp_eq_u32 s[sKH], 0
  s_waitcnt lgkmcnt(0)
  s_barrier
  STAMPE 1
  s_cbranch_scc0 .Lmerge1
  MERGE 0, 32
  s_branch .Lnext
.Lmerge1:
  MERGE 32, 0
  s_branch .Lnext
.LepiB:
  STAMP 7
  v_mov_b32 v[vT7], 0x71800000
  s_lshl_b32 s[sT3], s[sQR], 12
  s_add_u32 s[sT3], s[sT3], s[sHOFF]
  ROWSUM
  s_nop 0
  v_rcp_f32 v[vFA], v[vL0]
  s_waitcnt vmcnt(0)
  s_barrier
  .set i, 0
  .rept 32
    v_pk_mul_f32 v[vO+2*i:vO+2*i+1], v[vFA:vFA+1], v[vO+2*i:vO+2*i+1] op_sel_hi:[0,1]
    .set i, i+1
  .endr
  OUTHALF 0, vQ-vO
  v_add_u32 v[vSGW], 4096, v[vSGW]
  v_add_u32 v[vSGR], 4096, v[vSGR]
  s_add_u32 s[sT3], s[sT3], 128
  s_nop 0
  OUTHALF 32, vQ-vO+16
.Lnext:
  STAMP 8
  ; overflow check of the FAST path: any row sum that is not a normal-sized number -> redo this item with the online max
  v_cmp_nlt_f32 vcc, v[vL0], v[vT7]
  s_lshl_b32 s[sT1], s[sW], 2
  s_add_u32 s[sT1], s[sT1], LX+8192
  s_cmp_lg_u64 vcc, 0
  s_cselect_b32 s[sT2], 1, 0
  v_mov_b32 v[vT0], s[sT1]
  v_mov_b32 v[vT1], s[sT2]
  v_and_b32 v[vT2], 7, v[vLANE]
  v_lshlrev_b32 v[vT2], 2, v[vT2]
  v_add_u32 v[vT2], LX+8192, v[vT2]
  ds_write_b32 v[vT0], v[vT1]
  s_waitcnt lgkmcnt(0)
  s_barrier
  ds_read_b32 v[vT1], v[vT2]
  s_waitcnt lgkmcnt(0)
  v_cmp_ne_u32 vcc, 0, v[vT1]
  s_cmp_eq_u32 s[sSAFE], 1
  s_cbranch_scc1 .Lsafe_done
  s_cbranch_vccz .Lsafe_done
  s_mov_b32 s[sSAFE], 1
  s_barrier
  s_branch .Litem
.Lsafe_done:
  s_mov_b32 s[sSAFE], 0
  s_add_u32 s[sR], s[sR], 1
  s_lshl_b32 s[sT1], s[sR], 5
  s_cmp_ge_u32 s[sT1], s[sTOT]
  s_cbranch_scc1 .Ldone
  s_barrier
  s_branch .Litem
.Ldone:
  .if DIAG
    s_waitcnt vmcnt(0)
    STAMP 9
    s_memrealtime s[sA:sA+1]
    s_load_dwordx2 s[sB:sB+1], %0, 96
    s_lshl_b32 s[sT1], %1, 3
    s_add_u32 s[sT1], s[sT1], s[sW]
    s_lshl_b32 s[sT1], s[sT1], 8
    s_waitcnt lgkmcnt(0)
    s_add_u32 s[sB], s[sB], s[sT1]
    s_addc_u32 s[sB+1], s[sB+1], 0
    v_mov_b32 v[vT6], 0
    s_mov_b64 exec, 1
    .set i, 0
    .rept 10
      v_mov_b32 v[vT0], s[sST+2*i]
      v_mov_b32 v[vT1], s[sST+2*i+1]
      global_store_dwordx2 v[vT6], v[vT0:vT1], s[sB:sB+1] offset:8*i
      s_nop 1
      .set i, i+1
    .endr
    v_mov_b32 v[vT0], s[sRT]
    v_mov_b32 v[vT1], s[sRT+1]
    global_store_dwordx2 v[vT6], v[vT0:vT1], s[sB:sB+1] offset:80
    s_nop 1
    v_mov_b32 v[vT0], s[sA]
    v_mov_b32 v[vT1], s[sA+1]
    global_store_dwordx2 v[vT6], v[vT0:vT1], s[sB:sB+1] offset:88
    s_nop 1
  .endif
  s_endpgm
.Lfallback:
  s_waitcnt vmcnt(0) lgkmcnt(0)
  .purgem STAMP
  .purgem STAMPE
  .purgem PREFETCH
  .purgem DK1
  .purgem QK0
  .purgem P2
  .purgem DALL
  .purgem BAR4
  .purgem BAR0
  .purgem BARN
  .purgem FULLS
  .purgem LASTS
  .purgem FULLF
  .purgem LASTF
  .purgem DMAK1
  .purgem DMAK2
  .purgem DMAV1
  .purgem DMAV2
  .purgem DK2
  .purgem DV1
  .purgem DV2
  .purgem KREAD
  .purgem KADV
  .purgem QKM
  .purgem VREAD
  .purgem PVM
  .purgem MASK
  .purgem LSUM
  .purgem FMAE
  .purgem XPE
  .purgem ROWMAX1
  .purgem ROWMAX2
  .purgem EXPE
  .purgem SUMPK
  .purgem VADDR
  .purgem RESCALE
  .purgem ADDE
  .purgem CVTE
  .purgem OUTHALF
  .purgem MERGE
  .purgem ROWSUM
  .purgem PVJ
  .purgem SENDB
  .purgem LASTV
  )" :: "s"(__builtin_amdgcn_kernarg_segment_ptr()), "s"(blockIdx.x), "v"(threadIdx.x), "i"(NIT)
     : "memory", "vcc", "m0",
       "s8","s9","s10","s11","s12","s13","s14","s15","s16","s17","s18","s19","s20","s21","s22","s23","s24","s25","s26","s27","s28","s29","s30","s31",
       "s33","s34","s35","s36","s37","s38","s39","s40","s41","s42","s43","s44","s45","s46","s47","s48","s49","s50","s51","s52","s53","s54","s55",
       "s56","s57","s58","s59","s60","s61","s62","s63","s64","s65","s66","s67","s68","s69","s70","s71","s72","s73","s74","s75","s76","s77","s78","s79",
       "s80","s81","s82","s83","s84","s85","s86","s87","s88","s89","s90","s91","s92","s93","s94","s95","s96","s97","s98","s99",
       "v1","v2","v3","v4","v5","v6","v7","v8","v9","v10","v11","v12","v13","v14","v15","v16","v17","v18","v19","v20","v21","v22","v23","v24","v25","v26","v27","v28","v29","v30","v31",
       "v32","v33","v34","v35","v36","v37","v38","v39","v40","v41","v42","v43","v44","v45","v46","v47","v48","v49","v50","v51","v52","v53","v54","v55","v56","v57","v58","v59","v60","v61","v62","v63",
       "v64","v65","v66","v67","v68","v69","v70","v71","v72","v73","v74","v75","v76","v77","v78","v79","v80","v81","v82","v83","v84","v85","v86","v87","v88","v89","v90","v91","v92","v93","v94","v95",
       "v96","v97","v98","v99","v100","v101","v102","v103","v104","v105","v106","v107","v108","v109","v110","v111","v112","v113","v114","v115","v116","v117","v118","v119","v120","v121","v122","v123","v124","v125","v126","v127",
       "v128","v129","v130","v131","v132","v133","v134","v135","v136","v137","v138","v139","v140","v141","v142","v143","v144","v145","v146","v147","v148","v149","v150","v151","v152","v153","v154","v155","v156","v157","v158","v159",
       "v160","v161","v162","v163","v164","v165","v166","v167","v168","v169","v170","v171","v172","v173","v174","v175","v176","v177","v178","v179","v180","v181","v182","v183","v184","v185","v186","v187","v188","v189","v190","v191",
       "v192","v193","v194","v195","v196","v197","v198","v199","v200","v201","v202","v203","v204","v205","v206","v207","v208","v209","v210","v211","v212","v213","v214","v215","v216","v217","v218","v219","v220","v221","v222","v223",
       "v224","v225","v226","v227","v228","v229","v230","v231","v232","v233","v234","v235","v236","v237","v238","v239","v240","v241","v242","v243","v244","v245","v246","v247","v248","v249","v250","v251","v252","v253","v254","v255");
  generic_path((const unsigned short*)Q, (const unsigned short*)K, (const unsigned short*)V, (const int*)seg, (unsigned short*)O, T, lds);
}

__global__ __launch_bounds__(512, 2)
void ragged_attn_generic(const void* Q, const void* K, const void* V, const void* seg, void* O, int T, int pad) {
  __shared__ __attribute__((aligned(16))) char lds[LDS_BYTES];
  generic_path((const unsigned short*)Q, (const unsigned short*)K, (const unsigned short*)V, (const int*)seg, (unsigned short*)O, T, lds);
}

extern "C" void kernel_launch(void* const* d_in, const int* in_sizes, int n_in, void* d_out, int out_size, void* d_ws, size_t ws_size,
                              hipStream_t stream) {
  const int T = in_sizes[3];
  const unsigned nb = (unsigned)T * ROWB, F = 0x00020000u;
  if (T > 2048 && T <= 2560) {
    ragged_attn_kernel<5><<<256, 512, 0, stream>>>(d_in[0], nb, F, d_in[1], nb, F, d_in[2], nb, F, d_in[3], (unsigned)T * 4u, F, d_out, nb, F,
                                                   T, T - 1 - 512 * 4, ((T + 31) / 32) * (int)ROWB, 0, d_ws);
  } else {
    ragged_attn_generic<<<256, 512, 0, stream>>>(d_in[0], d_in[1], d_in[2], d_in[3], d_out, T, 0);
  }
}
```

```cpp
#include <hip/hip_runtime.h>
#include <stdint.h>

typedef short    bf16x8 __attribute__((ext_vector_type(8)));
typedef short    s16x4  __attribute__((ext_vector_type(4)));
typedef float    f32x16 __attribute__((ext_vector_type(16)));
typedef float    f32x4  __attribute__((ext_vector_type(4)));
typedef float    f32x2  __attribute__((ext_vector_type(2)));
typedef __bf16   bf2    __attribute__((ext_vector_type(2)));
typedef unsigned u32x4  __attribute__((ext_vector_type(4)));
typedef unsigned long long u64;
#define LDSP __attribute__((address_space(3)))
typedef LDSP const char* lds_cptr;
typedef LDSP char* lds_ptr;
typedef short v4i16_t __attribute__((ext_vector_type(4)));
#define DI __device__ __forceinline__
#define MFMA(a, b, c) __builtin_amdgcn_mfma_f32_32x32x16_bf16((a), (b), (c), 0, 0, 0)

constexpr int NH = 16, HD = 128;
constexpr unsigned ROWB = NH * HD * 2;
constexpr unsigned LDS_BYTES = 163840;
constexpr float CEXP = 0.08838834764831845f * 1.4426950408889634f;
constexpr float THR_RAW = 8.0f / CEXP;
constexpr float MINIT = -1.0e30f;

DI unsigned rfl(unsigned x) { return __builtin_amdgcn_readfirstlane(x); }
DI unsigned cvtpk(float lo, float hi) { f32x2 v = {lo, hi}; bf2 b = __builtin_convertvector(v, bf2); return __builtin_bit_cast(unsigned, b); }
DI float swap_max(float x) { auto r = __builtin_amdgcn_permlane32_swap(__float_as_uint(x), __float_as_uint(x), false, false); return __builtin_fmaxf(__uint_as_float(r[0]), __uint_as_float(r[1])); }
DI float swap_add(float x) { auto r = __builtin_amdgcn_permlane32_swap(__float_as_uint(x), __float_as_uint(x), false, false); return __uint_as_float(r[0]) + __uint_as_float(r[1]); }
DI s16x4 vtr(lds_cptr p) { return __builtin_bit_cast(s16x4, __builtin_amdgcn_ds_read_tr16_b64_v4i16((LDSP v4i16_t*)p)); }
#define MX3(a, b, c) __builtin_fmaxf(__builtin_fmaxf((a), (b)), (c))

DI void dma16(__amdgpu_buffer_rsrc_t srd, unsigned voff, unsigned soff, unsigned m0v) {
  unsigned keep;
  asm volatile("s_nop 4\n\ts_mov_b32 %0, m0\n\ts_mov_b32 m0, %1\n\ts_nop 0\n\tbuffer_load_dwordx4 %2, %3, %4 offen lds\n\ts_mov_b32 m0, %0"
               : "=&s"(keep) : "s"(m0v), "v"(voff), "s"(srd), "s"(soff) : "memory");
}

__device__ __forceinline__ void generic_path(const unsigned short* __restrict__ Q, const unsigned short* __restrict__ K, const unsigned short* __restrict__ V,
                                          const int* __restrict__ seg, unsigned short* __restrict__ O, int T, char* lds) {
  constexpr unsigned SLOT = 32768, VOFF = 16384, XREG = 4 * SLOT;
  const int tid = threadIdx.x, lane = tid & 63, r32 = lane & 31, hh = lane >> 5;
  const int w = (int)rfl((unsigned)tid >> 6);
  const int grp = blockIdx.x & 7, jj = blockIdx.x >> 3, NJ = 32;
  const unsigned ldsb = (unsigned)(uintptr_t)lds;
  const lds_ptr L = (lds_ptr)lds;
  const int total_items = 2 * ((T + 127) >> 7);
  const __amdgpu_buffer_rsrc_t srdQ = __builtin_amdgcn_make_buffer_rsrc((void*)Q, (short)0, T * (int)ROWB, 0x00020000);
  const __amdgpu_buffer_rsrc_t srdK = __builtin_amdgcn_make_buffer_rsrc((void*)K, (short)0, T * (int)ROWB, 0x00020000);
  const __amdgpu_buffer_rsrc_t srdV = __builtin_amdgcn_make_buffer_rsrc((void*)V, (short)0, T * (int)ROWB, 0x00020000);
  const unsigned prow = (lane >> 2) & 7, pcb = lane >> 5, pc = lane & 3;
  const unsigned voffK = prow * ROWB + pcb * 64 + ((pc ^ ((2u * (unsigned)w + (prow >> 2)) & 3u)) * 16);
  const unsigned voffV = prow * ROWB + pcb * 64 + pc * 16;
  const unsigned kb0 = 2048u * (r32 >> 3) + 64u * (r32 & 7) + 16u * ((unsigned)hh ^ ((r32 >> 2) & 3u));
  const unsigned vb0 = ((unsigned)hh * 4 + ((lane & 15) >> 2)) * 64 + ((lane >> 4) & 1) * 32 + (lane & 3) * 8;
  for (int idx = jj; idx < total_items; idx += NJ) {
    __syncthreads();
    const int head = grp + 8 * (idx & 1);
    const int q0 = (idx >> 1) * 128, qend = min(q0 + 128, T);
    LDSP int* red = (LDSP int*)L;
    if (tid == 0) { red[0] = 0x7fffffff; red[1] = (int)0x80000000; red[2] = 0x7fffffff; red[3] = -1; }
    __syncthreads();
    if (tid < 128 && q0 + tid < qend) { const int id = seg[q0 + tid];
      __hip_atomic_fetch_min((LDSP int*)&red[0], id, __ATOMIC_RELAXED, __HIP_MEMORY_SCOPE_WORKGROUP);
      __hip_atomic_fetch_max((LDSP int*)&red[1], id, __ATOMIC_RELAXED, __HIP_MEMORY_SCOPE_WORKGROUP); }
    __syncthreads();
    const int idlo = red[0], idhi = red[1];
    int lo = 0x7fffffff, hi = -1;
    for (int i = tid; i < T; i += 512) { const int id = seg[i]; if (id >= idlo && id <= idhi) { lo = min(lo, i); hi = max(hi, i); } }
    __hip_atomic_fetch_min((LDSP int*)&red[2], lo, __ATOMIC_RELAXED, __HIP_MEMORY_SCOPE_WORKGROUP);
    __hip_atomic_fetch_max((LDSP int*)&red[3], hi, __ATOMIC_RELAXED, __HIP_MEMORY_SCOPE_WORKGROUP);
    __syncthreads();
    const int k0 = (int)rfl((unsigned)red[2]), kend = (int)rfl((unsigned)red[3]) + 1;
    __syncthreads();
    const int nk = (kend - k0 + 63) >> 6;
    const unsigned hoff = (unsigned)head * 256u;
#pragma unroll
    for (int i = 0; i < 2; ++i) {
      const unsigned m = (unsigned)w + 8u * i;
      const unsigned so = (unsigned)(q0 + 8 * (int)m) * ROWB + hoff;
      dma16(srdQ, voffK, so, ldsb + XREG + m * 2048u);
      dma16(srdQ, voffK, so + 128u, ldsb + XREG + m * 2048u + 1024u);
    }
    const int qb = w & 3, sub = w >> 2;
    const int qrow = q0 + 32 * qb + r32;
    const int segq = seg[qrow < T ? qrow : T - 1];
    bf16x8 qf[8];
    f32x16 o[4];
#pragma unroll
    for (int d = 0; d < 4; ++d) o[d] = f32x16{};
    float m_run = MINIT, l_run = 0.f;
    for (int t = 0; t < nk; ++t) {
      __syncthreads();
      {
        const unsigned so = (unsigned)(k0 + 64 * t + 8 * w) * ROWB + hoff;
        const unsigned d = ldsb + (unsigned)w * 2048u;
        dma16(srdK, voffK, so, d); dma16(srdK, voffK, so + 128u, d + 1024u);
        dma16(srdV, voffV, so, d + VOFF); dma16(srdV, voffV, so + 128u, d + VOFF + 1024u);
      }
      asm volatile("s_waitcnt vmcnt(0)" ::: "memory");
      __syncthreads();
      if (t == 0) {
#pragma unroll
        for (int s = 0; s < 8; ++s)
          qf[s] = *(const LDSP bf16x8*)(L + XREG + (unsigned)qb * 8192u + ((kb0 ^ ((s & 1) * 32u)) + 512u * (s >> 1)));
      }
      const int kbase = k0 + 64 * t + 32 * sub;
      const lds_cptr kp = (lds_cptr)L + (unsigned)sub * 8192u;
      f32x16 sc = f32x16{};
#pragma unroll
      for (int s = 0; s < 8; ++s) {
        const bf16x8 kf = *(const LDSP bf16x8*)(kp + ((kb0 ^ ((s & 1) * 32u)) + 512u * (s >> 1)));
        sc = MFMA(kf, qf[s], sc);
      }
#pragma unroll
      for (int r = 0; r < 16; ++r) {
        const int kk = kbase + (r & 3) + 8 * (r >> 2) + 4 * hh;
        const int id = seg[kk < T ? kk : T - 1];
        sc[r] = (kk < kend && id == segq) ? sc[r] : -__builtin_inff();
      }
      float a = MX3(sc[0], sc[1], sc[2]), b = MX3(sc[3], sc[4], sc[5]);
      a = MX3(a, sc[6], sc[7]); b = MX3(b, sc[8], sc[9]); a = MX3(a, sc[10], sc[11]); b = MX3(b, sc[12], sc[13]);
      a = MX3(a, sc[14], sc[15]);
      const float pm = swap_max(__builtin_fmaxf(a, b));
      const float mn = __builtin_fmaxf(m_run, pm);
      const float al = __builtin_amdgcn_exp2f((m_run - mn) * CEXP);
      m_run = mn; l_run *= al;
#pragma unroll
      for (int d = 0; d < 4; ++d)
#pragma unroll
        for (int r = 0; r < 16; ++r) o[d][r] *= al;
      const float nmc = -m_run * CEXP;
      float ps = 0.f;
#pragma unroll
      for (int r = 0; r < 16; ++r) { sc[r] = __builtin_amdgcn_exp2f(__builtin_fmaf(sc[r], CEXP, nmc)); ps += sc[r]; }
      l_run += ps;
      u32x4 pw0, pw1;
#pragma unroll
      for (int e = 0; e < 4; ++e) { pw0[e] = cvtpk(sc[2 * e], sc[2 * e + 1]); pw1[e] = cvtpk(sc[8 + 2 * e], sc[8 + 2 * e + 1]); }
      const bf16x8 p0 = __builtin_bit_cast(bf16x8, pw0), p1 = __builtin_bit_cast(bf16x8, pw1);
      const lds_cptr vp = kp + VOFF + vb0;
#pragma unroll
      for (int d = 0; d < 4; ++d) {
        const s16x4 a0 = vtr(vp + d * 512), a1 = vtr(vp + d * 512 + 2048), b0 = vtr(vp + d * 512 + 4096), b1 = vtr(vp + d * 512 + 6144);
        const bf16x8 va = __builtin_shufflevector(a0, a1, 0, 1, 2, 3, 4, 5, 6, 7), vb = __builtin_shufflevector(b0, b1, 0, 1, 2, 3, 4, 5, 6, 7);
        o[d] = MFMA(va, p0, o[d]);
        o[d] = MFMA(vb, p1, o[d]);
      }
    }
    l_run = swap_add(l_run);
    unsigned short* orow = O + (size_t)(qrow < T ? qrow : 0) * (NH * HD) + head * HD + 8 * hh;
    const bool row_ok = qrow < qend;
    const int kh = sub;
    __syncthreads();
    const unsigned xa = SLOT, xb = 2 * SLOT, xs = 3 * SLOT;
    const unsigned mine = (kh ? xb : xa) + (unsigned)qb * 8192u + (unsigned)lane * 16u;
    const unsigned theirs = (kh ? xa : xb) + (unsigned)qb * 8192u + (unsigned)lane * 16u;
#pragma unroll
    for (int dd = 0; dd < 2; ++dd)
#pragma unroll
      for (int g = 0; g < 4; ++g) {
        f32x4 v;
#pragma unroll
        for (int e = 0; e < 4; ++e) v[e] = kh ? o[dd][4 * g + e] : o[2 + dd][4 * g + e];
        *(LDSP f32x4*)(L + mine + (unsigned)(dd * 4 + g) * 1024u) = v;
      }
    if (hh == 0) { f32x2 s2 = {m_run, l_run}; *(LDSP f32x2*)(L + xs + (unsigned)w * 256u + (unsigned)r32 * 8u) = s2; }
    __syncthreads();
    const f32x2 o2 = *(const LDSP f32x2*)(L + xs + (unsigned)(w ^ 4) * 256u + (unsigned)r32 * 8u);
    const float mn = __builtin_fmaxf(m_run, o2[0]);
    const float wa = __builtin_amdgcn_exp2f((m_run - mn) * CEXP), wb = __builtin_amdgcn_exp2f((o2[0] - mn) * CEXP);
    const float inv = 1.0f / (wa * l_run + wb * o2[1]);
    const float fa = wa * inv, fb = wb * inv;
#pragma unroll
    for (int dd = 0; dd < 2; ++dd) {
      float r[16];
#pragma unroll
      for (int g = 0; g < 4; ++g) {
        const f32x4 v = *(const LDSP f32x4*)(L + theirs + (unsigned)(dd * 4 + g) * 1024u);
#pragma unroll
        for (int e = 0; e < 4; ++e) r[4 * g + e] = fa * (kh ? o[2 + dd][4 * g + e] : o[dd][4 * g + e]) + fb * v[e];
      }
#pragma unroll
      for (int g = 0; g < 4; g += 2) {
        unsigned ax = cvtpk(r[4 * g + 0], r[4 * g + 1]), ay = cvtpk(r[4 * g + 2], r[4 * g + 3]);
        unsigned bx = cvtpk(r[4 * g + 4], r[4 * g + 5]), by = cvtpk(r[4 * g + 6], r[4 * g + 7]);
        auto rx = __builtin_amdgcn_permlane32_swap(ax, bx, false, false); auto ry = __builtin_amdgcn_permlane32_swap(ay, by, false, false);
        const u32x4 v = {rx[0], ry[0], rx[1], ry[1]};
        if (row_ok) *(u32x4*)(orow + 64 * kh + 32 * dd + 8 * g) = v;
      }
    }
  }
}

#define DIAG 0
#define PFQ 1
#define PFKV 0
#define ABL 0
#define PFAT 1
#define DBGW 0
#define TOUCH 0
#define PF1 3
#define PF2 5
#define WQ 1
#define PRIO 0
#define STR2(x) #x
#define STR(x) STR2(x)

template <int NIT>
__global__ __launch_bounds__(512, 2)
void ragged_attn_kernel(const void* Q, unsigned nQ, unsigned fQ, const void* K, unsigned nK, unsigned fK, const void* V, unsigned nV, unsigned fV,
                        const void* seg, unsigned nS, unsigned fS, void* O, unsigned nO, unsigned fO, int T, int lastc, int pfb, int pad, void* dbg) {
  __shared__ __attribute__((aligned(16))) char lds[LDS_BYTES];
  asm volatile(R"(
  .set NIT, %c3
  .set DIAG, )" STR(DIAG) R"(
  .set PFQ, )" STR(PFQ) R"(
  .set PFKV, )" STR(PFKV) R"(
  .set ABL, )" STR(ABL) R"(
  .set PFAT, )" STR(PFAT) R"(
  .set DBGW, )" STR(DBGW) R"(
  .set TOUCH, )" STR(TOUCH) R"(
  .set PRIO, )" STR(PRIO) R"(
  .set WQ, )" STR(WQ) R"(
  .set PF1, )" STR(PF1) R"(
  .set PF2, )" STR(PF2) R"(
  ; ---------------- SGPRs
  .set sQ, 8
  .set sK, 12
  .set sV, 16
  .set sS, 20
  .set sO, 24
  .set sT, 28
  .set sLASTC, 29
  .set sPFB, 30
  .set sF, 30
  .set sTM16, 31
  .set sG, 34
  .set sR, 35
  .set sNBIG, 36
  .set sTOT, 37
  .set sHOFF, 38
  .set sQ0, 39
  .set sQEND, 40
  .set sK0, 41
  .set sNK, 42
  .set sLEN, 43
  .set sMB, 44
  .set sNS, 45
  .set sU, 46
  .set sNU, 47
  .set sKOFF, 48
  .set sKSTEP, 49
  .set sVOFF, 50
  .set sKSO, 51
  .set sVSO, 52
  .set sKDST, 53
  .set sVDST, 54
  .set sMAXSO, 55
  .set sND, 56
  .set sMSTEP, 57
  .set sPEND, 58
  .set sW, 59
  .set sA, 60
  .set sB, 62
  .set sC, 64
  .set sT1, 66
  .set sT2, 67
  .set sT3, 68
  .set sT4, 69
  .set sCE, 70
  .set sTHR, 71
  .set sWD, 72
  .set sQB, 73
  .set sKH, 74
  .set sLIM, 75
  .set sJ, 76
  .set sQR, 77
  .set sRT, 78
  .set sLIVE, 30
  .set sSAFE, 64
  .set sNF, 65
  .set sNDF, 32+1
  .set sST, 80
  ; ---------------- VGPRs
  .set vLANE, 1
  .set vOK, 2
  .set vOK2, 3
  .set vOV, 4
  .set vOV2, 5
  .set vKB0, 6
  .set vKB1, 7
  .set vVB, 8
  .set vSTART, 9
  .set vLEN, 10
  .set vINCL, 11
  .set vEXCL, 12
  .set vM, 13
  .set vNMC, 14
  .set vL0, 15
  .set vL1, 16
  .set vKA0, 17
  .set vKA1, 18
  .set vVA, 19
  .set vAL, 20
  .set vSO, 21
  .set vHH4, 22
  .set vSW, 23
  .set vT0, 24
  .set vT1, 25
  .set vT2, 26
  .set vT3, 27
  .set vT4, 28
  .set vT5, 29
  .set vT6, 30
  .set vT7, 31
  .set vP, 32
  .set vX, 40
  .set vSR, 56
  .set vLS, 40
  .set vONE, 60
  .set vXO, 44
  .set vXP, 45
  .set vSTA, 46
  .set vSTP, 47
  .set vSGW, 48
  .set vSGR, 49
  .set vFA, 50
  .set vFB, 52
  .set vO, 64
  .set vQ, 128
  .set vSA, 160
  .set vSB, 176
  .set vK, 192
  .set vV, 224
  ; ---------------- LDS map
  .set LK, 0
  .set LV, 65536
  .set LX, 131072
  .set LQ2, 49152
  .set LQ3, 114688
  .set LSCR, 98304

  .macro STAMP i
    .if DIAG && ((DIAG == 1) || (\i == 0) || (\i >= 5))
      s_memtime s[sST+2*\i:sST+2*\i+1]
      s_waitcnt lgkmcnt(0)
    .endif
  .endm
  .macro STAMPE i
    .if DIAG == 2
      s_memtime s[sST+2*\i:sST+2*\i+1]
      s_waitcnt lgkmcnt(0)
    .endif
  .endm

  ; ======================================================================== entry
  s_load_dwordx16 s[8:23], %0, 0
  s_load_dwordx8 s[24:31], %0, 64
  .if DIAG
    s_memrealtime s[sRT:sRT+1]
  .endif
  STAMP 0
  s_getpc_b64 s[sA:sA+1]
.Lpc:
  v_lshlrev_b32 v[vT1], 6, %2
  v_min_u32 v[vT1], .Lfallback-.Lpc, v[vT1]
  global_load_dword v[vV+31], v[vT1], s[sA:sA+1]
  v_and_b32 v[vLANE], 63, %2
  v_readfirstlane_b32 s[sW], %2
  v_lshlrev_b32 v[vT0], 2, %2                     ; tid*4
  s_and_b32 s[sG], %1, 7
  s_lshr_b32 s[sJ], %1, 3
  s_lshr_b32 s[sW], s[sW], 6
  ; prefetch offset: (tid>>5)*4096 + ((tid>>4)&1)*2048 + ((tid>>3)&1)*128 + (tid&7)*16 + g*256
  v_lshrrev_b32 v[vT6], 5, %2
  v_lshlrev_b32 v[vT6], 12, v[vT6]
  v_bfe_u32 v[vT7], %2, 4, 1
  v_lshl_add_u32 v[vT6], v[vT7], 11, v[vT6]
  v_bfe_u32 v[vT7], %2, 3, 1
  v_lshl_add_u32 v[vT6], v[vT7], 7, v[vT6]
  v_and_b32 v[vT7], 7, %2
  v_lshl_add_u32 v[vT6], v[vT7], 4, v[vT6]
  s_lshl_b32 s[sT3], s[sG], 8
  v_add_u32 v[vT6], s[sT3], v[vT6]
  ; DMA lane offsets
  v_bfe_u32 v[vT1], v[vLANE], 2, 3                ; prow
  v_lshrrev_b32 v[vT2], 5, v[vLANE]               ; hh / pcb
  v_and_b32 v[vT3], 3, v[vLANE]                   ; pc
  v_lshlrev_b32 v[vT4], 12, v[vT1]
  v_lshl_add_u32 v[vT4], v[vT2], 6, v[vT4]        ; prow*4096 + pcb*64
  v_lshl_add_u32 v[vOV], v[vT3], 4, v[vT4]
  v_bfe_u32 v[vT5], v[vLANE], 4, 1
  s_and_b32 s[sT4], s[sW], 1
  s_lshl_b32 s[sT4], s[sT4], 1
  v_or_b32 v[vT5], s[sT4], v[vT5]                 ; x
  v_xor_b32 v[vT5], v[vT5], v[vT3]
  v_lshl_add_u32 v[vOK], v[vT5], 4, v[vT4]
  s_waitcnt lgkmcnt(0)
  STAMP 1
  ; ======================================================================== S1: segment ends per 64-token chunk
  s_lshl_b32 s[sT2], s[sT], 2
  s_sub_u32 s[sT2], s[sT2], 8                     ; (T-2)*4
  v_add_u32 v[vT7], 2048*(NIT-1), v[vT0]
  v_min_u32 v[vT7], s[sT2], v[vT7]                ; last chunk: clamped (no out-of-range lanes)
  .set i, 0
  .rept NIT
    .if i == NIT-1
      buffer_load_dwordx2 v[vX+2*i:vX+2*i+1], v[vT7], s[sS:sS+3], 0 offen
    .elseif i < 2
      buffer_load_dwordx2 v[vX+2*i:vX+2*i+1], v[vT0], s[sS:sS+3], 0 offen offset:2048*i
    .else
      s_movk_i32 s[sT1], 2048*i
      buffer_load_dwordx2 v[vX+2*i:vX+2*i+1], v[vT0], s[sS:sS+3], s[sT1] offen
    .endif
    .set i, i+1
  .endr
  .if TOUCH
    v_lshlrev_b32 v[vV+29], 20, v[vLANE]            ; 1 MiB stride
    s_sub_u32 s[sT3], s[sK+2], 4
    v_min_u32 v[vV+29], s[sT3], v[vV+29]
    s_mov_b64 exec, 0x3ff
    buffer_load_dword v[vV+20], v[vV+29], s[sK:sK+3], 0 offen
    buffer_load_dword v[vV+21], v[vV+29], s[sV:sV+3], 0 offen
    buffer_load_dword v[vV+22], v[vV+29], s[sQ:sQ+3], 0 offen
    buffer_load_dword v[vV+23], v[vV+29], s[sO:sO+3], 0 offen
    s_mov_b64 exec, -1
  .endif
  s_sub_u32 s[sTM16], s[sT], 16
  s_lshl_b32 s[sTM16], s[sTM16], 12               ; (T-16)*4096
  .macro PREFETCH lo, hi                            ; Q prefetch instructions lo..hi-1 (16 rows each)
    .set n, \lo
    .rept \hi-\lo
      s_mul_i32 s[sT3], s[sJ], s[sPFB]
      s_add_u32 s[sT3], s[sT3], n*16*4096
      s_min_u32 s[sT3], s[sT3], s[sTM16]
      buffer_load_dwordx4 v[vV+4*n:vV+4*n+3], v[vV+28], s[sQ:sQ+3], s[sT3] offen
      .set n, n+1
    .endr
  .endm
  v_mov_b32 v[vV+28], v[vT6]
  ; remaining lane constants (in the shadow of the loads)
  v_add_u32 v[vOK2], 128, v[vOK]
  v_add_u32 v[vOV2], 128, v[vOV]
  v_and_b32 v[vT1], 31, v[vLANE]                  ; r32
  v_lshrrev_b32 v[vT3], 3, v[vT1]
  v_lshlrev_b32 v[vT3], 11, v[vT3]
  v_and_b32 v[vT4], 7, v[vT1]
  v_lshl_add_u32 v[vT3], v[vT4], 6, v[vT3]
  v_bfe_u32 v[vT4], v[vT1], 2, 2
  v_xor_b32 v[vT4], v[vT4], v[vT2]
  v_lshl_add_u32 v[vKB0], v[vT4], 4, v[vT3]
  v_xor_b32 v[vKB1], 32, v[vKB0]
  v_bfe_u32 v[vT3], v[vLANE], 2, 2
  v_lshl_add_u32 v[vT3], v[vT2], 2, v[vT3]
  v_lshlrev_b32 v[vT3], 6, v[vT3]
  v_bfe_u32 v[vT4], v[vLANE], 4, 1
  v_lshl_add_u32 v[vT3], v[vT4], 5, v[vT3]
  v_and_b32 v[vT4], 3, v[vLANE]
  v_lshl_add_u32 v[vVB], v[vT4], 3, v[vT3]
  v_add_u32 v[vVB], LV, v[vVB]
  v_lshlrev_b32 v[vHH4], 2, v[vT2]
  ; output staging: write address  r32*128 + ((2*? + hh) ^ (r32&7))*16 handled per block: base = r32*128, xor term below
  v_and_b32 v[vT3], 7, v[vT1]
  v_xor_b32 v[vT3], v[vT3], v[vT2]                ; (r32&7) ^ hh
  v_lshlrev_b32 v[vT3], 4, v[vT3]
  v_lshl_add_u32 v[vSW], v[vT1], 7, v[vT3]        ; write addr for chunk pair 0 (chunk = hh); chunk pair p: xor with p*32
  ; read address: row = lane>>3, chunk = (lane&7) ^ (row&7) ; rows + 8i -> + 1024 i
  v_lshrrev_b32 v[vT3], 3, v[vLANE]
  v_and_b32 v[vT4], 7, v[vLANE]
  v_xor_b32 v[vT5], v[vT4], v[vT3]
  v_lshlrev_b32 v[vT5], 4, v[vT5]
  v_lshl_add_u32 v[vSR], v[vT3], 7, v[vT5]
  ; global store lane offset: (lane>>3)*4096 + (lane&7)*16
  v_lshlrev_b32 v[vT4], 4, v[vT4]
  v_lshl_add_u32 v[vSO], v[vT3], 12, v[vT4]
  ; row-sum MFMA A operand: row (lane&15) 0 takes k-groups 0,2 ; row 1 takes k-groups 1,3
  v_and_b32 v[vT3], 15, v[vLANE]
  v_bfe_u32 v[vT4], v[vLANE], 4, 1
  v_cmp_eq_u32 vcc, v[vT3], v[vT4]
  v_mov_b32 v[vT5], 0x3f803f80
  s_nop 0
  v_cndmask_b32 v[vONE], 0, v[vT5], vcc
  v_cndmask_b32 v[vONE+1], 0, v[vT5], vcc
  v_cndmask_b32 v[vONE+2], 0, v[vT5], vcc
  v_cndmask_b32 v[vONE+3], 0, v[vT5], vcc
  s_mov_b32 s[sCE], 0x3e0293ee
  s_mov_b32 s[sTHR], 0x427af232
  s_lshl_b32 s[sWD], s[sW], 11                    ; w*2048
  s_mov_b64 s[sC:sC+1], 0
  v_mov_b32 v[vT4], 0
  v_mov_b32 v[vT5], 0
  v_lshlrev_b32 v[vT7], 7, v[vLANE]               ; LDS address of chunk (w + 8*lane): 16 B each
  s_lshl_b32 s[sT1], s[sW], 4
  s_add_u32 s[sT1], s[sT1], LSCR
  v_add_u32 v[vT7], s[sT1], v[vT7]
  PREFETCH 0, PF1
  .set i, 0
  .rept NIT
    s_waitcnt vmcnt(NIT-1-i+PF1+4*TOUCH)
    v_cmp_ne_u32 s[sA:sA+1], v[vX+2*i], v[vX+2*i+1]
    v_cmp_gt_i32 vcc, v[vX+2*i], v[vX+2*i+1]
    .if i == NIT-1
      v_cmp_gt_i32 s[sB:sB+1], s[sLASTC], %2       ; element < T-1
      s_and_b64 s[sA:sA+1], s[sA:sA+1], s[sB:sB+1]
      s_and_b64 vcc, vcc, s[sB:sB+1]
      v_cmp_eq_u32 s[sB:sB+1], s[sLASTC], %2       ; element == T-1
      s_or_b64 s[sA:sA+1], s[sA:sA+1], s[sB:sB+1]
    .endif
    s_or_b64 s[sC:sC+1], s[sC:sC+1], vcc
    s_nop 0
    v_writelane_b32 v[vT4], s[sA], i
    v_writelane_b32 v[vT5], s[sA+1], i
    .set i, i+1
  .endr
  s_or_b32 s[sT1], s[sC], s[sC+1]
  v_mov_b32 v[vT6], s[sT1]
  s_mov_b64 exec, (1<<NIT)-1
  ds_write_b96 v[vT7], v[vT4:vT6]
  s_mov_b64 exec, -1
  v_mov_b32 v[vT0], 0
  v_mov_b32 v[vT1], 0
  v_mov_b32 v[vT2], 0
  v_lshlrev_b32 v[vT3], 4, v[vLANE]
  v_add_u32 v[vT3], LSCR, v[vT3]
  v_lshlrev_b32 v[vT7], 6, v[vLANE]
  v_add_u32 v[vT7], 1, v[vT7]                     ; lane*64 + 1
  PREFETCH PF1, PF2
  s_waitcnt lgkmcnt(0)
  s_barrier
  STAMP 2
  ; ======================================================================== S2
  s_bfm_b64 exec, 8*NIT, 0
  ds_read_b96 v[vT0:vT2], v[vT3]
  s_mov_b64 exec, -1
  s_movk_i32 s[sT1], 128
  s_movk_i32 s[sT2], 256
  s_mov_b32 s[sR], 0
  s_waitcnt lgkmcnt(0)
  v_cmp_ne_u32 vcc, 0, v[vT2]
  v_bcnt_u32_b32 v[vT3], v[vT0], 0
  v_bcnt_u32_b32 v[vT3], v[vT1], v[vT3]
  s_cbranch_vccnz .Lfallback
  v_cmp_lt_u32 vcc, 1, v[vT3]
  v_ffbl_b32 v[vT4], v[vT0]
  v_ffbl_b32 v[vT5], v[vT1]
  s_cbranch_vccnz .Lfallback
  v_cmp_ne_u32 s[sA:sA+1], 0, v[vT3]
  v_add_u32 v[vT5], 32, v[vT5]
  v_min_u32 v[vT4], v[vT4], v[vT5]
  v_add_u32 v[vT4], v[vT4], v[vT7]
  v_cndmask_b32 v[vT4], 0, v[vT4], s[sA:sA+1]     ; end (0 if none)
  s_nop 1
  v_mov_b32_dpp v[vSTART], v[vT4] wave_shr:1 row_mask:0xf bank_mask:0xf bound_ctrl:0
  s_nop 1
  v_max_u32_dpp v[vSTART], v[vSTART], v[vSTART] row_shr:1 row_mask:0xf bank_mask:0xf bound_ctrl:0
  s_nop 1
  v_max_u32_dpp v[vSTART], v[vSTART], v[vSTART] row_shr:2 row_mask:0xf bank_mask:0xf bound_ctrl:0
  s_nop 1
  v_max_u32_dpp v[vSTART], v[vSTART], v[vSTART] row_shr:4 row_mask:0xf bank_mask:0xf bound_ctrl:0
  s_nop 1
  v_max_u32_dpp v[vSTART], v[vSTART], v[vSTART] row_shr:8 row_mask:0xf bank_mask:0xf bound_ctrl:0
  s_nop 1
  v_max_u32_dpp v[vSTART], v[vSTART], v[vSTART] row_bcast:15 row_mask:0xa bank_mask:0xf
  s_nop 1
  v_max_u32_dpp v[vSTART], v[vSTART], v[vSTART] row_bcast:31 row_mask:0xc bank_mask:0xf
  s_nop 0
  v_sub_u32 v[vLEN], v[vT4], v[vSTART]
  v_cndmask_b32 v[vLEN], 0, v[vLEN], s[sA:sA+1]   ; len (0 if none)
  v_add_u32 v[vT5], 127, v[vLEN]
  v_lshrrev_b32 v[vT5], 7, v[vT5]                 ; nq (mode A)
  v_cmp_lt_u32 vcc, s[sT1], v[vLEN]               ; len > 128
  v_cmp_ge_u32 s[sB:sB+1], s[sT2], v[vLEN]        ; len <= 256
  s_and_b64 s[sB:sB+1], s[sB:sB+1], vcc           ; mode B
  v_lshlrev_b32 v[vT5], 1, v[vT5]
  s_nop 0
  v_cndmask_b32 v[vT5], v[vT5], 2, s[sB:sB+1]     ; items (2 heads)
  v_lshlrev_b32 v[vT6], 16, v[vT5]
  v_cndmask_b32 v[vINCL], v[vT6], v[vT5], vcc     ; small -> high half
  v_mov_b32 v[vEXCL], v[vINCL]
  s_nop 0
  v_add_u32_dpp v[vINCL], v[vINCL], v[vINCL] row_shr:1 row_mask:0xf bank_mask:0xf bound_ctrl:0
  s_nop 1
  v_add_u32_dpp v[vINCL], v[vINCL], v[vINCL] row_shr:2 row_mask:0xf bank_mask:0xf bound_ctrl:0
  s_nop 1
  v_add_u32_dpp v[vINCL], v[vINCL], v[vINCL] row_shr:4 row_mask:0xf bank_mask:0xf bound_ctrl:0
  s_nop 1
  v_add_u32_dpp v[vINCL], v[vINCL], v[vINCL] row_shr:8 row_mask:0xf bank_mask:0xf bound_ctrl:0
  s_nop 1
  v_add_u32_dpp v[vINCL], v[vINCL], v[vINCL] row_bcast:15 row_mask:0xa bank_mask:0xf
  s_nop 1
  v_add_u32_dpp v[vINCL], v[vINCL], v[vINCL] row_bcast:31 row_mask:0xc bank_mask:0xf
  s_nop 0
  v_sub_u32 v[vEXCL], v[vINCL], v[vEXCL]
  v_readlane_b32 s[sT1], v[vINCL], 63
  s_and_b32 s[sNBIG], s[sT1], 0xffff
  s_lshr_b32 s[sTOT], s[sT1], 16
  s_add_u32 s[sTOT], s[sTOT], s[sNBIG]
  .if DBGW & 8
  s_mov_b32 s[sSAFE], 1
  .else
  s_mov_b32 s[sSAFE], 0
  .endif
  STAMP 3

  .if PRIO == 1
    s_cmp_lt_u32 s[sW], 4
    s_cbranch_scc1 .Lnoprio
    s_setprio 1
.Lnoprio:
  .elseif PRIO == 2
    s_cmp_ge_u32 s[sW], 4
    s_cbranch_scc1 .Lnoprio
    s_setprio 1
.Lnoprio:
  .endif
  ; ---- DMA pieces.  sKDST / sVDST: ring offset + w*2048 of the next tile; rows beyond the segment end are out of range (zero fill, no traffic)
  .macro DK1
    s_mov_b32 m0, s[sKDST]
    s_add_u32 s[sT1], s[sKDST], 16384
    buffer_load_dwordx4 v[vOK], s[sK:sK+3], s[sKSO] offen lds
    .if DBGW & 4
    s_nop 3
    .endif
  .endm
  .macro DK2
    s_add_u32 m0, s[sKDST], 1024
    s_and_b32 s[sKDST], s[sT1], 0xffff
    buffer_load_dwordx4 v[vOK2], s[sK:sK+3], s[sKSO] offen lds
    .if DBGW & 4
    s_nop 3
    .endif
    s_add_u32 s[sKSO], s[sKSO], 64*4096
    s_min_u32 s[sKSO], s[sKSO], s[sMAXSO]
  .endm
  .macro DV1
    s_add_u32 m0, s[sVDST], LV
    s_add_u32 s[sT3], s[sVDST], 16384
    buffer_load_dwordx4 v[vOV], s[sV:sV+3], s[sVSO] offen lds
    .if DBGW & 4
    s_nop 3
    .endif
  .endm
  .macro DV2
    s_add_u32 m0, s[sVDST], LV+1024
    s_and_b32 s[sVDST], s[sT3], 0xffff
    buffer_load_dwordx4 v[vOV2], s[sV:sV+3], s[sVSO] offen lds
    .if DBGW & 4
    s_nop 3
    .endif
    s_add_u32 s[sVSO], s[sVSO], 64*4096
    s_min_u32 s[sVSO], s[sVSO], s[sMAXSO]
  .endm
  .macro DALL
    DK1
    DK2
    DV1
    DV2
  .endm
  .macro BAR4 modea=0, bit=2                        ; previous group had 4 pieces: everything older has landed
    .if \modea
      s_cmp_eq_u32 s[sMB], 1
      s_cbranch_scc1 .Lnob\@
    .endif
    .if DBGW & \bit
    s_waitcnt vmcnt(0) lgkmcnt(0)
    .else
    s_waitcnt vmcnt(4) lgkmcnt(0)
    .endif
    s_barrier
    .if \modea
    s_branch .Lbd\@
  .Lnob\@:
    s_waitcnt lgkmcnt(0)
  .Lbd\@:
    .endif
  .endm
  .macro BARN modea=0                               ; nothing was issued behind the needed pieces
    .if \modea
      s_cmp_eq_u32 s[sMB], 1
      s_cbranch_scc1 .Lnob\@
    .endif
    s_waitcnt vmcnt(0) lgkmcnt(0)
    s_barrier
    .if \modea
    s_branch .Lbd\@
  .Lnob\@:
    s_waitcnt lgkmcnt(0)
  .Lbd\@:
    .endif
  .endm
  .macro BAR0 modea=0
    .if \modea
      s_cmp_eq_u32 s[sMB], 1
      s_cbranch_scc1 .Lnob\@
    .endif
    s_waitcnt vmcnt(0) lgkmcnt(0)
    s_barrier
    .if \modea
  .Lnob\@:
    .endif
  .endm

.Litem:
  ; idx = r*32 + (r odd ? 31-j : j)
  s_lshl_b32 s[sT1], s[sR], 5
  s_sub_u32 s[sT2], 31, s[sJ]
  s_bitcmp1_b32 s[sR], 0
  s_cselect_b32 s[sT2], s[sT2], s[sJ]
  s_add_u32 s[sT1], s[sT1], s[sT2]                ; idx
  s_cmp_ge_u32 s[sT1], s[sTOT]
  s_cbranch_scc1 .Ldone
  s_sub_u32 s[sT2], s[sT1], s[sNBIG]
  s_cmp_lt_u32 s[sT1], s[sNBIG]
  s_cselect_b32 s[sT1], s[sT1], s[sT2]            ; index inside its class
  s_cselect_b32 s[sT2], 0, 16                     ; field shift
  v_bfe_u32 v[vT0], v[vINCL], s[sT2], 16
  v_bfe_u32 v[vT1], v[vEXCL], s[sT2], 16
  v_cmp_lt_u32 s[sA:sA+1], s[sT1], v[vT0]
  v_cmp_ge_u32 vcc, s[sT1], v[vT1]
  s_and_b64 s[sA:sA+1], s[sA:sA+1], vcc
  s_ff1_i32_b64 s[sT2], s[sA:sA+1]
  v_readlane_b32 s[sK0], v[vSTART], s[sT2]
  v_readlane_b32 s[sLEN], v[vLEN], s[sT2]
  v_readlane_b32 s[sT3], v[vT1], s[sT2]
  s_sub_u32 s[sT1], s[sT1], s[sT3]                ; local item
  s_and_b32 s[sT2], s[sT1], 1
  s_lshl_b32 s[sT2], s[sT2], 3
  s_add_u32 s[sT2], s[sT2], s[sG]
  s_lshl_b32 s[sHOFF], s[sT2], 8                  ; head*256 bytes
  s_lshr_b32 s[sT1], s[sT1], 1
  s_lshl_b32 s[sT1], s[sT1], 7
  s_add_u32 s[sQ0], s[sK0], s[sT1]
  s_lshl_b32 s[sT1], s[sW], 3
  s_add_u32 s[sT2], s[sQ0], s[sT1]
  s_lshl_b32 s[sT2], s[sT2], 12
  s_add_u32 s[sT2], s[sT2], s[sHOFF]              ; (q0 + 8w)*4096 + hoff
  ; ---- Q DMA
  s_add_u32 m0, s[sWD], LX
  s_add_u32 s[sT3], s[sT2], 64*4096
  buffer_load_dwordx4 v[vOK], s[sQ:sQ+3], s[sT2] offen lds
  s_add_u32 m0, s[sWD], LX+1024
  s_add_u32 s[sT4], s[sT2], 128*4096
  buffer_load_dwordx4 v[vOK2], s[sQ:sQ+3], s[sT2] offen lds
  s_add_u32 m0, s[sWD], LX+16384
  s_add_u32 s[sT1], s[sK0], s[sT1]
  buffer_load_dwordx4 v[vOK], s[sQ:sQ+3], s[sT3] offen lds
  s_add_u32 m0, s[sWD], LX+16384+1024
  s_lshl_b32 s[sT1], s[sT1], 12
  buffer_load_dwordx4 v[vOK2], s[sQ:sQ+3], s[sT3] offen lds
  s_add_u32 s[sKSO], s[sT1], s[sHOFF]             ; (k0 + 8w)*4096 + hoff
  s_add_u32 s[sMAXSO], s[sTM16], 15*4096
  s_add_u32 s[sMAXSO], s[sMAXSO], s[sHOFF]        ; (T-1)*4096 + hoff
  s_min_u32 s[sKSO], s[sKSO], s[sMAXSO]
  s_mov_b32 s[sVSO], s[sKSO]
  s_add_u32 s[sQEND], s[sK0], s[sLEN]
  s_add_u32 s[sNK], s[sLEN], 63
  s_lshr_b32 s[sNK], s[sNK], 6
  s_lshl_b32 s[sO+2], s[sQEND], 12                ; the output descriptor ends at the segment end
  s_sub_u32 s[sT1], s[sLEN], 129
  s_cmp_lt_u32 s[sT1], 128
  s_cselect_b32 s[sMB], 1, 0                      ; mode B: 128 < len <= 256
  s_mov_b32 s[sKDST], s[sWD]
  s_mov_b32 s[sVDST], s[sWD]
  s_cbranch_scc0 .Lq_done
  s_add_u32 m0, s[sWD], LQ2
  s_add_u32 s[sT3], s[sT4], 64*4096
  buffer_load_dwordx4 v[vOK], s[sQ:sQ+3], s[sT4] offen lds
  s_add_u32 m0, s[sWD], LQ2+1024
  s_nop 0
  buffer_load_dwordx4 v[vOK2], s[sQ:sQ+3], s[sT4] offen lds
  s_add_u32 m0, s[sWD], LQ3
  s_nop 0
  buffer_load_dwordx4 v[vOK], s[sQ:sQ+3], s[sT3] offen lds
  s_add_u32 m0, s[sWD], LQ3+1024
  s_nop 0
  buffer_load_dwordx4 v[vOK2], s[sQ:sQ+3], s[sT3] offen lds
.Lq_done:
  DK1
  DK2
  STAMP 4
  ; ---- per-wave geometry
  s_and_b32 s[sT1], s[sW], 3
  s_lshr_b32 s[sT2], s[sW], 2
  s_cmp_eq_u32 s[sMB], 1
  s_cselect_b32 s[sQB], s[sW], s[sT1]             ; q block
  s_cselect_b32 s[sKH], 0, s[sT2]                 ; key half (mode A)
  s_movk_i32 s[sKSTEP], 0x4000
  s_cselect_b32 s[sKSTEP], 8192, s[sKSTEP]
  s_add_u32 s[sT1], s[sLEN], 31
  s_lshr_b32 s[sT1], s[sT1], 5                    ; nst = ceil(len/32)
  s_add_u32 s[sT2], s[sT1], 1
  s_sub_u32 s[sT2], s[sT2], s[sKH]
  s_lshr_b32 s[sT2], s[sT2], 1
  s_cmp_eq_u32 s[sMB], 1
  s_cselect_b32 s[sNS], s[sT1], s[sT2]            ; steps of this wave
  s_cselect_b32 s[sNU], s[sT1], s[sNK]            ; step slots
  s_and_b32 s[sLIM], s[sLEN], 31
  s_sub_u32 s[sT2], s[sT1], 1
  s_and_b32 s[sT2], s[sT2], 1
  s_cmp_eq_u32 s[sT2], s[sKH]
  s_cselect_b32 s[sT3], 1, s[sMB]                 ; owner of the last sub-tile
  s_cmp_lg_u32 s[sLIM], 0
  s_cselect_b32 s[sT3], s[sT3], 0
  s_sub_u32 s[sMSTEP], s[sNS], 1
  s_cmp_eq_u32 s[sT3], 1
  s_cselect_b32 s[sMSTEP], s[sMSTEP], -1
  s_lshl_b32 s[sT1], s[sQB], 5
  s_add_u32 s[sQR], s[sT1], s[sQ0]                ; first q row of this wave
  s_cmp_lt_u32 s[sQR], s[sQEND]
  s_cselect_b32 s[sNS], s[sNS], 0
  s_cselect_b32 s[sLIVE], 1, 0
  ; FULL slots with DMA: tile iteration t <= nk-3
  s_sub_i32 s[sND], s[sNK], 2
  s_max_i32 s[sND], s[sND], 0
  s_lshl_b32 s[sND], s[sND], s[sMB]
  ; Q read base
  s_lshl_b32 s[sT1], s[sQB], 13
  s_add_u32 s[sT2], s[sT1], LX                    ; qb < 4
  s_add_u32 s[sT3], s[sT1], LQ2-4*8192            ; qb 4,5
  s_add_u32 s[sT4], s[sT1], LQ3-6*8192            ; qb 6,7
  s_cmp_lt_u32 s[sQB], 6
  s_cselect_b32 s[sT3], s[sT3], s[sT4]
  s_cmp_lt_u32 s[sQB], 4
  s_cselect_b32 s[sT2], s[sT2], s[sT3]
  v_add_u32 v[vT0], s[sT2], v[vKB0]
  v_add_u32 v[vT1], s[sT2], v[vKB1]
  s_lshl_b32 s[sKOFF], s[sKH], 13
  s_mov_b32 s[sVOFF], s[sKOFF]
  v_add_u32 v[vKA0], s[sKOFF], v[vKB0]
  v_add_u32 v[vKA1], s[sKOFF], v[vKB1]
  ; epilogue addresses
  v_lshlrev_b32 v[vT2], 4, v[vLANE]
  s_lshl_b32 s[sT1], s[sW], 13
  s_xor_b32 s[sT2], s[sT1], 4*8192
  v_add_u32 v[vXP], s[sT2], v[vT2]                ; partner's exchange area
  v_add_u32 v[vXO], s[sT1], v[vT2]                ; own exchange area
  v_and_b32 v[vT2], 31, v[vLANE]
  v_lshlrev_b32 v[vT2], 3, v[vT2]
  v_add_u32 v[vT2], LX, v[vT2]
  s_lshl_b32 s[sT1], s[sW], 8
  s_xor_b32 s[sT2], s[sT1], 4*256
  v_add_u32 v[vSTP], s[sT2], v[vT2]
  v_add_u32 v[vSTA], s[sT1], v[vT2]
  s_lshl_b32 s[sT1], s[sW], 12                    ; stage: mode A in the V ring, mode B in the X region... (4 KiB per wave)
  s_add_u32 s[sT2], s[sT1], LV
  s_lshl_b32 s[sT3], s[sW], 13
  s_cmp_eq_u32 s[sMB], 1
  s_cselect_b32 s[sT1], s[sT3], s[sT2]
  v_add_u32 v[vSGW], s[sT1], v[vSW]
  v_add_u32 v[vSGR], s[sT1], v[vSR]
  .set i, 0
  .rept 64
    v_mov_b32 v[vO+i], 0
    .set i, i+1
  .endr
  v_mov_b32 v[vL0], 0
  v_mov_b32 v[vL1], 0
  v_mov_b32 v[vLS], 0
  v_mov_b32 v[vLS+1], 0
  v_mov_b32 v[vLS+2], 0
  v_mov_b32 v[vLS+3], 0
  v_mov_b32 v[vM], 0xf149f2ca
  s_mov_b32 s[sPEND], 0
  s_mov_b32 s[sU], 0
  ; mid group: mode A: K1, K2, V0 ; mode B: K1, V0
  .if WQ
    s_waitcnt vmcnt(2)
  .endif
  DK1
  DK2
  s_cmp_eq_u32 s[sMB], 1
  s_cbranch_scc1 .Lmid_b
  DK1
  DK2
  DV1
  DV2
  s_waitcnt vmcnt(6)
  s_branch .Lmid_d
.Lmid_b:
  DV1
  DV2
  s_waitcnt vmcnt(4)
.Lmid_d:
  s_barrier                                         ; barrier P: Q, K0 landed
  STAMP 5

  .macro KREAD i
    .if (ABL & 8)
    .elseif (\i & 1)
      ds_read_b128 v[vK+4*\i:vK+4*\i+3], v[vKA1] offset:512*(\i>>1)
    .else
      ds_read_b128 v[vK+4*\i:vK+4*\i+3], v[vKA0] offset:512*(\i>>1)
    .endif
  .endm
  .macro KADV                                       ; advance K read addresses to the next step
    s_add_u32 s[sKOFF], s[sKOFF], s[sKSTEP]
    s_and_b32 s[sKOFF], s[sKOFF], 0xffff
    v_add_u32 v[vKA0], s[sKOFF], v[vKB0]
    v_add_u32 v[vKA1], s[sKOFF], v[vKB1]
  .endm
  .macro QKM c, i
    .if \i == 0
      v_mfma_f32_32x32x16_bf16 v[\c:\c+15], v[vK:vK+3], v[vQ:vQ+3], 0
    .else
      v_mfma_f32_32x32x16_bf16 v[\c:\c+15], v[vK+4*\i:vK+4*\i+3], v[vQ+4*\i:vQ+4*\i+3], v[\c:\c+15]
    .endif
  .endm
  .macro VREAD j                                    ; V fragment j: d = j&3, ks = j>>2
    .if (ABL & 4) == 0
    ds_read_b64_tr_b16 v[vV+4*\j:vV+4*\j+1], v[vVA] offset:512*(\j&3)+4096*(\j>>2)
    ds_read_b64_tr_b16 v[vV+4*\j+2:vV+4*\j+3], v[vVA] offset:512*(\j&3)+4096*(\j>>2)+2048
    .endif
  .endm
  .macro PVM j
    v_mfma_f32_32x32x16_bf16 v[vO+16*(\j&3):vO+16*(\j&3)+15], v[vV+4*\j:vV+4*\j+3], v[vP+4*(\j>>2):vP+4*(\j>>2)+3], v[vO+16*(\j&3):vO+16*(\j&3)+15]
  .endm
  .macro LSUM ks
    .if (ABL & 64) == 0
    v_mfma_f32_16x16x32_bf16 v[vLS:vLS+3], v[vONE:vONE+3], v[vP+4*\ks:vP+4*\ks+3], v[vLS:vLS+3]
    .endif
  .endm
  .macro FMAE c, r
    .if (ABL & 2) == 0
    v_fma_f32 v[\c+(\r)], v[\c+(\r)], s[sCE], v[vNMC]
    .endif
  .endm
  .macro XPE c, r
    .if (ABL & 2) == 0
    v_exp_f32 v[\c+(\r)], v[\c+(\r)]
    .endif
  .endm
  .macro MASK c                                     ; keys >= lim of this sub-tile -> -inf
    v_sub_u32 v[vT2], s[sLIM], v[vHH4]
    v_mov_b32 v[vT3], 0xff800000
    .set r, 0
    .rept 16
      v_cmp_lt_i32 vcc, (r&3)+8*(r>>2), v[vT2]
      s_nop 1
      v_cndmask_b32 v[\c+r], v[vT3], v[\c+r], vcc
      .set r, r+1
    .endr
  .endm
  .macro ROWMAX1 c
    v_max3_f32 v[vT2], v[\c+0], v[\c+1], v[\c+2]
    v_max3_f32 v[vT3], v[\c+3], v[\c+4], v[\c+5]
    v_max3_f32 v[vT2], v[vT2], v[\c+6], v[\c+7]
    v_max3_f32 v[vT3], v[vT3], v[\c+8], v[\c+9]
    v_max3_f32 v[vT2], v[vT2], v[\c+10], v[\c+11]
    v_max3_f32 v[vT3], v[vT3], v[\c+12], v[\c+13]
    v_max3_f32 v[vT2], v[vT2], v[\c+14], v[\c+15]
    v_max_f32 v[vT2], v[vT2], v[vT3]
    v_mov_b32 v[vT3], v[vT2]
  .endm
  .macro ROWMAX2                                    ; needs 2 wait states after ROWMAX1
    v_permlane32_swap_b32 v[vT2], v[vT3]
    s_nop 0
    v_max_f32 v[vT2], v[vT2], v[vT3]                ; vT2 = row max
  .endm
  .macro EXPE c, r
    .if (ABL & 2) == 0
    v_fma_f32 v[\c+(\r)], v[\c+(\r)], s[sCE], v[vNMC]
    v_exp_f32 v[\c+(\r)], v[\c+(\r)]
    .endif
  .endm
  .macro SUMPK p, i                                 ; row sums + pack of elements 2i, 2i+1
    .if (ABL & 32) == 0
    v_add_f32 v[vL0], v[vL0], v[\p+2*\i]
    v_add_f32 v[vL1], v[vL1], v[\p+2*\i+1]
    v_cvt_pk_bf16_f32 v[vP+\i], v[\p+2*\i], v[\p+2*\i+1]
    .endif
  .endm
  .macro ADDE p, e                                  ; row sum of element e (two chains)
    .if (ABL & 32) == 0
    .if ((\e) & 1)
      v_add_f32 v[vL1], v[vL1], v[\p+(\e)]
    .else
      v_add_f32 v[vL0], v[vL0], v[\p+(\e)]
    .endif
    .endif
  .endm
  .macro CVTE p, i                                  ; pack of elements 2i, 2i+1
    .if (ABL & 32) == 0
    v_cvt_pk_bf16_f32 v[vP+\i], v[\p+2*\i], v[\p+2*\i+1]
    .endif
  .endm
  .macro VADDR                                      ; V read address for the current step, then advance
    v_add_u32 v[vVA], s[sVOFF], v[vVB]
    s_add_u32 s[sVOFF], s[sVOFF], s[sKSTEP]
    s_and_b32 s[sVOFF], s[sVOFF], 0xffff
  .endm
  .macro RESCALE
    s_nop 7
    s_nop 7
    .set i, 0
    .rept 64
      v_mul_f32 v[vO+i], v[vAL], v[vO+i]
      .set i, i+1
    .endr
    v_mul_f32 v[vL0], v[vAL], v[vL0]
    v_mul_f32 v[vL1], v[vAL], v[vL1]
    s_mov_b32 s[sPEND], 0
  .endm

  ; ---- prologue compute: step 0
  s_cmp_eq_u32 s[sNS], 0
  s_cbranch_scc1 .Lpro_none
  .set i, 0
  .rept 8
    KREAD i
    .if (i & 1)
      ds_read_b128 v[vQ+4*i:vQ+4*i+3], v[vT1] offset:512*(i>>1)
    .else
      ds_read_b128 v[vQ+4*i:vQ+4*i+3], v[vT0] offset:512*(i>>1)
    .endif
    .set i, i+1
  .endr
  KADV
  .macro QK0 rd                                     ; rd: read the next step's K fragments behind each MFMA (mode B: same tile)
    .set i, 0
    .rept 8
      .if \rd
        .if i == 0
          s_waitcnt lgkmcnt(14)
        .elseif i == 1
          s_waitcnt lgkmcnt(13)
        .elseif i == 2
          s_waitcnt lgkmcnt(12)
        .elseif i == 3
          s_waitcnt lgkmcnt(11)
        .elseif i == 4
          s_waitcnt lgkmcnt(10)
        .elseif i == 5
          s_waitcnt lgkmcnt(9)
        .elseif i == 6
          s_waitcnt lgkmcnt(8)
        .else
          s_waitcnt lgkmcnt(7)
        .endif
      .else
        .if i == 0
          s_waitcnt lgkmcnt(14)
        .elseif i == 1
          s_waitcnt lgkmcnt(12)
        .elseif i == 2
          s_waitcnt lgkmcnt(10)
        .elseif i == 3
          s_waitcnt lgkmcnt(8)
        .elseif i == 4
          s_waitcnt lgkmcnt(6)
        .elseif i == 5
          s_waitcnt lgkmcnt(4)
        .elseif i == 6
          s_waitcnt lgkmcnt(2)
        .else
          s_waitcnt lgkmcnt(0)
        .endif
      .endif
      QKM vSA, i
      .if \rd
        KREAD i
      .endif
      .if i == 1
        DK1
      .elseif i == 3
        DK2
      .elseif i == 5
        DV1
      .elseif i == 7
        DV2
      .endif
      .set i, i+1
    .endr
  .endm
  s_cmp_eq_u32 s[sMB], 1
  s_cbranch_scc1 .Lqk0_b
  QK0 0
  s_branch .Lqk0_d
.Lqk0_b:
  QK0 1
  KADV
.Lqk0_d:
  s_nop 7
  s_cmp_lg_u32 s[sMSTEP], 0
  s_cbranch_scc1 .Lnomask0
  MASK vSA
.Lnomask0:
  ROWMAX1 vSA
  s_nop 1
  ROWMAX2
  v_max_f32 v[vM], v[vM], v[vT2]
  v_mul_f32 v[vNMC], 0xbe0293ee, v[vM]
  s_sub_u32 s[sNF], s[sNS], 1                     ; FULL slots of this wave
  s_min_u32 s[sNDF], s[sND], s[sNF]               ; ... of which issue DMA
  VADDR
  .macro P2                                         ; mode A: K1 landed -> its fragments
    s_cmp_eq_u32 s[sMB], 1
    s_cbranch_scc1 .Lp2\@
    s_waitcnt vmcnt(8)
    s_barrier
    .set i, 0
    .rept 8
      KREAD i
      .set i, i+1
    .endr
    KADV
  .Lp2\@:
  .endm
  s_cmp_eq_u32 s[sSAFE], 1
  s_cbranch_scc1 .Lpro_safe
  .set r, 0
  .rept 6
    EXPE vSA, r
    .set r, r+1
  .endr
  P2
  .set r, 6
  .rept 5
    EXPE vSA, r
    .set r, r+1
  .endr
  STAMP 6
  s_cmp_lt_u32 s[sU], s[sNDF]
  s_cbranch_scc1 .LloopF
  s_branch .LdispF0
.Lpro_safe:
  P2
  .set r, 0
  .rept 16
    EXPE vSA, r
    .set r, r+1
  .endr
  s_cmp_eq_u32 s[sNF], 0
  s_cbranch_scc1 .LlastS0
  s_branch .LloopS
.Lpro_none:
  DALL
  s_cmp_eq_u32 s[sMB], 1
  s_cbranch_scc1 .Lnone
  s_waitcnt vmcnt(8)
  s_barrier
  s_branch .Lnone

  ; ======================================================================== SAFE step (online max every step)
  .macro FULLS cur, prev, modea
    s_cmp_lt_u32 s[sU], s[sND]
    s_cselect_b32 s[sF], 1, 0
    s_cbranch_scc0 .Lbn\@
    BAR4 \modea
    s_branch .Lbq\@
  .Lbn\@:
    BARN \modea
  .Lbq\@:
    .set g, 0
    .rept 8
      QKM \cur, g
      VREAD g
      SUMPK \prev, g
      .if g == 1
        DMAK1 \modea, 2
      .elseif g == 5
        DMAK2 \modea, 2
      .endif
      .set g, g+1
    .endr
    s_waitcnt lgkmcnt(4)
    PVM 0
    KREAD 0
    KREAD 1
    s_add_u32 s[sU], s[sU], 1
    s_nop 0
    PVM 1
    s_cmp_lg_u32 s[sMSTEP], s[sU]
    s_cbranch_scc1 .Lnm\@
    MASK \cur
  .Lnm\@:
    ROWMAX1 \cur
    PVM 2
    KREAD 2
    ROWMAX2
    v_sub_f32 v[vT3], v[vT2], v[vM]
    v_cmp_lt_f32 vcc, s[sTHR], v[vT3]
    s_cbranch_vccz .Lkeep\@
    v_max_f32 v[vT3], v[vM], v[vT2]
    v_sub_f32 v[vAL], v[vM], v[vT3]
    v_mul_f32 v[vAL], s[sCE], v[vAL]
    v_exp_f32 v[vAL], v[vAL]
    v_mov_b32 v[vM], v[vT3]
    v_mul_f32 v[vNMC], 0xbe0293ee, v[vM]
    s_mov_b32 s[sPEND], 1
  .Lkeep\@:
    PVM 3
    KREAD 3
    DMAV1 \modea, 2
    EXPE \cur, 0
    EXPE \cur, 1
    EXPE \cur, 2
    PVM 4
    KREAD 4
    EXPE \cur, 3
    EXPE \cur, 4
    EXPE \cur, 5
    PVM 5
    KREAD 5
    EXPE \cur, 6
    EXPE \cur, 7
    EXPE \cur, 8
    s_waitcnt lgkmcnt(6)
    PVM 6
    KREAD 6
    DMAV2 \modea, 2
    EXPE \cur, 9
    EXPE \cur, 10
    EXPE \cur, 11
    PVM 7
    KREAD 7
    EXPE \cur, 12
    EXPE \cur, 13
    EXPE \cur, 14
    EXPE \cur, 15
    KADV
    VADDR
    s_cmp_eq_u32 s[sPEND], 0
    s_cbranch_scc1 .Lsend\@
    RESCALE
  .Lsend\@:
    s_cmp_eq_u32 s[sU], s[sNF]
  .endm
  .macro LASTS prev, modea
    BAR0 \modea
    .set g, 0
    .rept 8
      VREAD g
      SUMPK \prev, g
      .set g, g+1
    .endr
    s_add_u32 s[sU], s[sU], 1
    s_waitcnt lgkmcnt(0)
    .set g, 0
    .rept 8
      PVM g
      .set g, g+1
    .endr
    s_branch .Lnone_tail
  .endm

  ; DMA slots inside a step: odd slots of mode B issue nothing.  dma: 0 none, 1 always, 2 run-time flag sF
  .macro DMAK1 modea, dma=1
    .if (\dma) && ((ABL & 1) == 0)
    .if \dma == 2
      s_cmp_eq_u32 s[sF], 0
      s_cbranch_scc1 .Ld\@
    .endif
    .if \modea
      s_cmp_eq_u32 s[sMB], 1
      s_cbranch_scc1 .Ld\@
    .endif
    DK1
  .Ld\@:
    .endif
  .endm
  .macro DMAK2 modea, dma=1
    .if (\dma) && ((ABL & 1) == 0)
    .if \dma == 2
      s_cmp_eq_u32 s[sF], 0
      s_cbranch_scc1 .Ld\@
    .endif
    .if \modea
      s_cmp_eq_u32 s[sMB], 1
      s_cbranch_scc1 .Ld\@
    .endif
    DK2
  .Ld\@:
    .endif
  .endm
  .macro DMAV1 modea, dma=1
    .if (\dma) && ((ABL & 1) == 0)
    .if \dma == 2
      s_cmp_eq_u32 s[sF], 0
      s_cbranch_scc1 .Ld\@
    .endif
    .if \modea
      s_cmp_eq_u32 s[sMB], 1
      s_cbranch_scc1 .Ld\@
    .endif
    DV1
  .Ld\@:
    .endif
  .endm
  .macro DMAV2 modea, dma=1
    .if (\dma) && ((ABL & 1) == 0)
    .if \dma == 2
      s_cmp_eq_u32 s[sF], 0
      s_cbranch_scc1 .Ld\@
    .endif
    .if \modea
      s_cmp_eq_u32 s[sMB], 1
      s_cbranch_scc1 .Ld\@
    .endif
    DV2
  .Ld\@:
    .endif
  .endm

  ; ======================================================================== FAST step (reference = row max of the wave's first sub-tile;
  ; overflow is detected on the row sums at the end of the item and the item is redone with the SAFE steps)
  ; prev enters with exp done for e0..e10 and e0..e3 summed; vVA is set for this step
  .macro FULLF cur, prev, modea, dma
    .if \dma
      BAR4 \modea
    .else
      BARN \modea
    .endif
    ; ---------------- phase A
    QKM \cur, 0
    VREAD 0
    EXPE \prev, 11
    QKM \cur, 1
    VREAD 1
    EXPE \prev, 12
    CVTE \prev, 0
    QKM \cur, 2
    VREAD 2
    DMAK1 \modea, \dma
    EXPE \prev, 13
    CVTE \prev, 1
    QKM \cur, 3
    VREAD 3
    EXPE \prev, 14
    CVTE \prev, 2
    QKM \cur, 4
    VREAD 4
    EXPE \prev, 15
    CVTE \prev, 3
    QKM \cur, 5
    VREAD 5
    CVTE \prev, 4
    CVTE \prev, 5
    QKM \cur, 6
    VREAD 6
    DMAK2 \modea, \dma
    QKM \cur, 7
    VREAD 7
    CVTE \prev, 6
    CVTE \prev, 7
    s_add_u32 s[sU], s[sU], 1
    ; ---------------- phase B
    s_waitcnt lgkmcnt(4)
    PVM 0
    KREAD 0
    KREAD 1
    DMAV1 \modea, \dma
    s_cmp_lg_u32 s[sMSTEP], s[sU]
    s_nop 0
    s_cbranch_scc1 .Lnm\@
    MASK \cur
  .Lnm\@:
    EXPE \cur, 0
    PVM 1
    EXPE \cur, 1
    LSUM 0
    EXPE \cur, 2
    PVM 2
    KREAD 2
    KREAD 3
    EXPE \cur, 3
    PVM 3
    KREAD 4
    EXPE \cur, 4
    FMAE \cur, 5
    PVM 4
    KREAD 5
    XPE \cur, 5
    EXPE \cur, 6
    PVM 5
    KREAD 6
    KREAD 7
    EXPE \cur, 7
    LSUM 1
    EXPE \cur, 8
    s_waitcnt lgkmcnt(8)
    PVM 6
    DMAV2 \modea, \dma
    EXPE \cur, 9
    KADV
    VADDR
    .if \dma
      s_cmp_lt_u32 s[sU], s[sNDF]
    .else
      s_cmp_lt_u32 s[sU], s[sNF]
    .endif
    PVM 7
    EXPE \cur, 10
  .endm
  .macro PVJ d, ks
    v_mfma_f32_32x32x16_bf16 v[vO+16*\d:vO+16*\d+15], v[vV+4*(\d+4*\ks):vV+4*(\d+4*\ks)+3], v[vP+4*\ks:vP+4*\ks+3], v[vO+16*\d:vO+16*\d+15]
  .endm
  .macro SENDB d                                    ; d-block d (16 regs) -> own exchange area
    .set i, 0
    .rept 4
      ds_write_b128 v[vXO], v[vO+16*\d+4*i:vO+16*\d+4*i+3] offset:4096*(\d&1)+1024*i
      .set i, i+1
    .endr
  .endm
  .macro LASTV da, db, dc, dd                       ; last P.V: the blocks to send (da, db) first
    PVJ \da, 0
    PVJ \db, 0
    PVJ \da, 1
    PVJ \db, 1
    PVJ \dc, 0
    s_nop 7
    s_nop 1
    SENDB \da
    PVJ \dd, 0
    SENDB \db
    PVJ \dc, 1
    PVJ \dd, 1
    s_branch .Lepi_sent
  .endm
  .macro LASTF prev, modea
    BAR0 \modea
    .set g, 0
    .rept 8
      VREAD g
      .if g < 5
        EXPE \prev, 11+g
      .endif
      .set g, g+1
    .endr
    s_add_u32 s[sU], s[sU], 1
    .set g, 0
    .rept 8
      CVTE \prev, g
      .set g, g+1
    .endr
    ; the send-early form needs the final barrier behind it (mode A, last slot)
    s_cmp_eq_u32 s[sU], s[sNU]
    s_cselect_b32 s[sT1], 1, 0
    s_andn2_b32 s[sT1], s[sT1], s[sMB]
    s_cmp_eq_u32 s[sT1], 1
    s_waitcnt lgkmcnt(0)
    LSUM 0
    LSUM 1
    s_cbranch_scc0 .Lplain\@
    s_cmp_eq_u32 s[sKH], 0
    s_cbranch_scc0 .Lhi\@
    LASTV 2, 3, 0, 1
  .Lhi\@:
    LASTV 0, 1, 2, 3
  .Lplain\@:
    .set g, 0
    .rept 8
      PVM g
      .set g, g+1
    .endr
    s_branch .Lnone_tail
  .endm

.LloopS:
  FULLS vSB, vSA, 0
  s_cbranch_scc1 .LlastS1
  FULLS vSA, vSB, 1
  s_cbranch_scc0 .LloopS
.LlastS0:
  LASTS vSA, 0
.LlastS1:
  LASTS vSB, 1
.LloopF:
  FULLF vSB, vSA, 0, 1
  s_cbranch_scc0 .LdispF1
.LloopF1:
  FULLF vSA, vSB, 1, 1
  s_cbranch_scc1 .LloopF
.LdispF0:
  s_cmp_lt_u32 s[sU], s[sNF]
  s_cbranch_scc0 .LlastF0
.LfullN0:
  FULLF vSB, vSA, 0, 0
  s_cbranch_scc0 .LlastF1
.LfullN1:
  FULLF vSA, vSB, 1, 0
  s_cbranch_scc1 .LfullN0
.LlastF0:
  LASTF vSA, 0
.LdispF1:
  s_cmp_lt_u32 s[sU], s[sNF]
  s_cbranch_scc1 .LfullN1
.LlastF1:
  LASTF vSB, 1

  ; ---- slots in which this wave has nothing to compute
.Lnone:                                             ; wave without rows: keep loading its share of the tiles
  s_cmp_ge_u32 s[sU], s[sNU]
  s_cbranch_scc1 .Lepi
  s_bitcmp1_b32 s[sU], 0
  s_cselect_b32 s[sT1], s[sMB], 0
  s_cmp_eq_u32 s[sT1], 1
  s_cbranch_scc1 .Lnone_skip
  s_cmp_lt_u32 s[sU], s[sND]
  s_cbranch_scc0 .Lnone_fin
  s_waitcnt vmcnt(4) lgkmcnt(0)
  s_barrier
  DALL
  s_branch .Lnone_skip
.Lnone_fin:
  s_waitcnt vmcnt(0) lgkmcnt(0)
  s_barrier
.Lnone_skip:
  s_add_u32 s[sU], s[sU], 1
  s_branch .Lnone
.Lnone_tail:                                        ; after the wave's last step: only barriers remain
  s_cmp_ge_u32 s[sU], s[sNU]
  s_cbranch_scc1 .Lepi
  s_bitcmp1_b32 s[sU], 0
  s_cselect_b32 s[sT1], s[sMB], 0
  s_cmp_eq_u32 s[sT1], 1
  s_cbranch_scc1 .Lnt_skip
  s_barrier
.Lnt_skip:
  s_add_u32 s[sU], s[sU], 1
  s_branch .Lnone_tail

  ; ======================================================================== epilogue
  ; 32 f32 at vO+b.. (d 0..63 of a 64-wide half) -> bf16 -> LDS stage (row-major 128 B rows, 16-B chunks xor row) -> 4 full-line stores
  ; t: 16 dead registers (vO+t..) ; vSGW / vSGR: stage addresses ; sT3 = global offset of row 0 of this half
  .macro OUTHALF b, t
    .set q, 0
    .rept 4
      v_cvt_pk_bf16_f32 v[vO+\t+4*q+0], v[vO+\b+8*q+0], v[vO+\b+8*q+1]
      v_cvt_pk_bf16_f32 v[vO+\t+4*q+1], v[vO+\b+8*q+2], v[vO+\b+8*q+3]
      v_cvt_pk_bf16_f32 v[vO+\t+4*q+2], v[vO+\b+8*q+4], v[vO+\b+8*q+5]
      v_cvt_pk_bf16_f32 v[vO+\t+4*q+3], v[vO+\b+8*q+6], v[vO+\b+8*q+7]
      v_xor_b32 v[vT0+q], q*32, v[vSGW]
      .set q, q+1
    .endr
    .set q, 0
    .rept 4
      v_permlane32_swap_b32 v[vO+\t+4*q+0], v[vO+\t+4*q+2]
      v_permlane32_swap_b32 v[vO+\t+4*q+1], v[vO+\t+4*q+3]
      .set q, q+1
    .endr
    .set q, 0
    .rept 4
      ds_write_b128 v[vT0+q], v[vO+\t+4*q:vO+\t+4*q+3]
      .set q, q+1
    .endr
    s_add_u32 s[sT4], s[sT3], 8*4096
    s_add_u32 s[sT1], s[sT3], 16*4096
    s_add_u32 s[sT2], s[sT3], 24*4096
    s_waitcnt lgkmcnt(0)
    STAMPE 4
    ds_read_b128 v[vO+\t+0:vO+\t+3], v[vSGR]
    ds_read_b128 v[vO+\t+4:vO+\t+7], v[vSGR] offset:1024
    ds_read_b128 v[vO+\t+8:vO+\t+11], v[vSGR] offset:2048
    ds_read_b128 v[vO+\t+12:vO+\t+15], v[vSGR] offset:3072
    s_waitcnt lgkmcnt(3)
    buffer_store_dwordx4 v[vO+\t+0:vO+\t+3], v[vSO], s[sO:sO+3], s[sT3] offen
    s_waitcnt lgkmcnt(2)
    buffer_store_dwordx4 v[vO+\t+4:vO+\t+7], v[vSO], s[sO:sO+3], s[sT4] offen
    s_waitcnt lgkmcnt(1)
    buffer_store_dwordx4 v[vO+\t+8:vO+\t+11], v[vSO], s[sO:sO+3], s[sT1] offen
    s_waitcnt lgkmcnt(0)
    buffer_store_dwordx4 v[vO+\t+12:vO+\t+15], v[vSO], s[sO:sO+3], s[sT2] offen
  .endm
  .macro MERGE kb, rb                               ; kb: kept 32 regs, rb: 32 regs receiving the partner's half
    ds_read_b64 v[vT4:vT5], v[vSTP]
    .set i, 0
    .rept 8
      ds_read_b128 v[vO+\rb+4*i:vO+\rb+4*i+3], v[vXP] offset:1024*i
      .set i, i+1
    .endr
    s_waitcnt lgkmcnt(8)
    v_max_f32 v[vT6], v[vM], v[vT4]
    v_sub_f32 v[vFA], v[vM], v[vT6]
    v_sub_f32 v[vFB], v[vT4], v[vT6]
    v_mul_f32 v[vFA], s[sCE], v[vFA]
    v_mul_f32 v[vFB], s[sCE], v[vFB]
    v_exp_f32 v[vFA], v[vFA]
    v_exp_f32 v[vFB], v[vFB]
    s_nop 0
    v_mul_f32 v[vT6], v[vFA], v[vL0]
    v_fma_f32 v[vT6], v[vFB], v[vT5], v[vT6]
    v_rcp_f32 v[vT6], v[vT6]
    s_nop 0
    v_mul_f32 v[vFA], v[vFA], v[vT6]
    v_mul_f32 v[vFB], v[vFB], v[vT6]
    STAMPE 2
    .set i, 0
    .rept 16
      v_pk_mul_f32 v[vO+\kb+2*i:vO+\kb+2*i+1], v[vFA:vFA+1], v[vO+\kb+2*i:vO+\kb+2*i+1] op_sel_hi:[0,1]
      .set i, i+1
    .endr
    .set i, 0
    .rept 8
      .if i == 0
        s_waitcnt lgkmcnt(7)
      .elseif i == 1
        s_waitcnt lgkmcnt(6)
      .elseif i == 2
        s_waitcnt lgkmcnt(5)
      .elseif i == 3
        s_waitcnt lgkmcnt(4)
      .elseif i == 4
        s_waitcnt lgkmcnt(3)
      .elseif i == 5
        s_waitcnt lgkmcnt(2)
      .elseif i == 6
        s_waitcnt lgkmcnt(1)
      .else
        s_waitcnt lgkmcnt(0)
      .endif
      v_pk_fma_f32 v[vO+\kb+4*i:vO+\kb+4*i+1], v[vFB:vFB+1], v[vO+\rb+4*i:vO+\rb+4*i+1], v[vO+\kb+4*i:vO+\kb+4*i+1] op_sel_hi:[0,1,1]
      v_pk_fma_f32 v[vO+\kb+4*i+2:vO+\kb+4*i+3], v[vFB:vFB+1], v[vO+\rb+4*i+2:vO+\rb+4*i+3], v[vO+\kb+4*i+2:vO+\kb+4*i+3] op_sel_hi:[0,1,1]
      .set i, i+1
    .endr
    STAMPE 3
    OUTHALF \kb, \rb
  .endm
  .macro ROWSUM                                     ; complete row sum of this wave's keys -> vL0 (all lanes)
    s_cmp_eq_u32 s[sSAFE], 1
    s_cbranch_scc1 .Lss\@
    v_permlane16_swap_b32 v[vLS], v[vLS+1]
    s_nop 0
    v_mov_b32 v[vL0], v[vLS]
    s_nop 1
    v_permlane32_swap_b32 v[vL0], v[vLS]
    s_branch .Lsd\@
  .Lss\@:
    v_add_f32 v[vL0], v[vL0], v[vL1]
    v_mov_b32 v[vL1], v[vL0]
    s_nop 1
    v_permlane32_swap_b32 v[vL0], v[vL1]
    s_nop 0
    v_add_f32 v[vL0], v[vL0], v[vL1]
  .Lsd\@:
  .endm

.Lepi:                                              ; O complete, nothing sent yet
  s_cmp_eq_u32 s[sMB], 1
  s_cbranch_scc1 .LepiB
  s_nop 7
  s_cmp_eq_u32 s[sKH], 0
  s_cbranch_scc0 .Lsend_lo
  SENDB 2
  SENDB 3
  s_branch .Lepi_sent
.Lsend_lo:
  SENDB 0
  SENDB 1
.Lepi_sent:
  STAMP 7
  v_mov_b32 v[vT7], 0x71800000
  s_lshl_b32 s[sT3], s[sQR], 12
  s_add_u32 s[sT3], s[sT3], s[sHOFF]
  ROWSUM
  v_mov_b32 v[vT4], v[vM]
  v_mov_b32 v[vT5], v[vL0]
  s_lshl_b32 s[sT1], s[sKH], 7
  s_add_u32 s[sT3], s[sT3], s[sT1]
  ds_write_b64 v[vSTA], v[vT4:vT5]
  s_cmp_eq_u32 s[sKH], 0
  s_waitcnt lgkmcnt(0)
  s_barrier
  STAMPE 1
  s_cbranch_scc0 .Lmerge1
  MERGE 0, 32
  s_branch .Lnext
.Lmerge1:
  MERGE 32, 0
  s_branch .Lnext
.LepiB:
  STAMP 7
  v_mov_b32 v[vT7], 0x71800000
  s_lshl_b32 s[sT3], s[sQR], 12
  s_add_u32 s[sT3], s[sT3], s[sHOFF]
  ROWSUM
  s_nop 0
  v_rcp_f32 v[vFA], v[vL0]
  s_waitcnt vmcnt(0)
  s_barrier
  .set i, 0
  .rept 32
    v_pk_mul_f32 v[vO+2*i:vO+2*i+1], v[vFA:vFA+1], v[vO+2*i:vO+2*i+1] op_sel_hi:[0,1]
    .set i, i+1
  .endr
  OUTHALF 0, vQ-vO
  v_add_u32 v[vSGW], 4096, v[vSGW]
  v_add_u32 v[vSGR], 4096, v[vSGR]
  s_add_u32 s[sT3], s[sT3], 128
  s_nop 0
  OUTHALF 32, vQ-vO+16
.Lnext:
  STAMP 8
  ; overflow check of the FAST path: any row sum that is not a normal-sized number -> redo this item with the online max
  v_cmp_nlt_f32 vcc, v[vL0], v[vT7]
  s_lshl_b32 s[sT1], s[sW], 2
  s_add_u32 s[sT1], s[sT1], LX+8192
  s_cmp_lg_u64 vcc, 0
  s_cselect_b32 s[sT2], 1, 0
  v_mov_b32 v[vT0], s[sT1]
  v_mov_b32 v[vT1], s[sT2]
  v_and_b32 v[vT2], 7, v[vLANE]
  v_lshlrev_b32 v[vT2], 2, v[vT2]
  v_add_u32 v[vT2], LX+8192, v[vT2]
  ds_write_b32 v[vT0], v[vT1]
  s_waitcnt lgkmcnt(0)
  s_barrier
  ds_read_b32 v[vT1], v[vT2]
  s_waitcnt lgkmcnt(0)
  v_cmp_ne_u32 vcc, 0, v[vT1]
  s_cmp_eq_u32 s[sSAFE], 1
  s_cbranch_scc1 .Lsafe_done
  s_cbranch_vccz .Lsafe_done
  s_mov_b32 s[sSAFE], 1
  s_barrier
  s_branch .Litem
.Lsafe_done:
  s_mov_b32 s[sSAFE], 0
  s_add_u32 s[sR], s[sR], 1
  s_lshl_b32 s[sT1], s[sR], 5
  s_cmp_ge_u32 s[sT1], s[sTOT]
  s_cbranch_scc1 .Ldone
  s_barrier
  s_branch .Litem
.Ldone:
  .if DIAG
    s_waitcnt vmcnt(0)
    STAMP 9
    s_memrealtime s[sA:sA+1]
    s_load_dwordx2 s[sB:sB+1], %0, 96
    s_lshl_b32 s[sT1], %1, 3
    s_add_u32 s[sT1], s[sT1], s[sW]
    s_lshl_b32 s[sT1], s[sT1], 8
    s_waitcnt lgkmcnt(0)
    s_add_u32 s[sB], s[sB], s[sT1]
    s_addc_u32 s[sB+1], s[sB+1], 0
    v_mov_b32 v[vT6], 0
    s_mov_b64 exec, 1
    .set i, 0
    .rept 10
      v_mov_b32 v[vT0], s[sST+2*i]
      v_mov_b32 v[vT1], s[sST+2*i+1]
      global_store_dwordx2 v[vT6], v[vT0:vT1], s[sB:sB+1] offset:8*i
      s_nop 1
      .set i, i+1
    .endr
    v_mov_b32 v[vT0], s[sRT]
    v_mov_b32 v[vT1], s[sRT+1]
    global_store_dwordx2 v[vT6], v[vT0:vT1], s[sB:sB+1] offset:80
    s_nop 1
    v_mov_b32 v[vT0], s[sA]
    v_mov_b32 v[vT1], s[sA+1]
    global_store_dwordx2 v[vT6], v[vT0:vT1], s[sB:sB+1] offset:88
    s_nop 1
  .endif
  s_endpgm
.Lfallback:
  s_waitcnt vmcnt(0) lgkmcnt(0)
  .purgem STAMP
  .purgem STAMPE
  .purgem PREFETCH
  .purgem DK1
  .purgem QK0
  .purgem P2
  .purgem DALL
  .purgem BAR4
  .purgem BAR0
  .purgem BARN
  .purgem FULLS
  .purgem LASTS
  .purgem FULLF
  .purgem LASTF
  .purgem DMAK1
  .purgem DMAK2
  .purgem DMAV1
  .purgem DMAV2
  .purgem DK2
  .purgem DV1
  .purgem DV2
  .purgem KREAD
  .purgem KADV
  .purgem QKM
  .purgem VREAD
  .purgem PVM
  .purgem MASK
  .purgem LSUM
  .purgem FMAE
  .purgem XPE
  .purgem ROWMAX1
  .purgem ROWMAX2
  .purgem EXPE
  .purgem SUMPK
  .purgem VADDR
  .purgem RESCALE
  .purgem ADDE
  .purgem CVTE
  .purgem OUTHALF
  .purgem MERGE
  .purgem ROWSUM
  .purgem PVJ
  .purgem SENDB
  .purgem LASTV
  )" :: "s"(__builtin_amdgcn_kernarg_segment_ptr()), "s"(blockIdx.x), "v"(threadIdx.x), "i"(NIT)
     : "memory", "vcc", "m0",
       "s8","s9","s10","s11","s12","s13","s14","s15","s16","s17","s18","s19","s20","s21","s22","s23","s24","s25","s26","s27","s28","s29","s30","s31",
       "s33","s34","s35","s36","s37","s38","s39","s40","s41","s42","s43","s44","s45","s46","s47","s48","s49","s50","s51","s52","s53","s54","s55",
       "s56","s57","s58","s59","s60","s61","s62","s63","s64","s65","s66","s67","s68","s69","s70","s71","s72","s73","s74","s75","s76","s77","s78","s79",
       "s80","s81","s82","s83","s84","s85","s86","s87","s88","s89","s90","s91","s92","s93","s94","s95","s96","s97","s98","s99",
       "v1","v2","v3","v4","v5","v6","v7","v8","v9","v10","v11","v12","v13","v14","v15","v16","v17","v18","v19","v20","v21","v22","v23","v24","v25","v26","v27","v28","v29","v30","v31",
       "v32","v33","v34","v35","v36","v37","v38","v39","v40","v41","v42","v43","v44","v45","v46","v47","v48","v49","v50","v51","v52","v53","v54","v55","v56","v57","v58","v59","v60","v61","v62","v63",
       "v64","v65","v66","v67","v68","v69","v70","v71","v72","v73","v74","v75","v76","v77","v78","v79","v80","v81","v82","v83","v84","v85","v86","v87","v88","v89","v90","v91","v92","v93","v94","v95",
       "v96","v97","v98","v99","v100","v101","v102","v103","v104","v105","v106","v107","v108","v109","v110","v111","v112","v113","v114","v115","v116","v117","v118","v119","v120","v121","v122","v123","v124","v125","v126","v127",
       "v128","v129","v130","v131","v132","v133","v134","v135","v136","v137","v138","v139","v140","v141","v142","v143","v144","v145","v146","v147","v148","v149","v150","v151","v152","v153","v154","v155","v156","v157","v158","v159",
       "v160","v161","v162","v163","v164","v165","v166","v167","v168","v169","v170","v171","v172","v173","v174","v175","v176","v177","v178","v179","v180","v181","v182","v183","v184","v185","v186","v187","v188","v189","v190","v191",
       "v192","v193","v194","v195","v196","v197","v198","v199","v200","v201","v202","v203","v204","v205","v206","v207","v208","v209","v210","v211","v212","v213","v214","v215","v216","v217","v218","v219","v220","v221","v222","v223",
       "v224","v225","v226","v227","v228","v229","v230","v231","v232","v233","v234","v235","v236","v237","v238","v239","v240","v241","v242","v243","v244","v245","v246","v247","v248","v249","v250","v251","v252","v253","v254","v255");
  generic_path((const unsigned short*)Q, (const unsigned short*)K, (const unsigned short*)V, (const int*)seg, (unsigned short*)O, T, lds);
}

__global__ __launch_bounds__(512, 2)
void ragged_attn_generic(const void* Q, const void* K, const void* V, const void* seg, void* O, int T, int pad) {
  __shared__ __attribute__((aligned(16))) char lds[LDS_BYTES];
  generic_path((const unsigned short*)Q, (const unsigned short*)K, (const unsigned short*)V, (const int*)seg, (unsigned short*)O, T, lds);
}

extern "C" void kernel_launch(void* const* d_in, const int* in_sizes, int n_in, void* d_out, int out_size, void* d_ws, size_t ws_size,
                              hipStream_t stream) {
  const int T = in_sizes[3];
  const unsigned nb = (unsigned)T * ROWB, F = 0x00020000u;
  if (T > 2048 && T <= 2560) {
    ragged_attn_kernel<5><<<256, 512, 0, stream>>>(d_in[0], nb, F, d_in[1], nb, F, d_in[2], nb, F, d_in[3], (unsigned)T * 4u, F, d_out, nb, F,
                                                   T, T - 1 - 512 * 4, ((T + 31) / 32) * (int)ROWB, 0, d_ws);
  } else {
    ragged_attn_generic<<<256, 512, 0, stream>>>(d_in[0], d_in[1], d_in[2], d_in[3], d_out, T, 0);
  }
}
```

```cpp
#include <hip/hip_runtime.h>
#include <stdint.h>

typedef short    bf16x8 __attribute__((ext_vector_type(8)));
typedef short    s16x4  __attribute__((ext_vector_type(4)));
typedef float    f32x16 __attribute__((ext_vector_type(16)));
typedef float    f32x4  __attribute__((ext_vector_type(4)));
typedef float    f32x2  __attribute__((ext_vector_type(2)));
typedef __bf16   bf2    __attribute__((ext_vector_type(2)));
typedef unsigned u32x4  __attribute__((ext_vector_type(4)));
typedef unsigned long long u64;
#define LDSP __attribute__((address_space(3)))
typedef LDSP const char* lds_cptr;
typedef LDSP char* lds_ptr;
typedef short v4i16_t __attribute__((ext_vector_type(4)));
#define DI __device__ __forceinline__
#define MFMA(a, b, c) __builtin_amdgcn_mfma_f32_32x32x16_bf16((a), (b), (c), 0, 0, 0)

constexpr int NH = 16, HD = 128;
constexpr unsigned ROWB = NH * HD * 2;
constexpr unsigned LDS_BYTES = 163840;
constexpr float CEXP = 0.08838834764831845f * 1.4426950408889634f;
constexpr float THR_RAW = 8.0f / CEXP;
constexpr float MINIT = -1.0e30f;

DI unsigned rfl(unsigned x) { return __builtin_amdgcn_readfirstlane(x); }
DI unsigned cvtpk(float lo, float hi) { f32x2 v = {lo, hi}; bf2 b = __builtin_convertvector(v, bf2); return __builtin_bit_cast(unsigned, b); }
DI float swap_max(float x) { auto r = __builtin_amdgcn_permlane32_swap(__float_as_uint(x), __float_as_uint(x), false, false); return __builtin_fmaxf(__uint_as_float(r[0]), __uint_as_float(r[1])); }
DI float swap_add(float x) { auto r = __builtin_amdgcn_permlane32_swap(__float_as_uint(x), __float_as_uint(x), false, false); return __uint_as_float(r[0]) + __uint_as_float(r[1]); }
DI s16x4 vtr(lds_cptr p) { return __builtin_bit_cast(s16x4, __builtin_amdgcn_ds_read_tr16_b64_v4i16((LDSP v4i16_t*)p)); }
#define MX3(a, b, c) __builtin_fmaxf(__builtin_fmaxf((a), (b)), (c))

DI void dma16(__amdgpu_buffer_rsrc_t srd, unsigned voff, unsigned soff, unsigned m0v) {
  unsigned keep;
  asm volatile("s_nop 4\n\ts_mov_b32 %0, m0\n\ts_mov_b32 m0, %1\n\ts_nop 0\n\tbuffer_load_dwordx4 %2, %3, %4 offen lds\n\ts_mov_b32 m0, %0"
               : "=&s"(keep) : "s"(m0v), "v"(voff), "s"(srd), "s"(soff) : "memory");
}

__device__ __forceinline__ void generic_path(const unsigned short* __restrict__ Q, const unsigned short* __restrict__ K, const unsigned short* __restrict__ V,
                                          const int* __restrict__ seg, unsigned short* __restrict__ O, int T, char* lds) {
  constexpr unsigned SLOT = 32768, VOFF = 16384, XREG = 4 * SLOT;
  const int tid = threadIdx.x, lane = tid & 63, r32 = lane & 31, hh = lane >> 5;
  const int w = (int)rfl((unsigned)tid >> 6);
  const int grp = blockIdx.x & 7, jj = blockIdx.x >> 3, NJ = 32;
  const unsigned ldsb = (unsigned)(uintptr_t)lds;
  const lds_ptr L = (lds_ptr)lds;
  const int total_items = 2 * ((T + 127) >> 7);
  const __amdgpu_buffer_rsrc_t srdQ = __builtin_amdgcn_make_buffer_rsrc((void*)Q, (short)0, T * (int)ROWB, 0x00020000);
  const __amdgpu_buffer_rsrc_t srdK = __builtin_amdgcn_make_buffer_rsrc((void*)K, (short)0, T * (int)ROWB, 0x00020000);
  const __amdgpu_buffer_rsrc_t srdV = __builtin_amdgcn_make_buffer_rsrc((void*)V, (short)0, T * (int)ROWB, 0x00020000);
  const unsigned prow = (lane >> 2) & 7, pcb = lane >> 5, pc = lane & 3;
  const unsigned voffK = prow * ROWB + pcb * 64 + ((pc ^ ((2u * (unsigned)w + (prow >> 2)) & 3u)) * 16);
  const unsigned voffV = prow * ROWB + pcb * 64 + pc * 16;
  const unsigned kb0 = 2048u * (r32 >> 3) + 64u * (r32 & 7) + 16u * ((unsigned)hh ^ ((r32 >> 2) & 3u));
  const unsigned vb0 = ((unsigned)hh * 4 + ((lane & 15) >> 2)) * 64 + ((lane >> 4) & 1) * 32 + (lane & 3) * 8;
  for (int idx = jj; idx < total_items; idx += NJ) {
    __syncthreads();
    const int head = grp + 8 * (idx & 1);
    const int q0 = (idx >> 1) * 128, qend = min(q0 + 128, T);
    LDSP int* red = (LDSP int*)L;
    if (tid == 0) { red[0] = 0x7fffffff; red[1] = (int)0x80000000; red[2] = 0x7fffffff; red[3] = -1; }
    __syncthreads();
    if (tid < 128 && q0 + tid < qend) { const int id = seg[q0 + tid];
      __hip_atomic_fetch_min((LDSP int*)&red[0], id, __ATOMIC_RELAXED, __HIP_MEMORY_SCOPE_WORKGROUP);
      __hip_atomic_fetch_max((LDSP int*)&red[1], id, __ATOMIC_RELAXED, __HIP_MEMORY_SCOPE_WORKGROUP); }
    __syncthreads();
    const int idlo = red[0], idhi = red[1];
    int lo = 0x7fffffff, hi = -1;
    for (int i = tid; i < T; i += 512) { const int id = seg[i]; if (id >= idlo && id <= idhi) { lo = min(lo, i); hi = max(hi, i); } }
    __hip_atomic_fetch_min((LDSP int*)&red[2], lo, __ATOMIC_RELAXED, __HIP_MEMORY_SCOPE_WORKGROUP);
    __hip_atomic_fetch_max((LDSP int*)&red[3], hi, __ATOMIC_RELAXED, __HIP_MEMORY_SCOPE_WORKGROUP);
    __syncthreads();
    const int k0 = (int)rfl((unsigned)red[2]), kend = (int)rfl((unsigned)red[3]) + 1;
    __syncthreads();
    const int nk = (kend - k0 + 63) >> 6;
    const unsigned hoff = (unsigned)head * 256u;
#pragma unroll
    for (int i = 0; i < 2; ++i) {
      const unsigned m = (unsigned)w + 8u * i;
      const unsigned so = (unsigned)(q0 + 8 * (int)m) * ROWB + hoff;
      dma16(srdQ, voffK, so, ldsb + XREG + m * 2048u);
      dma16(srdQ, voffK, so + 128u, ldsb + XREG + m * 2048u + 1024u);
    }
    const int qb = w & 3, sub = w >> 2;
    const int qrow = q0 + 32 * qb + r32;
    const int segq = seg[qrow < T ? qrow : T - 1];
    bf16x8 qf[8];
    f32x16 o[4];
#pragma unroll
    for (int d = 0; d < 4; ++d) o[d] = f32x16{};
    float m_run = MINIT, l_run = 0.f;
    for (int t = 0; t < nk; ++t) {
      __syncthreads();
      {
        const unsigned so = (unsigned)(k0 + 64 * t + 8 * w) * ROWB + hoff;
        const unsigned d = ldsb + (unsigned)w * 2048u;
        dma16(srdK, voffK, so, d); dma16(srdK, voffK, so + 128u, d + 1024u);
        dma16(srdV, voffV, so, d + VOFF); dma16(srdV, voffV, so + 128u, d + VOFF + 1024u);
      }
      asm volatile("s_waitcnt vmcnt(0)" ::: "memory");
      __syncthreads();
      if (t == 0) {
#pragma unroll
        for (int s = 0; s < 8; ++s)
          qf[s] = *(const LDSP bf16x8*)(L + XREG + (unsigned)qb * 8192u + ((kb0 ^ ((s & 1) * 32u)) + 512u * (s >> 1)));
      }
      const int kbase = k0 + 64 * t + 32 * sub;
      const lds_cptr kp = (lds_cptr)L + (unsigned)sub * 8192u;
      f32x16 sc = f32x16{};
#pragma unroll
      for (int s = 0; s < 8; ++s) {
        const bf16x8 kf = *(const LDSP bf16x8*)(kp + ((kb0 ^ ((s & 1) * 32u)) + 512u * (s >> 1)));
        sc = MFMA(kf, qf[s], sc);
      }
#pragma unroll
      for (int r = 0; r < 16; ++r) {
        const int kk = kbase + (r & 3) + 8 * (r >> 2) + 4 * hh;
        const int id = seg[kk < T ? kk : T - 1];
        sc[r] = (kk < kend && id == segq) ? sc[r] : -__builtin_inff();
      }
      float a = MX3(sc[0], sc[1], sc[2]), b = MX3(sc[3], sc[4], sc[5]);
      a = MX3(a, sc[6], sc[7]); b = MX3(b, sc[8], sc[9]); a = MX3(a, sc[10], sc[11]); b = MX3(b, sc[12], sc[13]);
      a = MX3(a, sc[14], sc[15]);
      const float pm = swap_max(__builtin_fmaxf(a, b));
      const float mn = __builtin_fmaxf(m_run, pm);
      const float al = __builtin_amdgcn_exp2f((m_run - mn) * CEXP);
      m_run = mn; l_run *= al;
#pragma unroll
      for (int d = 0; d < 4; ++d)
#pragma unroll
        for (int r = 0; r < 16; ++r) o[d][r] *= al;
      const float nmc = -m_run * CEXP;
      float ps = 0.f;
#pragma unroll
      for (int r = 0; r < 16; ++r) { sc[r] = __builtin_amdgcn_exp2f(__builtin_fmaf(sc[r], CEXP, nmc)); ps += sc[r]; }
      l_run += ps;
      u32x4 pw0, pw1;
#pragma unroll
      for (int e = 0; e < 4; ++e) { pw0[e] = cvtpk(sc[2 * e], sc[2 * e + 1]); pw1[e] = cvtpk(sc[8 + 2 * e], sc[8 + 2 * e + 1]); }
      const bf16x8 p0 = __builtin_bit_cast(bf16x8, pw0), p1 = __builtin_bit_cast(bf16x8, pw1);
      const lds_cptr vp = kp + VOFF + vb0;
#pragma unroll
      for (int d = 0; d < 4; ++d) {
        const s16x4 a0 = vtr(vp + d * 512), a1 = vtr(vp + d * 512 + 2048), b0 = vtr(vp + d * 512 + 4096), b1 = vtr(vp + d * 512 + 6144);
        const bf16x8 va = __builtin_shufflevector(a0, a1, 0, 1, 2, 3, 4, 5, 6, 7), vb = __builtin_shufflevector(b0, b1, 0, 1, 2, 3, 4, 5, 6, 7);
        o[d] = MFMA(va, p0, o[d]);
        o[d] = MFMA(vb, p1, o[d]);
      }
    }
    l_run = swap_add(l_run);
    unsigned short* orow = O + (size_t)(qrow < T ? qrow : 0) * (NH * HD) + head * HD + 8 * hh;
    const bool row_ok = qrow < qend;
    const int kh = sub;
    __syncthreads();
    const unsigned xa = SLOT, xb = 2 * SLOT, xs = 3 * SLOT;
    const unsigned mine = (kh ? xb : xa) + (unsigned)qb * 8192u + (unsigned)lane * 16u;
    const unsigned theirs = (kh ? xa : xb) + (unsigned)qb * 8192u + (unsigned)lane * 16u;
#pragma unroll
    for (int dd = 0; dd < 2; ++dd)
#pragma unroll
      for (int g = 0; g < 4; ++g) {
        f32x4 v;
#pragma unroll
        for (int e = 0; e < 4; ++e) v[e] = kh ? o[dd][4 * g + e] : o[2 + dd][4 * g + e];
        *(LDSP f32x4*)(L + mine + (unsigned)(dd * 4 + g) * 1024u) = v;
      }
    if (hh == 0) { f32x2 s2 = {m_run, l_run}; *(LDSP f32x2*)(L + xs + (unsigned)w * 256u + (unsigned)r32 * 8u) = s2; }
    __syncthreads();
    const f32x2 o2 = *(const LDSP f32x2*)(L + xs + (unsigned)(w ^ 4) * 256u + (unsigned)r32 * 8u);
    const float mn = __builtin_fmaxf(m_run, o2[0]);
    const float wa = __builtin_amdgcn_exp2f((m_run - mn) * CEXP), wb = __builtin_amdgcn_exp2f((o2[0] - mn) * CEXP);
    const float inv = 1.0f / (wa * l_run + wb * o2[1]);
    const float fa = wa * inv, fb = wb * inv;
#pragma unroll
    for (int dd = 0; dd < 2; ++dd) {
      float r[16];
#pragma unroll
      for (int g = 0; g < 4; ++g) {
        const f32x4 v = *(const LDSP f32x4*)(L + theirs + (unsigned)(dd * 4 + g) * 1024u);
#pragma unroll
        for (int e = 0; e < 4; ++e) r[4 * g + e] = fa * (kh ? o[2 + dd][4 * g + e] : o[dd][4 * g + e]) + fb * v[e];
      }
#pragma unroll
      for (int g = 0; g < 4; g += 2) {
        unsigned ax = cvtpk(r[4 * g + 0], r[4 * g + 1]), ay = cvtpk(r[4 * g + 2], r[4 * g + 3]);
        unsigned bx = cvtpk(r[4 * g + 4], r[4 * g + 5]), by = cvtpk(r[4 * g + 6], r[4 * g + 7]);
        auto rx = __builtin_amdgcn_permlane32_swap(ax, bx, false, false); auto ry = __builtin_amdgcn_permlane32_swap(ay, by, false, false);
        const u32x4 v = {rx[0], ry[0], rx[1], ry[1]};
        if (row_ok) *(u32x4*)(orow + 64 * kh + 32 * dd + 8 * g) = v;
      }
    }
  }
}

#define DIAG 0
#define ABL 0
#define PF1 3
#define PF2 5
#define STR2(x) #x
#define STR(x) STR2(x)

template <int NIT>
__global__ __launch_bounds__(512, 2)
void ragged_attn_kernel(const void* Q, unsigned nQ, unsigned fQ, const void* K, unsigned nK, unsigned fK, const void* V, unsigned nV, unsigned fV,
                        const void* seg, unsigned nS, unsigned fS, void* O, unsigned nO, unsigned fO, int T, int lastc, int pfb, int pad, void* dbg) {
  __shared__ __attribute__((aligned(16))) char lds[LDS_BYTES];
  asm volatile(R"(
  .set NIT, %c3
  .set DIAG, )" STR(DIAG) R"(
  .set ABL, )" STR(ABL) R"(
  .set PF1, )" STR(PF1) R"(
  .set PF2, )" STR(PF2) R"(
  ; ---------------- SGPRs
  .set sQ, 8
  .set sK, 12
  .set sV, 16
  .set sS, 20
  .set sO, 24
  .set sT, 28
  .set sLASTC, 29
  .set sPFB, 30
  .set sF, 30
  .set sLIVE, 30
  .set sTM16, 31
  .set sNDF, 33
  .set sG, 34
  .set sR, 35
  .set sNBIG, 36
  .set sTOT, 37
  .set sHOFF, 38
  .set sQ0, 39
  .set sQEND, 40
  .set sK0, 41
  .set sNK, 42
  .set sLEN, 43
  .set sMB, 44
  .set sNS, 45
  .set sU, 46
  .set sNU, 47
  .set sKOFF, 48
  .set sKSTEP, 49
  .set sVOFF, 50
  .set sKSO, 51
  .set sVSO, 52
  .set sKDST, 53
  .set sVDST, 54
  .set sCNT, 55
  .set sND, 56
  .set sMSTEP, 57
  .set sPEND, 58
  .set sW, 59
  .set sA, 60
  .set sB, 62
  .set sC, 64
  .set sSAFE, 64
  .set sNF, 65
  .set sT1, 66
  .set sT2, 67
  .set sT3, 68
  .set sT4, 69
  .set sCE, 70
  .set sTHR, 71
  .set sWD, 72
  .set sQB, 73
  .set sKH, 74
  .set sLIM, 75
  .set sJ, 76
  .set sQR, 77
  .set sRT, 78
  .set sV2, 80
  .set sM, 84
  .set sMK, 86
  .set sMV, 87
  ; ---------------- VGPRs
  .set vLANE, 1
  .set vOK, 2
  .set vOK2, 3
  .set vOV, 4
  .set vOV2, 5
  .set vKB0, 6
  .set vKB1, 7
  .set vVB, 8
  .set vSTART, 9
  .set vLEN, 10
  .set vINCL, 11
  .set vEXCL, 12
  .set vM, 13
  .set vNMC, 14
  .set vL0, 15
  .set vL1, 16
  .set vKA0, 17
  .set vKA1, 18
  .set vVA, 19
  .set vAL, 20
  .set vSO, 21
  .set vHH4, 22
  .set vSW, 23
  .set vT0, 24
  .set vT1, 25
  .set vT2, 26
  .set vT3, 27
  .set vT4, 28
  .set vT5, 29
  .set vT6, 30
  .set vT7, 31
  .set vP, 32
  .set vX, 40
  .set vLS, 40
  .set vXO, 44
  .set vXP, 45
  .set vSTA, 46
  .set vSTP, 47
  .set vSGW, 48
  .set vSGR, 49
  .set vFA, 50
  .set vFB, 52
  .set vDG, 54
  .set vSR, 56
  .set vONE, 60
  .set vO, 64
  .set vQ, 128
  .set vSA, 160
  .set vSB, 176
  .set vK, 192
  .set vV, 224
  ; ---------------- LDS map: K ring 4 x 16 KiB | V ring 4 x 16 KiB | X (Q stage) 32 KiB
  .set LK, 0
  .set LV, 65536
  .set LX, 131072
  .set LQ2, 49152
  .set LQ3, 114688
  .set LSCR, 98304
  .set TILEB, 64*4096

  .macro STAMP i                                    ; diagnostic builds only: low word of the cycle counter -> lane i of vDG
    .if (DIAG == 1) || ((DIAG == 2) && ((\i == 0) || (\i >= 5)))
      s_memtime s[sM:sM+1]
      s_waitcnt lgkmcnt(0)
      v_writelane_b32 v[vDG], s[sM], \i
    .endif
  .endm
  .macro STAMPE i
    .if DIAG == 2
      s_memtime s[sM:sM+1]
      s_waitcnt lgkmcnt(0)
      v_writelane_b32 v[vDG], s[sM], \i
    .endif
  .endm

  ; ======================================================================== macros
  ; ---- DMA pieces (generic form).  sKDST / sVDST: ring offset + w*2048 of the next tile
  .macro DK1
    s_mov_b32 m0, s[sKDST]
    s_add_u32 s[sT1], s[sKDST], 16384
    buffer_load_dwordx4 v[vOK], s[sK:sK+3], s[sKSO] offen lds
  .endm
  .macro DK2
    s_add_u32 m0, s[sKDST], 1024
    s_and_b32 s[sKDST], s[sT1], 0xffff
    buffer_load_dwordx4 v[vOK2], s[sK:sK+3], s[sKSO] offen lds
    s_add_u32 s[sKSO], s[sKSO], TILEB
  .endm
  .macro DV1
    s_add_u32 m0, s[sVDST], LV
    s_add_u32 s[sT3], s[sVDST], 16384
    buffer_load_dwordx4 v[vOV], s[sV:sV+3], s[sVSO] offen lds
  .endm
  .macro DV2
    s_add_u32 m0, s[sVDST], LV+1024
    s_and_b32 s[sVDST], s[sT3], 0xffff
    buffer_load_dwordx4 v[vOV2], s[sV:sV+3], s[sVSO] offen lds
    s_add_u32 s[sVSO], s[sVSO], TILEB
  .endm
  .macro DALL
    DK1
    DK2
    DV1
    DV2
  .endm
  ; m: 0 this slot has a barrier, 1 barrier unless mode B (run time), 2 no barrier
  .macro BARX n, m
    .if \m == 1
      s_cmp_eq_u32 s[sMB], 1
      s_cbranch_scc1 .Lnob\@
    .endif
    .if \m == 2
      s_waitcnt lgkmcnt(0)
    .else
      s_waitcnt vmcnt(\n) lgkmcnt(0)
      s_barrier
    .endif
    .if \m == 1
      s_branch .Lbd\@
  .Lnob\@:
      s_waitcnt lgkmcnt(0)
  .Lbd\@:
    .endif
  .endm
  ; DMA piece k of a generic step.  dma: 0 none, 1 always, 2 run-time flag sF ; m as above
  .macro DMAP k, m, dma
    .if ((\dma) != 0) && ((\m) != 2) && ((ABL & 1) == 0)
      .if \dma == 2
        s_cmp_eq_u32 s[sF], 0
        s_cbranch_scc1 .Ld\@
      .endif
      .if \m == 1
        s_cmp_eq_u32 s[sMB], 1
        s_cbranch_scc1 .Ld\@
      .endif
      .if \k == 0
        DK1
      .elseif \k == 1
        DK2
      .elseif \k == 2
        DV1
      .else
        DV2
      .endif
  .Ld\@:
    .endif
  .endm
  .macro KREADO i, off
    .if (ABL & 8)
    .elseif (\i & 1)
      ds_read_b128 v[vK+4*\i:vK+4*\i+3], v[vKA1] offset:(\off)+512*(\i>>1)
    .else
      ds_read_b128 v[vK+4*\i:vK+4*\i+3], v[vKA0] offset:(\off)+512*(\i>>1)
    .endif
  .endm
  .macro KADV                                       ; generic form: advance the K read addresses to the next step
    s_add_u32 s[sKOFF], s[sKOFF], s[sKSTEP]
    s_and_b32 s[sKOFF], s[sKOFF], 0xffff
    v_add_u32 v[vKA0], s[sKOFF], v[vKB0]
    v_add_u32 v[vKA1], s[sKOFF], v[vKB1]
  .endm
  .macro VADDR                                      ; generic form: V read address of the coming step
    v_add_u32 v[vVA], s[sVOFF], v[vVB]
    s_add_u32 s[sVOFF], s[sVOFF], s[sKSTEP]
    s_and_b32 s[sVOFF], s[sVOFF], 0xffff
  .endm
  .macro QKM c, i
    .if \i == 0
      v_mfma_f32_32x32x16_bf16 v[\c:\c+15], v[vK:vK+3], v[vQ:vQ+3], 0
    .else
      v_mfma_f32_32x32x16_bf16 v[\c:\c+15], v[vK+4*\i:vK+4*\i+3], v[vQ+4*\i:vQ+4*\i+3], v[\c:\c+15]
    .endif
  .endm
  .macro VREADO j, off                              ; V fragment j: d = j&3, ks = j>>2
    .if (ABL & 4) == 0
    ds_read_b64_tr_b16 v[vV+4*\j:vV+4*\j+1], v[vVA] offset:(\off)+512*(\j&3)+4096*(\j>>2)
    ds_read_b64_tr_b16 v[vV+4*\j+2:vV+4*\j+3], v[vVA] offset:(\off)+512*(\j&3)+4096*(\j>>2)+2048
    .endif
  .endm
  .macro PVJ d, ks
    v_mfma_f32_32x32x16_bf16 v[vO+16*(\d):vO+16*(\d)+15], v[vV+4*((\d)+4*(\ks)):vV+4*((\d)+4*(\ks))+3], v[vP+4*(\ks):vP+4*(\ks)+3], v[vO+16*(\d):vO+16*(\d)+15]
  .endm
  .macro PVM j
    PVJ (\j&3), (\j>>2)
  .endm
  .macro LSUM ks                                    ; row sums of one packed P fragment
    .if (ABL & 64) == 0
    v_mfma_f32_16x16x32_bf16 v[vLS:vLS+3], v[vONE:vONE+3], v[vP+4*\ks:vP+4*\ks+3], v[vLS:vLS+3]
    .endif
  .endm
  .macro FMAE c, r
    .if (ABL & 2) == 0
    v_fma_f32 v[\c+(\r)], v[\c+(\r)], s[sCE], v[vNMC]
    .endif
  .endm
  .macro XPE c, r
    .if (ABL & 2) == 0
    v_exp_f32 v[\c+(\r)], v[\c+(\r)]
    .endif
  .endm
  .macro EXPE c, r
    FMAE \c, \r
    XPE \c, \r
  .endm
  .macro CVTE p, i                                  ; pack of elements 2i, 2i+1
    .if (ABL & 32) == 0
    v_cvt_pk_bf16_f32 v[vP+\i], v[\p+2*\i], v[\p+2*\i+1]
    .endif
  .endm
  .macro SUMPK p, i                                 ; SAFE form: row sums + pack of elements 2i, 2i+1
    v_add_f32 v[vL0], v[vL0], v[\p+2*\i]
    v_add_f32 v[vL1], v[vL1], v[\p+2*\i+1]
    v_cvt_pk_bf16_f32 v[vP+\i], v[\p+2*\i], v[\p+2*\i+1]
  .endm
  .macro MASK c                                     ; keys >= lim of this sub-tile -> -inf
    v_sub_u32 v[vT2], s[sLIM], v[vHH4]
    v_mov_b32 v[vT3], 0xff800000
    .set r, 0
    .rept 16
      v_cmp_lt_i32 vcc, (r&3)+8*(r>>2), v[vT2]
      s_nop 1
      v_cndmask_b32 v[\c+r], v[vT3], v[\c+r], vcc
      .set r, r+1
    .endr
  .endm
  .macro ROWMAX1 c
    v_max3_f32 v[vT2], v[\c+0], v[\c+1], v[\c+2]
    v_max3_f32 v[vT3], v[\c+3], v[\c+4], v[\c+5]
    v_max3_f32 v[vT2], v[vT2], v[\c+6], v[\c+7]
    v_max3_f32 v[vT3], v[vT3], v[\c+8], v[\c+9]
    v_max3_f32 v[vT2], v[vT2], v[\c+10], v[\c+11]
    v_max3_f32 v[vT3], v[vT3], v[\c+12], v[\c+13]
    v_max3_f32 v[vT2], v[vT2], v[\c+14], v[\c+15]
    v_max_f32 v[vT2], v[vT2], v[vT3]
    v_mov_b32 v[vT3], v[vT2]
  .endm
  .macro ROWMAX2                                    ; needs 2 wait states after ROWMAX1
    v_permlane32_swap_b32 v[vT2], v[vT3]
    s_nop 0
    v_max_f32 v[vT2], v[vT2], v[vT3]                ; vT2 = row max
  .endm
  .macro RESCALE
    s_nop 7
    s_nop 7
    .set i, 0
    .rept 64
      v_mul_f32 v[vO+i], v[vAL], v[vO+i]
      .set i, i+1
    .endr
    v_mul_f32 v[vL0], v[vAL], v[vL0]
    v_mul_f32 v[vL1], v[vAL], v[vL1]
    s_mov_b32 s[sPEND], 0
  .endm
  .macro SENDB d                                    ; d-block d (16 regs) -> own exchange area
    .set i, 0
    .rept 4
      ds_write_b128 v[vXO], v[vO+16*\d+4*i:vO+16*\d+4*i+3] offset:4096*(\d&1)+1024*i
      .set i, i+1
    .endr
  .endm
  .macro LASTV da, db, dc, dd                       ; last P.V: the blocks to send (da, db) first
    PVJ \da, 0
    PVJ \db, 0
    PVJ \da, 1
    PVJ \db, 1
    PVJ \dc, 0
    s_nop 7
    s_nop 1
    SENDB \da
    PVJ \dd, 0
    SENDB \db
    PVJ \dc, 1
    PVJ \dd, 1
    s_branch .Lepi_sent
  .endm

  ; ---- FAST step body (reference = row max of the wave's first sub-tile; overflow is detected on the row sums at the end of the item,
  ;      which is then redone with the SAFE steps).  prev enters with exp done for e0..e10.
  ;      gen = 1: generic addressing (address registers, generic DMA) ; gen = 0: mode A, ring position r as immediates
  .macro FDMA k, gen, r, m, dma
    .if \gen
      DMAP \k, \m, \dma
    .elseif (\dma) && ((ABL & 1) == 0)
      .if \k == 0
        s_add_u32 m0, s[sMK], 16384*(\r)
      .elseif \k == 1
        s_add_u32 m0, s[sMK], 16384*(\r)+1024
      .elseif \k == 2
        s_add_u32 m0, s[sMV], 16384*(\r)
      .else
        s_add_u32 m0, s[sMV], 16384*(\r)+1024
      .endif
    .endif
  .endm
  .macro FDMB k, gen, dma                            ; the load itself (mode A form), one instruction behind its M0 write
    .if ((\gen) == 0) && (\dma) && ((ABL & 1) == 0)
      .if \k == 0
        buffer_load_dwordx4 v[vOK], s[sK:sK+3], s[sKSO] offen lds
      .elseif \k == 1
        buffer_load_dwordx4 v[vOK2], s[sK:sK+3], s[sKSO] offen lds
      .elseif \k == 2
        buffer_load_dwordx4 v[vOV], s[sV2:sV2+3], s[sKSO] offen lds
      .else
        buffer_load_dwordx4 v[vOV2], s[sV2:sV2+3], s[sKSO] offen lds
        s_add_u32 s[sKSO], s[sKSO], TILEB
      .endif
    .endif
  .endm
  .macro FULLF cur, prev, gen, r, m
    .set KO, (1-\gen)*16384*(\r)
    .set VO, (1-\gen)*16384*(\r)
    BARX 4, \m
    ; ---------------- phase A
    QKM \cur, 0
    VREADO 0, VO
    EXPE \prev, 11
    QKM \cur, 1
    VREADO 1, VO
    EXPE \prev, 12
    CVTE \prev, 0
    QKM \cur, 2
    VREADO 2, VO
    FDMA 0, \gen, \r, \m, 1
    FMAE \prev, 13
    FDMB 0, \gen, 1
    XPE \prev, 13
    CVTE \prev, 1
    QKM \cur, 3
    VREADO 3, VO
    EXPE \prev, 14
    CVTE \prev, 2
    QKM \cur, 4
    VREADO 4, VO
    EXPE \prev, 15
    CVTE \prev, 3
    QKM \cur, 5
    VREADO 5, VO
    CVTE \prev, 4
    CVTE \prev, 5
    QKM \cur, 6
    VREADO 6, VO
    FDMA 1, \gen, \r, \m, 1
    CVTE \prev, 6
    FDMB 1, \gen, 1
    QKM \cur, 7
    VREADO 7, VO
    CVTE \prev, 7
    .if (\gen == 0) && (\r == 1)
      v_xor_b32 v[vVA], 0x8000, v[vVA]
    .endif
    ; ---------------- phase B
    s_waitcnt lgkmcnt(4)
    PVM 0
    KREADO 0, KO
    KREADO 1, KO
    FDMA 2, \gen, \r, \m, 1
    KREADO 2, KO
    FDMB 2, \gen, 1
    KREADO 3, KO
    s_nop 2
    PVM 1
    EXPE \cur, 0
    EXPE \cur, 1
    LSUM 0
    EXPE \cur, 2
    PVM 2
    KREADO 4, KO
    EXPE \cur, 3
    PVM 3
    KREADO 5, KO
    EXPE \cur, 4
    FMAE \cur, 5
    PVM 4
    KREADO 6, KO
    XPE \cur, 5
    EXPE \cur, 6
    PVM 5
    KREADO 7, KO
    EXPE \cur, 7
    LSUM 1
    EXPE \cur, 8
    s_waitcnt lgkmcnt(8)
    PVM 6
    FDMA 3, \gen, \r, \m, 1
    FMAE \cur, 9
    FDMB 3, \gen, 1
    XPE \cur, 9
    .if \gen
      KADV
      VADDR
    .elseif \r == 1
      v_xor_b32 v[vKA0], 0x8000, v[vKA0]
      v_xor_b32 v[vKA1], 0x8000, v[vKA1]
      s_xor_b32 s[sMK], s[sMK], 0x8000
      s_xor_b32 s[sMV], s[sMV], 0x8000
    .endif
    s_add_u32 s[sCNT], s[sCNT], 1
    PVM 7
    EXPE \cur, 10
  .endm
  .macro LASTF prev, gen, r, m
    .set VO, (1-\gen)*16384*(\r)
    .if \m == 2
    .else
      BARX 0, \m
    .endif
    .set g, 0
    .rept 8
      VREADO g, VO
      .if g < 5
        EXPE \prev, 11+g
      .endif
      .set g, g+1
    .endr
    s_mov_b32 s[sU], s[sNS]
    s_cmp_eq_i32 s[sMSTEP], -1
    s_cbranch_scc1 .Lnm\@
    ; partial sub-tile: keys >= lim get weight 0
    v_sub_u32 v[vT2], s[sLIM], v[vHH4]
    .set e, 0
    .rept 16
      v_cmp_lt_i32 vcc, (e&3)+8*(e>>2), v[vT2]
      s_nop 1
      v_cndmask_b32 v[\prev+e], 0, v[\prev+e], vcc
      .set e, e+1
    .endr
  .Lnm\@:
    .set g, 0
    .rept 8
      CVTE \prev, g
      .set g, g+1
    .endr
    s_cmp_eq_u32 s[sU], s[sNU]
    s_waitcnt lgkmcnt(0)
    LSUM 0
    LSUM 1
    .if \gen == 0                                    ; mode A, last slot: send the partner's half early
      s_cbranch_scc0 .Lplain\@
      s_cmp_eq_u32 s[sKH], 0
      s_cbranch_scc0 .Lhi\@
      LASTV 2, 3, 0, 1
  .Lhi\@:
      LASTV 0, 1, 2, 3
  .Lplain\@:
    .endif
    .set g, 0
    .rept 8
      PVM g
      .set g, g+1
    .endr
    s_branch .Lnone_tail
  .endm

  ; ---- SAFE step (online max every step), generic addressing, run-time mode
  .macro FULLS cur, prev, m
    s_cmp_lt_u32 s[sU], s[sND]
    s_cselect_b32 s[sF], 1, 0
    s_cbranch_scc0 .Lbn\@
    BARX 4, \m
    s_branch .Lbq\@
  .Lbn\@:
    BARX 0, \m
  .Lbq\@:
    .set g, 0
    .rept 8
      QKM \cur, g
      VREADO g, 0
      SUMPK \prev, g
      .if g == 1
        DMAP 0, \m, 2
      .elseif g == 5
        DMAP 1, \m, 2
      .endif
      .set g, g+1
    .endr
    s_waitcnt lgkmcnt(4)
    PVM 0
    KREADO 0, 0
    KREADO 1, 0
    s_add_u32 s[sU], s[sU], 1
    s_nop 1
    PVM 1
    s_cmp_lg_u32 s[sMSTEP], s[sU]
    s_cbranch_scc1 .Lnm\@
    MASK \cur
  .Lnm\@:
    ROWMAX1 \cur
    PVM 2
    KREADO 2, 0
    ROWMAX2
    v_sub_f32 v[vT3], v[vT2], v[vM]
    v_cmp_lt_f32 vcc, s[sTHR], v[vT3]
    s_cbranch_vccz .Lkeep\@
    v_max_f32 v[vT3], v[vM], v[vT2]
    v_sub_f32 v[vAL], v[vM], v[vT3]
    v_mul_f32 v[vAL], s[sCE], v[vAL]
    v_exp_f32 v[vAL], v[vAL]
    v_mov_b32 v[vM], v[vT3]
    v_mul_f32 v[vNMC], 0xbe0293ee, v[vM]
    s_mov_b32 s[sPEND], 1
  .Lkeep\@:
    PVM 3
    KREADO 3, 0
    DMAP 2, \m, 2
    EXPE \cur, 0
    EXPE \cur, 1
    EXPE \cur, 2
    PVM 4
    KREADO 4, 0
    EXPE \cur, 3
    EXPE \cur, 4
    EXPE \cur, 5
    PVM 5
    KREADO 5, 0
    EXPE \cur, 6
    EXPE \cur, 7
    EXPE \cur, 8
    s_waitcnt lgkmcnt(6)
    PVM 6
    KREADO 6, 0
    DMAP 3, \m, 2
    EXPE \cur, 9
    EXPE \cur, 10
    EXPE \cur, 11
    PVM 7
    KREADO 7, 0
    EXPE \cur, 12
    EXPE \cur, 13
    EXPE \cur, 14
    EXPE \cur, 15
    KADV
    VADDR
    s_cmp_eq_u32 s[sPEND], 0
    s_cbranch_scc1 .Lsend\@
    RESCALE
  .Lsend\@:
    s_cmp_eq_u32 s[sU], s[sNF]
  .endm
  .macro LASTS prev, m
    .if \m == 1
      s_cmp_eq_u32 s[sMB], 1
      s_cbranch_scc1 .Lnob\@
    .endif
    s_waitcnt vmcnt(0) lgkmcnt(0)
    s_barrier
  .Lnob\@:
    .set g, 0
    .rept 8
      VREADO g, 0
      SUMPK \prev, g
      .set g, g+1
    .endr
    s_add_u32 s[sU], s[sU], 1
    s_waitcnt lgkmcnt(0)
    .set g, 0
    .rept 8
      PVM g
      .set g, g+1
    .endr
    s_branch .Lnone_tail
  .endm

  ; ---- epilogue pieces
  ; 32 f32 at vO+b.. (one 64-wide half) -> bf16 -> LDS stage (row-major 128 B rows, 16-B chunks xor row) -> 4 full-line stores
  ; t: 16 dead registers (vO+t..) ; vSGW / vSGR: stage addresses ; sT3 = global offset of row 0 of this half
  .macro OUTHALF b, t
    .set q, 0
    .rept 4
      v_cvt_pk_bf16_f32 v[vO+\t+4*q+0], v[vO+\b+8*q+0], v[vO+\b+8*q+1]
      v_cvt_pk_bf16_f32 v[vO+\t+4*q+1], v[vO+\b+8*q+2], v[vO+\b+8*q+3]
      v_cvt_pk_bf16_f32 v[vO+\t+4*q+2], v[vO+\b+8*q+4], v[vO+\b+8*q+5]
      v_cvt_pk_bf16_f32 v[vO+\t+4*q+3], v[vO+\b+8*q+6], v[vO+\b+8*q+7]
      v_xor_b32 v[vT0+q], q*32, v[vSGW]
      .set q, q+1
    .endr
    .set q, 0
    .rept 4
      v_permlane32_swap_b32 v[vO+\t+4*q+0], v[vO+\t+4*q+2]
      v_permlane32_swap_b32 v[vO+\t+4*q+1], v[vO+\t+4*q+3]
      .set q, q+1
    .endr
    .set q, 0
    .rept 4
      ds_write_b128 v[vT0+q], v[vO+\t+4*q:vO+\t+4*q+3]
      .set q, q+1
    .endr
    s_add_u32 s[sT4], s[sT3], 8*4096
    s_add_u32 s[sT1], s[sT3], 16*4096
    s_add_u32 s[sT2], s[sT3], 24*4096
    s_waitcnt lgkmcnt(0)
    STAMPE 4
    ds_read_b128 v[vO+\t+0:vO+\t+3], v[vSGR]
    ds_read_b128 v[vO+\t+4:vO+\t+7], v[vSGR] offset:1024
    ds_read_b128 v[vO+\t+8:vO+\t+11], v[vSGR] offset:2048
    ds_read_b128 v[vO+\t+12:vO+\t+15], v[vSGR] offset:3072
    s_waitcnt lgkmcnt(3)
    buffer_store_dwordx4 v[vO+\t+0:vO+\t+3], v[vSO], s[sO:sO+3], s[sT3] offen
    s_waitcnt lgkmcnt(2)
    buffer_store_dwordx4 v[vO+\t+4:vO+\t+7], v[vSO], s[sO:sO+3], s[sT4] offen
    s_waitcnt lgkmcnt(1)
    buffer_store_dwordx4 v[vO+\t+8:vO+\t+11], v[vSO], s[sO:sO+3], s[sT1] offen
    s_waitcnt lgkmcnt(0)
    buffer_store_dwordx4 v[vO+\t+12:vO+\t+15], v[vSO], s[sO:sO+3], s[sT2] offen
  .endm
  .macro MERGE kb, rb                               ; kb: kept 32 regs, rb: 32 regs receiving the partner's half
    ds_read_b64 v[vT4:vT5], v[vSTP]
    .set i, 0
    .rept 8
      ds_read_b128 v[vO+\rb+4*i:vO+\rb+4*i+3], v[vXP] offset:1024*i
      .set i, i+1
    .endr
    s_waitcnt lgkmcnt(8)
    v_max_f32 v[vT6], v[vM], v[vT4]
    v_sub_f32 v[vFA], v[vM], v[vT6]
    v_sub_f32 v[vFB], v[vT4], v[vT6]
    v_mul_f32 v[vFA], s[sCE], v[vFA]
    v_mul_f32 v[vFB], s[sCE], v[vFB]
    v_exp_f32 v[vFA], v[vFA]
    v_exp_f32 v[vFB], v[vFB]
    s_nop 0
    v_mul_f32 v[vT6], v[vFA], v[vL0]
    v_fma_f32 v[vT6], v[vFB], v[vT5], v[vT6]
    v_rcp_f32 v[vT6], v[vT6]
    s_nop 0
    v_mul_f32 v[vFA], v[vFA], v[vT6]
    v_mul_f32 v[vFB], v[vFB], v[vT6]
    STAMPE 2
    .set i, 0
    .rept 16
      v_pk_mul_f32 v[vO+\kb+2*i:vO+\kb+2*i+1], v[vFA:vFA+1], v[vO+\kb+2*i:vO+\kb+2*i+1] op_sel_hi:[0,1]
      .set i, i+1
    .endr
    .set i, 0
    .rept 8
      .if i == 0
        s_waitcnt lgkmcnt(7)
      .elseif i == 1
        s_waitcnt lgkmcnt(6)
      .elseif i == 2
        s_waitcnt lgkmcnt(5)
      .elseif i == 3
        s_waitcnt lgkmcnt(4)
      .elseif i == 4
        s_waitcnt lgkmcnt(3)
      .elseif i == 5
        s_waitcnt lgkmcnt(2)
      .elseif i == 6
        s_waitcnt lgkmcnt(1)
      .else
        s_waitcnt lgkmcnt(0)
      .endif
      v_pk_fma_f32 v[vO+\kb+4*i:vO+\kb+4*i+1], v[vFB:vFB+1], v[vO+\rb+4*i:vO+\rb+4*i+1], v[vO+\kb+4*i:vO+\kb+4*i+1] op_sel_hi:[0,1,1]
      v_pk_fma_f32 v[vO+\kb+4*i+2:vO+\kb+4*i+3], v[vFB:vFB+1], v[vO+\rb+4*i+2:vO+\rb+4*i+3], v[vO+\kb+4*i+2:vO+\kb+4*i+3] op_sel_hi:[0,1,1]
      .set i, i+1
    .endr
    STAMPE 3
    OUTHALF \kb, \rb
  .endm
  .macro ROWSUM                                     ; complete row sum of this wave's keys -> vL0 (all lanes)
    s_cmp_eq_u32 s[sSAFE], 1
    s_cbranch_scc1 .Lss\@
    v_permlane16_swap_b32 v[vLS], v[vLS+1]
    s_nop 0
    v_mov_b32 v[vL0], v[vLS]
    s_nop 1
    v_permlane32_swap_b32 v[vL0], v[vLS]
    s_branch .Lsd\@
  .Lss\@:
    v_add_f32 v[vL0], v[vL0], v[vL1]
    v_mov_b32 v[vL1], v[vL0]
    s_nop 1
    v_permlane32_swap_b32 v[vL0], v[vL1]
    s_nop 0
    v_add_f32 v[vL0], v[vL0], v[vL1]
  .Lsd\@:
  .endm
  .macro PREFETCH lo, hi                            ; Q prefetch into L2, instructions lo..hi-1 (16 rows each)
    .set n, \lo
    .rept \hi-\lo
      s_mul_i32 s[sT3], s[sJ], s[sPFB]
      s_add_u32 s[sT3], s[sT3], n*16*4096
      s_min_u32 s[sT3], s[sT3], s[sTM16]
      buffer_load_dwordx4 v[vV+4*n:vV+4*n+3], v[vV+28], s[sQ:sQ+3], s[sT3] offen
      .set n, n+1
    .endr
  .endm

  ; ======================================================================== entry
  s_load_dwordx16 s[8:23], %0, 0
  s_load_dwordx8 s[24:31], %0, 64
  .if DIAG
    s_memrealtime s[sRT:sRT+1]
    v_mov_b32 v[vDG], 0
  .endif
  STAMP 0
  v_and_b32 v[vLANE], 63, %2
  v_readfirstlane_b32 s[sW], %2
  v_lshlrev_b32 v[vT0], 2, %2                     ; tid*4
  s_and_b32 s[sG], %1, 7
  s_lshr_b32 s[sJ], %1, 3
  s_lshr_b32 s[sW], s[sW], 6
  v_add_u32 v[vT7], 2048*(NIT-1), v[vT0]
  ; prefetch offset: (tid>>5)*4096 + ((tid>>4)&1)*2048 + ((tid>>3)&1)*128 + (tid&7)*16 + g*256
  v_lshrrev_b32 v[vT6], 5, %2
  v_lshlrev_b32 v[vT6], 12, v[vT6]
  v_bfe_u32 v[vT5], %2, 4, 1
  v_lshl_add_u32 v[vT6], v[vT5], 11, v[vT6]
  v_bfe_u32 v[vT5], %2, 3, 1
  v_lshl_add_u32 v[vT6], v[vT5], 7, v[vT6]
  v_and_b32 v[vT5], 7, %2
  v_lshl_add_u32 v[vT6], v[vT5], 4, v[vT6]
  s_lshl_b32 s[sT3], s[sG], 8
  v_add_u32 v[vV+28], s[sT3], v[vT6]
  ; DMA lane offsets
  v_bfe_u32 v[vT1], v[vLANE], 2, 3                ; prow
  v_lshrrev_b32 v[vT2], 5, v[vLANE]               ; hh / pcb
  v_and_b32 v[vT3], 3, v[vLANE]                   ; pc
  v_lshlrev_b32 v[vT4], 12, v[vT1]
  v_lshl_add_u32 v[vT4], v[vT2], 6, v[vT4]        ; prow*4096 + pcb*64
  v_lshl_add_u32 v[vOV], v[vT3], 4, v[vT4]
  v_bfe_u32 v[vT5], v[vLANE], 4, 1
  s_and_b32 s[sT4], s[sW], 1
  s_lshl_b32 s[sT4], s[sT4], 1
  v_or_b32 v[vT5], s[sT4], v[vT5]                 ; x
  v_xor_b32 v[vT5], v[vT5], v[vT3]
  v_lshl_add_u32 v[vOK], v[vT5], 4, v[vT4]
  s_waitcnt lgkmcnt(0)
  STAMP 1
  ; ======================================================================== S1: segment ends per 64-token chunk
  s_lshl_b32 s[sT2], s[sT], 2
  s_sub_u32 s[sT2], s[sT2], 8                     ; (T-2)*4
  v_min_u32 v[vT7], s[sT2], v[vT7]                ; last chunk: clamped
  .set i, 0
  .rept NIT
    .if i == NIT-1
      buffer_load_dwordx2 v[vX+2*i:vX+2*i+1], v[vT7], s[sS:sS+3], 0 offen
    .elseif i < 2
      buffer_load_dwordx2 v[vX+2*i:vX+2*i+1], v[vT0], s[sS:sS+3], 0 offen offset:2048*i
    .else
      s_movk_i32 s[sT1], 2048*i
      buffer_load_dwordx2 v[vX+2*i:vX+2*i+1], v[vT0], s[sS:sS+3], s[sT1] offen
    .endif
    .set i, i+1
  .endr
  s_sub_u32 s[sTM16], s[sT], 16
  s_lshl_b32 s[sTM16], s[sTM16], 12               ; (T-16)*4096
  ; V descriptor shifted by two tiles: V(u+2) is fetched with the source offset of K(u+4)
  s_sub_u32 s[sV2], s[sV], 2*TILEB
  s_subb_u32 s[sV2+1], s[sV+1], 0
  s_mov_b32 s[sV2+3], s[sV+3]
  ; remaining lane constants (in the shadow of the loads)
  v_add_u32 v[vOK2], 128, v[vOK]
  v_add_u32 v[vOV2], 128, v[vOV]
  v_and_b32 v[vT1], 31, v[vLANE]                  ; r32
  v_lshrrev_b32 v[vT3], 3, v[vT1]
  v_lshlrev_b32 v[vT3], 11, v[vT3]
  v_and_b32 v[vT4], 7, v[vT1]
  v_lshl_add_u32 v[vT3], v[vT4], 6, v[vT3]
  v_bfe_u32 v[vT4], v[vT1], 2, 2
  v_xor_b32 v[vT4], v[vT4], v[vT2]
  v_lshl_add_u32 v[vKB0], v[vT4], 4, v[vT3]
  v_xor_b32 v[vKB1], 32, v[vKB0]
  v_bfe_u32 v[vT3], v[vLANE], 2, 2
  v_lshl_add_u32 v[vT3], v[vT2], 2, v[vT3]
  v_lshlrev_b32 v[vT3], 6, v[vT3]
  v_bfe_u32 v[vT4], v[vLANE], 4, 1
  v_lshl_add_u32 v[vT3], v[vT4], 5, v[vT3]
  v_and_b32 v[vT4], 3, v[vLANE]
  v_lshl_add_u32 v[vVB], v[vT4], 3, v[vT3]
  v_add_u32 v[vVB], LV, v[vVB]
  v_lshlrev_b32 v[vHH4], 2, v[vT2]
  ; output staging: write address r32*128 + ((r32&7)^hh)*16 (chunk pair p: xor p*32)
  v_and_b32 v[vT3], 7, v[vT1]
  v_xor_b32 v[vT3], v[vT3], v[vT2]
  v_lshlrev_b32 v[vT3], 4, v[vT3]
  v_lshl_add_u32 v[vSW], v[vT1], 7, v[vT3]
  ; read address: row = lane>>3, chunk = (lane&7) ^ row ; rows + 8i -> + 1024 i
  v_lshrrev_b32 v[vT3], 3, v[vLANE]
  v_and_b32 v[vT4], 7, v[vLANE]
  v_xor_b32 v[vT5], v[vT4], v[vT3]
  v_lshlrev_b32 v[vT5], 4, v[vT5]
  v_lshl_add_u32 v[vSR], v[vT3], 7, v[vT5]
  ; global store lane offset: (lane>>3)*4096 + (lane&7)*16
  v_lshlrev_b32 v[vT4], 4, v[vT4]
  v_lshl_add_u32 v[vSO], v[vT3], 12, v[vT4]
  ; row-sum MFMA A operand: row (lane&15) 0 takes k-groups 0,2 ; row 1 takes k-groups 1,3
  v_and_b32 v[vT3], 15, v[vLANE]
  v_bfe_u32 v[vT4], v[vLANE], 4, 1
  v_cmp_eq_u32 vcc, v[vT3], v[vT4]
  v_mov_b32 v[vT5], 0x3f803f80
  s_mov_b32 s[sCE], 0x3e0293ee
  v_cndmask_b32 v[vONE], 0, v[vT5], vcc
  v_cndmask_b32 v[vONE+1], 0, v[vT5], vcc
  v_cndmask_b32 v[vONE+2], 0, v[vT5], vcc
  v_cndmask_b32 v[vONE+3], 0, v[vT5], vcc
  s_mov_b32 s[sTHR], 0x427af232
  s_lshl_b32 s[sWD], s[sW], 11                    ; w*2048
  s_mov_b64 s[sC:sC+1], 0
  v_mov_b32 v[vT4], 0
  v_mov_b32 v[vT5], 0
  v_lshlrev_b32 v[vT7], 7, v[vLANE]               ; LDS address of chunk (w + 8*lane): 16 B each
  s_lshl_b32 s[sT1], s[sW], 4
  s_add_u32 s[sT1], s[sT1], LSCR
  v_add_u32 v[vT7], s[sT1], v[vT7]
  PREFETCH 0, PF1
  .set i, 0
  .rept NIT
    s_waitcnt vmcnt(NIT-1-i+PF1)
    v_cmp_ne_u32 s[sA:sA+1], v[vX+2*i], v[vX+2*i+1]
    v_cmp_gt_i32 vcc, v[vX+2*i], v[vX+2*i+1]
    .if i == NIT-1
      v_cmp_gt_i32 s[sB:sB+1], s[sLASTC], %2       ; element < T-1
      s_and_b64 s[sA:sA+1], s[sA:sA+1], s[sB:sB+1]
      s_and_b64 vcc, vcc, s[sB:sB+1]
      v_cmp_eq_u32 s[sB:sB+1], s[sLASTC], %2       ; element == T-1
      s_or_b64 s[sA:sA+1], s[sA:sA+1], s[sB:sB+1]
    .endif
    s_or_b64 s[sC:sC+1], s[sC:sC+1], vcc
    s_nop 0
    v_writelane_b32 v[vT4], s[sA], i
    v_writelane_b32 v[vT5], s[sA+1], i
    .set i, i+1
  .endr
  s_or_b32 s[sT1], s[sC], s[sC+1]
  v_mov_b32 v[vT6], s[sT1]
  s_mov_b64 exec, (1<<NIT)-1
  ds_write_b96 v[vT7], v[vT4:vT6]
  s_mov_b64 exec, -1
  v_mov_b32 v[vT0], 0
  v_mov_b32 v[vT1], 0
  v_mov_b32 v[vT2], 0
  v_lshlrev_b32 v[vT3], 4, v[vLANE]
  v_add_u32 v[vT3], LSCR, v[vT3]
  v_lshlrev_b32 v[vT7], 6, v[vLANE]
  v_add_u32 v[vT7], 1, v[vT7]                     ; lane*64 + 1
  PREFETCH PF1, PF2
  s_waitcnt lgkmcnt(0)
  s_barrier
  STAMP 2
  ; ======================================================================== S2
  s_bfm_b64 exec, 8*NIT, 0
  ds_read_b96 v[vT0:vT2], v[vT3]
  s_mov_b64 exec, -1
  s_movk_i32 s[sT1], 128
  s_movk_i32 s[sT2], 256
  s_mov_b32 s[sR], 0
  s_waitcnt lgkmcnt(0)
  v_cmp_ne_u32 vcc, 0, v[vT2]
  v_bcnt_u32_b32 v[vT3], v[vT0], 0
  v_bcnt_u32_b32 v[vT3], v[vT1], v[vT3]
  s_cbranch_vccnz .Lfallback
  v_cmp_lt_u32 vcc, 1, v[vT3]
  v_ffbl_b32 v[vT4], v[vT0]
  v_ffbl_b32 v[vT5], v[vT1]
  s_cbranch_vccnz .Lfallback
  v_cmp_ne_u32 s[sA:sA+1], 0, v[vT3]
  v_add_u32 v[vT5], 32, v[vT5]
  v_min_u32 v[vT4], v[vT4], v[vT5]
  v_add_u32 v[vT4], v[vT4], v[vT7]
  v_cndmask_b32 v[vT4], 0, v[vT4], s[sA:sA+1]     ; end (0 if none)
  s_nop 1
  v_mov_b32_dpp v[vSTART], v[vT4] wave_shr:1 row_mask:0xf bank_mask:0xf bound_ctrl:0
  s_nop 1
  v_max_u32_dpp v[vSTART], v[vSTART], v[vSTART] row_shr:1 row_mask:0xf bank_mask:0xf bound_ctrl:0
  s_nop 1
  v_max_u32_dpp v[vSTART], v[vSTART], v[vSTART] row_shr:2 row_mask:0xf bank_mask:0xf bound_ctrl:0
  s_nop 1
  v_max_u32_dpp v[vSTART], v[vSTART], v[vSTART] row_shr:4 row_mask:0xf bank_mask:0xf bound_ctrl:0
  s_nop 1
  v_max_u32_dpp v[vSTART], v[vSTART], v[vSTART] row_shr:8 row_mask:0xf bank_mask:0xf bound_ctrl:0
  s_nop 1
  v_max_u32_dpp v[vSTART], v[vSTART], v[vSTART] row_bcast:15 row_mask:0xa bank_mask:0xf
  s_nop 1
  v_max_u32_dpp v[vSTART], v[vSTART], v[vSTART] row_bcast:31 row_mask:0xc bank_mask:0xf
  s_nop 0
  v_sub_u32 v[vLEN], v[vT4], v[vSTART]
  v_cndmask_b32 v[vLEN], 0, v[vLEN], s[sA:sA+1]   ; len (0 if none)
  v_add_u32 v[vT5], 127, v[vLEN]
  v_lshrrev_b32 v[vT5], 7, v[vT5]                 ; nq (mode A)
  v_cmp_lt_u32 vcc, s[sT1], v[vLEN]               ; len > 128
  v_cmp_ge_u32 s[sB:sB+1], s[sT2], v[vLEN]        ; len <= 256
  s_and_b64 s[sB:sB+1], s[sB:sB+1], vcc           ; mode B
  v_lshlrev_b32 v[vT5], 1, v[vT5]
  s_nop 0
  v_cndmask_b32 v[vT5], v[vT5], 2, s[sB:sB+1]     ; items (2 heads)
  v_lshlrev_b32 v[vT6], 16, v[vT5]
  v_cndmask_b32 v[vINCL], v[vT6], v[vT5], vcc     ; small -> high half
  v_mov_b32 v[vEXCL], v[vINCL]
  s_nop 0
  v_add_u32_dpp v[vINCL], v[vINCL], v[vINCL] row_shr:1 row_mask:0xf bank_mask:0xf bound_ctrl:0
  s_nop 1
  v_add_u32_dpp v[vINCL], v[vINCL], v[vINCL] row_shr:2 row_mask:0xf bank_mask:0xf bound_ctrl:0
  s_nop 1
  v_add_u32_dpp v[vINCL], v[vINCL], v[vINCL] row_shr:4 row_mask:0xf bank_mask:0xf bound_ctrl:0
  s_nop 1
  v_add_u32_dpp v[vINCL], v[vINCL], v[vINCL] row_shr:8 row_mask:0xf bank_mask:0xf bound_ctrl:0
  s_nop 1
  v_add_u32_dpp v[vINCL], v[vINCL], v[vINCL] row_bcast:15 row_mask:0xa bank_mask:0xf
  s_nop 1
  v_add_u32_dpp v[vINCL], v[vINCL], v[vINCL] row_bcast:31 row_mask:0xc bank_mask:0xf
  s_nop 0
  v_sub_u32 v[vEXCL], v[vINCL], v[vEXCL]
  v_readlane_b32 s[sT1], v[vINCL], 63
  s_and_b32 s[sNBIG], s[sT1], 0xffff
  s_lshr_b32 s[sTOT], s[sT1], 16
  s_add_u32 s[sTOT], s[sTOT], s[sNBIG]
  s_mov_b32 s[sSAFE], 0
  STAMP 3

.Litem:
  ; idx = r*32 + (r odd ? 31-j : j)
  s_lshl_b32 s[sT1], s[sR], 5
  s_sub_u32 s[sT2], 31, s[sJ]
  s_bitcmp1_b32 s[sR], 0
  s_cselect_b32 s[sT2], s[sT2], s[sJ]
  s_add_u32 s[sT1], s[sT1], s[sT2]                ; idx
  s_cmp_ge_u32 s[sT1], s[sTOT]
  s_cbranch_scc1 .Ldone
  s_sub_u32 s[sT2], s[sT1], s[sNBIG]
  s_cmp_lt_u32 s[sT1], s[sNBIG]
  s_cselect_b32 s[sT1], s[sT1], s[sT2]            ; index inside its class
  s_cselect_b32 s[sT2], 0, 16                     ; field shift
  v_bfe_u32 v[vT0], v[vINCL], s[sT2], 16
  v_bfe_u32 v[vT1], v[vEXCL], s[sT2], 16
  v_cmp_lt_u32 s[sA:sA+1], s[sT1], v[vT0]
  v_cmp_ge_u32 vcc, s[sT1], v[vT1]
  s_and_b64 s[sA:sA+1], s[sA:sA+1], vcc
  s_ff1_i32_b64 s[sT2], s[sA:sA+1]
  v_readlane_b32 s[sK0], v[vSTART], s[sT2]
  v_readlane_b32 s[sLEN], v[vLEN], s[sT2]
  v_readlane_b32 s[sT3], v[vT1], s[sT2]
  s_sub_u32 s[sT1], s[sT1], s[sT3]                ; local item
  s_and_b32 s[sT2], s[sT1], 1
  s_lshl_b32 s[sT2], s[sT2], 3
  s_add_u32 s[sT2], s[sT2], s[sG]
  s_lshl_b32 s[sHOFF], s[sT2], 8                  ; head*256 bytes
  s_lshr_b32 s[sT1], s[sT1], 1
  s_lshl_b32 s[sT1], s[sT1], 7
  s_add_u32 s[sQ0], s[sK0], s[sT1]
  s_lshl_b32 s[sT1], s[sW], 3
  s_add_u32 s[sT2], s[sQ0], s[sT1]
  s_lshl_b32 s[sT2], s[sT2], 12
  s_add_u32 s[sT2], s[sT2], s[sHOFF]              ; (q0 + 8w)*4096 + hoff
  ; ---- Q DMA
  s_add_u32 m0, s[sWD], LX
  s_add_u32 s[sT3], s[sT2], 64*4096
  buffer_load_dwordx4 v[vOK], s[sQ:sQ+3], s[sT2] offen lds
  s_add_u32 m0, s[sWD], LX+1024
  s_add_u32 s[sT4], s[sT2], 128*4096
  buffer_load_dwordx4 v[vOK2], s[sQ:sQ+3], s[sT2] offen lds
  s_add_u32 m0, s[sWD], LX+16384
  s_add_u32 s[sT1], s[sK0], s[sT1]
  buffer_load_dwordx4 v[vOK], s[sQ:sQ+3], s[sT3] offen lds
  s_add_u32 m0, s[sWD], LX+16384+1024
  s_lshl_b32 s[sT1], s[sT1], 12
  buffer_load_dwordx4 v[vOK2], s[sQ:sQ+3], s[sT3] offen lds
  s_add_u32 s[sKSO], s[sT1], s[sHOFF]             ; (k0 + 8w)*4096 + hoff
  s_mov_b32 s[sVSO], s[sKSO]
  s_add_u32 s[sQEND], s[sK0], s[sLEN]
  s_add_u32 s[sNK], s[sLEN], 63
  s_lshr_b32 s[sNK], s[sNK], 6
  s_lshl_b32 s[sO+2], s[sQEND], 12                ; the output, K and V descriptors end at the segment end
  s_mov_b32 s[sK+2], s[sO+2]
  s_mov_b32 s[sV+2], s[sO+2]
  s_add_u32 s[sV2+2], s[sO+2], 2*TILEB
  s_mov_b32 s[sKDST], s[sWD]
  s_mov_b32 s[sVDST], s[sWD]
  s_sub_u32 s[sT1], s[sLEN], 129
  s_cmp_lt_u32 s[sT1], 128
  s_cselect_b32 s[sMB], 1, 0                      ; mode B: 128 < len <= 256
  s_cbranch_scc0 .Lq_done
  s_add_u32 m0, s[sWD], LQ2
  s_add_u32 s[sT3], s[sT4], 64*4096
  buffer_load_dwordx4 v[vOK], s[sQ:sQ+3], s[sT4] offen lds
  s_add_u32 m0, s[sWD], LQ2+1024
  s_nop 0
  buffer_load_dwordx4 v[vOK2], s[sQ:sQ+3], s[sT4] offen lds
  s_add_u32 m0, s[sWD], LQ3
  s_nop 0
  buffer_load_dwordx4 v[vOK], s[sQ:sQ+3], s[sT3] offen lds
  s_add_u32 m0, s[sWD], LQ3+1024
  s_nop 0
  buffer_load_dwordx4 v[vOK2], s[sQ:sQ+3], s[sT3] offen lds
.Lq_done:
  DK1
  DK2
  STAMP 4
  ; ---- per-wave geometry
  s_and_b32 s[sT1], s[sW], 3
  s_lshr_b32 s[sT2], s[sW], 2
  s_cmp_eq_u32 s[sMB], 1
  s_cselect_b32 s[sQB], s[sW], s[sT1]             ; q block
  s_cselect_b32 s[sKH], 0, s[sT2]                 ; key half (mode A)
  s_movk_i32 s[sKSTEP], 0x4000
  s_cselect_b32 s[sKSTEP], 8192, s[sKSTEP]
  s_add_u32 s[sT1], s[sLEN], 31
  s_lshr_b32 s[sT1], s[sT1], 5                    ; nst = ceil(len/32)
  s_add_u32 s[sT2], s[sT1], 1
  s_sub_u32 s[sT2], s[sT2], s[sKH]
  s_lshr_b32 s[sT2], s[sT2], 1
  s_cmp_eq_u32 s[sMB], 1
  s_cselect_b32 s[sNS], s[sT1], s[sT2]            ; steps of this wave
  s_cselect_b32 s[sNU], s[sT1], s[sNK]            ; step slots
  s_and_b32 s[sLIM], s[sLEN], 31
  s_sub_u32 s[sT2], s[sT1], 1
  s_and_b32 s[sT2], s[sT2], 1
  s_cmp_eq_u32 s[sT2], s[sKH]
  s_cselect_b32 s[sT3], 1, s[sMB]                 ; owner of the last sub-tile
  s_cmp_lg_u32 s[sLIM], 0
  s_cselect_b32 s[sT3], s[sT3], 0
  s_sub_u32 s[sMSTEP], s[sNS], 1
  s_cmp_eq_u32 s[sT3], 1
  s_cselect_b32 s[sMSTEP], s[sMSTEP], -1
  s_lshl_b32 s[sT1], s[sQB], 5
  s_add_u32 s[sQR], s[sT1], s[sQ0]                ; first q row of this wave
  s_cmp_lt_u32 s[sQR], s[sQEND]
  s_cselect_b32 s[sNS], s[sNS], 0
  ; slots with DMA: tile iteration t <= nk-3
  s_sub_i32 s[sND], s[sNK], 2
  s_max_i32 s[sND], s[sND], 0
  s_lshl_b32 s[sND], s[sND], s[sMB]
  s_sub_u32 s[sNF], s[sNS], 1                     ; FULL slots of this wave
  s_sub_u32 s[sCNT], 0, s[sNF]
  s_mov_b32 s[sMK], s[sWD]
  s_add_u32 s[sMV], s[sWD], LV+0x8000
  ; Q read base
  s_lshl_b32 s[sT1], s[sQB], 13
  s_add_u32 s[sT2], s[sT1], LX                    ; qb < 4
  s_add_u32 s[sT3], s[sT1], LQ2-4*8192            ; qb 4,5
  s_add_u32 s[sT4], s[sT1], LQ3-6*8192            ; qb 6,7
  s_cmp_lt_u32 s[sQB], 6
  s_cselect_b32 s[sT3], s[sT3], s[sT4]
  s_cmp_lt_u32 s[sQB], 4
  s_cselect_b32 s[sT2], s[sT2], s[sT3]
  v_add_u32 v[vT0], s[sT2], v[vKB0]
  v_add_u32 v[vT1], s[sT2], v[vKB1]
  s_lshl_b32 s[sKOFF], s[sKH], 13
  s_mov_b32 s[sVOFF], s[sKOFF]
  v_add_u32 v[vKA0], s[sKOFF], v[vKB0]
  v_add_u32 v[vKA1], s[sKOFF], v[vKB1]
  VADDR
  ; epilogue addresses
  v_lshlrev_b32 v[vT2], 4, v[vLANE]
  s_lshl_b32 s[sT1], s[sW], 13
  s_xor_b32 s[sT2], s[sT1], 4*8192
  v_add_u32 v[vXP], s[sT2], v[vT2]                ; partner's exchange area
  v_add_u32 v[vXO], s[sT1], v[vT2]                ; own exchange area
  v_and_b32 v[vT2], 31, v[vLANE]
  v_lshlrev_b32 v[vT2], 3, v[vT2]
  v_add_u32 v[vT2], LX, v[vT2]
  s_lshl_b32 s[sT1], s[sW], 8
  s_xor_b32 s[sT2], s[sT1], 4*256
  v_add_u32 v[vSTP], s[sT2], v[vT2]
  v_add_u32 v[vSTA], s[sT1], v[vT2]
  s_lshl_b32 s[sT1], s[sW], 12                    ; output stage: mode A in the V ring (4 KiB per wave), mode B in the K ring (8 KiB)
  s_add_u32 s[sT2], s[sT1], LV
  s_lshl_b32 s[sT3], s[sW], 13
  s_cmp_eq_u32 s[sMB], 1
  s_cselect_b32 s[sT1], s[sT3], s[sT2]
  v_add_u32 v[vSGW], s[sT1], v[vSW]
  v_add_u32 v[vSGR], s[sT1], v[vSR]
  .set i, 0
  .rept 64
    v_mov_b32 v[vO+i], 0
    .set i, i+1
  .endr
  v_mov_b32 v[vL0], 0
  v_mov_b32 v[vL1], 0
  v_mov_b32 v[vLS], 0
  v_mov_b32 v[vLS+1], 0
  v_mov_b32 v[vLS+2], 0
  v_mov_b32 v[vLS+3], 0
  v_mov_b32 v[vM], 0xf149f2ca
  s_mov_b32 s[sPEND], 0
  s_mov_b32 s[sU], 0
  ; mid group: mode A: K1, K2, V0 ; mode B: K1, V0
  s_waitcnt vmcnt(2)                                ; own Q pieces landed
  DK1
  DK2
  s_cmp_eq_u32 s[sMB], 1
  s_cbranch_scc1 .Lmid_b
  DK1
  DK2
  DV1
  DV2
  s_waitcnt vmcnt(6)
  s_branch .Lmid_d
.Lmid_b:
  DV1
  DV2
  s_waitcnt vmcnt(4)
.Lmid_d:
  s_barrier                                         ; barrier P: Q, K0 landed
  STAMP 5
  ; ---- prologue compute: step 0
  s_cmp_eq_u32 s[sNS], 0
  s_cbranch_scc1 .Lpro_none
  .set i, 0
  .rept 8
    KREADO i, 0
    .if (i & 1)
      ds_read_b128 v[vQ+4*i:vQ+4*i+3], v[vT1] offset:512*(i>>1)
    .else
      ds_read_b128 v[vQ+4*i:vQ+4*i+3], v[vT0] offset:512*(i>>1)
    .endif
    .set i, i+1
  .endr
  .macro QK0 rd                                     ; rd: read the next step's K fragments behind each MFMA (mode B: same tile)
    .set i, 0
    .rept 8
      .if \rd
        .if i == 0
          s_waitcnt lgkmcnt(14)
        .elseif i == 1
          s_waitcnt lgkmcnt(13)
        .elseif i == 2
          s_waitcnt lgkmcnt(12)
        .elseif i == 3
          s_waitcnt lgkmcnt(11)
        .elseif i == 4
          s_waitcnt lgkmcnt(10)
        .elseif i == 5
          s_waitcnt lgkmcnt(9)
        .elseif i == 6
          s_waitcnt lgkmcnt(8)
        .else
          s_waitcnt lgkmcnt(7)
        .endif
      .else
        .if i == 0
          s_waitcnt lgkmcnt(14)
        .elseif i == 1
          s_waitcnt lgkmcnt(12)
        .elseif i == 2
          s_waitcnt lgkmcnt(10)
        .elseif i == 3
          s_waitcnt lgkmcnt(8)
        .elseif i == 4
          s_waitcnt lgkmcnt(6)
        .elseif i == 5
          s_waitcnt lgkmcnt(4)
        .elseif i == 6
          s_waitcnt lgkmcnt(2)
        .else
          s_waitcnt lgkmcnt(0)
        .endif
      .endif
      QKM vSA, i
      .if \rd
        KREADO i, 0
      .endif
      .if i == 1
        DK1
      .elseif i == 3
        DK2
      .elseif i == 5
        DV1
      .elseif i == 7
        DV2
      .endif
      .set i, i+1
    .endr
  .endm
  s_cmp_eq_u32 s[sMB], 1
  s_cbranch_scc1 .Lqk0_b
  QK0 0
  s_branch .Lqk0_d
.Lqk0_b:
  KADV
  QK0 1
  KADV
.Lqk0_d:
  s_nop 7
  s_cmp_lg_u32 s[sMSTEP], 0
  s_cbranch_scc1 .Lnomask0
  s_nop 3
  MASK vSA
.Lnomask0:
  s_nop 3
  ROWMAX1 vSA
  s_nop 1
  ROWMAX2
  v_max_f32 v[vM], v[vM], v[vT2]
  v_mul_f32 v[vNMC], 0xbe0293ee, v[vM]
  s_cmp_eq_u32 s[sSAFE], 1
  s_cbranch_scc1 .Lpro_safe
  .set r, 0
  .rept 6
    EXPE vSA, r
    .set r, r+1
  .endr
  s_cmp_eq_u32 s[sMB], 1
  s_cbranch_scc1 .Lpro_b
  ; ---- mode A: K1 landed -> its fragments
  s_waitcnt vmcnt(8)
  s_barrier
  .set i, 0
  .rept 8
    KREADO i, 16384
    .set i, i+1
  .endr
  .set r, 6
  .rept 5
    EXPE vSA, r
    .set r, r+1
  .endr
  v_xor_b32 v[vKA0], 0x8000, v[vKA0]                ; K(2) sits in ring slot 2
  v_xor_b32 v[vKA1], 0x8000, v[vKA1]
  STAMP 6
  s_cmp_eq_u32 s[sNF], 0
  s_cbranch_scc1 .LlA0
  ; ======================================================================== mode A loop
.LA0:
  FULLF vSB, vSA, 0, 0, 0
  s_cbranch_scc1 .LlA1
  FULLF vSA, vSB, 0, 1, 0
  s_cbranch_scc0 .LA0
.LlA0:
  LASTF vSA, 0, 0, 0
.LlA1:
  LASTF vSB, 0, 1, 0
  ; ======================================================================== mode B loop (two steps per tile)
.Lpro_b:
  .set r, 6
  .rept 5
    EXPE vSA, r
    .set r, r+1
  .endr
  STAMP 6
  s_cmp_eq_u32 s[sNF], 0
  s_cbranch_scc1 .LlB0
.LB0:
  FULLF vSB, vSA, 1, 0, 0
  s_cbranch_scc1 .LlB1
  FULLF vSA, vSB, 1, 0, 2
  s_cbranch_scc0 .LB0
.LlB0:
  LASTF vSA, 1, 0, 0
.LlB1:
  LASTF vSB, 1, 0, 2
  ; ======================================================================== SAFE loop
.Lpro_safe:
  s_cmp_eq_u32 s[sMB], 1
  s_cbranch_scc1 .Lps_b
  KADV
  s_waitcnt vmcnt(8)
  s_barrier
  .set i, 0
  .rept 8
    KREADO i, 0
    .set i, i+1
  .endr
  KADV
.Lps_b:
  .set r, 0
  .rept 16
    EXPE vSA, r
    .set r, r+1
  .endr
  s_cmp_eq_u32 s[sNF], 0
  s_cbranch_scc1 .LlastS0
.LloopS:
  FULLS vSB, vSA, 0
  s_cbranch_scc1 .LlastS1
  FULLS vSA, vSB, 1
  s_cbranch_scc0 .LloopS
.LlastS0:
  LASTS vSA, 0
.LlastS1:
  LASTS vSB, 1

  ; ---- slots in which this wave has nothing to compute
.Lpro_none:
  DALL
  s_cmp_eq_u32 s[sMB], 1
  s_cbranch_scc1 .Lnone
  s_waitcnt vmcnt(8)
  s_barrier
.Lnone:                                             ; wave without steps: keep loading its share of the tiles
  s_cmp_ge_u32 s[sU], s[sNU]
  s_cbranch_scc1 .Lepi
  s_bitcmp1_b32 s[sU], 0
  s_cselect_b32 s[sT1], s[sMB], 0
  s_cmp_eq_u32 s[sT1], 1
  s_cbranch_scc1 .Lnone_skip
  s_cmp_lt_u32 s[sU], s[sND]
  s_cbranch_scc0 .Lnone_fin
  s_waitcnt vmcnt(4) lgkmcnt(0)
  s_barrier
  DALL
  s_branch .Lnone_skip
.Lnone_fin:
  s_waitcnt vmcnt(0) lgkmcnt(0)
  s_barrier
.Lnone_skip:
  s_add_u32 s[sU], s[sU], 1
  s_branch .Lnone
.Lnone_tail:                                        ; after the wave's last step: only barriers remain
  s_cmp_ge_u32 s[sU], s[sNU]
  s_cbranch_scc1 .Lepi
  s_bitcmp1_b32 s[sU], 0
  s_cselect_b32 s[sT1], s[sMB], 0
  s_cmp_eq_u32 s[sT1], 1
  s_cbranch_scc1 .Lnt_skip
  s_barrier
.Lnt_skip:
  s_add_u32 s[sU], s[sU], 1
  s_branch .Lnone_tail

  ; ======================================================================== epilogue
.Lepi:                                              ; O complete, nothing sent yet
  s_cmp_eq_u32 s[sMB], 1
  s_cbranch_scc1 .LepiB
  s_nop 7
  s_cmp_eq_u32 s[sKH], 0
  s_cbranch_scc0 .Lsend_lo
  SENDB 2
  SENDB 3
  s_branch .Lepi_sent
.Lsend_lo:
  SENDB 0
  SENDB 1
.Lepi_sent:
  STAMP 7
  v_mov_b32 v[vT7], 0x71800000
  s_lshl_b32 s[sT3], s[sQR], 12
  s_add_u32 s[sT3], s[sT3], s[sHOFF]
  ROWSUM
  v_mov_b32 v[vT4], v[vM]
  v_mov_b32 v[vT5], v[vL0]
  s_lshl_b32 s[sT1], s[sKH], 7
  s_add_u32 s[sT3], s[sT3], s[sT1]
  ds_write_b64 v[vSTA], v[vT4:vT5]
  s_cmp_eq_u32 s[sKH], 0
  s_waitcnt lgkmcnt(0)
  s_barrier
  STAMPE 1
  s_cbranch_scc0 .Lmerge1
  MERGE 0, 32
  s_branch .Lnext
.Lmerge1:
  MERGE 32, 0
  s_branch .Lnext
.LepiB:
  STAMP 7
  v_mov_b32 v[vT7], 0x71800000
  s_lshl_b32 s[sT3], s[sQR], 12
  s_add_u32 s[sT3], s[sT3], s[sHOFF]
  ROWSUM
  s_nop 0
  v_rcp_f32 v[vFA], v[vL0]
  s_waitcnt vmcnt(0)
  s_barrier
  .set i, 0
  .rept 32
    v_pk_mul_f32 v[vO+2*i:vO+2*i+1], v[vFA:vFA+1], v[vO+2*i:vO+2*i+1] op_sel_hi:[0,1]
    .set i, i+1
  .endr
  OUTHALF 0, vQ-vO
  v_add_u32 v[vSGW], 4096, v[vSGW]
  v_add_u32 v[vSGR], 4096, v[vSGR]
  s_add_u32 s[sT3], s[sT3], 128
  s_nop 0
  OUTHALF 32, vQ-vO+16
.Lnext:
  STAMP 8
  ; overflow check of the FAST path: a row sum that is not a moderately sized number -> redo this item with the online max
  v_cmp_nlt_f32 vcc, v[vL0], v[vT7]
  s_lshl_b32 s[sT1], s[sW], 2
  s_add_u32 s[sT1], s[sT1], LX+8192
  s_cmp_lg_u64 vcc, 0
  s_cselect_b32 s[sT2], 1, 0
  v_mov_b32 v[vT0], s[sT1]
  v_mov_b32 v[vT1], s[sT2]
  v_and_b32 v[vT2], 7, v[vLANE]
  v_lshlrev_b32 v[vT2], 2, v[vT2]
  v_add_u32 v[vT2], LX+8192, v[vT2]
  ds_write_b32 v[vT0], v[vT1]
  s_waitcnt lgkmcnt(0)
  s_barrier
  ds_read_b32 v[vT1], v[vT2]
  s_waitcnt lgkmcnt(0)
  v_cmp_ne_u32 vcc, 0, v[vT1]
  s_cmp_eq_u32 s[sSAFE], 1
  s_cbranch_scc1 .Lsafe_done
  s_cbranch_vccz .Lsafe_done
  s_mov_b32 s[sSAFE], 1
  s_barrier
  s_branch .Litem
.Lsafe_done:
  s_mov_b32 s[sSAFE], 0
  s_add_u32 s[sR], s[sR], 1
  s_lshl_b32 s[sT1], s[sR], 5
  s_cmp_ge_u32 s[sT1], s[sTOT]
  s_cbranch_scc1 .Ldone
  s_barrier
  s_branch .Litem
.Ldone:
  .if DIAG
    s_waitcnt vmcnt(0)
    STAMP 9
    s_memrealtime s[sA:sA+1]
    s_load_dwordx2 s[sB:sB+1], %0, 96
    s_lshl_b32 s[sT1], %1, 3
    s_add_u32 s[sT1], s[sT1], s[sW]
    s_lshl_b32 s[sT1], s[sT1], 8
    s_waitcnt lgkmcnt(0)
    s_add_u32 s[sB], s[sB], s[sT1]
    s_addc_u32 s[sB+1], s[sB+1], 0
    v_writelane_b32 v[vDG], s[sRT], 10
    v_writelane_b32 v[vDG], s[sA], 11
    v_lshlrev_b32 v[vT6], 2, v[vLANE]
    s_mov_b64 exec, 0xfff
    global_store_dword v[vT6], v[vDG], s[sB:sB+1]
  .endif
  s_endpgm
.Lfallback:
  s_waitcnt vmcnt(0) lgkmcnt(0)
  .purgem STAMP
  .purgem STAMPE
  .purgem DK1
  .purgem DK2
  .purgem DV1
  .purgem DV2
  .purgem DALL
  .purgem BARX
  .purgem DMAP
  .purgem KREADO
  .purgem KADV
  .purgem VADDR
  .purgem QKM
  .purgem VREADO
  .purgem PVJ
  .purgem PVM
  .purgem LSUM
  .purgem FMAE
  .purgem XPE
  .purgem EXPE
  .purgem CVTE
  .purgem SUMPK
  .purgem MASK
  .purgem ROWMAX1
  .purgem ROWMAX2
  .purgem RESCALE
  .purgem SENDB
  .purgem LASTV
  .purgem FDMA
  .purgem FDMB
  .purgem FULLF
  .purgem LASTF
  .purgem FULLS
  .purgem LASTS
  .purgem OUTHALF
  .purgem MERGE
  .purgem ROWSUM
  .purgem PREFETCH
  .purgem QK0
  )" :: "s"(__builtin_amdgcn_kernarg_segment_ptr()), "s"(blockIdx.x), "v"(threadIdx.x), "i"(NIT)
     : "memory", "vcc", "m0",
       "s8","s9","s10","s11","s12","s13","s14","s15","s16","s17","s18","s19","s20","s21","s22","s23","s24","s25","s26","s27","s28","s29","s30","s31",
       "s33","s34","s35","s36","s37","s38","s39","s40","s41","s42","s43","s44","s45","s46","s47","s48","s49","s50","s51","s52","s53","s54","s55",
       "s56","s57","s58","s59","s60","s61","s62","s63","s64","s65","s66","s67","s68","s69","s70","s71","s72","s73","s74","s75","s76","s77","s78","s79",
       "s80","s81","s82","s83","s84","s85","s86","s87","s88","s89","s90","s91","s92","s93","s94","s95","s96","s97","s98","s99",
       "v1","v2","v3","v4","v5","v6","v7","v8","v9","v10","v11","v12","v13","v14","v15","v16","v17","v18","v19","v20","v21","v22","v23","v24","v25","v26","v27","v28","v29","v30","v31",
       "v32","v33","v34","v35","v36","v37","v38","v39","v40","v41","v42","v43","v44","v45","v46","v47","v48","v49","v50","v51","v52","v53","v54","v55","v56","v57","v58","v59","v60","v61","v62","v63",
       "v64","v65","v66","v67","v68","v69","v70","v71","v72","v73","v74","v75","v76","v77","v78","v79","v80","v81","v82","v83","v84","v85","v86","v87","v88","v89","v90","v91","v92","v93","v94","v95",
       "v96","v97","v98","v99","v100","v101","v102","v103","v104","v105","v106","v107","v108","v109","v110","v111","v112","v113","v114","v115","v116","v117","v118","v119","v120","v121","v122","v123","v124","v125","v126","v127",
       "v128","v129","v130","v131","v132","v133","v134","v135","v136","v137","v138","v139","v140","v141","v142","v143","v144","v145","v146","v147","v148","v149","v150","v151","v152","v153","v154","v155","v156","v157","v158","v159",
       "v160","v161","v162","v163","v164","v165","v166","v167","v168","v169","v170","v171","v172","v173","v174","v175","v176","v177","v178","v179","v180","v181","v182","v183","v184","v185","v186","v187","v188","v189","v190","v191",
       "v192","v193","v194","v195","v196","v197","v198","v199","v200","v201","v202","v203","v204","v205","v206","v207","v208","v209","v210","v211","v212","v213","v214","v215","v216","v217","v218","v219","v220","v221","v222","v223",
       "v224","v225","v226","v227","v228","v229","v230","v231","v232","v233","v234","v235","v236","v237","v238","v239","v240","v241","v242","v243","v244","v245","v246","v247","v248","v249","v250","v251","v252","v253","v254","v255");
  generic_path((const unsigned short*)Q, (const unsigned short*)K, (const unsigned short*)V, (const int*)seg, (unsigned short*)O, T, lds);
}

__global__ __launch_bounds__(512, 2)
void ragged_attn_generic(const void* Q, const void* K, const void* V, const void* seg, void* O, int T, int pad) {
  __shared__ __attribute__((aligned(16))) char lds[LDS_BYTES];
  generic_path((const unsigned short*)Q, (const unsigned short*)K, (const unsigned short*)V, (const int*)seg, (unsigned short*)O, T, lds);
}

extern "C" void kernel_launch(void* const* d_in, const int* in_sizes, int n_in, void* d_out, int out_size, void* d_ws, size_t ws_size,
                              hipStream_t stream) {
  const int T = in_sizes[3];
  const unsigned nb = (unsigned)T * ROWB, F = 0x00020000u;
  if (T > 2048 && T <= 2560) {
    ragged_attn_kernel<5><<<256, 512, 0, stream>>>(d_in[0], nb, F, d_in[1], nb, F, d_in[2], nb, F, d_in[3], (unsigned)T * 4u, F, d_out, nb, F,
                                                   T, T - 1 - 512 * 4, ((T + 31) / 32) * (int)ROWB, 0, d_ws);
  } else {
    ragged_attn_generic<<<256, 512, 0, stream>>>(d_in[0], d_in[1], d_in[2], d_in[3], d_out, T, 0);
  }
}
```

```cpp
#include <hip/hip_runtime.h>
#include <stdint.h>

typedef short    bf16x8 __attribute__((ext_vector_type(8)));
typedef short    s16x4  __attribute__((ext_vector_type(4)));
typedef float    f32x16 __attribute__((ext_vector_type(16)));
typedef float    f32x4  __attribute__((ext_vector_type(4)));
typedef float    f32x2  __attribute__((ext_vector_type(2)));
typedef __bf16   bf2    __attribute__((ext_vector_type(2)));
typedef unsigned u32x4  __attribute__((ext_vector_type(4)));
typedef unsigned long long u64;
#define LDSP __attribute__((address_space(3)))
typedef LDSP const char* lds_cptr;
typedef LDSP char* lds_ptr;
typedef short v4i16_t __attribute__((ext_vector_type(4)));
#define DI __device__ __forceinline__
#define MFMA(a, b, c) __builtin_amdgcn_mfma_f32_32x32x16_bf16((a), (b), (c), 0, 0, 0)

constexpr int NH = 16, HD = 128;
constexpr unsigned ROWB = NH * HD * 2;
constexpr unsigned LDS_BYTES = 163840;
constexpr float CEXP = 0.08838834764831845f * 1.4426950408889634f;
constexpr float THR_RAW = 8.0f / CEXP;
constexpr float MINIT = -1.0e30f;

DI unsigned rfl(unsigned x) { return __builtin_amdgcn_readfirstlane(x); }
DI unsigned cvtpk(float lo, float hi) { f32x2 v = {lo, hi}; bf2 b = __builtin_convertvector(v, bf2); return __builtin_bit_cast(unsigned, b); }
DI float swap_max(float x) { auto r = __builtin_amdgcn_permlane32_swap(__float_as_uint(x), __float_as_uint(x), false, false); return __builtin_fmaxf(__uint_as_float(r[0]), __uint_as_float(r[1])); }
DI float swap_add(float x) { auto r = __builtin_amdgcn_permlane32_swap(__float_as_uint(x), __float_as_uint(x), false, false); return __uint_as_float(r[0]) + __uint_as_float(r[1]); }
DI s16x4 vtr(lds_cptr p) { return __builtin_bit_cast(s16x4, __builtin_amdgcn_ds_read_tr16_b64_v4i16((LDSP v4i16_t*)p)); }
#define MX3(a, b, c) __builtin_fmaxf(__builtin_fmaxf((a), (b)), (c))

DI void dma16(__amdgpu_buffer_rsrc_t srd, unsigned voff, unsigned soff, unsigned m0v) {
  unsigned keep;
  asm volatile("s_nop 4\n\ts_mov_b32 %0, m0\n\ts_mov_b32 m0, %1\n\ts_nop 0\n\tbuffer_load_dwordx4 %2, %3, %4 offen lds\n\ts_mov_b32 m0, %0"
               : "=&s"(keep) : "s"(m0v), "v"(voff), "s"(srd), "s"(soff) : "memory");
}

__device__ __forceinline__ void generic_path(const unsigned short* __restrict__ Q, const unsigned short* __restrict__ K, const unsigned short* __restrict__ V,
                                          const int* __restrict__ seg, unsigned short* __restrict__ O, int T, char* lds) {
  constexpr unsigned SLOT = 32768, VOFF = 16384, XREG = 4 * SLOT;
  const int tid = threadIdx.x, lane = tid & 63, r32 = lane & 31, hh = lane >> 5;
  const int w = (int)rfl((unsigned)tid >> 6);
  const int grp = blockIdx.x & 7, jj = blockIdx.x >> 3, NJ = 32;
  const unsigned ldsb = (unsigned)(uintptr_t)lds;
  const lds_ptr L = (lds_ptr)lds;
  const int total_items = 2 * ((T + 127) >> 7);
  const __amdgpu_buffer_rsrc_t srdQ = __builtin_amdgcn_make_buffer_rsrc((void*)Q, (short)0, T * (int)ROWB, 0x00020000);
  const __amdgpu_buffer_rsrc_t srdK = __builtin_amdgcn_make_buffer_rsrc((void*)K, (short)0, T * (int)ROWB, 0x00020000);
  const __amdgpu_buffer_rsrc_t srdV = __builtin_amdgcn_make_buffer_rsrc((void*)V, (short)0, T * (int)ROWB, 0x00020000);
  const unsigned prow = (lane >> 2) & 7, pcb = lane >> 5, pc = lane & 3;
  const unsigned voffK = prow * ROWB + pcb * 64 + ((pc ^ ((2u * (unsigned)w + (prow >> 2)) & 3u)) * 16);
  const unsigned voffV = prow * ROWB + pcb * 64 + pc * 16;
  const unsigned kb0 = 2048u * (r32 >> 3) + 64u * (r32 & 7) + 16u * ((unsigned)hh ^ ((r32 >> 2) & 3u));
  const unsigned vb0 = ((unsigned)hh * 4 + ((lane & 15) >> 2)) * 64 + ((lane >> 4) & 1) * 32 + (lane & 3) * 8;
  for (int idx = jj; idx < total_items; idx += NJ) {
    __syncthreads();
    const int head = grp + 8 * (idx & 1);
    const int q0 = (idx >> 1) * 128, qend = min(q0 + 128, T);
    LDSP int* red = (LDSP int*)L;
    if (tid == 0) { red[0] = 0x7fffffff; red[1] = (int)0x80000000; red[2] = 0x7fffffff; red[3] = -1; }
    __syncthreads();
    if (tid < 128 && q0 + tid < qend) { const int id = seg[q0 + tid];
      __hip_atomic_fetch_min((LDSP int*)&red[0], id, __ATOMIC_RELAXED, __HIP_MEMORY_SCOPE_WORKGROUP);
      __hip_atomic_fetch_max((LDSP int*)&red[1], id, __ATOMIC_RELAXED, __HIP_MEMORY_SCOPE_WORKGROUP); }
    __syncthreads();
    const int idlo = red[0], idhi = red[1];
    int lo = 0x7fffffff, hi = -1;
    for (int i = tid; i < T; i += 512) { const int id = seg[i]; if (id >= idlo && id <= idhi) { lo = min(lo, i); hi = max(hi, i); } }
    __hip_atomic_fetch_min((LDSP int*)&red[2], lo, __ATOMIC_RELAXED, __HIP_MEMORY_SCOPE_WORKGROUP);
    __hip_atomic_fetch_max((LDSP int*)&red[3], hi, __ATOMIC_RELAXED, __HIP_MEMORY_SCOPE_WORKGROUP);
    __syncthreads();
    const int k0 = (int)rfl((unsigned)red[2]), kend = (int)rfl((unsigned)red[3]) + 1;
    __syncthreads();
    const int nk = (kend - k0 + 63) >> 6;
    const unsigned hoff = (unsigned)head * 256u;
#pragma unroll
    for (int i = 0; i < 2; ++i) {
      const unsigned m = (unsigned)w + 8u * i;
      const unsigned so = (unsigned)(q0 + 8 * (int)m) * ROWB + hoff;
      dma16(srdQ, voffK, so, ldsb + XREG + m * 2048u);
      dma16(srdQ, voffK, so + 128u, ldsb + XREG + m * 2048u + 1024u);
    }
    const int qb = w & 3, sub = w >> 2;
    const int qrow = q0 + 32 * qb + r32;
    const int segq = seg[qrow < T ? qrow : T - 1];
    bf16x8 qf[8];
    f32x16 o[4];
#pragma unroll
    for (int d = 0; d < 4; ++d) o[d] = f32x16{};
    float m_run = MINIT, l_run = 0.f;
    for (int t = 0; t < nk; ++t) {
      __syncthreads();
      {
        const unsigned so = (unsigned)(k0 + 64 * t + 8 * w) * ROWB + hoff;
        const unsigned d = ldsb + (unsigned)w * 2048u;
        dma16(srdK, voffK, so, d); dma16(srdK, voffK, so + 128u, d + 1024u);
        dma16(srdV, voffV, so, d + VOFF); dma16(srdV, voffV, so + 128u, d + VOFF + 1024u);
      }
      asm volatile("s_waitcnt vmcnt(0)" ::: "memory");
      __syncthreads();
      if (t == 0) {
#pragma unroll
        for (int s = 0; s < 8; ++s)
          qf[s] = *(const LDSP bf16x8*)(L + XREG + (unsigned)qb * 8192u + ((kb0 ^ ((s & 1) * 32u)) + 512u * (s >> 1)));
      }
      const int kbase = k0 + 64 * t + 32 * sub;
      const lds_cptr kp = (lds_cptr)L + (unsigned)sub * 8192u;
      f32x16 sc = f32x16{};
#pragma unroll
      for (int s = 0; s < 8; ++s) {
        const bf16x8 kf = *(const LDSP bf16x8*)(kp + ((kb0 ^ ((s & 1) * 32u)) + 512u * (s >> 1)));
        sc = MFMA(kf, qf[s], sc);
      }
#pragma unroll
      for (int r = 0; r < 16; ++r) {
        const int kk = kbase + (r & 3) + 8 * (r >> 2) + 4 * hh;
        const int id = seg[kk < T ? kk : T - 1];
        sc[r] = (kk < kend && id == segq) ? sc[r] : -__builtin_inff();
      }
      float a = MX3(sc[0], sc[1], sc[2]), b = MX3(sc[3], sc[4], sc[5]);
      a = MX3(a, sc[6], sc[7]); b = MX3(b, sc[8], sc[9]); a = MX3(a, sc[10], sc[11]); b = MX3(b, sc[12], sc[13]);
      a = MX3(a, sc[14], sc[15]);
      const float pm = swap_max(__builtin_fmaxf(a, b));
      const float mn = __builtin_fmaxf(m_run, pm);
      const float al = __builtin_amdgcn_exp2f((m_run - mn) * CEXP);
      m_run = mn; l_run *= al;
#pragma unroll
      for (int d = 0; d < 4; ++d)
#pragma unroll
        for (int r = 0; r < 16; ++r) o[d][r] *= al;
      const float nmc = -m_run * CEXP;
      float ps = 0.f;
#pragma unroll
      for (int r = 0; r < 16; ++r) { sc[r] = __builtin_amdgcn_exp2f(__builtin_fmaf(sc[r], CEXP, nmc)); ps += sc[r]; }
      l_run += ps;
      u32x4 pw0, pw1;
#pragma unroll
      for (int e = 0; e < 4; ++e) { pw0[e] = cvtpk(sc[2 * e], sc[2 * e + 1]); pw1[e] = cvtpk(sc[8 + 2 * e], sc[8 + 2 * e + 1]); }
      const bf16x8 p0 = __builtin_bit_cast(bf16x8, pw0), p1 = __builtin_bit_cast(bf16x8, pw1);
      const lds_cptr vp = kp + VOFF + vb0;
#pragma unroll
      for (int d = 0; d < 4; ++d) {
        const s16x4 a0 = vtr(vp + d * 512), a1 = vtr(vp + d * 512 + 2048), b0 = vtr(vp + d * 512 + 4096), b1 = vtr(vp + d * 512 + 6144);
        const bf16x8 va = __builtin_shufflevector(a0, a1, 0, 1, 2, 3, 4, 5, 6, 7), vb = __builtin_shufflevector(b0, b1, 0, 1, 2, 3, 4, 5, 6, 7);
        o[d] = MFMA(va, p0, o[d]);
        o[d] = MFMA(vb, p1, o[d]);
      }
    }
    l_run = swap_add(l_run);
    unsigned short* orow = O + (size_t)(qrow < T ? qrow : 0) * (NH * HD) + head * HD + 8 * hh;
    const bool row_ok = qrow < qend;
    const int kh = sub;
    __syncthreads();
    const unsigned xa = SLOT, xb = 2 * SLOT, xs = 3 * SLOT;
    const unsigned mine = (kh ? xb : xa) + (unsigned)qb * 8192u + (unsigned)lane * 16u;
    const unsigned theirs = (kh ? xa : xb) + (unsigned)qb * 8192u + (unsigned)lane * 16u;
#pragma unroll
    for (int dd = 0; dd < 2; ++dd)
#pragma unroll
      for (int g = 0; g < 4; ++g) {
        f32x4 v;
#pragma unroll
        for (int e = 0; e < 4; ++e) v[e] = kh ? o[dd][4 * g + e] : o[2 + dd][4 * g + e];
        *(LDSP f32x4*)(L + mine + (unsigned)(dd * 4 + g) * 1024u) = v;
      }
    if (hh == 0) { f32x2 s2 = {m_run, l_run}; *(LDSP f32x2*)(L + xs + (unsigned)w * 256u + (unsigned)r32 * 8u) = s2; }
    __syncthreads();
    const f32x2 o2 = *(const LDSP f32x2*)(L + xs + (unsigned)(w ^ 4) * 256u + (unsigned)r32 * 8u);
    const float mn = __builtin_fmaxf(m_run, o2[0]);
    const float wa = __builtin_amdgcn_exp2f((m_run - mn) * CEXP), wb = __builtin_amdgcn_exp2f((o2[0] - mn) * CEXP);
    const float inv = 1.0f / (wa * l_run + wb * o2[1]);
    const float fa = wa * inv, fb = wb * inv;
#pragma unroll
    for (int dd = 0; dd < 2; ++dd) {
      float r[16];
#pragma unroll
      for (int g = 0; g < 4; ++g) {
        const f32x4 v = *(const LDSP f32x4*)(L + theirs + (unsigned)(dd * 4 + g) * 1024u);
#pragma unroll
        for (int e = 0; e < 4; ++e) r[4 * g + e] = fa * (kh ? o[2 + dd][4 * g + e] : o[dd][4 * g + e]) + fb * v[e];
      }
#pragma unroll
      for (int g = 0; g < 4; g += 2) {
        unsigned ax = cvtpk(r[4 * g + 0], r[4 * g + 1]), ay = cvtpk(r[4 * g + 2], r[4 * g + 3]);
        unsigned bx = cvtpk(r[4 * g + 4], r[4 * g + 5]), by = cvtpk(r[4 * g + 6], r[4 * g + 7]);
        auto rx = __builtin_amdgcn_permlane32_swap(ax, bx, false, false); auto ry = __builtin_amdgcn_permlane32_swap(ay, by, false, false);
        const u32x4 v = {rx[0], ry[0], rx[1], ry[1]};
        if (row_ok) *(u32x4*)(orow + 64 * kh + 32 * dd + 8 * g) = v;
      }
    }
  }
}

#define DIAG 0
#define ABL 0
#define PF1 3
#define PF2 5
#define STR2(x) #x
#define STR(x) STR2(x)

template <int NIT>
__global__ __launch_bounds__(512, 2)
void ragged_attn_kernel(const void* Q, unsigned nQ, unsigned fQ, const void* K, unsigned nK, unsigned fK, const void* V, unsigned nV, unsigned fV,
                        const void* seg, unsigned nS, unsigned fS, void* O, unsigned nO, unsigned fO, int T, int lastc, int pfb, int pad, void* dbg) {
  __shared__ __attribute__((aligned(16))) char lds[LDS_BYTES];
  asm volatile(R"(
  .set NIT, %c3
  .set DIAG, )" STR(DIAG) R"(
  .set ABL, )" STR(ABL) R"(
  .set PF1, )" STR(PF1) R"(
  .set PF2, )" STR(PF2) R"(
  ; ---------------- SGPRs
  .set sQ, 8
  .set sK, 12
  .set sV, 16
  .set sS, 20
  .set sO, 24
  .set sT, 28
  .set sLASTC, 29
  .set sPFB, 30
  .set sF, 30
  .set sLIVE, 30
  .set sTM16, 31
  .set sNDF, 33
  .set sG, 34
  .set sR, 35
  .set sNBIG, 36
  .set sTOT, 37
  .set sHOFF, 38
  .set sQ0, 39
  .set sQEND, 40
  .set sK0, 41
  .set sNK, 42
  .set sLEN, 43
  .set sMB, 44
  .set sNS, 45
  .set sU, 46
  .set sNU, 47
  .set sKOFF, 48
  .set sKSTEP, 49
  .set sVOFF, 50
  .set sKSO, 51
  .set sVSO, 52
  .set sKDST, 53
  .set sVDST, 54
  .set sCNT, 55
  .set sND, 56
  .set sMSTEP, 57
  .set sPEND, 58
  .set sW, 59
  .set sA, 60
  .set sB, 62
  .set sC, 64
  .set sSAFE, 64
  .set sNF, 65
  .set sT1, 66
  .set sT2, 67
  .set sT3, 68
  .set sT4, 69
  .set sCE, 70
  .set sTHR, 71
  .set sWD, 72
  .set sQB, 73
  .set sKH, 74
  .set sLIM, 75
  .set sJ, 76
  .set sQR, 77
  .set sRT, 78
  .set sV2, 80
  .set sM, 84
  .set sMK, 86
  .set sMV, 87
  ; ---------------- VGPRs
  .set vLANE, 1
  .set vOK, 2
  .set vOK2, 3
  .set vOV, 4
  .set vOV2, 5
  .set vKB0, 6
  .set vKB1, 7
  .set vVB, 8
  .set vSTART, 9
  .set vLEN, 10
  .set vINCL, 11
  .set vEXCL, 12
  .set vM, 13
  .set vNMC, 14
  .set vL0, 15
  .set vL1, 16
  .set vKA0, 17
  .set vKA1, 18
  .set vVA, 19
  .set vAL, 20
  .set vSO, 21
  .set vHH4, 22
  .set vSW, 23
  .set vT0, 24
  .set vT1, 25
  .set vT2, 26
  .set vT3, 27
  .set vT4, 28
  .set vT5, 29
  .set vT6, 30
  .set vT7, 31
  .set vP, 32
  .set vX, 40
  .set vLS, 40
  .set vXO, 44
  .set vXP, 45
  .set vSTA, 46
  .set vSTP, 47
  .set vSGW, 48
  .set vSGR, 49
  .set vFA, 50
  .set vFB, 52
  .set vDG, 54
  .set vSR, 56
  .set vVH, 57
  .set vVBL, 58
  .set vVBH, 59
  .set vONE, 60
  .set vO, 64
  .set vQ, 128
  .set vSA, 160
  .set vSB, 176
  .set vK, 192
  .set vV, 224
  ; ---------------- LDS map: K ring 4 x 16 KiB | V ring 4 x 16 KiB | X (Q stage) 32 KiB
  .set LK, 0
  .set LV, 65536
  .set LX, 131072
  .set LQ2, 49152
  .set LQ3, 114688
  .set LSCR, 98304
  .set TILEB, 64*4096

  .macro STAMP i                                    ; diagnostic builds only: low word of the cycle counter -> lane i of vDG
    .if (DIAG == 1) || ((DIAG == 2) && ((\i == 0) || (\i >= 5)))
      s_memtime s[sM:sM+1]
      s_waitcnt lgkmcnt(0)
      v_writelane_b32 v[vDG], s[sM], \i
    .endif
  .endm
  .macro STAMPE i
    .if DIAG == 2
      s_memtime s[sM:sM+1]
      s_waitcnt lgkmcnt(0)
      v_writelane_b32 v[vDG], s[sM], \i
    .endif
  .endm

  ; ======================================================================== macros
  ; ---- DMA pieces (generic form).  sKDST / sVDST: ring offset + w*2048 of the next tile
  .macro DK1
    s_mov_b32 m0, s[sKDST]
    s_add_u32 s[sT1], s[sKDST], 16384
    buffer_load_dwordx4 v[vOK], s[sK:sK+3], s[sKSO] offen lds
  .endm
  .macro DK2
    s_add_u32 m0, s[sKDST], 1024
    s_and_b32 s[sKDST], s[sT1], 0xffff
    buffer_load_dwordx4 v[vOK2], s[sK:sK+3], s[sKSO] offen lds
    s_add_u32 s[sKSO], s[sKSO], TILEB
  .endm
  .macro DV1
    s_add_u32 m0, s[sVDST], LV
    s_add_u32 s[sT3], s[sVDST], 16384
    buffer_load_dwordx4 v[vOV], s[sV:sV+3], s[sVSO] offen lds
  .endm
  .macro DV2
    s_add_u32 m0, s[sVDST], LV+1024
    s_and_b32 s[sVDST], s[sT3], 0xffff
    buffer_load_dwordx4 v[vOV2], s[sV:sV+3], s[sVSO] offen lds
    s_add_u32 s[sVSO], s[sVSO], TILEB
  .endm
  .macro DALL
    DK1
    DK2
    DV1
    DV2
  .endm
  ; m: 0 this slot has a barrier, 1 barrier unless mode B (run time), 2 no barrier
  .macro BARX n, m
    .if \m == 1
      s_cmp_eq_u32 s[sMB], 1
      s_cbranch_scc1 .Lnob\@
    .endif
    .if \m == 2
      s_waitcnt lgkmcnt(0)
    .else
      s_waitcnt vmcnt(\n) lgkmcnt(0)
      s_barrier
    .endif
    .if \m == 1
      s_branch .Lbd\@
  .Lnob\@:
      s_waitcnt lgkmcnt(0)
  .Lbd\@:
    .endif
  .endm
  ; DMA piece k of a generic step.  dma: 0 none, 1 always, 2 run-time flag sF ; m as above
  .macro DMAP k, m, dma
    .if ((\dma) != 0) && ((\m) != 2) && ((ABL & 1) == 0)
      .if \dma == 2
        s_cmp_eq_u32 s[sF], 0
        s_cbranch_scc1 .Ld\@
      .endif
      .if \m == 1
        s_cmp_eq_u32 s[sMB], 1
        s_cbranch_scc1 .Ld\@
      .endif
      .if \k == 0
        DK1
      .elseif \k == 1
        DK2
      .elseif \k == 2
        DV1
      .else
        DV2
      .endif
  .Ld\@:
    .endif
  .endm
  .macro KREADO i, off
    .if (ABL & 8)
    .elseif (\i & 1)
      ds_read_b128 v[vK+4*\i:vK+4*\i+3], v[vKA1] offset:(\off)+512*(\i>>1)
    .else
      ds_read_b128 v[vK+4*\i:vK+4*\i+3], v[vKA0] offset:(\off)+512*(\i>>1)
    .endif
  .endm
  .macro KADV                                       ; generic form: advance the K read addresses to the next step
    s_add_u32 s[sKOFF], s[sKOFF], s[sKSTEP]
    s_and_b32 s[sKOFF], s[sKOFF], 0xffff
    v_add_u32 v[vKA0], s[sKOFF], v[vKB0]
    v_add_u32 v[vKA1], s[sKOFF], v[vKB1]
  .endm
  .macro VADDR                                      ; generic form: V read addresses of the coming step
    v_add_u32 v[vVA], s[sVOFF], v[vVBL]
    v_add_u32 v[vVH], s[sVOFF], v[vVBH]
    s_add_u32 s[sVOFF], s[sVOFF], s[sKSTEP]
    s_and_b32 s[sVOFF], s[sVOFF], 0xffff
  .endm
  .macro QKM c, i
    .if \i == 0
      v_mfma_f32_32x32x16_bf16 v[\c:\c+15], v[vK:vK+3], v[vQ:vQ+3], 0
    .else
      v_mfma_f32_32x32x16_bf16 v[\c:\c+15], v[vK+4*\i:vK+4*\i+3], v[vQ+4*\i:vQ+4*\i+3], v[\c:\c+15]
    .endif
  .endm
  .macro VREADO j, off                              ; V fragment j: register block d = j&3 (actual d-block = d ^ 2*kh), ks = j>>2
    .if (ABL & 4) == 0
    .if (\j & 2)
    ds_read_b64_tr_b16 v[vV+4*\j:vV+4*\j+1], v[vVH] offset:(\off)+512*(\j&3)+4096*(\j>>2)
    ds_read_b64_tr_b16 v[vV+4*\j+2:vV+4*\j+3], v[vVH] offset:(\off)+512*(\j&3)+4096*(\j>>2)+2048
    .else
    ds_read_b64_tr_b16 v[vV+4*\j:vV+4*\j+1], v[vVA] offset:(\off)+512*(\j&3)+4096*(\j>>2)
    ds_read_b64_tr_b16 v[vV+4*\j+2:vV+4*\j+3], v[vVA] offset:(\off)+512*(\j&3)+4096*(\j>>2)+2048
    .endif
    .endif
  .endm
  .macro PVJ d, ks
    v_mfma_f32_32x32x16_bf16 v[vO+16*(\d):vO+16*(\d)+15], v[vV+4*((\d)+4*(\ks)):vV+4*((\d)+4*(\ks))+3], v[vP+4*(\ks):vP+4*(\ks)+3], v[vO+16*(\d):vO+16*(\d)+15]
  .endm
  .macro PVM j
    PVJ (\j&3), (\j>>2)
  .endm
  .macro LSUM ks                                    ; row sums of one packed P fragment
    .if (ABL & 64) == 0
    v_mfma_f32_16x16x32_bf16 v[vLS:vLS+3], v[vONE:vONE+3], v[vP+4*\ks:vP+4*\ks+3], v[vLS:vLS+3]
    .endif
  .endm
  .macro FMAE c, r
    .if (ABL & 2) == 0
    v_fma_f32 v[\c+(\r)], v[\c+(\r)], s[sCE], v[vNMC]
    .endif
  .endm
  .macro XPE c, r
    .if (ABL & 2) == 0
    v_exp_f32 v[\c+(\r)], v[\c+(\r)]
    .endif
  .endm
  .macro EXPE c, r
    FMAE \c, \r
    XPE \c, \r
  .endm
  .macro CVTE p, i                                  ; pack of elements 2i, 2i+1
    .if (ABL & 32) == 0
    v_cvt_pk_bf16_f32 v[vP+\i], v[\p+2*\i], v[\p+2*\i+1]
    .endif
  .endm
  .macro SUMPK p, i                                 ; SAFE form: row sums + pack of elements 2i, 2i+1
    v_add_f32 v[vL0], v[vL0], v[\p+2*\i]
    v_add_f32 v[vL1], v[vL1], v[\p+2*\i+1]
    v_cvt_pk_bf16_f32 v[vP+\i], v[\p+2*\i], v[\p+2*\i+1]
  .endm
  .macro MASK c                                     ; keys >= lim of this sub-tile -> -inf
    v_sub_u32 v[vT2], s[sLIM], v[vHH4]
    v_mov_b32 v[vT3], 0xff800000
    .set r, 0
    .rept 16
      v_cmp_lt_i32 vcc, (r&3)+8*(r>>2), v[vT2]
      s_nop 1
      v_cndmask_b32 v[\c+r], v[vT3], v[\c+r], vcc
      .set r, r+1
    .endr
  .endm
  .macro ROWMAX1 c
    v_max3_f32 v[vT2], v[\c+0], v[\c+1], v[\c+2]
    v_max3_f32 v[vT3], v[\c+3], v[\c+4], v[\c+5]
    v_max3_f32 v[vT2], v[vT2], v[\c+6], v[\c+7]
    v_max3_f32 v[vT3], v[vT3], v[\c+8], v[\c+9]
    v_max3_f32 v[vT2], v[vT2], v[\c+10], v[\c+11]
    v_max3_f32 v[vT3], v[vT3], v[\c+12], v[\c+13]
    v_max3_f32 v[vT2], v[vT2], v[\c+14], v[\c+15]
    v_max_f32 v[vT2], v[vT2], v[vT3]
    v_mov_b32 v[vT3], v[vT2]
  .endm
  .macro ROWMAX2                                    ; needs 2 wait states after ROWMAX1
    v_permlane32_swap_b32 v[vT2], v[vT3]
    s_nop 0
    v_max_f32 v[vT2], v[vT2], v[vT3]                ; vT2 = row max
  .endm
  .macro RESCALE
    s_nop 7
    s_nop 7
    .set i, 0
    .rept 64
      v_mul_f32 v[vO+i], v[vAL], v[vO+i]
      .set i, i+1
    .endr
    v_mul_f32 v[vL0], v[vAL], v[vL0]
    v_mul_f32 v[vL1], v[vAL], v[vL1]
    s_mov_b32 s[sPEND], 0
  .endm
  .macro SENDB d                                    ; d-block d (16 regs) -> own exchange area
    .set i, 0
    .rept 4
      ds_write_b128 v[vXO], v[vO+16*\d+4*i:vO+16*\d+4*i+3] offset:4096*(\d&1)+1024*i
      .set i, i+1
    .endr
  .endm
  .macro LASTV da, db, dc, dd                       ; last P.V: the blocks to send (da, db) first
    PVJ \da, 0
    PVJ \db, 0
    PVJ \da, 1
    PVJ \db, 1
    PVJ \dc, 0
    s_nop 7
    s_nop 1
    SENDB \da
    PVJ \dd, 0
    SENDB \db
    PVJ \dc, 1
    PVJ \dd, 1
    s_branch .Lepi_sent
  .endm

  ; ---- FAST step body (reference = row max of the wave's first sub-tile; overflow is detected on the row sums at the end of the item,
  ;      which is then redone with the SAFE steps).  prev enters with exp done for e0..e10.
  ;      gen = 1: generic addressing (address registers, generic DMA) ; gen = 0: mode A, ring position r as immediates
  .macro FDMA k, gen, r, m, dma
    .if \gen
      DMAP \k, \m, \dma
    .elseif (\dma) && ((ABL & 1) == 0)
      .if \k == 0
        s_add_u32 m0, s[sMK], 16384*(\r)
      .elseif \k == 1
        s_add_u32 m0, s[sMK], 16384*(\r)+1024
      .elseif \k == 2
        s_add_u32 m0, s[sMV], 16384*(\r)
      .else
        s_add_u32 m0, s[sMV], 16384*(\r)+1024
      .endif
    .endif
  .endm
  .macro FDMB k, gen, dma                            ; the load itself (mode A form), one instruction behind its M0 write
    .if ((\gen) == 0) && (\dma) && ((ABL & 1) == 0)
      .if \k == 0
        buffer_load_dwordx4 v[vOK], s[sK:sK+3], s[sKSO] offen lds
      .elseif \k == 1
        buffer_load_dwordx4 v[vOK2], s[sK:sK+3], s[sKSO] offen lds
      .elseif \k == 2
        buffer_load_dwordx4 v[vOV], s[sV2:sV2+3], s[sKSO] offen lds
      .else
        buffer_load_dwordx4 v[vOV2], s[sV2:sV2+3], s[sKSO] offen lds
        s_add_u32 s[sKSO], s[sKSO], TILEB
      .endif
    .endif
  .endm
  .macro FULLF cur, prev, gen, r, m
    .set KO, (1-\gen)*16384*(\r)
    .set VO, (1-\gen)*16384*(\r)
    BARX 4, \m
    ; ---------------- phase A
    QKM \cur, 0
    VREADO 0, VO
    EXPE \prev, 11
    QKM \cur, 1
    VREADO 1, VO
    EXPE \prev, 12
    CVTE \prev, 0
    QKM \cur, 2
    VREADO 2, VO
    FDMA 0, \gen, \r, \m, 1
    FMAE \prev, 13
    FDMB 0, \gen, 1
    XPE \prev, 13
    CVTE \prev, 1
    QKM \cur, 3
    VREADO 3, VO
    EXPE \prev, 14
    CVTE \prev, 2
    QKM \cur, 4
    VREADO 4, VO
    EXPE \prev, 15
    CVTE \prev, 3
    QKM \cur, 5
    VREADO 5, VO
    CVTE \prev, 4
    CVTE \prev, 5
    QKM \cur, 6
    VREADO 6, VO
    FDMA 1, \gen, \r, \m, 1
    CVTE \prev, 6
    FDMB 1, \gen, 1
    QKM \cur, 7
    VREADO 7, VO
    CVTE \prev, 7
    .if (\gen == 0) && (\r == 1)
      v_xor_b32 v[vVA], 0x8000, v[vVA]
      v_xor_b32 v[vVH], 0x8000, v[vVH]
    .endif
    ; ---------------- phase B
    s_waitcnt lgkmcnt(4)
    PVM 0
    KREADO 0, KO
    KREADO 1, KO
    FDMA 2, \gen, \r, \m, 1
    KREADO 2, KO
    FDMB 2, \gen, 1
    KREADO 3, KO
    s_nop 2
    PVM 1
    EXPE \cur, 0
    EXPE \cur, 1
    LSUM 0
    EXPE \cur, 2
    PVM 2
    KREADO 4, KO
    EXPE \cur, 3
    PVM 3
    KREADO 5, KO
    EXPE \cur, 4
    FMAE \cur, 5
    PVM 4
    KREADO 6, KO
    XPE \cur, 5
    EXPE \cur, 6
    PVM 5
    KREADO 7, KO
    EXPE \cur, 7
    LSUM 1
    EXPE \cur, 8
    s_waitcnt lgkmcnt(8)
    PVM 6
    FDMA 3, \gen, \r, \m, 1
    FMAE \cur, 9
    FDMB 3, \gen, 1
    XPE \cur, 9
    .if \gen
      KADV
      VADDR
    .elseif \r == 1
      v_xor_b32 v[vKA0], 0x8000, v[vKA0]
      v_xor_b32 v[vKA1], 0x8000, v[vKA1]
      s_xor_b32 s[sMK], s[sMK], 0x8000
      s_xor_b32 s[sMV], s[sMV], 0x8000
    .endif
    s_add_u32 s[sCNT], s[sCNT], 1
    PVM 7
    EXPE \cur, 10
  .endm
  .macro LASTF prev, gen, r, m
    .set VO, (1-\gen)*16384*(\r)
    .if \m == 2
    .else
      BARX 0, \m
    .endif
    .set g, 0
    .rept 8
      VREADO g, VO
      .if g < 5
        EXPE \prev, 11+g
      .endif
      .set g, g+1
    .endr
    s_mov_b32 s[sU], s[sNS]
    s_cmp_eq_i32 s[sMSTEP], -1
    s_cbranch_scc1 .Lnm\@
    ; partial sub-tile: keys >= lim get weight 0
    v_sub_u32 v[vT2], s[sLIM], v[vHH4]
    .set e, 0
    .rept 16
      v_cmp_lt_i32 vcc, (e&3)+8*(e>>2), v[vT2]
      s_nop 1
      v_cndmask_b32 v[\prev+e], 0, v[\prev+e], vcc
      .set e, e+1
    .endr
  .Lnm\@:
    .set g, 0
    .rept 8
      CVTE \prev, g
      .set g, g+1
    .endr
    s_cmp_eq_u32 s[sU], s[sNU]
    s_waitcnt lgkmcnt(0)
    LSUM 0
    LSUM 1
    .if \gen == 0                                    ; mode A, last slot: send the partner's half early
      s_cbranch_scc0 .Lplain\@
      LASTV 2, 3, 0, 1
  .Lplain\@:
    .endif
    .set g, 0
    .rept 8
      PVM g
      .set g, g+1
    .endr
    s_branch .Lnone_tail
  .endm

  ; ---- SAFE step (online max every step), generic addressing, run-time mode
  .macro FULLS cur, prev, m
    s_cmp_lt_u32 s[sU], s[sND]
    s_cselect_b32 s[sF], 1, 0
    s_cbranch_scc0 .Lbn\@
    BARX 4, \m
    s_branch .Lbq\@
  .Lbn\@:
    BARX 0, \m
  .Lbq\@:
    .set g, 0
    .rept 8
      QKM \cur, g
      VREADO g, 0
      SUMPK \prev, g
      .if g == 1
        DMAP 0, \m, 2
      .elseif g == 5
        DMAP 1, \m, 2
      .endif
      .set g, g+1
    .endr
    s_waitcnt lgkmcnt(4)
    PVM 0
    KREADO 0, 0
    KREADO 1, 0
    s_add_u32 s[sU], s[sU], 1
    s_nop 1
    PVM 1
    s_cmp_lg_u32 s[sMSTEP], s[sU]
    s_cbranch_scc1 .Lnm\@
    MASK \cur
  .Lnm\@:
    ROWMAX1 \cur
    PVM 2
    KREADO 2, 0
    ROWMAX2
    v_sub_f32 v[vT3], v[vT2], v[vM]
    v_cmp_lt_f32 vcc, s[sTHR], v[vT3]
    s_cbranch_vccz .Lkeep\@
    v_max_f32 v[vT3], v[vM], v[vT2]
    v_sub_f32 v[vAL], v[vM], v[vT3]
    v_mul_f32 v[vAL], s[sCE], v[vAL]
    v_exp_f32 v[vAL], v[vAL]
    v_mov_b32 v[vM], v[vT3]
    v_mul_f32 v[vNMC], 0xbe0293ee, v[vM]
    s_mov_b32 s[sPEND], 1
  .Lkeep\@:
    PVM 3
    KREADO 3, 0
    DMAP 2, \m, 2
    EXPE \cur, 0
    EXPE \cur, 1
    EXPE \cur, 2
    PVM 4
    KREADO 4, 0
    EXPE \cur, 3
    EXPE \cur, 4
    EXPE \cur, 5
    PVM 5
    KREADO 5, 0
    EXPE \cur, 6
    EXPE \cur, 7
    EXPE \cur, 8
    s_waitcnt lgkmcnt(6)
    PVM 6
    KREADO 6, 0
    DMAP 3, \m, 2
    EXPE \cur, 9
    EXPE \cur, 10
    EXPE \cur, 11
    PVM 7
    KREADO 7, 0
    EXPE \cur, 12
    EXPE \cur, 13
    EXPE \cur, 14
    EXPE \cur, 15
    KADV
    VADDR
    s_cmp_eq_u32 s[sPEND], 0
    s_cbranch_scc1 .Lsend\@
    RESCALE
  .Lsend\@:
    s_cmp_eq_u32 s[sU], s[sNF]
  .endm
  .macro LASTS prev, m
    .if \m == 1
      s_cmp_eq_u32 s[sMB], 1
      s_cbranch_scc1 .Lnob\@
    .endif
    s_waitcnt vmcnt(0) lgkmcnt(0)
    s_barrier
  .Lnob\@:
    .set g, 0
    .rept 8
      VREADO g, 0
      SUMPK \prev, g
      .set g, g+1
    .endr
    s_add_u32 s[sU], s[sU], 1
    s_waitcnt lgkmcnt(0)
    .set g, 0
    .rept 8
      PVM g
      .set g, g+1
    .endr
    s_branch .Lnone_tail
  .endm

  ; ---- epilogue pieces
  ; 32 f32 at vO+b.. (one 64-wide half) -> bf16 -> LDS stage (row-major 128 B rows, 16-B chunks xor row) -> 4 full-line stores
  ; t: 16 dead registers (vO+t..) ; vSGW / vSGR: stage addresses ; sT3 = global offset of row 0 of this half
  .macro OUTHALF b, t
    .set q, 0
    .rept 4
      v_cvt_pk_bf16_f32 v[vO+\t+4*q+0], v[vO+\b+8*q+0], v[vO+\b+8*q+1]
      v_cvt_pk_bf16_f32 v[vO+\t+4*q+1], v[vO+\b+8*q+2], v[vO+\b+8*q+3]
      v_cvt_pk_bf16_f32 v[vO+\t+4*q+2], v[vO+\b+8*q+4], v[vO+\b+8*q+5]
      v_cvt_pk_bf16_f32 v[vO+\t+4*q+3], v[vO+\b+8*q+6], v[vO+\b+8*q+7]
      v_xor_b32 v[vT0+q], q*32, v[vSGW]
      .set q, q+1
    .endr
    .set q, 0
    .rept 4
      v_permlane32_swap_b32 v[vO+\t+4*q+0], v[vO+\t+4*q+2]
      v_permlane32_swap_b32 v[vO+\t+4*q+1], v[vO+\t+4*q+3]
      .set q, q+1
    .endr
    .set q, 0
    .rept 4
      ds_write_b128 v[vT0+q], v[vO+\t+4*q:vO+\t+4*q+3]
      .set q, q+1
    .endr
    s_add_u32 s[sT4], s[sT3], 8*4096
    s_add_u32 s[sT1], s[sT3], 16*4096
    s_add_u32 s[sT2], s[sT3], 24*4096
    s_waitcnt lgkmcnt(0)
    STAMPE 4
    ds_read_b128 v[vO+\t+0:vO+\t+3], v[vSGR]
    ds_read_b128 v[vO+\t+4:vO+\t+7], v[vSGR] offset:1024
    ds_read_b128 v[vO+\t+8:vO+\t+11], v[vSGR] offset:2048
    ds_read_b128 v[vO+\t+12:vO+\t+15], v[vSGR] offset:3072
    s_waitcnt lgkmcnt(3)
    buffer_store_dwordx4 v[vO+\t+0:vO+\t+3], v[vSO], s[sO:sO+3], s[sT3] offen
    s_waitcnt lgkmcnt(2)
    buffer_store_dwordx4 v[vO+\t+4:vO+\t+7], v[vSO], s[sO:sO+3], s[sT4] offen
    s_waitcnt lgkmcnt(1)
    buffer_store_dwordx4 v[vO+\t+8:vO+\t+11], v[vSO], s[sO:sO+3], s[sT1] offen
    s_waitcnt lgkmcnt(0)
    buffer_store_dwordx4 v[vO+\t+12:vO+\t+15], v[vSO], s[sO:sO+3], s[sT2] offen
  .endm
  .macro MERGE kb, rb                               ; kb: kept 32 regs, rb: 32 regs receiving the partner's half
    ds_read_b64 v[vT4:vT5], v[vSTP]
    .set i, 0
    .rept 8
      ds_read_b128 v[vO+\rb+4*i:vO+\rb+4*i+3], v[vXP] offset:1024*i
      .set i, i+1
    .endr
    s_waitcnt lgkmcnt(8)
    v_max_f32 v[vT6], v[vM], v[vT4]
    v_sub_f32 v[vFA], v[vM], v[vT6]
    v_sub_f32 v[vFB], v[vT4], v[vT6]
    v_mul_f32 v[vFA], s[sCE], v[vFA]
    v_mul_f32 v[vFB], s[sCE], v[vFB]
    v_exp_f32 v[vFA], v[vFA]
    v_exp_f32 v[vFB], v[vFB]
    s_nop 0
    v_mul_f32 v[vT6], v[vFA], v[vL0]
    v_fma_f32 v[vT6], v[vFB], v[vT5], v[vT6]
    v_rcp_f32 v[vT6], v[vT6]
    s_nop 0
    v_mul_f32 v[vFA], v[vFA], v[vT6]
    v_mul_f32 v[vFB], v[vFB], v[vT6]
    STAMPE 2
    .set i, 0
    .rept 16
      v_pk_mul_f32 v[vO+\kb+2*i:vO+\kb+2*i+1], v[vFA:vFA+1], v[vO+\kb+2*i:vO+\kb+2*i+1] op_sel_hi:[0,1]
      .set i, i+1
    .endr
    .set i, 0
    .rept 8
      .if i == 0
        s_waitcnt lgkmcnt(7)
      .elseif i == 1
        s_waitcnt lgkmcnt(6)
      .elseif i == 2
        s_waitcnt lgkmcnt(5)
      .elseif i == 3
        s_waitcnt lgkmcnt(4)
      .elseif i == 4
        s_waitcnt lgkmcnt(3)
      .elseif i == 5
        s_waitcnt lgkmcnt(2)
      .elseif i == 6
        s_waitcnt lgkmcnt(1)
      .else
        s_waitcnt lgkmcnt(0)
      .endif
      v_pk_fma_f32 v[vO+\kb+4*i:vO+\kb+4*i+1], v[vFB:vFB+1], v[vO+\rb+4*i:vO+\rb+4*i+1], v[vO+\kb+4*i:vO+\kb+4*i+1] op_sel_hi:[0,1,1]
      v_pk_fma_f32 v[vO+\kb+4*i+2:vO+\kb+4*i+3], v[vFB:vFB+1], v[vO+\rb+4*i+2:vO+\rb+4*i+3], v[vO+\kb+4*i+2:vO+\kb+4*i+3] op_sel_hi:[0,1,1]
      .set i, i+1
    .endr
    STAMPE 3
    OUTHALF \kb, \rb
  .endm
  .macro ROWSUM                                     ; complete row sum of this wave's keys -> vL0 (all lanes)
    s_cmp_eq_u32 s[sSAFE], 1
    s_cbranch_scc1 .Lss\@
    v_permlane16_swap_b32 v[vLS], v[vLS+1]
    s_nop 0
    v_mov_b32 v[vL0], v[vLS]
    s_nop 1
    v_permlane32_swap_b32 v[vL0], v[vLS]
    s_branch .Lsd\@
  .Lss\@:
    v_add_f32 v[vL0], v[vL0], v[vL1]
    v_mov_b32 v[vL1], v[vL0]
    s_nop 1
    v_permlane32_swap_b32 v[vL0], v[vL1]
    s_nop 0
    v_add_f32 v[vL0], v[vL0], v[vL1]
  .Lsd\@:
  .endm
  .macro PREFETCH lo, hi                            ; Q prefetch into L2, instructions lo..hi-1 (16 rows each)
    .set n, \lo
    .rept \hi-\lo
      s_mul_i32 s[sT3], s[sJ], s[sPFB]
      s_add_u32 s[sT3], s[sT3], n*16*4096
      s_min_u32 s[sT3], s[sT3], s[sTM16]
      buffer_load_dwordx4 v[vV+4*n:vV+4*n+3], v[vV+28], s[sQ:sQ+3], s[sT3] offen
      .set n, n+1
    .endr
  .endm

  ; ======================================================================== entry
  s_load_dwordx16 s[8:23], %0, 0
  s_load_dwordx8 s[24:31], %0, 64
  .if DIAG
    s_memrealtime s[sRT:sRT+1]
    v_mov_b32 v[vDG], 0
  .endif
  STAMP 0
  v_and_b32 v[vLANE], 63, %2
  v_readfirstlane_b32 s[sW], %2
  v_lshlrev_b32 v[vT0], 2, %2                     ; tid*4
  s_and_b32 s[sG], %1, 7
  s_lshr_b32 s[sJ], %1, 3
  s_lshr_b32 s[sW], s[sW], 6
  v_add_u32 v[vT7], 2048*(NIT-1), v[vT0]
  ; prefetch offset: (tid>>5)*4096 + ((tid>>4)&1)*2048 + ((tid>>3)&1)*128 + (tid&7)*16 + g*256
  v_lshrrev_b32 v[vT6], 5, %2
  v_lshlrev_b32 v[vT6], 12, v[vT6]
  v_bfe_u32 v[vT5], %2, 4, 1
  v_lshl_add_u32 v[vT6], v[vT5], 11, v[vT6]
  v_bfe_u32 v[vT5], %2, 3, 1
  v_lshl_add_u32 v[vT6], v[vT5], 7, v[vT6]
  v_and_b32 v[vT5], 7, %2
  v_lshl_add_u32 v[vT6], v[vT5], 4, v[vT6]
  s_lshl_b32 s[sT3], s[sG], 8
  v_add_u32 v[vV+28], s[sT3], v[vT6]
  ; DMA lane offsets
  v_bfe_u32 v[vT1], v[vLANE], 2, 3                ; prow
  v_lshrrev_b32 v[vT2], 5, v[vLANE]               ; hh / pcb
  v_and_b32 v[vT3], 3, v[vLANE]                   ; pc
  v_lshlrev_b32 v[vT4], 12, v[vT1]
  v_lshl_add_u32 v[vT4], v[vT2], 6, v[vT4]        ; prow*4096 + pcb*64
  v_lshl_add_u32 v[vOV], v[vT3], 4, v[vT4]
  v_bfe_u32 v[vT5], v[vLANE], 4, 1
  s_and_b32 s[sT4], s[sW], 1
  s_lshl_b32 s[sT4], s[sT4], 1
  v_or_b32 v[vT5], s[sT4], v[vT5]                 ; x
  v_xor_b32 v[vT5], v[vT5], v[vT3]
  v_lshl_add_u32 v[vOK], v[vT5], 4, v[vT4]
  s_waitcnt lgkmcnt(0)
  STAMP 1
  ; ======================================================================== S1: segment ends per 64-token chunk
  s_lshl_b32 s[sT2], s[sT], 2
  s_sub_u32 s[sT2], s[sT2], 8                     ; (T-2)*4
  v_min_u32 v[vT7], s[sT2], v[vT7]                ; last chunk: clamped
  .set i, 0
  .rept NIT
    .if i == NIT-1
      buffer_load_dwordx2 v[vX+2*i:vX+2*i+1], v[vT7], s[sS:sS+3], 0 offen
    .elseif i < 2
      buffer_load_dwordx2 v[vX+2*i:vX+2*i+1], v[vT0], s[sS:sS+3], 0 offen offset:2048*i
    .else
      s_movk_i32 s[sT1], 2048*i
      buffer_load_dwordx2 v[vX+2*i:vX+2*i+1], v[vT0], s[sS:sS+3], s[sT1] offen
    .endif
    .set i, i+1
  .endr
  s_sub_u32 s[sTM16], s[sT], 16
  s_lshl_b32 s[sTM16], s[sTM16], 12               ; (T-16)*4096
  ; V descriptor shifted by two tiles: V(u+2) is fetched with the source offset of K(u+4)
  s_sub_u32 s[sV2], s[sV], 2*TILEB
  s_subb_u32 s[sV2+1], s[sV+1], 0
  s_mov_b32 s[sV2+3], s[sV+3]
  ; remaining lane constants (in the shadow of the loads)
  v_add_u32 v[vOK2], 128, v[vOK]
  v_add_u32 v[vOV2], 128, v[vOV]
  v_and_b32 v[vT1], 31, v[vLANE]                  ; r32
  v_lshrrev_b32 v[vT3], 3, v[vT1]
  v_lshlrev_b32 v[vT3], 11, v[vT3]
  v_and_b32 v[vT4], 7, v[vT1]
  v_lshl_add_u32 v[vT3], v[vT4], 6, v[vT3]
  v_bfe_u32 v[vT4], v[vT1], 2, 2
  v_xor_b32 v[vT4], v[vT4], v[vT2]
  v_lshl_add_u32 v[vKB0], v[vT4], 4, v[vT3]
  v_xor_b32 v[vKB1], 32, v[vKB0]
  v_bfe_u32 v[vT3], v[vLANE], 2, 2
  v_lshl_add_u32 v[vT3], v[vT2], 2, v[vT3]
  v_lshlrev_b32 v[vT3], 6, v[vT3]
  v_bfe_u32 v[vT4], v[vLANE], 4, 1
  v_lshl_add_u32 v[vT3], v[vT4], 5, v[vT3]
  v_and_b32 v[vT4], 3, v[vLANE]
  v_lshl_add_u32 v[vVB], v[vT4], 3, v[vT3]
  v_add_u32 v[vVB], LV, v[vVB]
  v_lshlrev_b32 v[vHH4], 2, v[vT2]
  ; output staging: write address r32*128 + ((r32&7)^hh)*16 (chunk pair p: xor p*32)
  v_and_b32 v[vT3], 7, v[vT1]
  v_xor_b32 v[vT3], v[vT3], v[vT2]
  v_lshlrev_b32 v[vT3], 4, v[vT3]
  v_lshl_add_u32 v[vSW], v[vT1], 7, v[vT3]
  ; read address: row = lane>>3, chunk = (lane&7) ^ row ; rows + 8i -> + 1024 i
  v_lshrrev_b32 v[vT3], 3, v[vLANE]
  v_and_b32 v[vT4], 7, v[vLANE]
  v_xor_b32 v[vT5], v[vT4], v[vT3]
  v_lshlrev_b32 v[vT5], 4, v[vT5]
  v_lshl_add_u32 v[vSR], v[vT3], 7, v[vT5]
  ; global store lane offset: (lane>>3)*4096 + (lane&7)*16
  v_lshlrev_b32 v[vT4], 4, v[vT4]
  v_lshl_add_u32 v[vSO], v[vT3], 12, v[vT4]
  ; row-sum MFMA A operand: row (lane&15) 0 takes k-groups 0,2 ; row 1 takes k-groups 1,3
  v_and_b32 v[vT3], 15, v[vLANE]
  v_bfe_u32 v[vT4], v[vLANE], 4, 1
  v_cmp_eq_u32 vcc, v[vT3], v[vT4]
  v_mov_b32 v[vT5], 0x3f803f80
  s_mov_b32 s[sCE], 0x3e0293ee
  v_cndmask_b32 v[vONE], 0, v[vT5], vcc
  v_cndmask_b32 v[vONE+1], 0, v[vT5], vcc
  v_cndmask_b32 v[vONE+2], 0, v[vT5], vcc
  v_cndmask_b32 v[vONE+3], 0, v[vT5], vcc
  s_mov_b32 s[sTHR], 0x427af232
  s_lshl_b32 s[sWD], s[sW], 11                    ; w*2048
  s_mov_b64 s[sC:sC+1], 0
  v_mov_b32 v[vT4], 0
  v_mov_b32 v[vT5], 0
  v_lshlrev_b32 v[vT7], 7, v[vLANE]               ; LDS address of chunk (w + 8*lane): 16 B each
  s_lshl_b32 s[sT1], s[sW], 4
  s_add_u32 s[sT1], s[sT1], LSCR
  v_add_u32 v[vT7], s[sT1], v[vT7]
  PREFETCH 0, PF1
  .set i, 0
  .rept NIT
    s_waitcnt vmcnt(NIT-1-i+PF1)
    v_cmp_ne_u32 s[sA:sA+1], v[vX+2*i], v[vX+2*i+1]
    v_cmp_gt_i32 vcc, v[vX+2*i], v[vX+2*i+1]
    .if i == NIT-1
      v_cmp_gt_i32 s[sB:sB+1], s[sLASTC], %2       ; element < T-1
      s_and_b64 s[sA:sA+1], s[sA:sA+1], s[sB:sB+1]
      s_and_b64 vcc, vcc, s[sB:sB+1]
      v_cmp_eq_u32 s[sB:sB+1], s[sLASTC], %2       ; element == T-1
      s_or_b64 s[sA:sA+1], s[sA:sA+1], s[sB:sB+1]
    .endif
    s_or_b64 s[sC:sC+1], s[sC:sC+1], vcc
    s_nop 0
    v_writelane_b32 v[vT4], s[sA], i
    v_writelane_b32 v[vT5], s[sA+1], i
    .set i, i+1
  .endr
  s_or_b32 s[sT1], s[sC], s[sC+1]
  v_mov_b32 v[vT6], s[sT1]
  s_mov_b64 exec, (1<<NIT)-1
  ds_write_b96 v[vT7], v[vT4:vT6]
  s_mov_b64 exec, -1
  v_mov_b32 v[vT0], 0
  v_mov_b32 v[vT1], 0
  v_mov_b32 v[vT2], 0
  v_lshlrev_b32 v[vT3], 4, v[vLANE]
  v_add_u32 v[vT3], LSCR, v[vT3]
  v_lshlrev_b32 v[vT7], 6, v[vLANE]
  v_add_u32 v[vT7], 1, v[vT7]                     ; lane*64 + 1
  PREFETCH PF1, PF2
  s_waitcnt lgkmcnt(0)
  s_barrier
  STAMP 2
  ; ======================================================================== S2
  s_bfm_b64 exec, 8*NIT, 0
  ds_read_b96 v[vT0:vT2], v[vT3]
  s_mov_b64 exec, -1
  s_movk_i32 s[sT1], 128
  s_movk_i32 s[sT2], 256
  s_mov_b32 s[sR], 0
  s_waitcnt lgkmcnt(0)
  v_cmp_ne_u32 vcc, 0, v[vT2]
  v_bcnt_u32_b32 v[vT3], v[vT0], 0
  v_bcnt_u32_b32 v[vT3], v[vT1], v[vT3]
  s_cbranch_vccnz .Lfallback
  v_cmp_lt_u32 vcc, 1, v[vT3]
  v_ffbl_b32 v[vT4], v[vT0]
  v_ffbl_b32 v[vT5], v[vT1]
  s_cbranch_vccnz .Lfallback
  v_cmp_ne_u32 s[sA:sA+1], 0, v[vT3]
  v_add_u32 v[vT5], 32, v[vT5]
  v_min_u32 v[vT4], v[vT4], v[vT5]
  v_add_u32 v[vT4], v[vT4], v[vT7]
  v_cndmask_b32 v[vT4], 0, v[vT4], s[sA:sA+1]     ; end (0 if none)
  s_nop 1
  v_mov_b32_dpp v[vSTART], v[vT4] wave_shr:1 row_mask:0xf bank_mask:0xf bound_ctrl:0
  s_nop 1
  v_max_u32_dpp v[vSTART], v[vSTART], v[vSTART] row_shr:1 row_mask:0xf bank_mask:0xf bound_ctrl:0
  s_nop 1
  v_max_u32_dpp v[vSTART], v[vSTART], v[vSTART] row_shr:2 row_mask:0xf bank_mask:0xf bound_ctrl:0
  s_nop 1
  v_max_u32_dpp v[vSTART], v[vSTART], v[vSTART] row_shr:4 row_mask:0xf bank_mask:0xf bound_ctrl:0
  s_nop 1
  v_max_u32_dpp v[vSTART], v[vSTART], v[vSTART] row_shr:8 row_mask:0xf bank_mask:0xf bound_ctrl:0
  s_nop 1
  v_max_u32_dpp v[vSTART], v[vSTART], v[vSTART] row_bcast:15 row_mask:0xa bank_mask:0xf
  s_nop 1
  v_max_u32_dpp v[vSTART], v[vSTART], v[vSTART] row_bcast:31 row_mask:0xc bank_mask:0xf
  s_nop 0
  v_sub_u32 v[vLEN], v[vT4], v[vSTART]
  v_cndmask_b32 v[vLEN], 0, v[vLEN], s[sA:sA+1]   ; len (0 if none)
  v_add_u32 v[vT5], 127, v[vLEN]
  v_lshrrev_b32 v[vT5], 7, v[vT5]                 ; nq (mode A)
  v_cmp_lt_u32 vcc, s[sT1], v[vLEN]               ; len > 128
  v_cmp_ge_u32 s[sB:sB+1], s[sT2], v[vLEN]        ; len <= 256
  s_and_b64 s[sB:sB+1], s[sB:sB+1], vcc           ; mode B
  v_lshlrev_b32 v[vT5], 1, v[vT5]
  s_nop 0
  v_cndmask_b32 v[vT5], v[vT5], 2, s[sB:sB+1]     ; items (2 heads)
  v_lshlrev_b32 v[vT6], 16, v[vT5]
  v_cndmask_b32 v[vINCL], v[vT6], v[vT5], vcc     ; small -> high half
  v_mov_b32 v[vEXCL], v[vINCL]
  s_nop 0
  v_add_u32_dpp v[vINCL], v[vINCL], v[vINCL] row_shr:1 row_mask:0xf bank_mask:0xf bound_ctrl:0
  s_nop 1
  v_add_u32_dpp v[vINCL], v[vINCL], v[vINCL] row_shr:2 row_mask:0xf bank_mask:0xf bound_ctrl:0
  s_nop 1
  v_add_u32_dpp v[vINCL], v[vINCL], v[vINCL] row_shr:4 row_mask:0xf bank_mask:0xf bound_ctrl:0
  s_nop 1
  v_add_u32_dpp v[vINCL], v[vINCL], v[vINCL] row_shr:8 row_mask:0xf bank_mask:0xf bound_ctrl:0
  s_nop 1
  v_add_u32_dpp v[vINCL], v[vINCL], v[vINCL] row_bcast:15 row_mask:0xa bank_mask:0xf
  s_nop 1
  v_add_u32_dpp v[vINCL], v[vINCL], v[vINCL] row_bcast:31 row_mask:0xc bank_mask:0xf
  s_nop 0
  v_sub_u32 v[vEXCL], v[vINCL], v[vEXCL]
  v_readlane_b32 s[sT1], v[vINCL], 63
  s_and_b32 s[sNBIG], s[sT1], 0xffff
  s_lshr_b32 s[sTOT], s[sT1], 16
  s_add_u32 s[sTOT], s[sTOT], s[sNBIG]
  s_mov_b32 s[sSAFE], 0
  STAMP 3

.Litem:
  ; idx = r*32 + (r odd ? 31-j : j)
  s_lshl_b32 s[sT1], s[sR], 5
  s_sub_u32 s[sT2], 31, s[sJ]
  s_bitcmp1_b32 s[sR], 0
  s_cselect_b32 s[sT2], s[sT2], s[sJ]
  s_add_u32 s[sT1], s[sT1], s[sT2]                ; idx
  s_cmp_ge_u32 s[sT1], s[sTOT]
  s_cbranch_scc1 .Ldone
  s_sub_u32 s[sT2], s[sT1], s[sNBIG]
  s_cmp_lt_u32 s[sT1], s[sNBIG]
  s_cselect_b32 s[sT1], s[sT1], s[sT2]            ; index inside its class
  s_cselect_b32 s[sT2], 0, 16                     ; field shift
  v_bfe_u32 v[vT0], v[vINCL], s[sT2], 16
  v_bfe_u32 v[vT1], v[vEXCL], s[sT2], 16
  v_cmp_lt_u32 s[sA:sA+1], s[sT1], v[vT0]
  v_cmp_ge_u32 vcc, s[sT1], v[vT1]
  s_and_b64 s[sA:sA+1], s[sA:sA+1], vcc
  s_ff1_i32_b64 s[sT2], s[sA:sA+1]
  v_readlane_b32 s[sK0], v[vSTART], s[sT2]
  v_readlane_b32 s[sLEN], v[vLEN], s[sT2]
  v_readlane_b32 s[sT3], v[vT1], s[sT2]
  s_sub_u32 s[sT1], s[sT1], s[sT3]                ; local item
  s_and_b32 s[sT2], s[sT1], 1
  s_lshl_b32 s[sT2], s[sT2], 3
  s_add_u32 s[sT2], s[sT2], s[sG]
  s_lshl_b32 s[sHOFF], s[sT2], 8                  ; head*256 bytes
  s_lshr_b32 s[sT1], s[sT1], 1
  s_lshl_b32 s[sT1], s[sT1], 7
  s_add_u32 s[sQ0], s[sK0], s[sT1]
  s_lshl_b32 s[sT1], s[sW], 3
  s_add_u32 s[sT2], s[sQ0], s[sT1]
  s_lshl_b32 s[sT2], s[sT2], 12
  s_add_u32 s[sT2], s[sT2], s[sHOFF]              ; (q0 + 8w)*4096 + hoff
  ; ---- Q DMA
  s_add_u32 m0, s[sWD], LX
  s_add_u32 s[sT3], s[sT2], 64*4096
  buffer_load_dwordx4 v[vOK], s[sQ:sQ+3], s[sT2] offen lds
  s_add_u32 m0, s[sWD], LX+1024
  s_add_u32 s[sT4], s[sT2], 128*4096
  buffer_load_dwordx4 v[vOK2], s[sQ:sQ+3], s[sT2] offen lds
  s_add_u32 m0, s[sWD], LX+16384
  s_add_u32 s[sT1], s[sK0], s[sT1]
  buffer_load_dwordx4 v[vOK], s[sQ:sQ+3], s[sT3] offen lds
  s_add_u32 m0, s[sWD], LX+16384+1024
  s_lshl_b32 s[sT1], s[sT1], 12
  buffer_load_dwordx4 v[vOK2], s[sQ:sQ+3], s[sT3] offen lds
  s_add_u32 s[sKSO], s[sT1], s[sHOFF]             ; (k0 + 8w)*4096 + hoff
  s_mov_b32 s[sVSO], s[sKSO]
  s_add_u32 s[sQEND], s[sK0], s[sLEN]
  s_add_u32 s[sNK], s[sLEN], 63
  s_lshr_b32 s[sNK], s[sNK], 6
  s_lshl_b32 s[sO+2], s[sQEND], 12                ; the output, K and V descriptors end at the segment end
  s_mov_b32 s[sK+2], s[sO+2]
  s_mov_b32 s[sV+2], s[sO+2]
  s_add_u32 s[sV2+2], s[sO+2], 2*TILEB
  s_mov_b32 s[sKDST], s[sWD]
  s_mov_b32 s[sVDST], s[sWD]
  s_sub_u32 s[sT1], s[sLEN], 129
  s_cmp_lt_u32 s[sT1], 128
  s_cselect_b32 s[sMB], 1, 0                      ; mode B: 128 < len <= 256
  s_cbranch_scc0 .Lq_done
  s_add_u32 m0, s[sWD], LQ2
  s_add_u32 s[sT3], s[sT4], 64*4096
  buffer_load_dwordx4 v[vOK], s[sQ:sQ+3], s[sT4] offen lds
  s_add_u32 m0, s[sWD], LQ2+1024
  s_nop 0
  buffer_load_dwordx4 v[vOK2], s[sQ:sQ+3], s[sT4] offen lds
  s_add_u32 m0, s[sWD], LQ3
  s_nop 0
  buffer_load_dwordx4 v[vOK], s[sQ:sQ+3], s[sT3] offen lds
  s_add_u32 m0, s[sWD], LQ3+1024
  s_nop 0
  buffer_load_dwordx4 v[vOK2], s[sQ:sQ+3], s[sT3] offen lds
.Lq_done:
  DK1
  DK2
  STAMP 4
  ; ---- per-wave geometry
  s_and_b32 s[sT1], s[sW], 3
  s_lshr_b32 s[sT2], s[sW], 2
  s_cmp_eq_u32 s[sMB], 1
  s_cselect_b32 s[sQB], s[sW], s[sT1]             ; q block
  s_cselect_b32 s[sKH], 0, s[sT2]                 ; key half (mode A)
  s_movk_i32 s[sKSTEP], 0x4000
  s_cselect_b32 s[sKSTEP], 8192, s[sKSTEP]
  s_add_u32 s[sT1], s[sLEN], 31
  s_lshr_b32 s[sT1], s[sT1], 5                    ; nst = ceil(len/32)
  s_add_u32 s[sT2], s[sT1], 1
  s_sub_u32 s[sT2], s[sT2], s[sKH]
  s_lshr_b32 s[sT2], s[sT2], 1
  s_cmp_eq_u32 s[sMB], 1
  s_cselect_b32 s[sNS], s[sT1], s[sT2]            ; steps of this wave
  s_cselect_b32 s[sNU], s[sT1], s[sNK]            ; step slots
  s_and_b32 s[sLIM], s[sLEN], 31
  s_sub_u32 s[sT2], s[sT1], 1
  s_and_b32 s[sT2], s[sT2], 1
  s_cmp_eq_u32 s[sT2], s[sKH]
  s_cselect_b32 s[sT3], 1, s[sMB]                 ; owner of the last sub-tile
  s_cmp_lg_u32 s[sLIM], 0
  s_cselect_b32 s[sT3], s[sT3], 0
  s_sub_u32 s[sMSTEP], s[sNS], 1
  s_cmp_eq_u32 s[sT3], 1
  s_cselect_b32 s[sMSTEP], s[sMSTEP], -1
  s_lshl_b32 s[sT1], s[sQB], 5
  s_add_u32 s[sQR], s[sT1], s[sQ0]                ; first q row of this wave
  s_cmp_lt_u32 s[sQR], s[sQEND]
  s_cselect_b32 s[sNS], s[sNS], 0
  ; slots with DMA: tile iteration t <= nk-3
  s_sub_i32 s[sND], s[sNK], 2
  s_max_i32 s[sND], s[sND], 0
  s_lshl_b32 s[sND], s[sND], s[sMB]
  s_sub_u32 s[sNF], s[sNS], 1                     ; FULL slots of this wave
  s_sub_u32 s[sCNT], 0, s[sNF]
  s_mov_b32 s[sMK], s[sWD]
  s_add_u32 s[sMV], s[sWD], LV+0x8000
  ; Q read base
  s_lshl_b32 s[sT1], s[sQB], 13
  s_add_u32 s[sT2], s[sT1], LX                    ; qb < 4
  s_add_u32 s[sT3], s[sT1], LQ2-4*8192            ; qb 4,5
  s_add_u32 s[sT4], s[sT1], LQ3-6*8192            ; qb 6,7
  s_cmp_lt_u32 s[sQB], 6
  s_cselect_b32 s[sT3], s[sT3], s[sT4]
  s_cmp_lt_u32 s[sQB], 4
  s_cselect_b32 s[sT2], s[sT2], s[sT3]
  v_add_u32 v[vT0], s[sT2], v[vKB0]
  v_add_u32 v[vT1], s[sT2], v[vKB1]
  s_lshl_b32 s[sKOFF], s[sKH], 13
  s_mov_b32 s[sVOFF], s[sKOFF]
  v_add_u32 v[vKA0], s[sKOFF], v[vKB0]
  v_add_u32 v[vKA1], s[sKOFF], v[vKB1]
  s_lshl_b32 s[sT1], s[sKH], 10
  v_add_u32 v[vVBL], s[sT1], v[vVB]
  v_subrev_u32 v[vVBH], s[sT1], v[vVB]
  VADDR
  ; epilogue addresses
  v_lshlrev_b32 v[vT2], 4, v[vLANE]
  s_lshl_b32 s[sT1], s[sW], 13
  s_xor_b32 s[sT2], s[sT1], 4*8192
  v_add_u32 v[vXP], s[sT2], v[vT2]                ; partner's exchange area
  v_add_u32 v[vXO], s[sT1], v[vT2]                ; own exchange area
  v_and_b32 v[vT2], 31, v[vLANE]
  v_lshlrev_b32 v[vT2], 3, v[vT2]
  v_add_u32 v[vT2], LX, v[vT2]
  s_lshl_b32 s[sT1], s[sW], 8
  s_xor_b32 s[sT2], s[sT1], 4*256
  v_add_u32 v[vSTP], s[sT2], v[vT2]
  v_add_u32 v[vSTA], s[sT1], v[vT2]
  s_lshl_b32 s[sT1], s[sW], 12                    ; output stage: mode A in the V ring (4 KiB per wave), mode B in the K ring (8 KiB)
  s_add_u32 s[sT2], s[sT1], LV
  s_lshl_b32 s[sT3], s[sW], 13
  s_cmp_eq_u32 s[sMB], 1
  s_cselect_b32 s[sT1], s[sT3], s[sT2]
  v_add_u32 v[vSGW], s[sT1], v[vSW]
  v_add_u32 v[vSGR], s[sT1], v[vSR]
  v_mov_b32 v[vL0], 0
  v_mov_b32 v[vL1], 0
  v_mov_b32 v[vLS], 0
  v_mov_b32 v[vLS+1], 0
  v_mov_b32 v[vLS+2], 0
  v_mov_b32 v[vLS+3], 0
  s_nop 1
  .set i, 0
  .rept 4
    v_mfma_f32_32x32x16_bf16 v[vO+16*i:vO+16*i+15], v[vLS:vLS+3], v[vLS:vLS+3], 0     ; O = 0
    .set i, i+1
  .endr
  v_mov_b32 v[vM], 0xf149f2ca
  s_mov_b32 s[sPEND], 0
  s_mov_b32 s[sU], 0
  ; mid group: mode A: K1, K2, V0 ; mode B: K1, V0
  s_waitcnt vmcnt(2)                                ; own Q pieces landed
  DK1
  DK2
  s_cmp_eq_u32 s[sMB], 1
  s_cbranch_scc1 .Lmid_b
  DK1
  DK2
  DV1
  DV2
  s_waitcnt vmcnt(6)
  s_branch .Lmid_d
.Lmid_b:
  DV1
  DV2
  s_waitcnt vmcnt(4)
.Lmid_d:
  s_barrier                                         ; barrier P: Q, K0 landed
  STAMP 5
  ; ---- prologue compute: step 0
  s_cmp_eq_u32 s[sNS], 0
  s_cbranch_scc1 .Lpro_none
  .set i, 0
  .rept 8
    KREADO i, 0
    .if (i & 1)
      ds_read_b128 v[vQ+4*i:vQ+4*i+3], v[vT1] offset:512*(i>>1)
    .else
      ds_read_b128 v[vQ+4*i:vQ+4*i+3], v[vT0] offset:512*(i>>1)
    .endif
    .set i, i+1
  .endr
  .macro QK0 rd                                     ; rd: read the next step's K fragments behind each MFMA (mode B: same tile)
    .set i, 0
    .rept 8
      .if \rd
        .if i == 0
          s_waitcnt lgkmcnt(14)
        .elseif i == 1
          s_waitcnt lgkmcnt(13)
        .elseif i == 2
          s_waitcnt lgkmcnt(12)
        .elseif i == 3
          s_waitcnt lgkmcnt(11)
        .elseif i == 4
          s_waitcnt lgkmcnt(10)
        .elseif i == 5
          s_waitcnt lgkmcnt(9)
        .elseif i == 6
          s_waitcnt lgkmcnt(8)
        .else
          s_waitcnt lgkmcnt(7)
        .endif
      .else
        .if i == 0
          s_waitcnt lgkmcnt(14)
        .elseif i == 1
          s_waitcnt lgkmcnt(12)
        .elseif i == 2
          s_waitcnt lgkmcnt(10)
        .elseif i == 3
          s_waitcnt lgkmcnt(8)
        .elseif i == 4
          s_waitcnt lgkmcnt(6)
        .elseif i == 5
          s_waitcnt lgkmcnt(4)
        .elseif i == 6
          s_waitcnt lgkmcnt(2)
        .else
          s_waitcnt lgkmcnt(0)
        .endif
      .endif
      QKM vSA, i
      .if \rd
        KREADO i, 0
      .endif
      .if i == 1
        DK1
      .elseif i == 3
        DK2
      .elseif i == 5
        DV1
      .elseif i == 7
        DV2
      .endif
      .set i, i+1
    .endr
  .endm
  s_cmp_eq_u32 s[sMB], 1
  s_cbranch_scc1 .Lqk0_b
  QK0 0
  s_branch .Lqk0_d
.Lqk0_b:
  KADV
  QK0 1
  KADV
.Lqk0_d:
  s_nop 7
  s_cmp_lg_u32 s[sMSTEP], 0
  s_cbranch_scc1 .Lnomask0
  s_nop 3
  MASK vSA
.Lnomask0:
  s_nop 3
  ROWMAX1 vSA
  s_nop 1
  ROWMAX2
  v_max_f32 v[vM], v[vM], v[vT2]
  v_mul_f32 v[vNMC], 0xbe0293ee, v[vM]
  s_cmp_eq_u32 s[sSAFE], 1
  s_cbranch_scc1 .Lpro_safe
  .set r, 0
  .rept 6
    EXPE vSA, r
    .set r, r+1
  .endr
  s_cmp_eq_u32 s[sMB], 1
  s_cbranch_scc1 .Lpro_b
  ; ---- mode A: K1 landed -> its fragments
  s_waitcnt vmcnt(8)
  s_barrier
  .set i, 0
  .rept 8
    KREADO i, 16384
    .set i, i+1
  .endr
  .set r, 6
  .rept 5
    EXPE vSA, r
    .set r, r+1
  .endr
  v_xor_b32 v[vKA0], 0x8000, v[vKA0]                ; K(2) sits in ring slot 2
  v_xor_b32 v[vKA1], 0x8000, v[vKA1]
  STAMP 6
  s_cmp_eq_u32 s[sNF], 0
  s_cbranch_scc1 .LlA0
  ; ======================================================================== mode A loop
.LA0:
  FULLF vSB, vSA, 0, 0, 0
  s_cbranch_scc1 .LlA1
  FULLF vSA, vSB, 0, 1, 0
  s_cbranch_scc0 .LA0
.LlA0:
  LASTF vSA, 0, 0, 0
.LlA1:
  LASTF vSB, 0, 1, 0
  ; ======================================================================== epilogue (mode A)
.Lepi_sent:
  STAMP 7
  v_mov_b32 v[vT7], 0x71800000
  s_lshl_b32 s[sT3], s[sQR], 12
  s_add_u32 s[sT3], s[sT3], s[sHOFF]
  ROWSUM
  v_mov_b32 v[vT4], v[vM]
  v_mov_b32 v[vT5], v[vL0]
  s_lshl_b32 s[sT1], s[sKH], 7
  s_add_u32 s[sT3], s[sT3], s[sT1]
  ds_write_b64 v[vSTA], v[vT4:vT5]
  s_waitcnt lgkmcnt(0)
  s_barrier
  STAMPE 1
  MERGE 0, 32
.Lnext:
  STAMP 8
  ; overflow check of the FAST path: a row sum that is not a moderately sized number -> redo this item with the online max
  v_cmp_nlt_f32 vcc, v[vL0], v[vT7]
  s_lshl_b32 s[sT1], s[sW], 2
  s_add_u32 s[sT1], s[sT1], LX+8192
  s_cmp_lg_u64 vcc, 0
  s_cselect_b32 s[sT2], 1, 0
  v_mov_b32 v[vT0], s[sT1]
  v_mov_b32 v[vT1], s[sT2]
  v_and_b32 v[vT2], 7, v[vLANE]
  v_lshlrev_b32 v[vT2], 2, v[vT2]
  v_add_u32 v[vT2], LX+8192, v[vT2]
  ds_write_b32 v[vT0], v[vT1]
  s_waitcnt lgkmcnt(0)
  s_barrier
  ds_read_b32 v[vT1], v[vT2]
  s_waitcnt lgkmcnt(0)
  v_cmp_ne_u32 vcc, 0, v[vT1]
  s_cmp_eq_u32 s[sSAFE], 1
  s_cbranch_scc1 .Lsafe_done
  s_cbranch_vccz .Lsafe_done
  s_mov_b32 s[sSAFE], 1
  s_barrier
  s_branch .Litem
.Lsafe_done:
  s_mov_b32 s[sSAFE], 0
  s_add_u32 s[sR], s[sR], 1
  s_lshl_b32 s[sT1], s[sR], 5
  s_cmp_ge_u32 s[sT1], s[sTOT]
  s_cbranch_scc1 .Ldone
  s_barrier
  s_branch .Litem
.Ldone:
  .if DIAG
    s_waitcnt vmcnt(0)
    STAMP 9
    s_memrealtime s[sA:sA+1]
    s_load_dwordx2 s[sB:sB+1], %0, 96
    s_lshl_b32 s[sT1], %1, 3
    s_add_u32 s[sT1], s[sT1], s[sW]
    s_lshl_b32 s[sT1], s[sT1], 8
    s_waitcnt lgkmcnt(0)
    s_add_u32 s[sB], s[sB], s[sT1]
    s_addc_u32 s[sB+1], s[sB+1], 0
    v_writelane_b32 v[vDG], s[sRT], 10
    v_writelane_b32 v[vDG], s[sA], 11
    v_lshlrev_b32 v[vT6], 2, v[vLANE]
    s_mov_b64 exec, 0xfff
    global_store_dword v[vT6], v[vDG], s[sB:sB+1]
  .endif
  s_endpgm
  ; ======================================================================== cold paths
.Lepi:                                              ; O complete, nothing sent yet
  s_cmp_eq_u32 s[sMB], 1
  s_cbranch_scc1 .LepiB
  s_nop 7
  SENDB 2
  SENDB 3
  s_branch .Lepi_sent
.LepiB:
  STAMP 7
  v_mov_b32 v[vT7], 0x71800000
  s_lshl_b32 s[sT3], s[sQR], 12
  s_add_u32 s[sT3], s[sT3], s[sHOFF]
  ROWSUM
  s_nop 0
  v_rcp_f32 v[vFA], v[vL0]
  s_waitcnt vmcnt(0)
  s_barrier
  .set i, 0
  .rept 32
    v_pk_mul_f32 v[vO+2*i:vO+2*i+1], v[vFA:vFA+1], v[vO+2*i:vO+2*i+1] op_sel_hi:[0,1]
    .set i, i+1
  .endr
  OUTHALF 0, vQ-vO
  v_add_u32 v[vSGW], 4096, v[vSGW]
  v_add_u32 v[vSGR], 4096, v[vSGR]
  s_add_u32 s[sT3], s[sT3], 128
  s_nop 0
  OUTHALF 32, vQ-vO+16
  s_branch .Lnext
  ; ======================================================================== mode B loop (two steps per tile)
.Lpro_b:
  .set r, 6
  .rept 5
    EXPE vSA, r
    .set r, r+1
  .endr
  STAMP 6
  s_cmp_eq_u32 s[sNF], 0
  s_cbranch_scc1 .LlB0
.LB0:
  FULLF vSB, vSA, 1, 0, 0
  s_cbranch_scc1 .LlB1
  FULLF vSA, vSB, 1, 0, 2
  s_cbranch_scc0 .LB0
.LlB0:
  LASTF vSA, 1, 0, 0
.LlB1:
  LASTF vSB, 1, 0, 2
  ; ======================================================================== SAFE loop
.Lpro_safe:
  s_cmp_eq_u32 s[sMB], 1
  s_cbranch_scc1 .Lps_b
  KADV
  s_waitcnt vmcnt(8)
  s_barrier
  .set i, 0
  .rept 8
    KREADO i, 0
    .set i, i+1
  .endr
  KADV
.Lps_b:
  .set r, 0
  .rept 16
    EXPE vSA, r
    .set r, r+1
  .endr
  s_cmp_eq_u32 s[sNF], 0
  s_cbranch_scc1 .LlastS0
.LloopS:
  FULLS vSB, vSA, 0
  s_cbranch_scc1 .LlastS1
  FULLS vSA, vSB, 1
  s_cbranch_scc0 .LloopS
.LlastS0:
  LASTS vSA, 0
.LlastS1:
  LASTS vSB, 1

  ; ---- slots in which this wave has nothing to compute
.Lpro_none:
  DALL
  s_cmp_eq_u32 s[sMB], 1
  s_cbranch_scc1 .Lnone
  s_waitcnt vmcnt(8)
  s_barrier
.Lnone:                                             ; wave without steps: keep loading its share of the tiles
  s_cmp_ge_u32 s[sU], s[sNU]
  s_cbranch_scc1 .Lepi
  s_bitcmp1_b32 s[sU], 0
  s_cselect_b32 s[sT1], s[sMB], 0
  s_cmp_eq_u32 s[sT1], 1
  s_cbranch_scc1 .Lnone_skip
  s_cmp_lt_u32 s[sU], s[sND]
  s_cbranch_scc0 .Lnone_fin
  s_waitcnt vmcnt(4) lgkmcnt(0)
  s_barrier
  DALL
  s_branch .Lnone_skip
.Lnone_fin:
  s_waitcnt vmcnt(0) lgkmcnt(0)
  s_barrier
.Lnone_skip:
  s_add_u32 s[sU], s[sU], 1
  s_branch .Lnone
.Lnone_tail:                                        ; after the wave's last step: only barriers remain
  s_cmp_ge_u32 s[sU], s[sNU]
  s_cbranch_scc1 .Lepi
  s_bitcmp1_b32 s[sU], 0
  s_cselect_b32 s[sT1], s[sMB], 0
  s_cmp_eq_u32 s[sT1], 1
  s_cbranch_scc1 .Lnt_skip
  s_barrier
.Lnt_skip:
  s_add_u32 s[sU], s[sU], 1
  s_branch .Lnone_tail

.Lfallback:
  s_waitcnt vmcnt(0) lgkmcnt(0)
  .purgem STAMP
  .purgem STAMPE
  .purgem DK1
  .purgem DK2
  .purgem DV1
  .purgem DV2
  .purgem DALL
  .purgem BARX
  .purgem DMAP
  .purgem KREADO
  .purgem KADV
  .purgem VADDR
  .purgem QKM
  .purgem VREADO
  .purgem PVJ
  .purgem PVM
  .purgem LSUM
  .purgem FMAE
  .purgem XPE
  .purgem EXPE
  .purgem CVTE
  .purgem SUMPK
  .purgem MASK
  .purgem ROWMAX1
  .purgem ROWMAX2
  .purgem RESCALE
  .purgem SENDB
  .purgem LASTV
  .purgem FDMA
  .purgem FDMB
  .purgem FULLF
  .purgem LASTF
  .purgem FULLS
  .purgem LASTS
  .purgem OUTHALF
  .purgem MERGE
  .purgem ROWSUM
  .purgem PREFETCH
  .purgem QK0
  )" :: "s"(__builtin_amdgcn_kernarg_segment_ptr()), "s"(blockIdx.x), "v"(threadIdx.x), "i"(NIT)
     : "memory", "vcc", "m0",
       "s8","s9","s10","s11","s12","s13","s14","s15","s16","s17","s18","s19","s20","s21","s22","s23","s24","s25","s26","s27","s28","s29","s30","s31",
       "s33","s34","s35","s36","s37","s38","s39","s40","s41","s42","s43","s44","s45","s46","s47","s48","s49","s50","s51","s52","s53","s54","s55",
       "s56","s57","s58","s59","s60","s61","s62","s63","s64","s65","s66","s67","s68","s69","s70","s71","s72","s73","s74","s75","s76","s77","s78","s79",
       "s80","s81","s82","s83","s84","s85","s86","s87","s88","s89","s90","s91","s92","s93","s94","s95","s96","s97","s98","s99",
       "v1","v2","v3","v4","v5","v6","v7","v8","v9","v10","v11","v12","v13","v14","v15","v16","v17","v18","v19","v20","v21","v22","v23","v24","v25","v26","v27","v28","v29","v30","v31",
       "v32","v33","v34","v35","v36","v37","v38","v39","v40","v41","v42","v43","v44","v45","v46","v47","v48","v49","v50","v51","v52","v53","v54","v55","v56","v57","v58","v59","v60","v61","v62","v63",
       "v64","v65","v66","v67","v68","v69","v70","v71","v72","v73","v74","v75","v76","v77","v78","v79","v80","v81","v82","v83","v84","v85","v86","v87","v88","v89","v90","v91","v92","v93","v94","v95",
       "v96","v97","v98","v99","v100","v101","v102","v103","v104","v105","v106","v107","v108","v109","v110","v111","v112","v113","v114","v115","v116","v117","v118","v119","v120","v121","v122","v123","v124","v125","v126","v127",
       "v128","v129","v130","v131","v132","v133","v134","v135","v136","v137","v138","v139","v140","v141","v142","v143","v144","v145","v146","v147","v148","v149","v150","v151","v152","v153","v154","v155","v156","v157","v158","v159",
       "v160","v161","v162","v163","v164","v165","v166","v167","v168","v169","v170","v171","v172","v173","v174","v175","v176","v177","v178","v179","v180","v181","v182","v183","v184","v185","v186","v187","v188","v189","v190","v191",
       "v192","v193","v194","v195","v196","v197","v198","v199","v200","v201","v202","v203","v204","v205","v206","v207","v208","v209","v210","v211","v212","v213","v214","v215","v216","v217","v218","v219","v220","v221","v222","v223",
       "v224","v225","v226","v227","v228","v229","v230","v231","v232","v233","v234","v235","v236","v237","v238","v239","v240","v241","v242","v243","v244","v245","v246","v247","v248","v249","v250","v251","v252","v253","v254","v255");
  generic_path((const unsigned short*)Q, (const unsigned short*)K, (const unsigned short*)V, (const int*)seg, (unsigned short*)O, T, lds);
}

__global__ __launch_bounds__(512, 2)
void ragged_attn_generic(const void* Q, const void* K, const void* V, const void* seg, void* O, int T, int pad) {
  __shared__ __attribute__((aligned(16))) char lds[LDS_BYTES];
  generic_path((const unsigned short*)Q, (const unsigned short*)K, (const unsigned short*)V, (const int*)seg, (unsigned short*)O, T, lds);
}

extern "C" void kernel_launch(void* const* d_in, const int* in_sizes, int n_in, void* d_out, int out_size, void* d_ws, size_t ws_size,
                              hipStream_t stream) {
  const int T = in_sizes[3];
  const unsigned nb = (unsigned)T * ROWB, F = 0x00020000u;
  if (T > 2048 && T <= 2560) {
    ragged_attn_kernel<5><<<256, 512, 0, stream>>>(d_in[0], nb, F, d_in[1], nb, F, d_in[2], nb, F, d_in[3], (unsigned)T * 4u, F, d_out, nb, F,
                                                   T, T - 1 - 512 * 4, ((T + 31) / 32) * (int)ROWB, 0, d_ws);
  } else {
    ragged_attn_generic<<<256, 512, 0, stream>>>(d_in[0], d_in[1], d_in[2], d_in[3], d_out, T, 0);
  }
}
```

```cpp
#include <hip/hip_runtime.h>
#include <stdint.h>

typedef short    bf16x8 __attribute__((ext_vector_type(8)));
typedef short    s16x4  __attribute__((ext_vector_type(4)));
typedef float    f32x16 __attribute__((ext_vector_type(16)));
typedef float    f32x4  __attribute__((ext_vector_type(4)));
typedef float    f32x2  __attribute__((ext_vector_type(2)));
typedef __bf16   bf2    __attribute__((ext_vector_type(2)));
typedef unsigned u32x4  __attribute__((ext_vector_type(4)));
typedef unsigned long long u64;
#define LDSP __attribute__((address_space(3)))
typedef LDSP const char* lds_cptr;
typedef LDSP char* lds_ptr;
typedef short v4i16_t __attribute__((ext_vector_type(4)));
#define DI __device__ __forceinline__
#define MFMA(a, b, c) __builtin_amdgcn_mfma_f32_32x32x16_bf16((a), (b), (c), 0, 0, 0)

constexpr int NH = 16, HD = 128;
constexpr unsigned ROWB = NH * HD * 2;
constexpr unsigned LDS_BYTES = 163840;
constexpr float CEXP = 0.08838834764831845f * 1.4426950408889634f;
constexpr float THR_RAW = 8.0f / CEXP;
constexpr float MINIT = -1.0e30f;

DI unsigned rfl(unsigned x) { return __builtin_amdgcn_readfirstlane(x); }
DI unsigned cvtpk(float lo, float hi) { f32x2 v = {lo, hi}; bf2 b = __builtin_convertvector(v, bf2); return __builtin_bit_cast(unsigned, b); }
DI float swap_max(float x) { auto r = __builtin_amdgcn_permlane32_swap(__float_as_uint(x), __float_as_uint(x), false, false); return __builtin_fmaxf(__uint_as_float(r[0]), __uint_as_float(r[1])); }
DI float swap_add(float x) { auto r = __builtin_amdgcn_permlane32_swap(__float_as_uint(x), __float_as_uint(x), false, false); return __uint_as_float(r[0]) + __uint_as_float(r[1]); }
DI s16x4 vtr(lds_cptr p) { return __builtin_bit_cast(s16x4, __builtin_amdgcn_ds_read_tr16_b64_v4i16((LDSP v4i16_t*)p)); }
#define MX3(a, b, c) __builtin_fmaxf(__builtin_fmaxf((a), (b)), (c))

DI void dma16(__amdgpu_buffer_rsrc_t srd, unsigned voff, unsigned soff, unsigned m0v) {
  unsigned keep;
  asm volatile("s_nop 4\n\ts_mov_b32 %0, m0\n\ts_mov_b32 m0, %1\n\ts_nop 0\n\tbuffer_load_dwordx4 %2, %3, %4 offen lds\n\ts_mov_b32 m0, %0"
               : "=&s"(keep) : "s"(m0v), "v"(voff), "s"(srd), "s"(soff) : "memory");
}

__device__ __forceinline__ void generic_path(const unsigned short* __restrict__ Q, const unsigned short* __restrict__ K, const unsigned short* __restrict__ V,
                                          const int* __restrict__ seg, unsigned short* __restrict__ O, int T, char* lds) {
  constexpr unsigned SLOT = 32768, VOFF = 16384, XREG = 4 * SLOT;
  const int tid = threadIdx.x, lane = tid & 63, r32 = lane & 31, hh = lane >> 5;
  const int w = (int)rfl((unsigned)tid >> 6);
  const int grp = blockIdx.x & 7, jj = blockIdx.x >> 3, NJ = 32;
  const unsigned ldsb = (unsigned)(uintptr_t)lds;
  const lds_ptr L = (lds_ptr)lds;
  const int total_items = 2 * ((T + 127) >> 7);
  const __amdgpu_buffer_rsrc_t srdQ = __builtin_amdgcn_make_buffer_rsrc((void*)Q, (short)0, T * (int)ROWB, 0x00020000);
  const __amdgpu_buffer_rsrc_t srdK = __builtin_amdgcn_make_buffer_rsrc((void*)K, (short)0, T * (int)ROWB, 0x00020000);
  const __amdgpu_buffer_rsrc_t srdV = __builtin_amdgcn_make_buffer_rsrc((void*)V, (short)0, T * (int)ROWB, 0x00020000);
  const unsigned prow = (lane >> 2) & 7, pcb = lane >> 5, pc = lane & 3;
  const unsigned voffK = prow * ROWB + pcb * 64 + ((pc ^ ((2u * (unsigned)w + (prow >> 2)) & 3u)) * 16);
  const unsigned voffV = prow * ROWB + pcb * 64 + pc * 16;
  const unsigned kb0 = 2048u * (r32 >> 3) + 64u * (r32 & 7) + 16u * ((unsigned)hh ^ ((r32 >> 2) & 3u));
  const unsigned vb0 = ((unsigned)hh * 4 + ((lane & 15) >> 2)) * 64 + ((lane >> 4) & 1) * 32 + (lane & 3) * 8;
  for (int idx = jj; idx < total_items; idx += NJ) {
    __syncthreads();
    const int head = grp + 8 * (idx & 1);
    const int q0 = (idx >> 1) * 128, qend = min(q0 + 128, T);
    LDSP int* red = (LDSP int*)L;
    if (tid == 0) { red[0] = 0x7fffffff; red[1] = (int)0x80000000; red[2] = 0x7fffffff; red[3] = -1; }
    __syncthreads();
    if (tid < 128 && q0 + tid < qend) { const int id = seg[q0 + tid];
      __hip_atomic_fetch_min((LDSP int*)&red[0], id, __ATOMIC_RELAXED, __HIP_MEMORY_SCOPE_WORKGROUP);
      __hip_atomic_fetch_max((LDSP int*)&red[1], id, __ATOMIC_RELAXED, __HIP_MEMORY_SCOPE_WORKGROUP); }
    __syncthreads();
    const int idlo = red[0], idhi = red[1];
    int lo = 0x7fffffff, hi = -1;
    for (int i = tid; i < T; i += 512) { const int id = seg[i]; if (id >= idlo && id <= idhi) { lo = min(lo, i); hi = max(hi, i); } }
    __hip_atomic_fetch_min((LDSP int*)&red[2], lo, __ATOMIC_RELAXED, __HIP_MEMORY_SCOPE_WORKGROUP);
    __hip_atomic_fetch_max((LDSP int*)&red[3], hi, __ATOMIC_RELAXED, __HIP_MEMORY_SCOPE_WORKGROUP);
    __syncthreads();
    const int k0 = (int)rfl((unsigned)red[2]), kend = (int)rfl((unsigned)red[3]) + 1;
    __syncthreads();
    const int nk = (kend - k0 + 63) >> 6;
    const unsigned hoff = (unsigned)head * 256u;
#pragma unroll
    for (int i = 0; i < 2; ++i) {
      const unsigned m = (unsigned)w + 8u * i;
      const unsigned so = (unsigned)(q0 + 8 * (int)m) * ROWB + hoff;
      dma16(srdQ, voffK, so, ldsb + XREG + m * 2048u);
      dma16(srdQ, voffK, so + 128u, ldsb + XREG + m * 2048u + 1024u);
    }
    const int qb = w & 3, sub = w >> 2;
    const int qrow = q0 + 32 * qb + r32;
    const int segq = seg[qrow < T ? qrow : T - 1];
    bf16x8 qf[8];
    f32x16 o[4];
#pragma unroll
    for (int d = 0; d < 4; ++d) o[d] = f32x16{};
    float m_run = MINIT, l_run = 0.f;
    for (int t = 0; t < nk; ++t) {
      __syncthreads();
      {
        const unsigned so = (unsigned)(k0 + 64 * t + 8 * w) * ROWB + hoff;
        const unsigned d = ldsb + (unsigned)w * 2048u;
        dma16(srdK, voffK, so, d); dma16(srdK, voffK, so + 128u, d + 1024u);
        dma16(srdV, voffV, so, d + VOFF); dma16(srdV, voffV, so + 128u, d + VOFF + 1024u);
      }
      asm volatile("s_waitcnt vmcnt(0)" ::: "memory");
      __syncthreads();
      if (t == 0) {
#pragma unroll
        for (int s = 0; s < 8; ++s)
          qf[s] = *(const LDSP bf16x8*)(L + XREG + (unsigned)qb * 8192u + ((kb0 ^ ((s & 1) * 32u)) + 512u * (s >> 1)));
      }
      const int kbase = k0 + 64 * t + 32 * sub;
      const lds_cptr kp = (lds_cptr)L + (unsigned)sub * 8192u;
      f32x16 sc = f32x16{};
#pragma unroll
      for (int s = 0; s < 8; ++s) {
        const bf16x8 kf = *(const LDSP bf16x8*)(kp + ((kb0 ^ ((s & 1) * 32u)) + 512u * (s >> 1)));
        sc = MFMA(kf, qf[s], sc);
      }
#pragma unroll
      for (int r = 0; r < 16; ++r) {
        const int kk = kbase + (r & 3) + 8 * (r >> 2) + 4 * hh;
        const int id = seg[kk < T ? kk : T - 1];
        sc[r] = (kk < kend && id == segq) ? sc[r] : -__builtin_inff();
      }
      float a = MX3(sc[0], sc[1], sc[2]), b = MX3(sc[3], sc[4], sc[5]);
      a = MX3(a, sc[6], sc[7]); b = MX3(b, sc[8], sc[9]); a = MX3(a, sc[10], sc[11]); b = MX3(b, sc[12], sc[13]);
      a = MX3(a, sc[14], sc[15]);
      const float pm = swap_max(__builtin_fmaxf(a, b));
      const float mn = __builtin_fmaxf(m_run, pm);
      const float al = __builtin_amdgcn_exp2f((m_run - mn) * CEXP);
      m_run = mn; l_run *= al;
#pragma unroll
      for (int d = 0; d < 4; ++d)
#pragma unroll
        for (int r = 0; r < 16; ++r) o[d][r] *= al;
      const float nmc = -m_run * CEXP;
      float ps = 0.f;
#pragma unroll
      for (int r = 0; r < 16; ++r) { sc[r] = __builtin_amdgcn_exp2f(__builtin_fmaf(sc[r], CEXP, nmc)); ps += sc[r]; }
      l_run += ps;
      u32x4 pw0, pw1;
#pragma unroll
      for (int e = 0; e < 4; ++e) { pw0[e] = cvtpk(sc[2 * e], sc[2 * e + 1]); pw1[e] = cvtpk(sc[8 + 2 * e], sc[8 + 2 * e + 1]); }
      const bf16x8 p0 = __builtin_bit_cast(bf16x8, pw0), p1 = __builtin_bit_cast(bf16x8, pw1);
      const lds_cptr vp = kp + VOFF + vb0;
#pragma unroll
      for (int d = 0; d < 4; ++d) {
        const s16x4 a0 = vtr(vp + d * 512), a1 = vtr(vp + d * 512 + 2048), b0 = vtr(vp + d * 512 + 4096), b1 = vtr(vp + d * 512 + 6144);
        const bf16x8 va = __builtin_shufflevector(a0, a1, 0, 1, 2, 3, 4, 5, 6, 7), vb = __builtin_shufflevector(b0, b1, 0, 1, 2, 3, 4, 5, 6, 7);
        o[d] = MFMA(va, p0, o[d]);
        o[d] = MFMA(vb, p1, o[d]);
      }
    }
    l_run = swap_add(l_run);
    unsigned short* orow = O + (size_t)(qrow < T ? qrow : 0) * (NH * HD) + head * HD + 8 * hh;
    const bool row_ok = qrow < qend;
    const int kh = sub;
    __syncthreads();
    const unsigned xa = SLOT, xb = 2 * SLOT, xs = 3 * SLOT;
    const unsigned mine = (kh ? xb : xa) + (unsigned)qb * 8192u + (unsigned)lane * 16u;
    const unsigned theirs = (kh ? xa : xb) + (unsigned)qb * 8192u + (unsigned)lane * 16u;
#pragma unroll
    for (int dd = 0; dd < 2; ++dd)
#pragma unroll
      for (int g = 0; g < 4; ++g) {
        f32x4 v;
#pragma unroll
        for (int e = 0; e < 4; ++e) v[e] = kh ? o[dd][4 * g + e] : o[2 + dd][4 * g + e];
        *(LDSP f32x4*)(L + mine + (unsigned)(dd * 4 + g) * 1024u) = v;
      }
    if (hh == 0) { f32x2 s2 = {m_run, l_run}; *(LDSP f32x2*)(L + xs + (unsigned)w * 256u + (unsigned)r32 * 8u) = s2; }
    __syncthreads();
    const f32x2 o2 = *(const LDSP f32x2*)(L + xs + (unsigned)(w ^ 4) * 256u + (unsigned)r32 * 8u);
    const float mn = __builtin_fmaxf(m_run, o2[0]);
    const float wa = __builtin_amdgcn_exp2f((m_run - mn) * CEXP), wb = __builtin_amdgcn_exp2f((o2[0] - mn) * CEXP);
    const float inv = 1.0f / (wa * l_run + wb * o2[1]);
    const float fa = wa * inv, fb = wb * inv;
#pragma unroll
    for (int dd = 0; dd < 2; ++dd) {
      float r[16];
#pragma unroll
      for (int g = 0; g < 4; ++g) {
        const f32x4 v = *(const LDSP f32x4*)(L + theirs + (unsigned)(dd * 4 + g) * 1024u);
#pragma unroll
        for (int e = 0; e < 4; ++e) r[4 * g + e] = fa * (kh ? o[2 + dd][4 * g + e] : o[dd][4 * g + e]) + fb * v[e];
      }
#pragma unroll
      for (int g = 0; g < 4; g += 2) {
        unsigned ax = cvtpk(r[4 * g + 0], r[4 * g + 1]), ay = cvtpk(r[4 * g + 2], r[4 * g + 3]);
        unsigned bx = cvtpk(r[4 * g + 4], r[4 * g + 5]), by = cvtpk(r[4 * g + 6], r[4 * g + 7]);
        auto rx = __builtin_amdgcn_permlane32_swap(ax, bx, false, false); auto ry = __builtin_amdgcn_permlane32_swap(ay, by, false, false);
        const u32x4 v = {rx[0], ry[0], rx[1], ry[1]};
        if (row_ok) *(u32x4*)(orow + 64 * kh + 32 * dd + 8 * g) = v;
      }
    }
  }
}

#define DIAG 0
#define ABL 0
#define PF1 3
#define PF2 5
#define STR2(x) #x
#define STR(x) STR2(x)

template <int NIT>
__global__ __launch_bounds__(512, 2)
void ragged_attn_kernel(const void* Q, unsigned nQ, unsigned fQ, const void* K, unsigned nK, unsigned fK, const void* V, unsigned nV, unsigned fV,
                        const void* seg, unsigned nS, unsigned fS, void* O, unsigned nO, unsigned fO, int T, int lastc, int pfb, int pad, void* dbg) {
  __shared__ __attribute__((aligned(16))) char lds[LDS_BYTES];
  asm volatile(R"(
  .set NIT, %c3
  .set DIAG, )" STR(DIAG) R"(
  .set ABL, )" STR(ABL) R"(
  .set PF1, )" STR(PF1) R"(
  .set PF2, )" STR(PF2) R"(
  ; ---------------- SGPRs
  .set sQ, 8
  .set sK, 12
  .set sV, 16
  .set sS, 20
  .set sO, 24
  .set sT, 28
  .set sLASTC, 29
  .set sPFB, 30
  .set sF, 30
  .set sLIVE, 30
  .set sTM16, 31
  .set sNDF, 33
  .set sG, 34
  .set sR, 35
  .set sNBIG, 36
  .set sTOT, 37
  .set sHOFF, 38
  .set sQ0, 39
  .set sQEND, 40
  .set sK0, 41
  .set sNK, 42
  .set sLEN, 43
  .set sMB, 44
  .set sNS, 45
  .set sU, 46
  .set sNU, 47
  .set sKOFF, 48
  .set sKSTEP, 49
  .set sVOFF, 50
  .set sKSO, 51
  .set sVSO, 52
  .set sKDST, 53
  .set sVDST, 54
  .set sCNT, 55
  .set sND, 56
  .set sMSTEP, 57
  .set sPEND, 58
  .set sW, 59
  .set sA, 60
  .set sB, 62
  .set sC, 64
  .set sSAFE, 64
  .set sNF, 65
  .set sT1, 66
  .set sT2, 67
  .set sT3, 68
  .set sT4, 69
  .set sCE, 70
  .set sTHR, 71
  .set sWD, 72
  .set sQB, 73
  .set sKH, 74
  .set sLIM, 75
  .set sJ, 76
  .set sQR, 77
  .set sRT, 78
  .set sV2, 80
  .set sM, 84
  .set sMK, 86
  .set sMV, 87
  ; ---------------- VGPRs
  .set vLANE, 1
  .set vOK, 2
  .set vOK2, 3
  .set vOV, 4
  .set vOV2, 5
  .set vKB0, 6
  .set vKB1, 7
  .set vVB, 8
  .set vSTART, 9
  .set vLEN, 10
  .set vINCL, 11
  .set vEXCL, 12
  .set vM, 13
  .set vNMC, 14
  .set vL0, 15
  .set vL1, 16
  .set vKA0, 17
  .set vKA1, 18
  .set vVA, 19
  .set vAL, 20
  .set vSO, 21
  .set vHH4, 22
  .set vSW, 23
  .set vT0, 24
  .set vT1, 25
  .set vT2, 26
  .set vT3, 27
  .set vT4, 28
  .set vT5, 29
  .set vT6, 30
  .set vT7, 31
  .set vP, 32
  .set vX, 40
  .set vLS, 40
  .set vXO, 44
  .set vXP, 45
  .set vSTA, 46
  .set vSTP, 47
  .set vSGW, 48
  .set vSGR, 49
  .set vFA, 50
  .set vFB, 52
  .set vDG, 54
  .set vSR, 56
  .set vVH, 57
  .set vVBL, 58
  .set vVBH, 59
  .set vONE, 60
  .set vO, 64
  .set vQ, 128
  .set vSA, 160
  .set vSB, 176
  .set vK, 192
  .set vV, 224
  ; ---------------- LDS map: K ring 4 x 16 KiB | V ring 4 x 16 KiB | X (Q stage) 32 KiB
  .set LK, 0
  .set LV, 65536
  .set LX, 131072
  .set LQ2, 49152
  .set LQ3, 114688
  .set LSCR, 98304
  .set TILEB, 64*4096

  .macro STAMP i                                    ; diagnostic builds only: low word of the cycle counter -> lane i of vDG
    .if (DIAG == 1) || ((DIAG == 2) && ((\i == 0) || (\i >= 5)))
      s_memtime s[sM:sM+1]
      s_waitcnt lgkmcnt(0)
      v_writelane_b32 v[vDG], s[sM], \i
    .endif
  .endm
  .macro STAMPE i
    .if DIAG == 2
      s_memtime s[sM:sM+1]
      s_waitcnt lgkmcnt(0)
      v_writelane_b32 v[vDG], s[sM], \i
    .endif
  .endm

  ; ======================================================================== macros
  ; ---- DMA pieces (generic form).  sKDST / sVDST: ring offset + w*2048 of the next tile
  .macro DK1
    s_mov_b32 m0, s[sKDST]
    s_add_u32 s[sT1], s[sKDST], 16384
    buffer_load_dwordx4 v[vOK], s[sK:sK+3], s[sKSO] offen lds
  .endm
  .macro DK2
    s_add_u32 m0, s[sKDST], 1024
    s_and_b32 s[sKDST], s[sT1], 0xffff
    buffer_load_dwordx4 v[vOK2], s[sK:sK+3], s[sKSO] offen lds
    s_add_u32 s[sKSO], s[sKSO], TILEB
  .endm
  .macro DV1
    s_add_u32 m0, s[sVDST], LV
    s_add_u32 s[sT3], s[sVDST], 16384
    buffer_load_dwordx4 v[vOV], s[sV:sV+3], s[sVSO] offen lds
  .endm
  .macro DV2
    s_add_u32 m0, s[sVDST], LV+1024
    s_and_b32 s[sVDST], s[sT3], 0xffff
    buffer_load_dwordx4 v[vOV2], s[sV:sV+3], s[sVSO] offen lds
    s_add_u32 s[sVSO], s[sVSO], TILEB
  .endm
  .macro DALL
    DK1
    DK2
    DV1
    DV2
  .endm
  ; m: 0 this slot has a barrier, 1 barrier unless mode B (run time), 2 no barrier
  .macro BARX n, m
    .if \m == 1
      s_cmp_eq_u32 s[sMB], 1
      s_cbranch_scc1 .Lnob\@
    .endif
    .if \m == 2
      s_waitcnt lgkmcnt(0)
    .else
      s_waitcnt vmcnt(\n) lgkmcnt(0)
      s_barrier
    .endif
    .if \m == 1
      s_branch .Lbd\@
  .Lnob\@:
      s_waitcnt lgkmcnt(0)
  .Lbd\@:
    .endif
  .endm
  ; DMA piece k of a generic step.  dma: 0 none, 1 always, 2 run-time flag sF ; m as above
  .macro DMAP k, m, dma
    .if ((\dma) != 0) && ((\m) != 2) && ((ABL & 1) == 0)
      .if \dma == 2
        s_cmp_eq_u32 s[sF], 0
        s_cbranch_scc1 .Ld\@
      .endif
      .if \m == 1
        s_cmp_eq_u32 s[sMB], 1
        s_cbranch_scc1 .Ld\@
      .endif
      .if \k == 0
        DK1
      .elseif \k == 1
        DK2
      .elseif \k == 2
        DV1
      .else
        DV2
      .endif
  .Ld\@:
    .endif
  .endm
  .macro KREADO i, off
    .if (ABL & 8)
    .elseif (\i & 1)
      ds_read_b128 v[vK+4*\i:vK+4*\i+3], v[vKA1] offset:(\off)+512*(\i>>1)
    .else
      ds_read_b128 v[vK+4*\i:vK+4*\i+3], v[vKA0] offset:(\off)+512*(\i>>1)
    .endif
  .endm
  .macro KADV                                       ; generic form: advance the K read addresses to the next step
    s_add_u32 s[sKOFF], s[sKOFF], s[sKSTEP]
    s_and_b32 s[sKOFF], s[sKOFF], 0xffff
    v_add_u32 v[vKA0], s[sKOFF], v[vKB0]
    v_add_u32 v[vKA1], s[sKOFF], v[vKB1]
  .endm
  .macro VADDR                                      ; generic form: V read addresses of the coming step
    v_add_u32 v[vVA], s[sVOFF], v[vVBL]
    v_add_u32 v[vVH], s[sVOFF], v[vVBH]
    s_add_u32 s[sVOFF], s[sVOFF], s[sKSTEP]
    s_and_b32 s[sVOFF], s[sVOFF], 0xffff
  .endm
  .macro QKM c, i
    .if \i == 0
      v_mfma_f32_32x32x16_bf16 v[\c:\c+15], v[vK:vK+3], v[vQ:vQ+3], 0
    .else
      v_mfma_f32_32x32x16_bf16 v[\c:\c+15], v[vK+4*\i:vK+4*\i+3], v[vQ+4*\i:vQ+4*\i+3], v[\c:\c+15]
    .endif
  .endm
  .macro VREADO j, off                              ; V fragment j: register block d = j&3 (actual d-block = d ^ 2*kh), ks = j>>2
    .if (ABL & 4) == 0
    .if (\j & 2)
    ds_read_b64_tr_b16 v[vV+4*\j:vV+4*\j+1], v[vVH] offset:(\off)+512*(\j&3)+4096*(\j>>2)
    ds_read_b64_tr_b16 v[vV+4*\j+2:vV+4*\j+3], v[vVH] offset:(\off)+512*(\j&3)+4096*(\j>>2)+2048
    .else
    ds_read_b64_tr_b16 v[vV+4*\j:vV+4*\j+1], v[vVA] offset:(\off)+512*(\j&3)+4096*(\j>>2)
    ds_read_b64_tr_b16 v[vV+4*\j+2:vV+4*\j+3], v[vVA] offset:(\off)+512*(\j&3)+4096*(\j>>2)+2048
    .endif
    .endif
  .endm
  .macro PVJ d, ks
    v_mfma_f32_32x32x16_bf16 v[vO+16*(\d):vO+16*(\d)+15], v[vV+4*((\d)+4*(\ks)):vV+4*((\d)+4*(\ks))+3], v[vP+4*(\ks):vP+4*(\ks)+3], v[vO+16*(\d):vO+16*(\d)+15]
  .endm
  .macro PVM j
    PVJ (\j&3), (\j>>2)
  .endm
  .macro LSUM ks                                    ; row sums of one packed P fragment
    .if (ABL & 64) == 0
    v_mfma_f32_16x16x32_bf16 v[vLS:vLS+3], v[vONE:vONE+3], v[vP+4*\ks:vP+4*\ks+3], v[vLS:vLS+3]
    .endif
  .endm
  .macro FMAE c, r
    .if (ABL & 2) == 0
    v_fma_f32 v[\c+(\r)], v[\c+(\r)], s[sCE], v[vNMC]
    .endif
  .endm
  .macro XPE c, r
    .if (ABL & 2) == 0
    v_exp_f32 v[\c+(\r)], v[\c+(\r)]
    .endif
  .endm
  .macro EXPE c, r
    FMAE \c, \r
    XPE \c, \r
  .endm
  .macro CVTE p, i                                  ; pack of elements 2i, 2i+1
    .if (ABL & 32) == 0
    v_cvt_pk_bf16_f32 v[vP+\i], v[\p+2*\i], v[\p+2*\i+1]
    .endif
  .endm
  .macro SUMPK p, i                                 ; SAFE form: row sums + pack of elements 2i, 2i+1
    v_add_f32 v[vL0], v[vL0], v[\p+2*\i]
    v_add_f32 v[vL1], v[vL1], v[\p+2*\i+1]
    v_cvt_pk_bf16_f32 v[vP+\i], v[\p+2*\i], v[\p+2*\i+1]
  .endm
  .macro MASK c                                     ; keys >= lim of this sub-tile -> -inf
    v_sub_u32 v[vT2], s[sLIM], v[vHH4]
    v_mov_b32 v[vT3], 0xff800000
    .set r, 0
    .rept 16
      v_cmp_lt_i32 vcc, (r&3)+8*(r>>2), v[vT2]
      s_nop 1
      v_cndmask_b32 v[\c+r], v[vT3], v[\c+r], vcc
      .set r, r+1
    .endr
  .endm
  .macro ROWMAX1 c
    v_max3_f32 v[vT2], v[\c+0], v[\c+1], v[\c+2]
    v_max3_f32 v[vT3], v[\c+3], v[\c+4], v[\c+5]
    v_max3_f32 v[vT2], v[vT2], v[\c+6], v[\c+7]
    v_max3_f32 v[vT3], v[vT3], v[\c+8], v[\c+9]
    v_max3_f32 v[vT2], v[vT2], v[\c+10], v[\c+11]
    v_max3_f32 v[vT3], v[vT3], v[\c+12], v[\c+13]
    v_max3_f32 v[vT2], v[vT2], v[\c+14], v[\c+15]
    v_max_f32 v[vT2], v[vT2], v[vT3]
    v_mov_b32 v[vT3], v[vT2]
  .endm
  .macro ROWMAX2                                    ; needs 2 wait states after ROWMAX1
    v_permlane32_swap_b32 v[vT2], v[vT3]
    s_nop 0
    v_max_f32 v[vT2], v[vT2], v[vT3]                ; vT2 = row max
  .endm
  .macro RESCALE
    s_nop 7
    s_nop 7
    .set i, 0
    .rept 64
      v_mul_f32 v[vO+i], v[vAL], v[vO+i]
      .set i, i+1
    .endr
    v_mul_f32 v[vL0], v[vAL], v[vL0]
    v_mul_f32 v[vL1], v[vAL], v[vL1]
    s_mov_b32 s[sPEND], 0
  .endm
  .macro SENDB d                                    ; d-block d (16 regs) -> own exchange area
    .set i, 0
    .rept 4
      ds_write_b128 v[vXO], v[vO+16*\d+4*i:vO+16*\d+4*i+3] offset:4096*(\d&1)+1024*i
      .set i, i+1
    .endr
  .endm
  .macro LASTV da, db, dc, dd                       ; last P.V: the blocks to send (da, db) first
    PVJ \da, 0
    PVJ \db, 0
    PVJ \da, 1
    PVJ \db, 1
    PVJ \dc, 0
    s_nop 7
    s_nop 1
    SENDB \da
    PVJ \dd, 0
    SENDB \db
    PVJ \dc, 1
    PVJ \dd, 1
    s_branch .Lepi_sent
  .endm

  ; ---- FAST step body (reference = row max of the wave's first sub-tile; overflow is detected on the row sums at the end of the item,
  ;      which is then redone with the SAFE steps).  prev enters with exp done for e0..e10.
  ;      gen = 1: generic addressing (address registers, generic DMA) ; gen = 0: mode A, ring position r as immediates
  .macro FDMA k, gen, r, m, dma
    .if \gen
      DMAP \k, \m, \dma
    .elseif (\dma) && ((ABL & 1) == 0)
      .if \k == 0
        s_add_u32 m0, s[sMK], 16384*(\r)
      .elseif \k == 1
        s_add_u32 m0, s[sMK], 16384*(\r)+1024
      .elseif \k == 2
        s_add_u32 m0, s[sMV], 16384*(\r)
      .else
        s_add_u32 m0, s[sMV], 16384*(\r)+1024
      .endif
    .endif
  .endm
  .macro FDMB k, gen, dma                            ; the load itself (mode A form), one instruction behind its M0 write
    .if ((\gen) == 0) && (\dma) && ((ABL & 1) == 0)
      .if \k == 0
        buffer_load_dwordx4 v[vOK], s[sK:sK+3], s[sKSO] offen lds
      .elseif \k == 1
        buffer_load_dwordx4 v[vOK2], s[sK:sK+3], s[sKSO] offen lds
      .elseif \k == 2
        buffer_load_dwordx4 v[vOV], s[sV2:sV2+3], s[sKSO] offen lds
      .else
        buffer_load_dwordx4 v[vOV2], s[sV2:sV2+3], s[sKSO] offen lds
        s_add_u32 s[sKSO], s[sKSO], TILEB
      .endif
    .endif
  .endm
  .macro FULLF cur, prev, gen, r, m
    .set KO, (1-\gen)*16384*(\r)
    .set VO, (1-\gen)*16384*(\r)
    BARX 4, \m
    ; ---------------- phase A
    QKM \cur, 0
    VREADO 0, VO
    EXPE \prev, 11
    QKM \cur, 1
    VREADO 1, VO
    EXPE \prev, 12
    CVTE \prev, 0
    QKM \cur, 2
    VREADO 2, VO
    FDMA 0, \gen, \r, \m, 1
    FMAE \prev, 13
    FDMB 0, \gen, 1
    XPE \prev, 13
    CVTE \prev, 1
    QKM \cur, 3
    VREADO 3, VO
    EXPE \prev, 14
    CVTE \prev, 2
    QKM \cur, 4
    VREADO 4, VO
    EXPE \prev, 15
    CVTE \prev, 3
    QKM \cur, 5
    VREADO 5, VO
    CVTE \prev, 4
    CVTE \prev, 5
    QKM \cur, 6
    VREADO 6, VO
    FDMA 1, \gen, \r, \m, 1
    CVTE \prev, 6
    FDMB 1, \gen, 1
    QKM \cur, 7
    VREADO 7, VO
    CVTE \prev, 7
    .if (\gen == 0) && (\r == 1)
      v_xor_b32 v[vVA], 0x8000, v[vVA]
      v_xor_b32 v[vVH], 0x8000, v[vVH]
    .endif
    ; ---------------- phase B
    s_waitcnt lgkmcnt(4)
    PVM 0
    KREADO 0, KO
    KREADO 1, KO
    FDMA 2, \gen, \r, \m, 1
    KREADO 2, KO
    FDMB 2, \gen, 1
    KREADO 3, KO
    s_nop 2
    PVM 1
    EXPE \cur, 0
    EXPE \cur, 1
    LSUM 0
    EXPE \cur, 2
    PVM 2
    KREADO 4, KO
    EXPE \cur, 3
    PVM 3
    KREADO 5, KO
    EXPE \cur, 4
    FMAE \cur, 5
    PVM 4
    KREADO 6, KO
    XPE \cur, 5
    EXPE \cur, 6
    PVM 5
    KREADO 7, KO
    EXPE \cur, 7
    LSUM 1
    EXPE \cur, 8
    s_waitcnt lgkmcnt(8)
    PVM 6
    FDMA 3, \gen, \r, \m, 1
    FMAE \cur, 9
    FDMB 3, \gen, 1
    XPE \cur, 9
    .if \gen
      KADV
      VADDR
    .elseif \r == 1
      v_xor_b32 v[vKA0], 0x8000, v[vKA0]
      v_xor_b32 v[vKA1], 0x8000, v[vKA1]
      s_xor_b32 s[sMK], s[sMK], 0x8000
      s_xor_b32 s[sMV], s[sMV], 0x8000
    .endif
    s_add_u32 s[sCNT], s[sCNT], 1
    PVM 7
    EXPE \cur, 10
  .endm
  .macro LASTF prev, gen, r, m
    .set VO, (1-\gen)*16384*(\r)
    .if \m == 2
    .else
      BARX 0, \m
    .endif
    .set g, 0
    .rept 8
      VREADO g, VO
      .if g < 5
        EXPE \prev, 11+g
      .endif
      .set g, g+1
    .endr
    s_mov_b32 s[sU], s[sNS]
    s_cmp_eq_i32 s[sMSTEP], -1
    s_cbranch_scc1 .Lnm\@
    ; partial sub-tile: keys >= lim get weight 0
    v_sub_u32 v[vT2], s[sLIM], v[vHH4]
    .set e, 0
    .rept 16
      v_cmp_lt_i32 vcc, (e&3)+8*(e>>2), v[vT2]
      s_nop 1
      v_cndmask_b32 v[\prev+e], 0, v[\prev+e], vcc
      .set e, e+1
    .endr
  .Lnm\@:
    .set g, 0
    .rept 8
      CVTE \prev, g
      .set g, g+1
    .endr
    s_cmp_eq_u32 s[sU], s[sNU]
    s_waitcnt lgkmcnt(0)
    LSUM 0
    LSUM 1
    .if \gen == 0                                    ; mode A, last slot: send the partner's half early
      s_cbranch_scc0 .Lplain\@
      LASTV 2, 3, 0, 1
  .Lplain\@:
    .endif
    .set g, 0
    .rept 8
      PVM g
      .set g, g+1
    .endr
    s_branch .Lnone_tail
  .endm

  ; ---- SAFE step (online max every step), generic addressing, run-time mode
  .macro FULLS cur, prev, m
    s_cmp_lt_u32 s[sU], s[sND]
    s_cselect_b32 s[sF], 1, 0
    s_cbranch_scc0 .Lbn\@
    BARX 4, \m
    s_branch .Lbq\@
  .Lbn\@:
    BARX 0, \m
  .Lbq\@:
    .set g, 0
    .rept 8
      QKM \cur, g
      VREADO g, 0
      SUMPK \prev, g
      .if g == 1
        DMAP 0, \m, 2
      .elseif g == 5
        DMAP 1, \m, 2
      .endif
      .set g, g+1
    .endr
    s_waitcnt lgkmcnt(4)
    PVM 0
    KREADO 0, 0
    KREADO 1, 0
    s_add_u32 s[sU], s[sU], 1
    s_nop 1
    PVM 1
    s_cmp_lg_u32 s[sMSTEP], s[sU]
    s_cbranch_scc1 .Lnm\@
    MASK \cur
  .Lnm\@:
    ROWMAX1 \cur
    PVM 2
    KREADO 2, 0
    ROWMAX2
    v_sub_f32 v[vT3], v[vT2], v[vM]
    v_cmp_lt_f32 vcc, s[sTHR], v[vT3]
    s_cbranch_vccz .Lkeep\@
    v_max_f32 v[vT3], v[vM], v[vT2]
    v_sub_f32 v[vAL], v[vM], v[vT3]
    v_mul_f32 v[vAL], s[sCE], v[vAL]
    v_exp_f32 v[vAL], v[vAL]
    v_mov_b32 v[vM], v[vT3]
    v_mul_f32 v[vNMC], 0xbe0293ee, v[vM]
    s_mov_b32 s[sPEND], 1
  .Lkeep\@:
    PVM 3
    KREADO 3, 0
    DMAP 2, \m, 2
    EXPE \cur, 0
    EXPE \cur, 1
    EXPE \cur, 2
    PVM 4
    KREADO 4, 0
    EXPE \cur, 3
    EXPE \cur, 4
    EXPE \cur, 5
    PVM 5
    KREADO 5, 0
    EXPE \cur, 6
    EXPE \cur, 7
    EXPE \cur, 8
    s_waitcnt lgkmcnt(6)
    PVM 6
    KREADO 6, 0
    DMAP 3, \m, 2
    EXPE \cur, 9
    EXPE \cur, 10
    EXPE \cur, 11
    PVM 7
    KREADO 7, 0
    EXPE \cur, 12
    EXPE \cur, 13
    EXPE \cur, 14
    EXPE \cur, 15
    KADV
    VADDR
    s_cmp_eq_u32 s[sPEND], 0
    s_cbranch_scc1 .Lsend\@
    RESCALE
  .Lsend\@:
    s_cmp_eq_u32 s[sU], s[sNF]
  .endm
  .macro LASTS prev, m
    .if \m == 1
      s_cmp_eq_u32 s[sMB], 1
      s_cbranch_scc1 .Lnob\@
    .endif
    s_waitcnt vmcnt(0) lgkmcnt(0)
    s_barrier
  .Lnob\@:
    .set g, 0
    .rept 8
      VREADO g, 0
      SUMPK \prev, g
      .set g, g+1
    .endr
    s_add_u32 s[sU], s[sU], 1
    s_waitcnt lgkmcnt(0)
    .set g, 0
    .rept 8
      PVM g
      .set g, g+1
    .endr
    s_branch .Lnone_tail
  .endm

  ; ---- epilogue pieces
  ; 32 f32 at vO+b.. (one 64-wide half) -> bf16 -> LDS stage (row-major 128 B rows, 16-B chunks xor row) -> 4 full-line stores
  ; t: 16 dead registers (vO+t..) ; vSGW / vSGR: stage addresses ; sT3 = global offset of row 0 of this half
  .macro OUTHALF b, t
    .set q, 0
    .rept 4
      v_cvt_pk_bf16_f32 v[vO+\t+4*q+0], v[vO+\b+8*q+0], v[vO+\b+8*q+1]
      v_cvt_pk_bf16_f32 v[vO+\t+4*q+1], v[vO+\b+8*q+2], v[vO+\b+8*q+3]
      v_cvt_pk_bf16_f32 v[vO+\t+4*q+2], v[vO+\b+8*q+4], v[vO+\b+8*q+5]
      v_cvt_pk_bf16_f32 v[vO+\t+4*q+3], v[vO+\b+8*q+6], v[vO+\b+8*q+7]
      v_xor_b32 v[vT0+q], q*32, v[vSGW]
      .set q, q+1
    .endr
    .set q, 0
    .rept 4
      v_permlane32_swap_b32 v[vO+\t+4*q+0], v[vO+\t+4*q+2]
      v_permlane32_swap_b32 v[vO+\t+4*q+1], v[vO+\t+4*q+3]
      .set q, q+1
    .endr
    .set q, 0
    .rept 4
      ds_write_b128 v[vT0+q], v[vO+\t+4*q:vO+\t+4*q+3]
      .set q, q+1
    .endr
    s_add_u32 s[sT4], s[sT3], 8*4096
    s_add_u32 s[sT1], s[sT3], 16*4096
    s_add_u32 s[sT2], s[sT3], 24*4096
    s_waitcnt lgkmcnt(0)
    STAMPE 4
    ds_read_b128 v[vO+\t+0:vO+\t+3], v[vSGR]
    ds_read_b128 v[vO+\t+4:vO+\t+7], v[vSGR] offset:1024
    ds_read_b128 v[vO+\t+8:vO+\t+11], v[vSGR] offset:2048
    ds_read_b128 v[vO+\t+12:vO+\t+15], v[vSGR] offset:3072
    s_waitcnt lgkmcnt(3)
    buffer_store_dwordx4 v[vO+\t+0:vO+\t+3], v[vSO], s[sO:sO+3], s[sT3] offen sc0 sc1
    s_waitcnt lgkmcnt(2)
    buffer_store_dwordx4 v[vO+\t+4:vO+\t+7], v[vSO], s[sO:sO+3], s[sT4] offen sc0 sc1
    s_waitcnt lgkmcnt(1)
    buffer_store_dwordx4 v[vO+\t+8:vO+\t+11], v[vSO], s[sO:sO+3], s[sT1] offen sc0 sc1
    s_waitcnt lgkmcnt(0)
    buffer_store_dwordx4 v[vO+\t+12:vO+\t+15], v[vSO], s[sO:sO+3], s[sT2] offen sc0 sc1
  .endm
  .macro MERGE kb, rb                               ; kb: kept 32 regs, rb: 32 regs receiving the partner's half
    ds_read_b64 v[vT4:vT5], v[vSTP]
    .set i, 0
    .rept 8
      ds_read_b128 v[vO+\rb+4*i:vO+\rb+4*i+3], v[vXP] offset:1024*i
      .set i, i+1
    .endr
    s_waitcnt lgkmcnt(8)
    v_max_f32 v[vT6], v[vM], v[vT4]
    v_sub_f32 v[vFA], v[vM], v[vT6]
    v_sub_f32 v[vFB], v[vT4], v[vT6]
    v_mul_f32 v[vFA], s[sCE], v[vFA]
    v_mul_f32 v[vFB], s[sCE], v[vFB]
    v_exp_f32 v[vFA], v[vFA]
    v_exp_f32 v[vFB], v[vFB]
    s_nop 0
    v_mul_f32 v[vT6], v[vFA], v[vL0]
    v_fma_f32 v[vT6], v[vFB], v[vT5], v[vT6]
    v_rcp_f32 v[vT6], v[vT6]
    s_nop 0
    v_mul_f32 v[vFA], v[vFA], v[vT6]
    v_mul_f32 v[vFB], v[vFB], v[vT6]
    STAMPE 2
    .set i, 0
    .rept 16
      v_pk_mul_f32 v[vO+\kb+2*i:vO+\kb+2*i+1], v[vFA:vFA+1], v[vO+\kb+2*i:vO+\kb+2*i+1] op_sel_hi:[0,1]
      .set i, i+1
    .endr
    .set i, 0
    .rept 8
      .if i == 0
        s_waitcnt lgkmcnt(7)
      .elseif i == 1
        s_waitcnt lgkmcnt(6)
      .elseif i == 2
        s_waitcnt lgkmcnt(5)
      .elseif i == 3
        s_waitcnt lgkmcnt(4)
      .elseif i == 4
        s_waitcnt lgkmcnt(3)
      .elseif i == 5
        s_waitcnt lgkmcnt(2)
      .elseif i == 6
        s_waitcnt lgkmcnt(1)
      .else
        s_waitcnt lgkmcnt(0)
      .endif
      v_pk_fma_f32 v[vO+\kb+4*i:vO+\kb+4*i+1], v[vFB:vFB+1], v[vO+\rb+4*i:vO+\rb+4*i+1], v[vO+\kb+4*i:vO+\kb+4*i+1] op_sel_hi:[0,1,1]
      v_pk_fma_f32 v[vO+\kb+4*i+2:vO+\kb+4*i+3], v[vFB:vFB+1], v[vO+\rb+4*i+2:vO+\rb+4*i+3], v[vO+\kb+4*i+2:vO+\kb+4*i+3] op_sel_hi:[0,1,1]
      .set i, i+1
    .endr
    STAMPE 3
    OUTHALF \kb, \rb
  .endm
  .macro ROWSUM                                     ; complete row sum of this wave's keys -> vL0 (all lanes)
    s_cmp_eq_u32 s[sSAFE], 1
    s_cbranch_scc1 .Lss\@
    v_permlane16_swap_b32 v[vLS], v[vLS+1]
    s_nop 0
    v_mov_b32 v[vL0], v[vLS]
    s_nop 1
    v_permlane32_swap_b32 v[vL0], v[vLS]
    s_branch .Lsd\@
  .Lss\@:
    v_add_f32 v[vL0], v[vL0], v[vL1]
    v_mov_b32 v[vL1], v[vL0]
    s_nop 1
    v_permlane32_swap_b32 v[vL0], v[vL1]
    s_nop 0
    v_add_f32 v[vL0], v[vL0], v[vL1]
  .Lsd\@:
  .endm
  .macro PREFETCH lo, hi                            ; Q prefetch into L2, instructions lo..hi-1 (16 rows each)
    .set n, \lo
    .rept \hi-\lo
      s_mul_i32 s[sT3], s[sJ], s[sPFB]
      s_add_u32 s[sT3], s[sT3], n*16*4096
      s_min_u32 s[sT3], s[sT3], s[sTM16]
      buffer_load_dwordx4 v[vV+4*n:vV+4*n+3], v[vV+28], s[sQ:sQ+3], s[sT3] offen
      .set n, n+1
    .endr
  .endm

  ; ======================================================================== entry
  s_load_dwordx16 s[8:23], %0, 0
  s_load_dwordx8 s[24:31], %0, 64
  .if DIAG
    s_memrealtime s[sRT:sRT+1]
    v_mov_b32 v[vDG], 0
  .endif
  STAMP 0
  v_and_b32 v[vLANE], 63, %2
  v_readfirstlane_b32 s[sW], %2
  v_lshlrev_b32 v[vT0], 2, %2                     ; tid*4
  s_and_b32 s[sG], %1, 7
  s_lshr_b32 s[sJ], %1, 3
  s_lshr_b32 s[sW], s[sW], 6
  v_add_u32 v[vT7], 2048*(NIT-1), v[vT0]
  ; prefetch offset: (tid>>5)*4096 + ((tid>>4)&1)*2048 + ((tid>>3)&1)*128 + (tid&7)*16 + g*256
  v_lshrrev_b32 v[vT6], 5, %2
  v_lshlrev_b32 v[vT6], 12, v[vT6]
  v_bfe_u32 v[vT5], %2, 4, 1
  v_lshl_add_u32 v[vT6], v[vT5], 11, v[vT6]
  v_bfe_u32 v[vT5], %2, 3, 1
  v_lshl_add_u32 v[vT6], v[vT5], 7, v[vT6]
  v_and_b32 v[vT5], 7, %2
  v_lshl_add_u32 v[vT6], v[vT5], 4, v[vT6]
  s_lshl_b32 s[sT3], s[sG], 8
  v_add_u32 v[vV+28], s[sT3], v[vT6]
  ; DMA lane offsets
  v_bfe_u32 v[vT1], v[vLANE], 2, 3                ; prow
  v_lshrrev_b32 v[vT2], 5, v[vLANE]               ; hh / pcb
  v_and_b32 v[vT3], 3, v[vLANE]                   ; pc
  v_lshlrev_b32 v[vT4], 12, v[vT1]
  v_lshl_add_u32 v[vT4], v[vT2], 6, v[vT4]        ; prow*4096 + pcb*64
  v_lshl_add_u32 v[vOV], v[vT3], 4, v[vT4]
  v_bfe_u32 v[vT5], v[vLANE], 4, 1
  s_and_b32 s[sT4], s[sW], 1
  s_lshl_b32 s[sT4], s[sT4], 1
  v_or_b32 v[vT5], s[sT4], v[vT5]                 ; x
  v_xor_b32 v[vT5], v[vT5], v[vT3]
  v_lshl_add_u32 v[vOK], v[vT5], 4, v[vT4]
  s_waitcnt lgkmcnt(0)
  STAMP 1
  ; ======================================================================== S1: segment ends per 64-token chunk
  s_lshl_b32 s[sT2], s[sT], 2
  s_sub_u32 s[sT2], s[sT2], 8                     ; (T-2)*4
  v_min_u32 v[vT7], s[sT2], v[vT7]                ; last chunk: clamped
  .set i, 0
  .rept NIT
    .if i == NIT-1
      buffer_load_dwordx2 v[vX+2*i:vX+2*i+1], v[vT7], s[sS:sS+3], 0 offen
    .elseif i < 2
      buffer_load_dwordx2 v[vX+2*i:vX+2*i+1], v[vT0], s[sS:sS+3], 0 offen offset:2048*i
    .else
      s_movk_i32 s[sT1], 2048*i
      buffer_load_dwordx2 v[vX+2*i:vX+2*i+1], v[vT0], s[sS:sS+3], s[sT1] offen
    .endif
    .set i, i+1
  .endr
  s_sub_u32 s[sTM16], s[sT], 16
  s_lshl_b32 s[sTM16], s[sTM16], 12               ; (T-16)*4096
  ; V descriptor shifted by two tiles: V(u+2) is fetched with the source offset of K(u+4)
  s_sub_u32 s[sV2], s[sV], 2*TILEB
  s_subb_u32 s[sV2+1], s[sV+1], 0
  s_mov_b32 s[sV2+3], s[sV+3]
  ; remaining lane constants (in the shadow of the loads)
  v_add_u32 v[vOK2], 128, v[vOK]
  v_add_u32 v[vOV2], 128, v[vOV]
  v_and_b32 v[vT1], 31, v[vLANE]                  ; r32
  v_lshrrev_b32 v[vT3], 3, v[vT1]
  v_lshlrev_b32 v[vT3], 11, v[vT3]
  v_and_b32 v[vT4], 7, v[vT1]
  v_lshl_add_u32 v[vT3], v[vT4], 6, v[vT3]
  v_bfe_u32 v[vT4], v[vT1], 2, 2
  v_xor_b32 v[vT4], v[vT4], v[vT2]
  v_lshl_add_u32 v[vKB0], v[vT4], 4, v[vT3]
  v_xor_b32 v[vKB1], 32, v[vKB0]
  v_bfe_u32 v[vT3], v[vLANE], 2, 2
  v_lshl_add_u32 v[vT3], v[vT2], 2, v[vT3]
  v_lshlrev_b32 v[vT3], 6, v[vT3]
  v_bfe_u32 v[vT4], v[vLANE], 4, 1
  v_lshl_add_u32 v[vT3], v[vT4], 5, v[vT3]
  v_and_b32 v[vT4], 3, v[vLANE]
  v_lshl_add_u32 v[vVB], v[vT4], 3, v[vT3]
  v_add_u32 v[vVB], LV, v[vVB]
  v_lshlrev_b32 v[vHH4], 2, v[vT2]
  ; output staging: write address r32*128 + ((r32&7)^hh)*16 (chunk pair p: xor p*32)
  v_and_b32 v[vT3], 7, v[vT1]
  v_xor_b32 v[vT3], v[vT3], v[vT2]
  v_lshlrev_b32 v[vT3], 4, v[vT3]
  v_lshl_add_u32 v[vSW], v[vT1], 7, v[vT3]
  ; read address: row = lane>>3, chunk = (lane&7) ^ row ; rows + 8i -> + 1024 i
  v_lshrrev_b32 v[vT3], 3, v[vLANE]
  v_and_b32 v[vT4], 7, v[vLANE]
  v_xor_b32 v[vT5], v[vT4], v[vT3]
  v_lshlrev_b32 v[vT5], 4, v[vT5]
  v_lshl_add_u32 v[vSR], v[vT3], 7, v[vT5]
  ; global store lane offset: (lane>>3)*4096 + (lane&7)*16
  v_lshlrev_b32 v[vT4], 4, v[vT4]
  v_lshl_add_u32 v[vSO], v[vT3], 12, v[vT4]
  ; row-sum MFMA A operand: row (lane&15) 0 takes k-groups 0,2 ; row 1 takes k-groups 1,3
  v_and_b32 v[vT3], 15, v[vLANE]
  v_bfe_u32 v[vT4], v[vLANE], 4, 1
  v_cmp_eq_u32 vcc, v[vT3], v[vT4]
  v_mov_b32 v[vT5], 0x3f803f80
  s_mov_b32 s[sCE], 0x3e0293ee
  v_cndmask_b32 v[vONE], 0, v[vT5], vcc
  v_cndmask_b32 v[vONE+1], 0, v[vT5], vcc
  v_cndmask_b32 v[vONE+2], 0, v[vT5], vcc
  v_cndmask_b32 v[vONE+3], 0, v[vT5], vcc
  s_mov_b32 s[sTHR], 0x427af232
  s_lshl_b32 s[sWD], s[sW], 11                    ; w*2048
  s_mov_b64 s[sC:sC+1], 0
  v_mov_b32 v[vT4], 0
  v_mov_b32 v[vT5], 0
  v_lshlrev_b32 v[vT7], 7, v[vLANE]               ; LDS address of chunk (w + 8*lane): 16 B each
  s_lshl_b32 s[sT1], s[sW], 4
  s_add_u32 s[sT1], s[sT1], LSCR
  v_add_u32 v[vT7], s[sT1], v[vT7]
  PREFETCH 0, PF1
  .set i, 0
  .rept NIT
    s_waitcnt vmcnt(NIT-1-i+PF1)
    v_cmp_ne_u32 s[sA:sA+1], v[vX+2*i], v[vX+2*i+1]
    v_cmp_gt_i32 vcc, v[vX+2*i], v[vX+2*i+1]
    .if i == NIT-1
      v_cmp_gt_i32 s[sB:sB+1], s[sLASTC], %2       ; element < T-1
      s_and_b64 s[sA:sA+1], s[sA:sA+1], s[sB:sB+1]
      s_and_b64 vcc, vcc, s[sB:sB+1]
      v_cmp_eq_u32 s[sB:sB+1], s[sLASTC], %2       ; element == T-1
      s_or_b64 s[sA:sA+1], s[sA:sA+1], s[sB:sB+1]
    .endif
    s_or_b64 s[sC:sC+1], s[sC:sC+1], vcc
    s_nop 0
    v_writelane_b32 v[vT4], s[sA], i
    v_writelane_b32 v[vT5], s[sA+1], i
    .set i, i+1
  .endr
  s_or_b32 s[sT1], s[sC], s[sC+1]
  v_mov_b32 v[vT6], s[sT1]
  s_mov_b64 exec, (1<<NIT)-1
  ds_write_b96 v[vT7], v[vT4:vT6]
  s_mov_b64 exec, -1
  v_mov_b32 v[vT0], 0
  v_mov_b32 v[vT1], 0
  v_mov_b32 v[vT2], 0
  v_lshlrev_b32 v[vT3], 4, v[vLANE]
  v_add_u32 v[vT3], LSCR, v[vT3]
  v_lshlrev_b32 v[vT7], 6, v[vLANE]
  v_add_u32 v[vT7], 1, v[vT7]                     ; lane*64 + 1
  PREFETCH PF1, PF2
  s_waitcnt lgkmcnt(0)
  s_barrier
  STAMP 2
  ; ======================================================================== S2
  s_bfm_b64 exec, 8*NIT, 0
  ds_read_b96 v[vT0:vT2], v[vT3]
  s_mov_b64 exec, -1
  s_movk_i32 s[sT1], 128
  s_movk_i32 s[sT2], 256
  s_mov_b32 s[sR], 0
  s_waitcnt lgkmcnt(0)
  v_cmp_ne_u32 vcc, 0, v[vT2]
  v_bcnt_u32_b32 v[vT3], v[vT0], 0
  v_bcnt_u32_b32 v[vT3], v[vT1], v[vT3]
  s_cbranch_vccnz .Lfallback
  v_cmp_lt_u32 vcc, 1, v[vT3]
  v_ffbl_b32 v[vT4], v[vT0]
  v_ffbl_b32 v[vT5], v[vT1]
  s_cbranch_vccnz .Lfallback
  v_cmp_ne_u32 s[sA:sA+1], 0, v[vT3]
  v_add_u32 v[vT5], 32, v[vT5]
  v_min_u32 v[vT4], v[vT4], v[vT5]
  v_add_u32 v[vT4], v[vT4], v[vT7]
  v_cndmask_b32 v[vT4], 0, v[vT4], s[sA:sA+1]     ; end (0 if none)
  s_nop 1
  v_mov_b32_dpp v[vSTART], v[vT4] wave_shr:1 row_mask:0xf bank_mask:0xf bound_ctrl:0
  s_nop 1
  v_max_u32_dpp v[vSTART], v[vSTART], v[vSTART] row_shr:1 row_mask:0xf bank_mask:0xf bound_ctrl:0
  s_nop 1
  v_max_u32_dpp v[vSTART], v[vSTART], v[vSTART] row_shr:2 row_mask:0xf bank_mask:0xf bound_ctrl:0
  s_nop 1
  v_max_u32_dpp v[vSTART], v[vSTART], v[vSTART] row_shr:4 row_mask:0xf bank_mask:0xf bound_ctrl:0
  s_nop 1
  v_max_u32_dpp v[vSTART], v[vSTART], v[vSTART] row_shr:8 row_mask:0xf bank_mask:0xf bound_ctrl:0
  s_nop 1
  v_max_u32_dpp v[vSTART], v[vSTART], v[vSTART] row_bcast:15 row_mask:0xa bank_mask:0xf
  s_nop 1
  v_max_u32_dpp v[vSTART], v[vSTART], v[vSTART] row_bcast:31 row_mask:0xc bank_mask:0xf
  s_nop 0
  v_sub_u32 v[vLEN], v[vT4], v[vSTART]
  v_cndmask_b32 v[vLEN], 0, v[vLEN], s[sA:sA+1]   ; len (0 if none)
  v_add_u32 v[vT5], 127, v[vLEN]
  v_lshrrev_b32 v[vT5], 7, v[vT5]                 ; nq (mode A)
  v_cmp_lt_u32 vcc, s[sT1], v[vLEN]               ; len > 128
  v_cmp_ge_u32 s[sB:sB+1], s[sT2], v[vLEN]        ; len <= 256
  s_and_b64 s[sB:sB+1], s[sB:sB+1], vcc           ; mode B
  v_lshlrev_b32 v[vT5], 1, v[vT5]
  s_nop 0
  v_cndmask_b32 v[vT5], v[vT5], 2, s[sB:sB+1]     ; items (2 heads)
  v_lshlrev_b32 v[vT6], 16, v[vT5]
  v_cndmask_b32 v[vINCL], v[vT6], v[vT5], vcc     ; small -> high half
  v_mov_b32 v[vEXCL], v[vINCL]
  s_nop 0
  v_add_u32_dpp v[vINCL], v[vINCL], v[vINCL] row_shr:1 row_mask:0xf bank_mask:0xf bound_ctrl:0
  s_nop 1
  v_add_u32_dpp v[vINCL], v[vINCL], v[vINCL] row_shr:2 row_mask:0xf bank_mask:0xf bound_ctrl:0
  s_nop 1
  v_add_u32_dpp v[vINCL], v[vINCL], v[vINCL] row_shr:4 row_mask:0xf bank_mask:0xf bound_ctrl:0
  s_nop 1
  v_add_u32_dpp v[vINCL], v[vINCL], v[vINCL] row_shr:8 row_mask:0xf bank_mask:0xf bound_ctrl:0
  s_nop 1
  v_add_u32_dpp v[vINCL], v[vINCL], v[vINCL] row_bcast:15 row_mask:0xa bank_mask:0xf
  s_nop 1
  v_add_u32_dpp v[vINCL], v[vINCL], v[vINCL] row_bcast:31 row_mask:0xc bank_mask:0xf
  s_nop 0
  v_sub_u32 v[vEXCL], v[vINCL], v[vEXCL]
  v_readlane_b32 s[sT1], v[vINCL], 63
  s_and_b32 s[sNBIG], s[sT1], 0xffff
  s_lshr_b32 s[sTOT], s[sT1], 16
  s_add_u32 s[sTOT], s[sTOT], s[sNBIG]
  s_mov_b32 s[sSAFE], 0
  STAMP 3

.Litem:
  ; idx = r*32 + (r odd ? 31-j : j)
  s_lshl_b32 s[sT1], s[sR], 5
  s_sub_u32 s[sT2], 31, s[sJ]
  s_bitcmp1_b32 s[sR], 0
  s_cselect_b32 s[sT2], s[sT2], s[sJ]
  s_add_u32 s[sT1], s[sT1], s[sT2]                ; idx
  s_cmp_ge_u32 s[sT1], s[sTOT]
  s_cbranch_scc1 .Ldone
  s_sub_u32 s[sT2], s[sT1], s[sNBIG]
  s_cmp_lt_u32 s[sT1], s[sNBIG]
  s_cselect_b32 s[sT1], s[sT1], s[sT2]            ; index inside its class
  s_cselect_b32 s[sT2], 0, 16                     ; field shift
  v_bfe_u32 v[vT0], v[vINCL], s[sT2], 16
  v_bfe_u32 v[vT1], v[vEXCL], s[sT2], 16
  v_cmp_lt_u32 s[sA:sA+1], s[sT1], v[vT0]
  v_cmp_ge_u32 vcc, s[sT1], v[vT1]
  s_and_b64 s[sA:sA+1], s[sA:sA+1], vcc
  s_ff1_i32_b64 s[sT2], s[sA:sA+1]
  v_readlane_b32 s[sK0], v[vSTART], s[sT2]
  v_readlane_b32 s[sLEN], v[vLEN], s[sT2]
  v_readlane_b32 s[sT3], v[vT1], s[sT2]
  s_sub_u32 s[sT1], s[sT1], s[sT3]                ; local item
  s_and_b32 s[sT2], s[sT1], 1
  s_lshl_b32 s[sT2], s[sT2], 3
  s_add_u32 s[sT2], s[sT2], s[sG]
  s_lshl_b32 s[sHOFF], s[sT2], 8                  ; head*256 bytes
  s_lshr_b32 s[sT1], s[sT1], 1
  s_lshl_b32 s[sT1], s[sT1], 7
  s_add_u32 s[sQ0], s[sK0], s[sT1]
  s_lshl_b32 s[sT1], s[sW], 3
  s_add_u32 s[sT2], s[sQ0], s[sT1]
  s_lshl_b32 s[sT2], s[sT2], 12
  s_add_u32 s[sT2], s[sT2], s[sHOFF]              ; (q0 + 8w)*4096 + hoff
  ; ---- Q DMA
  s_add_u32 m0, s[sWD], LX
  s_add_u32 s[sT3], s[sT2], 64*4096
  buffer_load_dwordx4 v[vOK], s[sQ:sQ+3], s[sT2] offen lds
  s_add_u32 m0, s[sWD], LX+1024
  s_add_u32 s[sT4], s[sT2], 128*4096
  buffer_load_dwordx4 v[vOK2], s[sQ:sQ+3], s[sT2] offen lds
  s_add_u32 m0, s[sWD], LX+16384
  s_add_u32 s[sT1], s[sK0], s[sT1]
  buffer_load_dwordx4 v[vOK], s[sQ:sQ+3], s[sT3] offen lds
  s_add_u32 m0, s[sWD], LX+16384+1024
  s_lshl_b32 s[sT1], s[sT1], 12
  buffer_load_dwordx4 v[vOK2], s[sQ:sQ+3], s[sT3] offen lds
  s_add_u32 s[sKSO], s[sT1], s[sHOFF]             ; (k0 + 8w)*4096 + hoff
  s_mov_b32 s[sVSO], s[sKSO]
  s_add_u32 s[sQEND], s[sK0], s[sLEN]
  s_add_u32 s[sNK], s[sLEN], 63
  s_lshr_b32 s[sNK], s[sNK], 6
  s_lshl_b32 s[sO+2], s[sQEND], 12                ; the output, K and V descriptors end at the segment end
  s_mov_b32 s[sK+2], s[sO+2]
  s_mov_b32 s[sV+2], s[sO+2]
  s_add_u32 s[sV2+2], s[sO+2], 2*TILEB
  s_mov_b32 s[sKDST], s[sWD]
  s_mov_b32 s[sVDST], s[sWD]
  s_sub_u32 s[sT1], s[sLEN], 129
  s_cmp_lt_u32 s[sT1], 128
  s_cselect_b32 s[sMB], 1, 0                      ; mode B: 128 < len <= 256
  s_cbranch_scc0 .Lq_done
  s_add_u32 m0, s[sWD], LQ2
  s_add_u32 s[sT3], s[sT4], 64*4096
  buffer_load_dwordx4 v[vOK], s[sQ:sQ+3], s[sT4] offen lds
  s_add_u32 m0, s[sWD], LQ2+1024
  s_nop 0
  buffer_load_dwordx4 v[vOK2], s[sQ:sQ+3], s[sT4] offen lds
  s_add_u32 m0, s[sWD], LQ3
  s_nop 0
  buffer_load_dwordx4 v[vOK], s[sQ:sQ+3], s[sT3] offen lds
  s_add_u32 m0, s[sWD], LQ3+1024
  s_nop 0
  buffer_load_dwordx4 v[vOK2], s[sQ:sQ+3], s[sT3] offen lds
.Lq_done:
  DK1
  DK2
  STAMP 4
  ; ---- per-wave geometry
  s_and_b32 s[sT1], s[sW], 3
  s_lshr_b32 s[sT2], s[sW], 2
  s_cmp_eq_u32 s[sMB], 1
  s_cselect_b32 s[sQB], s[sW], s[sT1]             ; q block
  s_cselect_b32 s[sKH], 0, s[sT2]                 ; key half (mode A)
  s_movk_i32 s[sKSTEP], 0x4000
  s_cselect_b32 s[sKSTEP], 8192, s[sKSTEP]
  s_add_u32 s[sT1], s[sLEN], 31
  s_lshr_b32 s[sT1], s[sT1], 5                    ; nst = ceil(len/32)
  s_add_u32 s[sT2], s[sT1], 1
  s_sub_u32 s[sT2], s[sT2], s[sKH]
  s_lshr_b32 s[sT2], s[sT2], 1
  s_cmp_eq_u32 s[sMB], 1
  s_cselect_b32 s[sNS], s[sT1], s[sT2]            ; steps of this wave
  s_cselect_b32 s[sNU], s[sT1], s[sNK]            ; step slots
  s_and_b32 s[sLIM], s[sLEN], 31
  s_sub_u32 s[sT2], s[sT1], 1
  s_and_b32 s[sT2], s[sT2], 1
  s_cmp_eq_u32 s[sT2], s[sKH]
  s_cselect_b32 s[sT3], 1, s[sMB]                 ; owner of the last sub-tile
  s_cmp_lg_u32 s[sLIM], 0
  s_cselect_b32 s[sT3], s[sT3], 0
  s_sub_u32 s[sMSTEP], s[sNS], 1
  s_cmp_eq_u32 s[sT3], 1
  s_cselect_b32 s[sMSTEP], s[sMSTEP], -1
  s_lshl_b32 s[sT1], s[sQB], 5
  s_add_u32 s[sQR], s[sT1], s[sQ0]                ; first q row of this wave
  s_cmp_lt_u32 s[sQR], s[sQEND]
  s_cselect_b32 s[sNS], s[sNS], 0
  ; slots with DMA: tile iteration t <= nk-3
  s_sub_i32 s[sND], s[sNK], 2
  s_max_i32 s[sND], s[sND], 0
  s_lshl_b32 s[sND], s[sND], s[sMB]
  s_sub_u32 s[sNF], s[sNS], 1                     ; FULL slots of this wave
  s_sub_u32 s[sCNT], 0, s[sNF]
  s_mov_b32 s[sMK], s[sWD]
  s_add_u32 s[sMV], s[sWD], LV+0x8000
  ; Q read base
  s_lshl_b32 s[sT1], s[sQB], 13
  s_add_u32 s[sT2], s[sT1], LX                    ; qb < 4
  s_add_u32 s[sT3], s[sT1], LQ2-4*8192            ; qb 4,5
  s_add_u32 s[sT4], s[sT1], LQ3-6*8192            ; qb 6,7
  s_cmp_lt_u32 s[sQB], 6
  s_cselect_b32 s[sT3], s[sT3], s[sT4]
  s_cmp_lt_u32 s[sQB], 4
  s_cselect_b32 s[sT2], s[sT2], s[sT3]
  v_add_u32 v[vT0], s[sT2], v[vKB0]
  v_add_u32 v[vT1], s[sT2], v[vKB1]
  s_lshl_b32 s[sKOFF], s[sKH], 13
  s_mov_b32 s[sVOFF], s[sKOFF]
  v_add_u32 v[vKA0], s[sKOFF], v[vKB0]
  v_add_u32 v[vKA1], s[sKOFF], v[vKB1]
  s_lshl_b32 s[sT1], s[sKH], 10
  v_add_u32 v[vVBL], s[sT1], v[vVB]
  v_subrev_u32 v[vVBH], s[sT1], v[vVB]
  VADDR
  ; epilogue addresses
  v_lshlrev_b32 v[vT2], 4, v[vLANE]
  s_lshl_b32 s[sT1], s[sW], 13
  s_xor_b32 s[sT2], s[sT1], 4*8192
  v_add_u32 v[vXP], s[sT2], v[vT2]                ; partner's exchange area
  v_add_u32 v[vXO], s[sT1], v[vT2]                ; own exchange area
  v_and_b32 v[vT2], 31, v[vLANE]
  v_lshlrev_b32 v[vT2], 3, v[vT2]
  v_add_u32 v[vT2], LX, v[vT2]
  s_lshl_b32 s[sT1], s[sW], 8
  s_xor_b32 s[sT2], s[sT1], 4*256
  v_add_u32 v[vSTP], s[sT2], v[vT2]
  v_add_u32 v[vSTA], s[sT1], v[vT2]
  s_lshl_b32 s[sT1], s[sW], 12                    ; output stage: mode A in the V ring (4 KiB per wave), mode B in the K ring (8 KiB)
  s_add_u32 s[sT2], s[sT1], LV
  s_lshl_b32 s[sT3], s[sW], 13
  s_cmp_eq_u32 s[sMB], 1
  s_cselect_b32 s[sT1], s[sT3], s[sT2]
  v_add_u32 v[vSGW], s[sT1], v[vSW]
  v_add_u32 v[vSGR], s[sT1], v[vSR]
  v_mov_b32 v[vL0], 0
  v_mov_b32 v[vL1], 0
  v_mov_b32 v[vLS], 0
  v_mov_b32 v[vLS+1], 0
  v_mov_b32 v[vLS+2], 0
  v_mov_b32 v[vLS+3], 0
  s_nop 1
  .set i, 0
  .rept 4
    v_mfma_f32_32x32x16_bf16 v[vO+16*i:vO+16*i+15], v[vLS:vLS+3], v[vLS:vLS+3], 0     ; O = 0
    .set i, i+1
  .endr
  v_mov_b32 v[vM], 0xf149f2ca
  s_mov_b32 s[sPEND], 0
  s_mov_b32 s[sU], 0
  ; mid group: mode A: K1, K2, V0 ; mode B: K1, V0
  s_waitcnt vmcnt(2)                                ; own Q pieces landed
  DK1
  DK2
  s_cmp_eq_u32 s[sMB], 1
  s_cbranch_scc1 .Lmid_b
  DK1
  DK2
  DV1
  DV2
  s_waitcnt vmcnt(6)
  s_branch .Lmid_d
.Lmid_b:
  DV1
  DV2
  s_waitcnt vmcnt(4)
.Lmid_d:
  s_barrier                                         ; barrier P: Q, K0 landed
  STAMP 5
  ; ---- prologue compute: step 0
  s_cmp_eq_u32 s[sNS], 0
  s_cbranch_scc1 .Lpro_none
  .set i, 0
  .rept 8
    KREADO i, 0
    .if (i & 1)
      ds_read_b128 v[vQ+4*i:vQ+4*i+3], v[vT1] offset:512*(i>>1)
    .else
      ds_read_b128 v[vQ+4*i:vQ+4*i+3], v[vT0] offset:512*(i>>1)
    .endif
    .set i, i+1
  .endr
  .macro QK0 rd                                     ; rd: read the next step's K fragments behind each MFMA (mode B: same tile)
    .set i, 0
    .rept 8
      .if \rd
        .if i == 0
          s_waitcnt lgkmcnt(14)
        .elseif i == 1
          s_waitcnt lgkmcnt(13)
        .elseif i == 2
          s_waitcnt lgkmcnt(12)
        .elseif i == 3
          s_waitcnt lgkmcnt(11)
        .elseif i == 4
          s_waitcnt lgkmcnt(10)
        .elseif i == 5
          s_waitcnt lgkmcnt(9)
        .elseif i == 6
          s_waitcnt lgkmcnt(8)
        .else
          s_waitcnt lgkmcnt(7)
        .endif
      .else
        .if i == 0
          s_waitcnt lgkmcnt(14)
        .elseif i == 1
          s_waitcnt lgkmcnt(12)
        .elseif i == 2
          s_waitcnt lgkmcnt(10)
        .elseif i == 3
          s_waitcnt lgkmcnt(8)
        .elseif i == 4
          s_waitcnt lgkmcnt(6)
        .elseif i == 5
          s_waitcnt lgkmcnt(4)
        .elseif i == 6
          s_waitcnt lgkmcnt(2)
        .else
          s_waitcnt lgkmcnt(0)
        .endif
      .endif
      QKM vSA, i
      .if \rd
        KREADO i, 0
      .endif
      .if i == 1
        DK1
      .elseif i == 3
        DK2
      .elseif i == 5
        DV1
      .elseif i == 7
        DV2
      .endif
      .set i, i+1
    .endr
  .endm
  s_cmp_eq_u32 s[sMB], 1
  s_cbranch_scc1 .Lqk0_b
  QK0 0
  s_branch .Lqk0_d
.Lqk0_b:
  KADV
  QK0 1
  KADV
.Lqk0_d:
  s_nop 7
  s_cmp_lg_u32 s[sMSTEP], 0
  s_cbranch_scc1 .Lnomask0
  s_nop 3
  MASK vSA
.Lnomask0:
  s_nop 3
  ROWMAX1 vSA
  s_nop 1
  ROWMAX2
  v_max_f32 v[vM], v[vM], v[vT2]
  v_mul_f32 v[vNMC], 0xbe0293ee, v[vM]
  s_cmp_eq_u32 s[sSAFE], 1
  s_cbranch_scc1 .Lpro_safe
  .set r, 0
  .rept 6
    EXPE vSA, r
    .set r, r+1
  .endr
  s_cmp_eq_u32 s[sMB], 1
  s_cbranch_scc1 .Lpro_b
  ; ---- mode A: K1 landed -> its fragments
  s_waitcnt vmcnt(8)
  s_barrier
  .set i, 0
  .rept 8
    KREADO i, 16384
    .set i, i+1
  .endr
  .set r, 6
  .rept 5
    EXPE vSA, r
    .set r, r+1
  .endr
  v_xor_b32 v[vKA0], 0x8000, v[vKA0]                ; K(2) sits in ring slot 2
  v_xor_b32 v[vKA1], 0x8000, v[vKA1]
  STAMP 6
  s_cmp_eq_u32 s[sNF], 0
  s_cbranch_scc1 .LlA0
  ; ======================================================================== mode A loop
.LA0:
  FULLF vSB, vSA, 0, 0, 0
  s_cbranch_scc1 .LlA1
  FULLF vSA, vSB, 0, 1, 0
  s_cbranch_scc0 .LA0
.LlA0:
  LASTF vSA, 0, 0, 0
.LlA1:
  LASTF vSB, 0, 1, 0
  ; ======================================================================== epilogue (mode A)
.Lepi_sent:
  STAMP 7
  v_mov_b32 v[vT7], 0x71800000
  s_lshl_b32 s[sT3], s[sQR], 12
  s_add_u32 s[sT3], s[sT3], s[sHOFF]
  ROWSUM
  v_mov_b32 v[vT4], v[vM]
  v_mov_b32 v[vT5], v[vL0]
  s_lshl_b32 s[sT1], s[sKH], 7
  s_add_u32 s[sT3], s[sT3], s[sT1]
  ds_write_b64 v[vSTA], v[vT4:vT5]
  s_waitcnt lgkmcnt(0)
  s_barrier
  STAMPE 1
  MERGE 0, 32
.Lnext:
  STAMP 8
  ; overflow check of the FAST path: a row sum that is not a moderately sized number -> redo this item with the online max
  v_cmp_nlt_f32 vcc, v[vL0], v[vT7]
  s_lshl_b32 s[sT1], s[sW], 2
  s_add_u32 s[sT1], s[sT1], LX+8192
  s_cmp_lg_u64 vcc, 0
  s_cselect_b32 s[sT2], 1, 0
  v_mov_b32 v[vT0], s[sT1]
  v_mov_b32 v[vT1], s[sT2]
  v_and_b32 v[vT2], 7, v[vLANE]
  v_lshlrev_b32 v[vT2], 2, v[vT2]
  v_add_u32 v[vT2], LX+8192, v[vT2]
  ds_write_b32 v[vT0], v[vT1]
  s_waitcnt lgkmcnt(0)
  s_barrier
  ds_read_b32 v[vT1], v[vT2]
  s_waitcnt lgkmcnt(0)
  v_cmp_ne_u32 vcc, 0, v[vT1]
  s_cmp_eq_u32 s[sSAFE], 1
  s_cbranch_scc1 .Lsafe_done
  s_cbranch_vccz .Lsafe_done
  s_mov_b32 s[sSAFE], 1
  s_barrier
  s_branch .Litem
.Lsafe_done:
  s_mov_b32 s[sSAFE], 0
  s_add_u32 s[sR], s[sR], 1
  s_lshl_b32 s[sT1], s[sR], 5
  s_cmp_ge_u32 s[sT1], s[sTOT]
  s_cbranch_scc1 .Ldone
  s_barrier
  s_branch .Litem
.Ldone:
  .if DIAG
    s_waitcnt vmcnt(0)
    STAMP 9
    s_memrealtime s[sA:sA+1]
    s_load_dwordx2 s[sB:sB+1], %0, 96
    s_lshl_b32 s[sT1], %1, 3
    s_add_u32 s[sT1], s[sT1], s[sW]
    s_lshl_b32 s[sT1], s[sT1], 8
    s_waitcnt lgkmcnt(0)
    s_add_u32 s[sB], s[sB], s[sT1]
    s_addc_u32 s[sB+1], s[sB+1], 0
    v_writelane_b32 v[vDG], s[sRT], 10
    v_writelane_b32 v[vDG], s[sA], 11
    v_lshlrev_b32 v[vT6], 2, v[vLANE]
    s_mov_b64 exec, 0xfff
    global_store_dword v[vT6], v[vDG], s[sB:sB+1]
  .endif
  s_endpgm
  ; ======================================================================== cold paths
.Lepi:                                              ; O complete, nothing sent yet
  s_cmp_eq_u32 s[sMB], 1
  s_cbranch_scc1 .LepiB
  s_nop 7
  SENDB 2
  SENDB 3
  s_branch .Lepi_sent
.LepiB:
  STAMP 7
  v_mov_b32 v[vT7], 0x71800000
  s_lshl_b32 s[sT3], s[sQR], 12
  s_add_u32 s[sT3], s[sT3], s[sHOFF]
  ROWSUM
  s_nop 0
  v_rcp_f32 v[vFA], v[vL0]
  s_waitcnt vmcnt(0)
  s_barrier
  .set i, 0
  .rept 32
    v_pk_mul_f32 v[vO+2*i:vO+2*i+1], v[vFA:vFA+1], v[vO+2*i:vO+2*i+1] op_sel_hi:[0,1]
    .set i, i+1
  .endr
  OUTHALF 0, vQ-vO
  v_add_u32 v[vSGW], 4096, v[vSGW]
  v_add_u32 v[vSGR], 4096, v[vSGR]
  s_add_u32 s[sT3], s[sT3], 128
  s_nop 0
  OUTHALF 32, vQ-vO+16
  s_branch .Lnext
  ; ======================================================================== mode B loop (two steps per tile)
.Lpro_b:
  .set r, 6
  .rept 5
    EXPE vSA, r
    .set r, r+1
  .endr
  STAMP 6
  s_cmp_eq_u32 s[sNF], 0
  s_cbranch_scc1 .LlB0
.LB0:
  FULLF vSB, vSA, 1, 0, 0
  s_cbranch_scc1 .LlB1
  FULLF vSA, vSB, 1, 0, 2
  s_cbranch_scc0 .LB0
.LlB0:
  LASTF vSA, 1, 0, 0
.LlB1:
  LASTF vSB, 1, 0, 2
  ; ======================================================================== SAFE loop
.Lpro_safe:
  s_cmp_eq_u32 s[sMB], 1
  s_cbranch_scc1 .Lps_b
  KADV
  s_waitcnt vmcnt(8)
  s_barrier
  .set i, 0
  .rept 8
    KREADO i, 0
    .set i, i+1
  .endr
  KADV
.Lps_b:
  .set r, 0
  .rept 16
    EXPE vSA, r
    .set r, r+1
  .endr
  s_cmp_eq_u32 s[sNF], 0
  s_cbranch_scc1 .LlastS0
.LloopS:
  FULLS vSB, vSA, 0
  s_cbranch_scc1 .LlastS1
  FULLS vSA, vSB, 1
  s_cbranch_scc0 .LloopS
.LlastS0:
  LASTS vSA, 0
.LlastS1:
  LASTS vSB, 1

  ; ---- slots in which this wave has nothing to compute
.Lpro_none:
  DALL
  s_cmp_eq_u32 s[sMB], 1
  s_cbranch_scc1 .Lnone
  s_waitcnt vmcnt(8)
  s_barrier
.Lnone:                                             ; wave without steps: keep loading its share of the tiles
  s_cmp_ge_u32 s[sU], s[sNU]
  s_cbranch_scc1 .Lepi
  s_bitcmp1_b32 s[sU], 0
  s_cselect_b32 s[sT1], s[sMB], 0
  s_cmp_eq_u32 s[sT1], 1
  s_cbranch_scc1 .Lnone_skip
  s_cmp_lt_u32 s[sU], s[sND]
  s_cbranch_scc0 .Lnone_fin
  s_waitcnt vmcnt(4) lgkmcnt(0)
  s_barrier
  DALL
  s_branch .Lnone_skip
.Lnone_fin:
  s_waitcnt vmcnt(0) lgkmcnt(0)
  s_barrier
.Lnone_skip:
  s_add_u32 s[sU], s[sU], 1
  s_branch .Lnone
.Lnone_tail:                                        ; after the wave's last step: only barriers remain
  s_cmp_ge_u32 s[sU], s[sNU]
  s_cbranch_scc1 .Lepi
  s_bitcmp1_b32 s[sU], 0
  s_cselect_b32 s[sT1], s[sMB], 0
  s_cmp_eq_u32 s[sT1], 1
  s_cbranch_scc1 .Lnt_skip
  s_barrier
.Lnt_skip:
  s_add_u32 s[sU], s[sU], 1
  s_branch .Lnone_tail

.Lfallback:
  s_waitcnt vmcnt(0) lgkmcnt(0)
  .purgem STAMP
  .purgem STAMPE
  .purgem DK1
  .purgem DK2
  .purgem DV1
  .purgem DV2
  .purgem DALL
  .purgem BARX
  .purgem DMAP
  .purgem KREADO
  .purgem KADV
  .purgem VADDR
  .purgem QKM
  .purgem VREADO
  .purgem PVJ
  .purgem PVM
  .purgem LSUM
  .purgem FMAE
  .purgem XPE
  .purgem EXPE
  .purgem CVTE
  .purgem SUMPK
  .purgem MASK
  .purgem ROWMAX1
  .purgem ROWMAX2
  .purgem RESCALE
  .purgem SENDB
  .purgem LASTV
  .purgem FDMA
  .purgem FDMB
  .purgem FULLF
  .purgem LASTF
  .purgem FULLS
  .purgem LASTS
  .purgem OUTHALF
  .purgem MERGE
  .purgem ROWSUM
  .purgem PREFETCH
  .purgem QK0
  )" :: "s"(__builtin_amdgcn_kernarg_segment_ptr()), "s"(blockIdx.x), "v"(threadIdx.x), "i"(NIT)
     : "memory", "vcc", "m0",
       "s8","s9","s10","s11","s12","s13","s14","s15","s16","s17","s18","s19","s20","s21","s22","s23","s24","s25","s26","s27","s28","s29","s30","s31",
       "s33","s34","s35","s36","s37","s38","s39","s40","s41","s42","s43","s44","s45","s46","s47","s48","s49","s50","s51","s52","s53","s54","s55",
       "s56","s57","s58","s59","s60","s61","s62","s63","s64","s65","s66","s67","s68","s69","s70","s71","s72","s73","s74","s75","s76","s77","s78","s79",
       "s80","s81","s82","s83","s84","s85","s86","s87","s88","s89","s90","s91","s92","s93","s94","s95","s96","s97","s98","s99",
       "v1","v2","v3","v4","v5","v6","v7","v8","v9","v10","v11","v12","v13","v14","v15","v16","v17","v18","v19","v20","v21","v22","v23","v24","v25","v26","v27","v28","v29","v30","v31",
       "v32","v33","v34","v35","v36","v37","v38","v39","v40","v41","v42","v43","v44","v45","v46","v47","v48","v49","v50","v51","v52","v53","v54","v55","v56","v57","v58","v59","v60","v61","v62","v63",
       "v64","v65","v66","v67","v68","v69","v70","v71","v72","v73","v74","v75","v76","v77","v78","v79","v80","v81","v82","v83","v84","v85","v86","v87","v88","v89","v90","v91","v92","v93","v94","v95",
       "v96","v97","v98","v99","v100","v101","v102","v103","v104","v105","v106","v107","v108","v109","v110","v111","v112","v113","v114","v115","v116","v117","v118","v119","v120","v121","v122","v123","v124","v125","v126","v127",
       "v128","v129","v130","v131","v132","v133","v134","v135","v136","v137","v138","v139","v140","v141","v142","v143","v144","v145","v146","v147","v148","v149","v150","v151","v152","v153","v154","v155","v156","v157","v158","v159",
       "v160","v161","v162","v163","v164","v165","v166","v167","v168","v169","v170","v171","v172","v173","v174","v175","v176","v177","v178","v179","v180","v181","v182","v183","v184","v185","v186","v187","v188","v189","v190","v191",
       "v192","v193","v194","v195","v196","v197","v198","v199","v200","v201","v202","v203","v204","v205","v206","v207","v208","v209","v210","v211","v212","v213","v214","v215","v216","v217","v218","v219","v220","v221","v222","v223",
       "v224","v225","v226","v227","v228","v229","v230","v231","v232","v233","v234","v235","v236","v237","v238","v239","v240","v241","v242","v243","v244","v245","v246","v247","v248","v249","v250","v251","v252","v253","v254","v255");
  generic_path((const unsigned short*)Q, (const unsigned short*)K, (const unsigned short*)V, (const int*)seg, (unsigned short*)O, T, lds);
}

__global__ __launch_bounds__(512, 2)
void ragged_attn_generic(const void* Q, const void* K, const void* V, const void* seg, void* O, int T, int pad) {
  __shared__ __attribute__((aligned(16))) char lds[LDS_BYTES];
  generic_path((const unsigned short*)Q, (const unsigned short*)K, (const unsigned short*)V, (const int*)seg, (unsigned short*)O, T, lds);
}

extern "C" void kernel_launch(void* const* d_in, const int* in_sizes, int n_in, void* d_out, int out_size, void* d_ws, size_t ws_size,
                              hipStream_t stream) {
  const int T = in_sizes[3];
  const unsigned nb = (unsigned)T * ROWB, F = 0x00020000u;
  if (T > 2048 && T <= 2560) {
    ragged_attn_kernel<5><<<256, 512, 0, stream>>>(d_in[0], nb, F, d_in[1], nb, F, d_in[2], nb, F, d_in[3], (unsigned)T * 4u, F, d_out, nb, F,
                                                   T, T - 1 - 512 * 4, ((T + 31) / 32) * (int)ROWB, 0, d_ws);
  } else {
    ragged_attn_generic<<<256, 512, 0, stream>>>(d_in[0], d_in[1], d_in[2], d_in[3], d_out, T, 0);
  }
}
```

```cpp
#include <hip/hip_runtime.h>
#include <stdint.h>

typedef short    bf16x8 __attribute__((ext_vector_type(8)));
typedef short    s16x4  __attribute__((ext_vector_type(4)));
typedef float    f32x16 __attribute__((ext_vector_type(16)));
typedef float    f32x4  __attribute__((ext_vector_type(4)));
typedef float    f32x2  __attribute__((ext_vector_type(2)));
typedef __bf16   bf2    __attribute__((ext_vector_type(2)));
typedef unsigned u32x4  __attribute__((ext_vector_type(4)));
typedef unsigned long long u64;
#define LDSP __attribute__((address_space(3)))
typedef LDSP const char* lds_cptr;
typedef LDSP char* lds_ptr;
typedef short v4i16_t __attribute__((ext_vector_type(4)));
#define DI __device__ __forceinline__
#define MFMA(a, b, c) __builtin_amdgcn_mfma_f32_32x32x16_bf16((a), (b), (c), 0, 0, 0)

constexpr int NH = 16, HD = 128;
constexpr unsigned ROWB = NH * HD * 2;
constexpr unsigned LDS_BYTES = 163840;
constexpr float CEXP = 0.08838834764831845f * 1.4426950408889634f;
constexpr float THR_RAW = 8.0f / CEXP;
constexpr float MINIT = -1.0e30f;

DI unsigned rfl(unsigned x) { return __builtin_amdgcn_readfirstlane(x); }
DI unsigned cvtpk(float lo, float hi) { f32x2 v = {lo, hi}; bf2 b = __builtin_convertvector(v, bf2); return __builtin_bit_cast(unsigned, b); }
DI float swap_max(float x) { auto r = __builtin_amdgcn_permlane32_swap(__float_as_uint(x), __float_as_uint(x), false, false); return __builtin_fmaxf(__uint_as_float(r[0]), __uint_as_float(r[1])); }
DI float swap_add(float x) { auto r = __builtin_amdgcn_permlane32_swap(__float_as_uint(x), __float_as_uint(x), false, false); return __uint_as_float(r[0]) + __uint_as_float(r[1]); }
DI s16x4 vtr(lds_cptr p) { return __builtin_bit_cast(s16x4, __builtin_amdgcn_ds_read_tr16_b64_v4i16((LDSP v4i16_t*)p)); }
#define MX3(a, b, c) __builtin_fmaxf(__builtin_fmaxf((a), (b)), (c))

DI void dma16(__amdgpu_buffer_rsrc_t srd, unsigned voff, unsigned soff, unsigned m0v) {
  unsigned keep;
  asm volatile("s_nop 4\n\ts_mov_b32 %0, m0\n\ts_mov_b32 m0, %1\n\ts_nop 0\n\tbuffer_load_dwordx4 %2, %3, %4 offen lds\n\ts_mov_b32 m0, %0"
               : "=&s"(keep) : "s"(m0v), "v"(voff), "s"(srd), "s"(soff) : "memory");
}

__device__ __forceinline__ void generic_path(const unsigned short* __restrict__ Q, const unsigned short* __restrict__ K, const unsigned short* __restrict__ V,
                                          const int* __restrict__ seg, unsigned short* __restrict__ O, int T, char* lds) {
  constexpr unsigned SLOT = 32768, VOFF = 16384, XREG = 4 * SLOT;
  const int tid = threadIdx.x, lane = tid & 63, r32 = lane & 31, hh = lane >> 5;
  const int w = (int)rfl((unsigned)tid >> 6);
  const int grp = blockIdx.x & 7, jj = blockIdx.x >> 3, NJ = 32;
  const unsigned ldsb = (unsigned)(uintptr_t)lds;
  const lds_ptr L = (lds_ptr)lds;
  const int total_items = 2 * ((T + 127) >> 7);
  const __amdgpu_buffer_rsrc_t srdQ = __builtin_amdgcn_make_buffer_rsrc((void*)Q, (short)0, T * (int)ROWB, 0x00020000);
  const __amdgpu_buffer_rsrc_t srdK = __builtin_amdgcn_make_buffer_rsrc((void*)K, (short)0, T * (int)ROWB, 0x00020000);
  const __amdgpu_buffer_rsrc_t srdV = __builtin_amdgcn_make_buffer_rsrc((void*)V, (short)0, T * (int)ROWB, 0x00020000);
  const unsigned prow = (lane >> 2) & 7, pcb = lane >> 5, pc = lane & 3;
  const unsigned voffK = prow * ROWB + pcb * 64 + ((pc ^ ((2u * (unsigned)w + (prow >> 2)) & 3u)) * 16);
  const unsigned voffV = prow * ROWB + pcb * 64 + pc * 16;
  const unsigned kb0 = 2048u * (r32 >> 3) + 64u * (r32 & 7) + 16u * ((unsigned)hh ^ ((r32 >> 2) & 3u));
  const unsigned vb0 = ((unsigned)hh * 4 + ((lane & 15) >> 2)) * 64 + ((lane >> 4) & 1) * 32 + (lane & 3) * 8;
  for (int idx = jj; idx < total_items; idx += NJ) {
    __syncthreads();
    const int head = grp + 8 * (idx & 1);
    const int q0 = (idx >> 1) * 128, qend = min(q0 + 128, T);
    LDSP int* red = (LDSP int*)L;
    if (tid == 0) { red[0] = 0x7fffffff; red[1] = (int)0x80000000; red[2] = 0x7fffffff; red[3] = -1; }
    __syncthreads();
    if (tid < 128 && q0 + tid < qend) { const int id = seg[q0 + tid];
      __hip_atomic_fetch_min((LDSP int*)&red[0], id, __ATOMIC_RELAXED, __HIP_MEMORY_SCOPE_WORKGROUP);
      __hip_atomic_fetch_max((LDSP int*)&red[1], id, __ATOMIC_RELAXED, __HIP_MEMORY_SCOPE_WORKGROUP); }
    __syncthreads();
    const int idlo = red[0], idhi = red[1];
    int lo = 0x7fffffff, hi = -1;
    for (int i = tid; i < T; i += 512) { const int id = seg[i]; if (id >= idlo && id <= idhi) { lo = min(lo, i); hi = max(hi, i); } }
    __hip_atomic_fetch_min((LDSP int*)&red[2], lo, __ATOMIC_RELAXED, __HIP_MEMORY_SCOPE_WORKGROUP);
    __hip_atomic_fetch_max((LDSP int*)&red[3], hi, __ATOMIC_RELAXED, __HIP_MEMORY_SCOPE_WORKGROUP);
    __syncthreads();
    const int k0 = (int)rfl((unsigned)red[2]), kend = (int)rfl((unsigned)red[3]) + 1;
    __syncthreads();
    const int nk = (kend - k0 + 63) >> 6;
    const unsigned hoff = (unsigned)head * 256u;
#pragma unroll
    for (int i = 0; i < 2; ++i) {
      const unsigned m = (unsigned)w + 8u * i;
      const unsigned so = (unsigned)(q0 + 8 * (int)m) * ROWB + hoff;
      dma16(srdQ, voffK, so, ldsb + XREG + m * 2048u);
      dma16(srdQ, voffK, so + 128u, ldsb + XREG + m * 2048u + 1024u);
    }
    const int qb = w & 3, sub = w >> 2;
    const int qrow = q0 + 32 * qb + r32;
    const int segq = seg[qrow < T ? qrow : T - 1];
    bf16x8 qf[8];
    f32x16 o[4];
#pragma unroll
    for (int d = 0; d < 4; ++d) o[d] = f32x16{};
    float m_run = MINIT, l_run = 0.f;
    for (int t = 0; t < nk; ++t) {
      __syncthreads();
      {
        const unsigned so = (unsigned)(k0 + 64 * t + 8 * w) * ROWB + hoff;
        const unsigned d = ldsb + (unsigned)w * 2048u;
        dma16(srdK, voffK, so, d); dma16(srdK, voffK, so + 128u, d + 1024u);
        dma16(srdV, voffV, so, d + VOFF); dma16(srdV, voffV, so + 128u, d + VOFF + 1024u);
      }
      asm volatile("s_waitcnt vmcnt(0)" ::: "memory");
      __syncthreads();
      if (t == 0) {
#pragma unroll
        for (int s = 0; s < 8; ++s)
          qf[s] = *(const LDSP bf16x8*)(L + XREG + (unsigned)qb * 8192u + ((kb0 ^ ((s & 1) * 32u)) + 512u * (s >> 1)));
      }
      const int kbase = k0 + 64 * t + 32 * sub;
      const lds_cptr kp = (lds_cptr)L + (unsigned)sub * 8192u;
      f32x16 sc = f32x16{};
#pragma unroll
      for (int s = 0; s < 8; ++s) {
        const bf16x8 kf = *(const LDSP bf16x8*)(kp + ((kb0 ^ ((s & 1) * 32u)) + 512u * (s >> 1)));
        sc = MFMA(kf, qf[s], sc);
      }
#pragma unroll
      for (int r = 0; r < 16; ++r) {
        const int kk = kbase + (r & 3) + 8 * (r >> 2) + 4 * hh;
        const int id = seg[kk < T ? kk : T - 1];
        sc[r] = (kk < kend && id == segq) ? sc[r] : -__builtin_inff();
      }
      float a = MX3(sc[0], sc[1], sc[2]), b = MX3(sc[3], sc[4], sc[5]);
      a = MX3(a, sc[6], sc[7]); b = MX3(b, sc[8], sc[9]); a = MX3(a, sc[10], sc[11]); b = MX3(b, sc[12], sc[13]);
      a = MX3(a, sc[14], sc[15]);
      const float pm = swap_max(__builtin_fmaxf(a, b));
      const float mn = __builtin_fmaxf(m_run, pm);
      const float al = __builtin_amdgcn_exp2f((m_run - mn) * CEXP);
      m_run = mn; l_run *= al;
#pragma unroll
      for (int d = 0; d < 4; ++d)
#pragma unroll
        for (int r = 0; r < 16; ++r) o[d][r] *= al;
      const float nmc = -m_run * CEXP;
      float ps = 0.f;
#pragma unroll
      for (int r = 0; r < 16; ++r) { sc[r] = __builtin_amdgcn_exp2f(__builtin_fmaf(sc[r], CEXP, nmc)); ps += sc[r]; }
      l_run += ps;
      u32x4 pw0, pw1;
#pragma unroll
      for (int e = 0; e < 4; ++e) { pw0[e] = cvtpk(sc[2 * e], sc[2 * e + 1]); pw1[e] = cvtpk(sc[8 + 2 * e], sc[8 + 2 * e + 1]); }
      const bf16x8 p0 = __builtin_bit_cast(bf16x8, pw0), p1 = __builtin_bit_cast(bf16x8, pw1);
      const lds_cptr vp = kp + VOFF + vb0;
#pragma unroll
      for (int d = 0; d < 4; ++d) {
        const s16x4 a0 = vtr(vp + d * 512), a1 = vtr(vp + d * 512 + 2048), b0 = vtr(vp + d * 512 + 4096), b1 = vtr(vp + d * 512 + 6144);
        const bf16x8 va = __builtin_shufflevector(a0, a1, 0, 1, 2, 3, 4, 5, 6, 7), vb = __builtin_shufflevector(b0, b1, 0, 1, 2, 3, 4, 5, 6, 7);
        o[d] = MFMA(va, p0, o[d]);
        o[d] = MFMA(vb, p1, o[d]);
      }
    }
    l_run = swap_add(l_run);
    unsigned short* orow = O + (size_t)(qrow < T ? qrow : 0) * (NH * HD) + head * HD + 8 * hh;
    const bool row_ok = qrow < qend;
    const int kh = sub;
    __syncthreads();
    const unsigned xa = SLOT, xb = 2 * SLOT, xs = 3 * SLOT;
    const unsigned mine = (kh ? xb : xa) + (unsigned)qb * 8192u + (unsigned)lane * 16u;
    const unsigned theirs = (kh ? xa : xb) + (unsigned)qb * 8192u + (unsigned)lane * 16u;
#pragma unroll
    for (int dd = 0; dd < 2; ++dd)
#pragma unroll
      for (int g = 0; g < 4; ++g) {
        f32x4 v;
#pragma unroll
        for (int e = 0; e < 4; ++e) v[e] = kh ? o[dd][4 * g + e] : o[2 + dd][4 * g + e];
        *(LDSP f32x4*)(L + mine + (unsigned)(dd * 4 + g) * 1024u) = v;
      }
    if (hh == 0) { f32x2 s2 = {m_run, l_run}; *(LDSP f32x2*)(L + xs + (unsigned)w * 256u + (unsigned)r32 * 8u) = s2; }
    __syncthreads();
    const f32x2 o2 = *(const LDSP f32x2*)(L + xs + (unsigned)(w ^ 4) * 256u + (unsigned)r32 * 8u);
    const float mn = __builtin_fmaxf(m_run, o2[0]);
    const float wa = __builtin_amdgcn_exp2f((m_run - mn) * CEXP), wb = __builtin_amdgcn_exp2f((o2[0] - mn) * CEXP);
    const float inv = 1.0f / (wa * l_run + wb * o2[1]);
    const float fa = wa * inv, fb = wb * inv;
#pragma unroll
    for (int dd = 0; dd < 2; ++dd) {
      float r[16];
#pragma unroll
      for (int g = 0; g < 4; ++g) {
        const f32x4 v = *(const LDSP f32x4*)(L + theirs + (unsigned)(dd * 4 + g) * 1024u);
#pragma unroll
        for (int e = 0; e < 4; ++e) r[4 * g + e] = fa * (kh ? o[2 + dd][4 * g + e] : o[dd][4 * g + e]) + fb * v[e];
      }
#pragma unroll
      for (int g = 0; g < 4; g += 2) {
        unsigned ax = cvtpk(r[4 * g + 0], r[4 * g + 1]), ay = cvtpk(r[4 * g + 2], r[4 * g + 3]);
        unsigned bx = cvtpk(r[4 * g + 4], r[4 * g + 5]), by = cvtpk(r[4 * g + 6], r[4 * g + 7]);
        auto rx = __builtin_amdgcn_permlane32_swap(ax, bx, false, false); auto ry = __builtin_amdgcn_permlane32_swap(ay, by, false, false);
        const u32x4 v = {rx[0], ry[0], rx[1], ry[1]};
        if (row_ok) *(u32x4*)(orow + 64 * kh + 32 * dd + 8 * g) = v;
      }
    }
  }
}

#define DIAG 0
#define ABL 0
#define PF1 3
#define PF2 5
#define STR2(x) #x
#define STR(x) STR2(x)

template <int NIT>
__global__ __launch_bounds__(512, 2)
void ragged_attn_kernel(const void* Q, unsigned nQ, unsigned fQ, const void* K, unsigned nK, unsigned fK, const void* V, unsigned nV, unsigned fV,
                        const void* seg, unsigned nS, unsigned fS, void* O, unsigned nO, unsigned fO, int T, int lastc, int pfb, int pad, void* dbg) {
  __shared__ __attribute__((aligned(16))) char lds[LDS_BYTES];
  asm volatile(R"(
  .set NIT, %c3
  .set DIAG, )" STR(DIAG) R"(
  .set ABL, )" STR(ABL) R"(
  .set PF1, )" STR(PF1) R"(
  .set PF2, )" STR(PF2) R"(
  ; ---------------- SGPRs
  .set sQ, 8
  .set sK, 12
  .set sV, 16
  .set sS, 20
  .set sO, 24
  .set sT, 28
  .set sLASTC, 29
  .set sPFB, 30
  .set sF, 30
  .set sLIVE, 30
  .set sTM16, 31
  .set sNDF, 33
  .set sG, 34
  .set sR, 35
  .set sNBIG, 36
  .set sTOT, 37
  .set sHOFF, 38
  .set sQ0, 39
  .set sQEND, 40
  .set sK0, 41
  .set sNK, 42
  .set sLEN, 43
  .set sMB, 44
  .set sNS, 45
  .set sU, 46
  .set sNU, 47
  .set sKOFF, 48
  .set sKSTEP, 49
  .set sVOFF, 50
  .set sKSO, 51
  .set sVSO, 52
  .set sKDST, 53
  .set sVDST, 54
  .set sCNT, 55
  .set sND, 56
  .set sMSTEP, 57
  .set sPEND, 58
  .set sW, 59
  .set sA, 60
  .set sB, 62
  .set sC, 64
  .set sSAFE, 64
  .set sNF, 65
  .set sT1, 66
  .set sT2, 67
  .set sT3, 68
  .set sT4, 69
  .set sCE, 70
  .set sTHR, 71
  .set sWD, 72
  .set sQB, 73
  .set sKH, 74
  .set sLIM, 75
  .set sJ, 76
  .set sQR, 77
  .set sRT, 78
  .set sV2, 80
  .set sM, 84
  .set sMK, 86
  .set sMV, 87
  ; ---------------- VGPRs
  .set vLANE, 1
  .set vOK, 2
  .set vOK2, 3
  .set vOV, 4
  .set vOV2, 5
  .set vKB0, 6
  .set vKB1, 7
  .set vVB, 8
  .set vSTART, 9
  .set vLEN, 10
  .set vINCL, 11
  .set vEXCL, 12
  .set vM, 13
  .set vNMC, 14
  .set vL0, 15
  .set vL1, 16
  .set vKA0, 17
  .set vKA1, 18
  .set vVA, 19
  .set vAL, 20
  .set vSO, 21
  .set vHH4, 22
  .set vSW, 23
  .set vT0, 24
  .set vT1, 25
  .set vT2, 26
  .set vT3, 27
  .set vT4, 28
  .set vT5, 29
  .set vT6, 30
  .set vT7, 31
  .set vP, 32
  .set vX, 40
  .set vLS, 40
  .set vXO, 44
  .set vXP, 45
  .set vSTA, 46
  .set vSTP, 47
  .set vSGW, 48
  .set vSGR, 49
  .set vFA, 50
  .set vFB, 52
  .set vDG, 54
  .set vSR, 56
  .set vVH, 57
  .set vVBL, 58
  .set vVBH, 59
  .set vONE, 60
  .set vO, 64
  .set vQ, 128
  .set vSA, 160
  .set vSB, 176
  .set vK, 192
  .set vV, 224
  ; ---------------- LDS map: K ring 4 x 16 KiB | V ring 4 x 16 KiB | X (Q stage) 32 KiB
  .set LK, 0
  .set LV, 65536
  .set LX, 131072
  .set LQ2, 49152
  .set LQ3, 114688
  .set LSCR, 98304
  .set TILEB, 64*4096

  .macro STAMP i                                    ; diagnostic builds only: low word of the cycle counter -> lane i of vDG
    .if (DIAG == 1) || ((DIAG == 2) && ((\i == 0) || (\i >= 5)))
      s_memtime s[sM:sM+1]
      s_waitcnt lgkmcnt(0)
      v_writelane_b32 v[vDG], s[sM], \i
    .endif
  .endm
  .macro STAMPE i
    .if DIAG == 2
      s_memtime s[sM:sM+1]
      s_waitcnt lgkmcnt(0)
      v_writelane_b32 v[vDG], s[sM], \i
    .endif
  .endm

  ; ======================================================================== macros
  ; ---- DMA pieces (generic form).  sKDST / sVDST: ring offset + w*2048 of the next tile
  .macro DK1
    s_mov_b32 m0, s[sKDST]
    s_add_u32 s[sT1], s[sKDST], 16384
    buffer_load_dwordx4 v[vOK], s[sK:sK+3], s[sKSO] offen lds
  .endm
  .macro DK2
    s_add_u32 m0, s[sKDST], 1024
    s_and_b32 s[sKDST], s[sT1], 0xffff
    buffer_load_dwordx4 v[vOK2], s[sK:sK+3], s[sKSO] offen lds
    s_add_u32 s[sKSO], s[sKSO], TILEB
  .endm
  .macro DV1
    s_add_u32 m0, s[sVDST], LV
    s_add_u32 s[sT3], s[sVDST], 16384
    buffer_load_dwordx4 v[vOV], s[sV:sV+3], s[sVSO] offen lds
  .endm
  .macro DV2
    s_add_u32 m0, s[sVDST], LV+1024
    s_and_b32 s[sVDST], s[sT3], 0xffff
    buffer_load_dwordx4 v[vOV2], s[sV:sV+3], s[sVSO] offen lds
    s_add_u32 s[sVSO], s[sVSO], TILEB
  .endm
  .macro DALL
    DK1
    DK2
    DV1
    DV2
  .endm
  ; m: 0 this slot has a barrier, 1 barrier unless mode B (run time), 2 no barrier
  .macro BARX n, m
    .if \m == 1
      s_cmp_eq_u32 s[sMB], 1
      s_cbranch_scc1 .Lnob\@
    .endif
    .if \m == 2
      s_waitcnt lgkmcnt(0)
    .else
      s_waitcnt vmcnt(\n) lgkmcnt(0)
      s_barrier
    .endif
    .if \m == 1
      s_branch .Lbd\@
  .Lnob\@:
      s_waitcnt lgkmcnt(0)
  .Lbd\@:
    .endif
  .endm
  ; DMA piece k of a generic step.  dma: 0 none, 1 always, 2 run-time flag sF ; m as above
  .macro DMAP k, m, dma
    .if ((\dma) != 0) && ((\m) != 2) && ((ABL & 1) == 0)
      .if \dma == 2
        s_cmp_eq_u32 s[sF], 0
        s_cbranch_scc1 .Ld\@
      .endif
      .if \m == 1
        s_cmp_eq_u32 s[sMB], 1
        s_cbranch_scc1 .Ld\@
      .endif
      .if \k == 0
        DK1
      .elseif \k == 1
        DK2
      .elseif \k == 2
        DV1
      .else
        DV2
      .endif
  .Ld\@:
    .endif
  .endm
  .macro KREADO i, off
    .if (ABL & 8)
    .elseif (\i & 1)
      ds_read_b128 v[vK+4*\i:vK+4*\i+3], v[vKA1] offset:(\off)+512*(\i>>1)
    .else
      ds_read_b128 v[vK+4*\i:vK+4*\i+3], v[vKA0] offset:(\off)+512*(\i>>1)
    .endif
  .endm
  .macro KADV                                       ; generic form: advance the K read addresses to the next step
    s_add_u32 s[sKOFF], s[sKOFF], s[sKSTEP]
    s_and_b32 s[sKOFF], s[sKOFF], 0xffff
    v_add_u32 v[vKA0], s[sKOFF], v[vKB0]
    v_add_u32 v[vKA1], s[sKOFF], v[vKB1]
  .endm
  .macro VADDR                                      ; generic form: V read addresses of the coming step
    v_add_u32 v[vVA], s[sVOFF], v[vVBL]
    v_add_u32 v[vVH], s[sVOFF], v[vVBH]
    s_add_u32 s[sVOFF], s[sVOFF], s[sKSTEP]
    s_and_b32 s[sVOFF], s[sVOFF], 0xffff
  .endm
  .macro QKM c, i
    .if \i == 0
      v_mfma_f32_32x32x16_bf16 v[\c:\c+15], v[vK:vK+3], v[vQ:vQ+3], 0
    .else
      v_mfma_f32_32x32x16_bf16 v[\c:\c+15], v[vK+4*\i:vK+4*\i+3], v[vQ+4*\i:vQ+4*\i+3], v[\c:\c+15]
    .endif
  .endm
  .macro VREADO j, off                              ; V fragment j: register block d = j&3 (actual d-block = d ^ 2*kh), ks = j>>2
    .if (ABL & 4) == 0
    .if (\j & 2)
    ds_read_b64_tr_b16 v[vV+4*\j:vV+4*\j+1], v[vVH] offset:(\off)+512*(\j&3)+4096*(\j>>2)
    ds_read_b64_tr_b16 v[vV+4*\j+2:vV+4*\j+3], v[vVH] offset:(\off)+512*(\j&3)+4096*(\j>>2)+2048
    .else
    ds_read_b64_tr_b16 v[vV+4*\j:vV+4*\j+1], v[vVA] offset:(\off)+512*(\j&3)+4096*(\j>>2)
    ds_read_b64_tr_b16 v[vV+4*\j+2:vV+4*\j+3], v[vVA] offset:(\off)+512*(\j&3)+4096*(\j>>2)+2048
    .endif
    .endif
  .endm
  .macro PVJ d, ks
    v_mfma_f32_32x32x16_bf16 v[vO+16*(\d):vO+16*(\d)+15], v[vV+4*((\d)+4*(\ks)):vV+4*((\d)+4*(\ks))+3], v[vP+4*(\ks):vP+4*(\ks)+3], v[vO+16*(\d):vO+16*(\d)+15]
  .endm
  .macro PVM j
    PVJ (\j&3), (\j>>2)
  .endm
  .macro LSUM ks                                    ; row sums of one packed P fragment
    .if (ABL & 64) == 0
    v_mfma_f32_16x16x32_bf16 v[vLS:vLS+3], v[vONE:vONE+3], v[vP+4*\ks:vP+4*\ks+3], v[vLS:vLS+3]
    .endif
  .endm
  .macro FMAE c, r
    .if (ABL & 2) == 0
    v_fma_f32 v[\c+(\r)], v[\c+(\r)], s[sCE], v[vNMC]
    .endif
  .endm
  .macro XPE c, r
    .if (ABL & 2) == 0
    v_exp_f32 v[\c+(\r)], v[\c+(\r)]
    .endif
  .endm
  .macro EXPE c, r
    FMAE \c, \r
    XPE \c, \r
  .endm
  .macro CVTE p, i                                  ; pack of elements 2i, 2i+1
    .if (ABL & 32) == 0
    v_cvt_pk_bf16_f32 v[vP+\i], v[\p+2*\i], v[\p+2*\i+1]
    .endif
  .endm
  .macro SUMPK p, i                                 ; SAFE form: row sums + pack of elements 2i, 2i+1
    v_add_f32 v[vL0], v[vL0], v[\p+2*\i]
    v_add_f32 v[vL1], v[vL1], v[\p+2*\i+1]
    v_cvt_pk_bf16_f32 v[vP+\i], v[\p+2*\i], v[\p+2*\i+1]
  .endm
  .macro MASK c                                     ; keys >= lim of this sub-tile -> -inf
    v_sub_u32 v[vT2], s[sLIM], v[vHH4]
    v_mov_b32 v[vT3], 0xff800000
    .set r, 0
    .rept 16
      v_cmp_lt_i32 vcc, (r&3)+8*(r>>2), v[vT2]
      s_nop 1
      v_cndmask_b32 v[\c+r], v[vT3], v[\c+r], vcc
      .set r, r+1
    .endr
  .endm
  .macro ROWMAX1 c
    v_max3_f32 v[vT2], v[\c+0], v[\c+1], v[\c+2]
    v_max3_f32 v[vT3], v[\c+3], v[\c+4], v[\c+5]
    v_max3_f32 v[vT2], v[vT2], v[\c+6], v[\c+7]
    v_max3_f32 v[vT3], v[vT3], v[\c+8], v[\c+9]
    v_max3_f32 v[vT2], v[vT2], v[\c+10], v[\c+11]
    v_max3_f32 v[vT3], v[vT3], v[\c+12], v[\c+13]
    v_max3_f32 v[vT2], v[vT2], v[\c+14], v[\c+15]
    v_max_f32 v[vT2], v[vT2], v[vT3]
    v_mov_b32 v[vT3], v[vT2]
  .endm
  .macro ROWMAX2                                    ; needs 2 wait states after ROWMAX1
    v_permlane32_swap_b32 v[vT2], v[vT3]
    s_nop 0
    v_max_f32 v[vT2], v[vT2], v[vT3]                ; vT2 = row max
  .endm
  .macro RESCALE
    s_nop 7
    s_nop 7
    .set i, 0
    .rept 64
      v_mul_f32 v[vO+i], v[vAL], v[vO+i]
      .set i, i+1
    .endr
    v_mul_f32 v[vL0], v[vAL], v[vL0]
    v_mul_f32 v[vL1], v[vAL], v[vL1]
    s_mov_b32 s[sPEND], 0
  .endm
  .macro SENDB d                                    ; d-block d (16 regs) -> own exchange area
    .set i, 0
    .rept 4
      ds_write_b128 v[vXO], v[vO+16*\d+4*i:vO+16*\d+4*i+3] offset:4096*(\d&1)+1024*i
      .set i, i+1
    .endr
  .endm
  .macro LASTV da, db, dc, dd                       ; last P.V: the blocks to send (da, db) first
    PVJ \da, 0
    PVJ \db, 0
    PVJ \da, 1
    PVJ \db, 1
    PVJ \dc, 0
    s_nop 7
    s_nop 1
    SENDB \da
    PVJ \dd, 0
    SENDB \db
    PVJ \dc, 1
    PVJ \dd, 1
    s_branch .Lepi_sent
  .endm

  ; ---- FAST step body (reference = row max of the wave's first sub-tile; overflow is detected on the row sums at the end of the item,
  ;      which is then redone with the SAFE steps).  prev enters with exp done for e0..e10.
  ;      gen = 1: generic addressing (address registers, generic DMA) ; gen = 0: mode A, ring position r as immediates
  .macro FDMA k, gen, r, m, dma
    .if \gen
      DMAP \k, \m, \dma
    .elseif (\dma) && ((ABL & 1) == 0)
      .if \k == 0
        s_add_u32 m0, s[sMK], 16384*(\r)
      .elseif \k == 1
        s_add_u32 m0, s[sMK], 16384*(\r)+1024
      .elseif \k == 2
        s_add_u32 m0, s[sMV], 16384*(\r)
      .else
        s_add_u32 m0, s[sMV], 16384*(\r)+1024
      .endif
    .endif
  .endm
  .macro FDMB k, gen, dma                            ; the load itself (mode A form), one instruction behind its M0 write
    .if ((\gen) == 0) && (\dma) && ((ABL & 1) == 0)
      .if \k == 0
        buffer_load_dwordx4 v[vOK], s[sK:sK+3], s[sKSO] offen lds
      .elseif \k == 1
        buffer_load_dwordx4 v[vOK2], s[sK:sK+3], s[sKSO] offen lds
      .elseif \k == 2
        buffer_load_dwordx4 v[vOV], s[sV2:sV2+3], s[sKSO] offen lds
      .else
        buffer_load_dwordx4 v[vOV2], s[sV2:sV2+3], s[sKSO] offen lds
        s_add_u32 s[sKSO], s[sKSO], TILEB
      .endif
    .endif
  .endm
  .macro FULLF cur, prev, gen, r, m
    .set KO, (1-\gen)*16384*(\r)
    .set VO, (1-\gen)*16384*(\r)
    BARX 4, \m
    ; ---------------- phase A
    QKM \cur, 0
    VREADO 0, VO
    EXPE \prev, 11
    QKM \cur, 1
    VREADO 1, VO
    EXPE \prev, 12
    CVTE \prev, 0
    QKM \cur, 2
    VREADO 2, VO
    FDMA 0, \gen, \r, \m, 1
    FMAE \prev, 13
    FDMB 0, \gen, 1
    XPE \prev, 13
    CVTE \prev, 1
    QKM \cur, 3
    VREADO 3, VO
    EXPE \prev, 14
    CVTE \prev, 2
    QKM \cur, 4
    VREADO 4, VO
    EXPE \prev, 15
    CVTE \prev, 3
    QKM \cur, 5
    VREADO 5, VO
    CVTE \prev, 4
    CVTE \prev, 5
    QKM \cur, 6
    VREADO 6, VO
    FDMA 1, \gen, \r, \m, 1
    CVTE \prev, 6
    FDMB 1, \gen, 1
    QKM \cur, 7
    VREADO 7, VO
    CVTE \prev, 7
    .if (\gen == 0) && (\r == 1)
      v_xor_b32 v[vVA], 0x8000, v[vVA]
      v_xor_b32 v[vVH], 0x8000, v[vVH]
    .endif
    ; ---------------- phase B
    s_waitcnt lgkmcnt(4)
    PVM 0
    KREADO 0, KO
    KREADO 1, KO
    FDMA 2, \gen, \r, \m, 1
    KREADO 2, KO
    FDMB 2, \gen, 1
    KREADO 3, KO
    s_nop 2
    PVM 1
    EXPE \cur, 0
    EXPE \cur, 1
    LSUM 0
    EXPE \cur, 2
    PVM 2
    KREADO 4, KO
    EXPE \cur, 3
    PVM 3
    KREADO 5, KO
    EXPE \cur, 4
    FMAE \cur, 5
    PVM 4
    KREADO 6, KO
    XPE \cur, 5
    EXPE \cur, 6
    PVM 5
    KREADO 7, KO
    EXPE \cur, 7
    LSUM 1
    EXPE \cur, 8
    s_waitcnt lgkmcnt(8)
    PVM 6
    FDMA 3, \gen, \r, \m, 1
    FMAE \cur, 9
    FDMB 3, \gen, 1
    XPE \cur, 9
    .if \gen
      KADV
      VADDR
    .elseif \r == 1
      v_xor_b32 v[vKA0], 0x8000, v[vKA0]
      v_xor_b32 v[vKA1], 0x8000, v[vKA1]
      s_xor_b32 s[sMK], s[sMK], 0x8000
      s_xor_b32 s[sMV], s[sMV], 0x8000
    .endif
    s_add_u32 s[sCNT], s[sCNT], 1
    PVM 7
    EXPE \cur, 10
  .endm
  .macro LASTF prev, gen, r, m
    .set VO, (1-\gen)*16384*(\r)
    .if \m == 2
    .else
      BARX 0, \m
    .endif
    .set g, 0
    .rept 8
      VREADO g, VO
      .if g < 5
        EXPE \prev, 11+g
      .endif
      .set g, g+1
    .endr
    s_mov_b32 s[sU], s[sNS]
    s_cmp_eq_i32 s[sMSTEP], -1
    s_cbranch_scc1 .Lnm\@
    ; partial sub-tile: keys >= lim get weight 0
    v_sub_u32 v[vT2], s[sLIM], v[vHH4]
    .set e, 0
    .rept 16
      v_cmp_lt_i32 vcc, (e&3)+8*(e>>2), v[vT2]
      s_nop 1
      v_cndmask_b32 v[\prev+e], 0, v[\prev+e], vcc
      .set e, e+1
    .endr
  .Lnm\@:
    .set g, 0
    .rept 8
      CVTE \prev, g
      .set g, g+1
    .endr
    s_cmp_eq_u32 s[sU], s[sNU]
    s_waitcnt lgkmcnt(0)
    LSUM 0
    LSUM 1
    .if \gen == 0                                    ; mode A, last slot: send the partner's half early
      s_cbranch_scc0 .Lplain\@
      LASTV 2, 3, 0, 1
  .Lplain\@:
    .endif
    .set g, 0
    .rept 8
      PVM g
      .set g, g+1
    .endr
    s_branch .Lnone_tail
  .endm

  ; ---- SAFE step (online max every step), generic addressing, run-time mode
  .macro FULLS cur, prev, m
    s_cmp_lt_u32 s[sU], s[sND]
    s_cselect_b32 s[sF], 1, 0
    s_cbranch_scc0 .Lbn\@
    BARX 4, \m
    s_branch .Lbq\@
  .Lbn\@:
    BARX 0, \m
  .Lbq\@:
    .set g, 0
    .rept 8
      QKM \cur, g
      VREADO g, 0
      SUMPK \prev, g
      .if g == 1
        DMAP 0, \m, 2
      .elseif g == 5
        DMAP 1, \m, 2
      .endif
      .set g, g+1
    .endr
    s_waitcnt lgkmcnt(4)
    PVM 0
    KREADO 0, 0
    KREADO 1, 0
    s_add_u32 s[sU], s[sU], 1
    s_nop 1
    PVM 1
    s_cmp_lg_u32 s[sMSTEP], s[sU]
    s_cbranch_scc1 .Lnm\@
    MASK \cur
  .Lnm\@:
    ROWMAX1 \cur
    PVM 2
    KREADO 2, 0
    ROWMAX2
    v_sub_f32 v[vT3], v[vT2], v[vM]
    v_cmp_lt_f32 vcc, s[sTHR], v[vT3]
    s_cbranch_vccz .Lkeep\@
    v_max_f32 v[vT3], v[vM], v[vT2]
    v_sub_f32 v[vAL], v[vM], v[vT3]
    v_mul_f32 v[vAL], s[sCE], v[vAL]
    v_exp_f32 v[vAL], v[vAL]
    v_mov_b32 v[vM], v[vT3]
    v_mul_f32 v[vNMC], 0xbe0293ee, v[vM]
    s_mov_b32 s[sPEND], 1
  .Lkeep\@:
    PVM 3
    KREADO 3, 0
    DMAP 2, \m, 2
    EXPE \cur, 0
    EXPE \cur, 1
    EXPE \cur, 2
    PVM 4
    KREADO 4, 0
    EXPE \cur, 3
    EXPE \cur, 4
    EXPE \cur, 5
    PVM 5
    KREADO 5, 0
    EXPE \cur, 6
    EXPE \cur, 7
    EXPE \cur, 8
    s_waitcnt lgkmcnt(6)
    PVM 6
    KREADO 6, 0
    DMAP 3, \m, 2
    EXPE \cur, 9
    EXPE \cur, 10
    EXPE \cur, 11
    PVM 7
    KREADO 7, 0
    EXPE \cur, 12
    EXPE \cur, 13
    EXPE \cur, 14
    EXPE \cur, 15
    KADV
    VADDR
    s_cmp_eq_u32 s[sPEND], 0
    s_cbranch_scc1 .Lsend\@
    RESCALE
  .Lsend\@:
    s_cmp_eq_u32 s[sU], s[sNF]
  .endm
  .macro LASTS prev, m
    .if \m == 1
      s_cmp_eq_u32 s[sMB], 1
      s_cbranch_scc1 .Lnob\@
    .endif
    s_waitcnt vmcnt(0) lgkmcnt(0)
    s_barrier
  .Lnob\@:
    .set g, 0
    .rept 8
      VREADO g, 0
      SUMPK \prev, g
      .set g, g+1
    .endr
    s_add_u32 s[sU], s[sU], 1
    s_waitcnt lgkmcnt(0)
    .set g, 0
    .rept 8
      PVM g
      .set g, g+1
    .endr
    s_branch .Lnone_tail
  .endm

  ; ---- epilogue pieces
  ; 32 f32 at vO+b.. (one 64-wide half) -> bf16 -> LDS stage (row-major 128 B rows, 16-B chunks xor row) -> 4 full-line stores
  ; t: 16 dead registers (vO+t..) ; vSGW / vSGR: stage addresses ; sT3 = global offset of row 0 of this half
  .macro OUTHALF b, t
    .set q, 0
    .rept 4
      v_cvt_pk_bf16_f32 v[vO+\t+4*q+0], v[vO+\b+8*q+0], v[vO+\b+8*q+1]
      v_cvt_pk_bf16_f32 v[vO+\t+4*q+1], v[vO+\b+8*q+2], v[vO+\b+8*q+3]
      v_cvt_pk_bf16_f32 v[vO+\t+4*q+2], v[vO+\b+8*q+4], v[vO+\b+8*q+5]
      v_cvt_pk_bf16_f32 v[vO+\t+4*q+3], v[vO+\b+8*q+6], v[vO+\b+8*q+7]
      v_xor_b32 v[vT0+q], q*32, v[vSGW]
      .set q, q+1
    .endr
    .set q, 0
    .rept 4
      v_permlane32_swap_b32 v[vO+\t+4*q+0], v[vO+\t+4*q+2]
      v_permlane32_swap_b32 v[vO+\t+4*q+1], v[vO+\t+4*q+3]
      .set q, q+1
    .endr
    .set q, 0
    .rept 4
      ds_write_b128 v[vT0+q], v[vO+\t+4*q:vO+\t+4*q+3]
      .set q, q+1
    .endr
    s_add_u32 s[sT4], s[sT3], 8*4096
    s_add_u32 s[sT1], s[sT3], 16*4096
    s_add_u32 s[sT2], s[sT3], 24*4096
    s_waitcnt lgkmcnt(0)
    STAMPE 4
    ds_read_b128 v[vO+\t+0:vO+\t+3], v[vSGR]
    ds_read_b128 v[vO+\t+4:vO+\t+7], v[vSGR] offset:1024
    ds_read_b128 v[vO+\t+8:vO+\t+11], v[vSGR] offset:2048
    ds_read_b128 v[vO+\t+12:vO+\t+15], v[vSGR] offset:3072
    s_waitcnt lgkmcnt(3)
    buffer_store_dwordx4 v[vO+\t+0:vO+\t+3], v[vSO], s[sO:sO+3], s[sT3] offen sc1
    s_waitcnt lgkmcnt(2)
    buffer_store_dwordx4 v[vO+\t+4:vO+\t+7], v[vSO], s[sO:sO+3], s[sT4] offen sc1
    s_waitcnt lgkmcnt(1)
    buffer_store_dwordx4 v[vO+\t+8:vO+\t+11], v[vSO], s[sO:sO+3], s[sT1] offen sc1
    s_waitcnt lgkmcnt(0)
    buffer_store_dwordx4 v[vO+\t+12:vO+\t+15], v[vSO], s[sO:sO+3], s[sT2] offen sc1
  .endm
  .macro MERGE kb, rb                               ; kb: kept 32 regs, rb: 32 regs receiving the partner's half
    ds_read_b64 v[vT4:vT5], v[vSTP]
    .set i, 0
    .rept 8
      ds_read_b128 v[vO+\rb+4*i:vO+\rb+4*i+3], v[vXP] offset:1024*i
      .set i, i+1
    .endr
    s_waitcnt lgkmcnt(8)
    v_max_f32 v[vT6], v[vM], v[vT4]
    v_sub_f32 v[vFA], v[vM], v[vT6]
    v_sub_f32 v[vFB], v[vT4], v[vT6]
    v_mul_f32 v[vFA], s[sCE], v[vFA]
    v_mul_f32 v[vFB], s[sCE], v[vFB]
    v_exp_f32 v[vFA], v[vFA]
    v_exp_f32 v[vFB], v[vFB]
    s_nop 0
    v_mul_f32 v[vT6], v[vFA], v[vL0]
    v_fma_f32 v[vT6], v[vFB], v[vT5], v[vT6]
    v_rcp_f32 v[vT6], v[vT6]
    s_nop 0
    v_mul_f32 v[vFA], v[vFA], v[vT6]
    v_mul_f32 v[vFB], v[vFB], v[vT6]
    STAMPE 2
    .set i, 0
    .rept 16
      v_pk_mul_f32 v[vO+\kb+2*i:vO+\kb+2*i+1], v[vFA:vFA+1], v[vO+\kb+2*i:vO+\kb+2*i+1] op_sel_hi:[0,1]
      .set i, i+1
    .endr
    .set i, 0
    .rept 8
      .if i == 0
        s_waitcnt lgkmcnt(7)
      .elseif i == 1
        s_waitcnt lgkmcnt(6)
      .elseif i == 2
        s_waitcnt lgkmcnt(5)
      .elseif i == 3
        s_waitcnt lgkmcnt(4)
      .elseif i == 4
        s_waitcnt lgkmcnt(3)
      .elseif i == 5
        s_waitcnt lgkmcnt(2)
      .elseif i == 6
        s_waitcnt lgkmcnt(1)
      .else
        s_waitcnt lgkmcnt(0)
      .endif
      v_pk_fma_f32 v[vO+\kb+4*i:vO+\kb+4*i+1], v[vFB:vFB+1], v[vO+\rb+4*i:vO+\rb+4*i+1], v[vO+\kb+4*i:vO+\kb+4*i+1] op_sel_hi:[0,1,1]
      v_pk_fma_f32 v[vO+\kb+4*i+2:vO+\kb+4*i+3], v[vFB:vFB+1], v[vO+\rb+4*i+2:vO+\rb+4*i+3], v[vO+\kb+4*i+2:vO+\kb+4*i+3] op_sel_hi:[0,1,1]
      .set i, i+1
    .endr
    STAMPE 3
    OUTHALF \kb, \rb
  .endm
  .macro ROWSUM                                     ; complete row sum of this wave's keys -> vL0 (all lanes)
    s_cmp_eq_u32 s[sSAFE], 1
    s_cbranch_scc1 .Lss\@
    v_permlane16_swap_b32 v[vLS], v[vLS+1]
    s_nop 0
    v_mov_b32 v[vL0], v[vLS]
    s_nop 1
    v_permlane32_swap_b32 v[vL0], v[vLS]
    s_branch .Lsd\@
  .Lss\@:
    v_add_f32 v[vL0], v[vL0], v[vL1]
    v_mov_b32 v[vL1], v[vL0]
    s_nop 1
    v_permlane32_swap_b32 v[vL0], v[vL1]
    s_nop 0
    v_add_f32 v[vL0], v[vL0], v[vL1]
  .Lsd\@:
  .endm
  .macro PREFETCH lo, hi                            ; Q prefetch into L2, instructions lo..hi-1 (16 rows each)
    .set n, \lo
    .rept \hi-\lo
      s_mul_i32 s[sT3], s[sJ], s[sPFB]
      s_add_u32 s[sT3], s[sT3], n*16*4096
      s_min_u32 s[sT3], s[sT3], s[sTM16]
      buffer_load_dwordx4 v[vV+4*n:vV+4*n+3], v[vV+28], s[sQ:sQ+3], s[sT3] offen
      .set n, n+1
    .endr
  .endm

  ; ======================================================================== entry
  s_load_dwordx16 s[8:23], %0, 0
  s_load_dwordx8 s[24:31], %0, 64
  .if DIAG
    s_memrealtime s[sRT:sRT+1]
    v_mov_b32 v[vDG], 0
  .endif
  STAMP 0
  v_and_b32 v[vLANE], 63, %2
  v_readfirstlane_b32 s[sW], %2
  v_lshlrev_b32 v[vT0], 2, %2                     ; tid*4
  s_and_b32 s[sG], %1, 7
  s_lshr_b32 s[sJ], %1, 3
  s_lshr_b32 s[sW], s[sW], 6
  v_add_u32 v[vT7], 2048*(NIT-1), v[vT0]
  ; prefetch offset: (tid>>5)*4096 + ((tid>>4)&1)*2048 + ((tid>>3)&1)*128 + (tid&7)*16 + g*256
  v_lshrrev_b32 v[vT6], 5, %2
  v_lshlrev_b32 v[vT6], 12, v[vT6]
  v_bfe_u32 v[vT5], %2, 4, 1
  v_lshl_add_u32 v[vT6], v[vT5], 11, v[vT6]
  v_bfe_u32 v[vT5], %2, 3, 1
  v_lshl_add_u32 v[vT6], v[vT5], 7, v[vT6]
  v_and_b32 v[vT5], 7, %2
  v_lshl_add_u32 v[vT6], v[vT5], 4, v[vT6]
  s_lshl_b32 s[sT3], s[sG], 8
  v_add_u32 v[vV+28], s[sT3], v[vT6]
  ; DMA lane offsets
  v_bfe_u32 v[vT1], v[vLANE], 2, 3                ; prow
  v_lshrrev_b32 v[vT2], 5, v[vLANE]               ; hh / pcb
  v_and_b32 v[vT3], 3, v[vLANE]                   ; pc
  v_lshlrev_b32 v[vT4], 12, v[vT1]
  v_lshl_add_u32 v[vT4], v[vT2], 6, v[vT4]        ; prow*4096 + pcb*64
  v_lshl_add_u32 v[vOV], v[vT3], 4, v[vT4]
  v_bfe_u32 v[vT5], v[vLANE], 4, 1
  s_and_b32 s[sT4], s[sW], 1
  s_lshl_b32 s[sT4], s[sT4], 1
  v_or_b32 v[vT5], s[sT4], v[vT5]                 ; x
  v_xor_b32 v[vT5], v[vT5], v[vT3]
  v_lshl_add_u32 v[vOK], v[vT5], 4, v[vT4]
  s_waitcnt lgkmcnt(0)
  STAMP 1
  ; ======================================================================== S1: segment ends per 64-token chunk
  s_lshl_b32 s[sT2], s[sT], 2
  s_sub_u32 s[sT2], s[sT2], 8                     ; (T-2)*4
  v_min_u32 v[vT7], s[sT2], v[vT7]                ; last chunk: clamped
  .set i, 0
  .rept NIT
    .if i == NIT-1
      buffer_load_dwordx2 v[vX+2*i:vX+2*i+1], v[vT7], s[sS:sS+3], 0 offen
    .elseif i < 2
      buffer_load_dwordx2 v[vX+2*i:vX+2*i+1], v[vT0], s[sS:sS+3], 0 offen offset:2048*i
    .else
      s_movk_i32 s[sT1], 2048*i
      buffer_load_dwordx2 v[vX+2*i:vX+2*i+1], v[vT0], s[sS:sS+3], s[sT1] offen
    .endif
    .set i, i+1
  .endr
  s_sub_u32 s[sTM16], s[sT], 16
  s_lshl_b32 s[sTM16], s[sTM16], 12               ; (T-16)*4096
  ; V descriptor shifted by two tiles: V(u+2) is fetched with the source offset of K(u+4)
  s_sub_u32 s[sV2], s[sV], 2*TILEB
  s_subb_u32 s[sV2+1], s[sV+1], 0
  s_mov_b32 s[sV2+3], s[sV+3]
  ; remaining lane constants (in the shadow of the loads)
  v_add_u32 v[vOK2], 128, v[vOK]
  v_add_u32 v[vOV2], 128, v[vOV]
  v_and_b32 v[vT1], 31, v[vLANE]                  ; r32
  v_lshrrev_b32 v[vT3], 3, v[vT1]
  v_lshlrev_b32 v[vT3], 11, v[vT3]
  v_and_b32 v[vT4], 7, v[vT1]
  v_lshl_add_u32 v[vT3], v[vT4], 6, v[vT3]
  v_bfe_u32 v[vT4], v[vT1], 2, 2
  v_xor_b32 v[vT4], v[vT4], v[vT2]
  v_lshl_add_u32 v[vKB0], v[vT4], 4, v[vT3]
  v_xor_b32 v[vKB1], 32, v[vKB0]
  v_bfe_u32 v[vT3], v[vLANE], 2, 2
  v_lshl_add_u32 v[vT3], v[vT2], 2, v[vT3]
  v_lshlrev_b32 v[vT3], 6, v[vT3]
  v_bfe_u32 v[vT4], v[vLANE], 4, 1
  v_lshl_add_u32 v[vT3], v[vT4], 5, v[vT3]
  v_and_b32 v[vT4], 3, v[vLANE]
  v_lshl_add_u32 v[vVB], v[vT4], 3, v[vT3]
  v_add_u32 v[vVB], LV, v[vVB]
  v_lshlrev_b32 v[vHH4], 2, v[vT2]
  ; output staging: write address r32*128 + ((r32&7)^hh)*16 (chunk pair p: xor p*32)
  v_and_b32 v[vT3], 7, v[vT1]
  v_xor_b32 v[vT3], v[vT3], v[vT2]
  v_lshlrev_b32 v[vT3], 4, v[vT3]
  v_lshl_add_u32 v[vSW], v[vT1], 7, v[vT3]
  ; read address: row = lane>>3, chunk = (lane&7) ^ row ; rows + 8i -> + 1024 i
  v_lshrrev_b32 v[vT3], 3, v[vLANE]
  v_and_b32 v[vT4], 7, v[vLANE]
  v_xor_b32 v[vT5], v[vT4], v[vT3]
  v_lshlrev_b32 v[vT5], 4, v[vT5]
  v_lshl_add_u32 v[vSR], v[vT3], 7, v[vT5]
  ; global store lane offset: (lane>>3)*4096 + (lane&7)*16
  v_lshlrev_b32 v[vT4], 4, v[vT4]
  v_lshl_add_u32 v[vSO], v[vT3], 12, v[vT4]
  ; row-sum MFMA A operand: row (lane&15) 0 takes k-groups 0,2 ; row 1 takes k-groups 1,3
  v_and_b32 v[vT3], 15, v[vLANE]
  v_bfe_u32 v[vT4], v[vLANE], 4, 1
  v_cmp_eq_u32 vcc, v[vT3], v[vT4]
  v_mov_b32 v[vT5], 0x3f803f80
  s_mov_b32 s[sCE], 0x3e0293ee
  v_cndmask_b32 v[vONE], 0, v[vT5], vcc
  v_cndmask_b32 v[vONE+1], 0, v[vT5], vcc
  v_cndmask_b32 v[vONE+2], 0, v[vT5], vcc
  v_cndmask_b32 v[vONE+3], 0, v[vT5], vcc
  s_mov_b32 s[sTHR], 0x427af232
  s_lshl_b32 s[sWD], s[sW], 11                    ; w*2048
  s_mov_b64 s[sC:sC+1], 0
  v_mov_b32 v[vT4], 0
  v_mov_b32 v[vT5], 0
  v_lshlrev_b32 v[vT7], 7, v[vLANE]               ; LDS address of chunk (w + 8*lane): 16 B each
  s_lshl_b32 s[sT1], s[sW], 4
  s_add_u32 s[sT1], s[sT1], LSCR
  v_add_u32 v[vT7], s[sT1], v[vT7]
  PREFETCH 0, PF1
  .set i, 0
  .rept NIT
    s_waitcnt vmcnt(NIT-1-i+PF1)
    v_cmp_ne_u32 s[sA:sA+1], v[vX+2*i], v[vX+2*i+1]
    v_cmp_gt_i32 vcc, v[vX+2*i], v[vX+2*i+1]
    .if i == NIT-1
      v_cmp_gt_i32 s[sB:sB+1], s[sLASTC], %2       ; element < T-1
      s_and_b64 s[sA:sA+1], s[sA:sA+1], s[sB:sB+1]
      s_and_b64 vcc, vcc, s[sB:sB+1]
      v_cmp_eq_u32 s[sB:sB+1], s[sLASTC], %2       ; element == T-1
      s_or_b64 s[sA:sA+1], s[sA:sA+1], s[sB:sB+1]
    .endif
    s_or_b64 s[sC:sC+1], s[sC:sC+1], vcc
    s_nop 0
    v_writelane_b32 v[vT4], s[sA], i
    v_writelane_b32 v[vT5], s[sA+1], i
    .set i, i+1
  .endr
  s_or_b32 s[sT1], s[sC], s[sC+1]
  v_mov_b32 v[vT6], s[sT1]
  s_mov_b64 exec, (1<<NIT)-1
  ds_write_b96 v[vT7], v[vT4:vT6]
  s_mov_b64 exec, -1
  v_mov_b32 v[vT0], 0
  v_mov_b32 v[vT1], 0
  v_mov_b32 v[vT2], 0
  v_lshlrev_b32 v[vT3], 4, v[vLANE]
  v_add_u32 v[vT3], LSCR, v[vT3]
  v_lshlrev_b32 v[vT7], 6, v[vLANE]
  v_add_u32 v[vT7], 1, v[vT7]                     ; lane*64 + 1
  PREFETCH PF1, PF2
  s_waitcnt lgkmcnt(0)
  s_barrier
  STAMP 2
  ; ======================================================================== S2
  s_bfm_b64 exec, 8*NIT, 0
  ds_read_b96 v[vT0:vT2], v[vT3]
  s_mov_b64 exec, -1
  s_movk_i32 s[sT1], 128
  s_movk_i32 s[sT2], 256
  s_mov_b32 s[sR], 0
  s_waitcnt lgkmcnt(0)
  v_cmp_ne_u32 vcc, 0, v[vT2]
  v_bcnt_u32_b32 v[vT3], v[vT0], 0
  v_bcnt_u32_b32 v[vT3], v[vT1], v[vT3]
  s_cbranch_vccnz .Lfallback
  v_cmp_lt_u32 vcc, 1, v[vT3]
  v_ffbl_b32 v[vT4], v[vT0]
  v_ffbl_b32 v[vT5], v[vT1]
  s_cbranch_vccnz .Lfallback
  v_cmp_ne_u32 s[sA:sA+1], 0, v[vT3]
  v_add_u32 v[vT5], 32, v[vT5]
  v_min_u32 v[vT4], v[vT4], v[vT5]
  v_add_u32 v[vT4], v[vT4], v[vT7]
  v_cndmask_b32 v[vT4], 0, v[vT4], s[sA:sA+1]     ; end (0 if none)
  s_nop 1
  v_mov_b32_dpp v[vSTART], v[vT4] wave_shr:1 row_mask:0xf bank_mask:0xf bound_ctrl:0
  s_nop 1
  v_max_u32_dpp v[vSTART], v[vSTART], v[vSTART] row_shr:1 row_mask:0xf bank_mask:0xf bound_ctrl:0
  s_nop 1
  v_max_u32_dpp v[vSTART], v[vSTART], v[vSTART] row_shr:2 row_mask:0xf bank_mask:0xf bound_ctrl:0
  s_nop 1
  v_max_u32_dpp v[vSTART], v[vSTART], v[vSTART] row_shr:4 row_mask:0xf bank_mask:0xf bound_ctrl:0
  s_nop 1
  v_max_u32_dpp v[vSTART], v[vSTART], v[vSTART] row_shr:8 row_mask:0xf bank_mask:0xf bound_ctrl:0
  s_nop 1
  v_max_u32_dpp v[vSTART], v[vSTART], v[vSTART] row_bcast:15 row_mask:0xa bank_mask:0xf
  s_nop 1
  v_max_u32_dpp v[vSTART], v[vSTART], v[vSTART] row_bcast:31 row_mask:0xc bank_mask:0xf
  s_nop 0
  v_sub_u32 v[vLEN], v[vT4], v[vSTART]
  v_cndmask_b32 v[vLEN], 0, v[vLEN], s[sA:sA+1]   ; len (0 if none)
  v_add_u32 v[vT5], 127, v[vLEN]
  v_lshrrev_b32 v[vT5], 7, v[vT5]                 ; nq (mode A)
  v_cmp_lt_u32 vcc, s[sT1], v[vLEN]               ; len > 128
  v_cmp_ge_u32 s[sB:sB+1], s[sT2], v[vLEN]        ; len <= 256
  s_and_b64 s[sB:sB+1], s[sB:sB+1], vcc           ; mode B
  v_lshlrev_b32 v[vT5], 1, v[vT5]
  s_nop 0
  v_cndmask_b32 v[vT5], v[vT5], 2, s[sB:sB+1]     ; items (2 heads)
  v_lshlrev_b32 v[vT6], 16, v[vT5]
  v_cndmask_b32 v[vINCL], v[vT6], v[vT5], vcc     ; small -> high half
  v_mov_b32 v[vEXCL], v[vINCL]
  s_nop 0
  v_add_u32_dpp v[vINCL], v[vINCL], v[vINCL] row_shr:1 row_mask:0xf bank_mask:0xf bound_ctrl:0
  s_nop 1
  v_add_u32_dpp v[vINCL], v[vINCL], v[vINCL] row_shr:2 row_mask:0xf bank_mask:0xf bound_ctrl:0
  s_nop 1
  v_add_u32_dpp v[vINCL], v[vINCL], v[vINCL] row_shr:4 row_mask:0xf bank_mask:0xf bound_ctrl:0
  s_nop 1
  v_add_u32_dpp v[vINCL], v[vINCL], v[vINCL] row_shr:8 row_mask:0xf bank_mask:0xf bound_ctrl:0
  s_nop 1
  v_add_u32_dpp v[vINCL], v[vINCL], v[vINCL] row_bcast:15 row_mask:0xa bank_mask:0xf
  s_nop 1
  v_add_u32_dpp v[vINCL], v[vINCL], v[vINCL] row_bcast:31 row_mask:0xc bank_mask:0xf
  s_nop 0
  v_sub_u32 v[vEXCL], v[vINCL], v[vEXCL]
  v_readlane_b32 s[sT1], v[vINCL], 63
  s_and_b32 s[sNBIG], s[sT1], 0xffff
  s_lshr_b32 s[sTOT], s[sT1], 16
  s_add_u32 s[sTOT], s[sTOT], s[sNBIG]
  s_mov_b32 s[sSAFE], 0
  STAMP 3

.Litem:
  ; idx = r*32 + (r odd ? 31-j : j)
  s_lshl_b32 s[sT1], s[sR], 5
  s_sub_u32 s[sT2], 31, s[sJ]
  s_bitcmp1_b32 s[sR], 0
  s_cselect_b32 s[sT2], s[sT2], s[sJ]
  s_add_u32 s[sT1], s[sT1], s[sT2]                ; idx
  s_cmp_ge_u32 s[sT1], s[sTOT]
  s_cbranch_scc1 .Ldone
  s_sub_u32 s[sT2], s[sT1], s[sNBIG]
  s_cmp_lt_u32 s[sT1], s[sNBIG]
  s_cselect_b32 s[sT1], s[sT1], s[sT2]            ; index inside its class
  s_cselect_b32 s[sT2], 0, 16                     ; field shift
  v_bfe_u32 v[vT0], v[vINCL], s[sT2], 16
  v_bfe_u32 v[vT1], v[vEXCL], s[sT2], 16
  v_cmp_lt_u32 s[sA:sA+1], s[sT1], v[vT0]
  v_cmp_ge_u32 vcc, s[sT1], v[vT1]
  s_and_b64 s[sA:sA+1], s[sA:sA+1], vcc
  s_ff1_i32_b64 s[sT2], s[sA:sA+1]
  v_readlane_b32 s[sK0], v[vSTART], s[sT2]
  v_readlane_b32 s[sLEN], v[vLEN], s[sT2]
  v_readlane_b32 s[sT3], v[vT1], s[sT2]
  s_sub_u32 s[sT1], s[sT1], s[sT3]                ; local item
  s_and_b32 s[sT2], s[sT1], 1
  s_lshl_b32 s[sT2], s[sT2], 3
  s_add_u32 s[sT2], s[sT2], s[sG]
  s_lshl_b32 s[sHOFF], s[sT2], 8                  ; head*256 bytes
  s_lshr_b32 s[sT1], s[sT1], 1
  s_lshl_b32 s[sT1], s[sT1], 7
  s_add_u32 s[sQ0], s[sK0], s[sT1]
  s_lshl_b32 s[sT1], s[sW], 3
  s_add_u32 s[sT2], s[sQ0], s[sT1]
  s_lshl_b32 s[sT2], s[sT2], 12
  s_add_u32 s[sT2], s[sT2], s[sHOFF]              ; (q0 + 8w)*4096 + hoff
  ; ---- Q DMA
  s_add_u32 m0, s[sWD], LX
  s_add_u32 s[sT3], s[sT2], 64*4096
  buffer_load_dwordx4 v[vOK], s[sQ:sQ+3], s[sT2] offen lds
  s_add_u32 m0, s[sWD], LX+1024
  s_add_u32 s[sT4], s[sT2], 128*4096
  buffer_load_dwordx4 v[vOK2], s[sQ:sQ+3], s[sT2] offen lds
  s_add_u32 m0, s[sWD], LX+16384
  s_add_u32 s[sT1], s[sK0], s[sT1]
  buffer_load_dwordx4 v[vOK], s[sQ:sQ+3], s[sT3] offen lds
  s_add_u32 m0, s[sWD], LX+16384+1024
  s_lshl_b32 s[sT1], s[sT1], 12
  buffer_load_dwordx4 v[vOK2], s[sQ:sQ+3], s[sT3] offen lds
  s_add_u32 s[sKSO], s[sT1], s[sHOFF]             ; (k0 + 8w)*4096 + hoff
  s_mov_b32 s[sVSO], s[sKSO]
  s_add_u32 s[sQEND], s[sK0], s[sLEN]
  s_add_u32 s[sNK], s[sLEN], 63
  s_lshr_b32 s[sNK], s[sNK], 6
  s_lshl_b32 s[sO+2], s[sQEND], 12                ; the output, K and V descriptors end at the segment end
  s_mov_b32 s[sK+2], s[sO+2]
  s_mov_b32 s[sV+2], s[sO+2]
  s_add_u32 s[sV2+2], s[sO+2], 2*TILEB
  s_mov_b32 s[sKDST], s[sWD]
  s_mov_b32 s[sVDST], s[sWD]
  s_sub_u32 s[sT1], s[sLEN], 129
  s_cmp_lt_u32 s[sT1], 128
  s_cselect_b32 s[sMB], 1, 0                      ; mode B: 128 < len <= 256
  s_cbranch_scc0 .Lq_done
  s_add_u32 m0, s[sWD], LQ2
  s_add_u32 s[sT3], s[sT4], 64*4096
  buffer_load_dwordx4 v[vOK], s[sQ:sQ+3], s[sT4] offen lds
  s_add_u32 m0, s[sWD], LQ2+1024
  s_nop 0
  buffer_load_dwordx4 v[vOK2], s[sQ:sQ+3], s[sT4] offen lds
  s_add_u32 m0, s[sWD], LQ3
  s_nop 0
  buffer_load_dwordx4 v[vOK], s[sQ:sQ+3], s[sT3] offen lds
  s_add_u32 m0, s[sWD], LQ3+1024
  s_nop 0
  buffer_load_dwordx4 v[vOK2], s[sQ:sQ+3], s[sT3] offen lds
.Lq_done:
  DK1
  DK2
  STAMP 4
  ; ---- per-wave geometry
  s_and_b32 s[sT1], s[sW], 3
  s_lshr_b32 s[sT2], s[sW], 2
  s_cmp_eq_u32 s[sMB], 1
  s_cselect_b32 s[sQB], s[sW], s[sT1]             ; q block
  s_cselect_b32 s[sKH], 0, s[sT2]                 ; key half (mode A)
  s_movk_i32 s[sKSTEP], 0x4000
  s_cselect_b32 s[sKSTEP], 8192, s[sKSTEP]
  s_add_u32 s[sT1], s[sLEN], 31
  s_lshr_b32 s[sT1], s[sT1], 5                    ; nst = ceil(len/32)
  s_add_u32 s[sT2], s[sT1], 1
  s_sub_u32 s[sT2], s[sT2], s[sKH]
  s_lshr_b32 s[sT2], s[sT2], 1
  s_cmp_eq_u32 s[sMB], 1
  s_cselect_b32 s[sNS], s[sT1], s[sT2]            ; steps of this wave
  s_cselect_b32 s[sNU], s[sT1], s[sNK]            ; step slots
  s_and_b32 s[sLIM], s[sLEN], 31
  s_sub_u32 s[sT2], s[sT1], 1
  s_and_b32 s[sT2], s[sT2], 1
  s_cmp_eq_u32 s[sT2], s[sKH]
  s_cselect_b32 s[sT3], 1, s[sMB]                 ; owner of the last sub-tile
  s_cmp_lg_u32 s[sLIM], 0
  s_cselect_b32 s[sT3], s[sT3], 0
  s_sub_u32 s[sMSTEP], s[sNS], 1
  s_cmp_eq_u32 s[sT3], 1
  s_cselect_b32 s[sMSTEP], s[sMSTEP], -1
  s_lshl_b32 s[sT1], s[sQB], 5
  s_add_u32 s[sQR], s[sT1], s[sQ0]                ; first q row of this wave
  s_cmp_lt_u32 s[sQR], s[sQEND]
  s_cselect_b32 s[sNS], s[sNS], 0
  ; slots with DMA: tile iteration t <= nk-3
  s_sub_i32 s[sND], s[sNK], 2
  s_max_i32 s[sND], s[sND], 0
  s_lshl_b32 s[sND], s[sND], s[sMB]
  s_sub_u32 s[sNF], s[sNS], 1                     ; FULL slots of this wave
  s_sub_u32 s[sCNT], 0, s[sNF]
  s_mov_b32 s[sMK], s[sWD]
  s_add_u32 s[sMV], s[sWD], LV+0x8000
  ; Q read base
  s_lshl_b32 s[sT1], s[sQB], 13
  s_add_u32 s[sT2], s[sT1], LX                    ; qb < 4
  s_add_u32 s[sT3], s[sT1], LQ2-4*8192            ; qb 4,5
  s_add_u32 s[sT4], s[sT1], LQ3-6*8192            ; qb 6,7
  s_cmp_lt_u32 s[sQB], 6
  s_cselect_b32 s[sT3], s[sT3], s[sT4]
  s_cmp_lt_u32 s[sQB], 4
  s_cselect_b32 s[sT2], s[sT2], s[sT3]
  v_add_u32 v[vT0], s[sT2], v[vKB0]
  v_add_u32 v[vT1], s[sT2], v[vKB1]
  s_lshl_b32 s[sKOFF], s[sKH], 13
  s_mov_b32 s[sVOFF], s[sKOFF]
  v_add_u32 v[vKA0], s[sKOFF], v[vKB0]
  v_add_u32 v[vKA1], s[sKOFF], v[vKB1]
  s_lshl_b32 s[sT1], s[sKH], 10
  v_add_u32 v[vVBL], s[sT1], v[vVB]
  v_subrev_u32 v[vVBH], s[sT1], v[vVB]
  VADDR
  ; epilogue addresses
  v_lshlrev_b32 v[vT2], 4, v[vLANE]
  s_lshl_b32 s[sT1], s[sW], 13
  s_xor_b32 s[sT2], s[sT1], 4*8192
  v_add_u32 v[vXP], s[sT2], v[vT2]                ; partner's exchange area
  v_add_u32 v[vXO], s[sT1], v[vT2]                ; own exchange area
  v_and_b32 v[vT2], 31, v[vLANE]
  v_lshlrev_b32 v[vT2], 3, v[vT2]
  v_add_u32 v[vT2], LX, v[vT2]
  s_lshl_b32 s[sT1], s[sW], 8
  s_xor_b32 s[sT2], s[sT1], 4*256
  v_add_u32 v[vSTP], s[sT2], v[vT2]
  v_add_u32 v[vSTA], s[sT1], v[vT2]
  s_lshl_b32 s[sT1], s[sW], 12                    ; output stage: mode A in the V ring (4 KiB per wave), mode B in the K ring (8 KiB)
  s_add_u32 s[sT2], s[sT1], LV
  s_lshl_b32 s[sT3], s[sW], 13
  s_cmp_eq_u32 s[sMB], 1
  s_cselect_b32 s[sT1], s[sT3], s[sT2]
  v_add_u32 v[vSGW], s[sT1], v[vSW]
  v_add_u32 v[vSGR], s[sT1], v[vSR]
  v_mov_b32 v[vL0], 0
  v_mov_b32 v[vL1], 0
  v_mov_b32 v[vLS], 0
  v_mov_b32 v[vLS+1], 0
  v_mov_b32 v[vLS+2], 0
  v_mov_b32 v[vLS+3], 0
  s_nop 1
  .set i, 0
  .rept 4
    v_mfma_f32_32x32x16_bf16 v[vO+16*i:vO+16*i+15], v[vLS:vLS+3], v[vLS:vLS+3], 0     ; O = 0
    .set i, i+1
  .endr
  v_mov_b32 v[vM], 0xf149f2ca
  s_mov_b32 s[sPEND], 0
  s_mov_b32 s[sU], 0
  ; mid group: mode A: K1, K2, V0 ; mode B: K1, V0
  s_waitcnt vmcnt(2)                                ; own Q pieces landed
  DK1
  DK2
  s_cmp_eq_u32 s[sMB], 1
  s_cbranch_scc1 .Lmid_b
  DK1
  DK2
  DV1
  DV2
  s_waitcnt vmcnt(6)
  s_branch .Lmid_d
.Lmid_b:
  DV1
  DV2
  s_waitcnt vmcnt(4)
.Lmid_d:
  s_barrier                                         ; barrier P: Q, K0 landed
  STAMP 5
  ; ---- prologue compute: step 0
  s_cmp_eq_u32 s[sNS], 0
  s_cbranch_scc1 .Lpro_none
  .set i, 0
  .rept 8
    KREADO i, 0
    .if (i & 1)
      ds_read_b128 v[vQ+4*i:vQ+4*i+3], v[vT1] offset:512*(i>>1)
    .else
      ds_read_b128 v[vQ+4*i:vQ+4*i+3], v[vT0] offset:512*(i>>1)
    .endif
    .set i, i+1
  .endr
  .macro QK0 rd                                     ; rd: read the next step's K fragments behind each MFMA (mode B: same tile)
    .set i, 0
    .rept 8
      .if \rd
        .if i == 0
          s_waitcnt lgkmcnt(14)
        .elseif i == 1
          s_waitcnt lgkmcnt(13)
        .elseif i == 2
          s_waitcnt lgkmcnt(12)
        .elseif i == 3
          s_waitcnt lgkmcnt(11)
        .elseif i == 4
          s_waitcnt lgkmcnt(10)
        .elseif i == 5
          s_waitcnt lgkmcnt(9)
        .elseif i == 6
          s_waitcnt lgkmcnt(8)
        .else
          s_waitcnt lgkmcnt(7)
        .endif
      .else
        .if i == 0
          s_waitcnt lgkmcnt(14)
        .elseif i == 1
          s_waitcnt lgkmcnt(12)
        .elseif i == 2
          s_waitcnt lgkmcnt(10)
        .elseif i == 3
          s_waitcnt lgkmcnt(8)
        .elseif i == 4
          s_waitcnt lgkmcnt(6)
        .elseif i == 5
          s_waitcnt lgkmcnt(4)
        .elseif i == 6
          s_waitcnt lgkmcnt(2)
        .else
          s_waitcnt lgkmcnt(0)
        .endif
      .endif
      QKM vSA, i
      .if \rd
        KREADO i, 0
      .endif
      .if i == 1
        DK1
      .elseif i == 3
        DK2
      .elseif i == 5
        DV1
      .elseif i == 7
        DV2
      .endif
      .set i, i+1
    .endr
  .endm
  s_cmp_eq_u32 s[sMB], 1
  s_cbranch_scc1 .Lqk0_b
  QK0 0
  s_branch .Lqk0_d
.Lqk0_b:
  KADV
  QK0 1
  KADV
.Lqk0_d:
  s_nop 7
  s_cmp_lg_u32 s[sMSTEP], 0
  s_cbranch_scc1 .Lnomask0
  s_nop 3
  MASK vSA
.Lnomask0:
  s_nop 3
  ROWMAX1 vSA
  s_nop 1
  ROWMAX2
  v_max_f32 v[vM], v[vM], v[vT2]
  v_mul_f32 v[vNMC], 0xbe0293ee, v[vM]
  s_cmp_eq_u32 s[sSAFE], 1
  s_cbranch_scc1 .Lpro_safe
  .set r, 0
  .rept 6
    EXPE vSA, r
    .set r, r+1
  .endr
  s_cmp_eq_u32 s[sMB], 1
  s_cbranch_scc1 .Lpro_b
  ; ---- mode A: K1 landed -> its fragments
  s_waitcnt vmcnt(8)
  s_barrier
  .set i, 0
  .rept 8
    KREADO i, 16384
    .set i, i+1
  .endr
  .set r, 6
  .rept 5
    EXPE vSA, r
    .set r, r+1
  .endr
  v_xor_b32 v[vKA0], 0x8000, v[vKA0]                ; K(2) sits in ring slot 2
  v_xor_b32 v[vKA1], 0x8000, v[vKA1]
  STAMP 6
  s_cmp_eq_u32 s[sNF], 0
  s_cbranch_scc1 .LlA0
  ; ======================================================================== mode A loop
.LA0:
  FULLF vSB, vSA, 0, 0, 0
  s_cbranch_scc1 .LlA1
  FULLF vSA, vSB, 0, 1, 0
  s_cbranch_scc0 .LA0
.LlA0:
  LASTF vSA, 0, 0, 0
.LlA1:
  LASTF vSB, 0, 1, 0
  ; ======================================================================== epilogue (mode A)
.Lepi_sent:
  STAMP 7
  v_mov_b32 v[vT7], 0x71800000
  s_lshl_b32 s[sT3], s[sQR], 12
  s_add_u32 s[sT3], s[sT3], s[sHOFF]
  ROWSUM
  v_mov_b32 v[vT4], v[vM]
  v_mov_b32 v[vT5], v[vL0]
  s_lshl_b32 s[sT1], s[sKH], 7
  s_add_u32 s[sT3], s[sT3], s[sT1]
  ds_write_b64 v[vSTA], v[vT4:vT5]
  s_waitcnt lgkmcnt(0)
  s_barrier
  STAMPE 1
  MERGE 0, 32
.Lnext:
  STAMP 8
  ; overflow check of the FAST path: a row sum that is not a moderately sized number -> redo this item with the online max
  v_cmp_nlt_f32 vcc, v[vL0], v[vT7]
  s_lshl_b32 s[sT1], s[sW], 2
  s_add_u32 s[sT1], s[sT1], LX+8192
  s_cmp_lg_u64 vcc, 0
  s_cselect_b32 s[sT2], 1, 0
  v_mov_b32 v[vT0], s[sT1]
  v_mov_b32 v[vT1], s[sT2]
  v_and_b32 v[vT2], 7, v[vLANE]
  v_lshlrev_b32 v[vT2], 2, v[vT2]
  v_add_u32 v[vT2], LX+8192, v[vT2]
  ds_write_b32 v[vT0], v[vT1]
  s_waitcnt lgkmcnt(0)
  s_barrier
  ds_read_b32 v[vT1], v[vT2]
  s_waitcnt lgkmcnt(0)
  v_cmp_ne_u32 vcc, 0, v[vT1]
  s_cmp_eq_u32 s[sSAFE], 1
  s_cbranch_scc1 .Lsafe_done
  s_cbranch_vccz .Lsafe_done
  s_mov_b32 s[sSAFE], 1
  s_barrier
  s_branch .Litem
.Lsafe_done:
  s_mov_b32 s[sSAFE], 0
  s_add_u32 s[sR], s[sR], 1
  s_lshl_b32 s[sT1], s[sR], 5
  s_cmp_ge_u32 s[sT1], s[sTOT]
  s_cbranch_scc1 .Ldone
  s_barrier
  s_branch .Litem
.Ldone:
  .if DIAG
    s_waitcnt vmcnt(0)
    STAMP 9
    s_memrealtime s[sA:sA+1]
    s_load_dwordx2 s[sB:sB+1], %0, 96
    s_lshl_b32 s[sT1], %1, 3
    s_add_u32 s[sT1], s[sT1], s[sW]
    s_lshl_b32 s[sT1], s[sT1], 8
    s_waitcnt lgkmcnt(0)
    s_add_u32 s[sB], s[sB], s[sT1]
    s_addc_u32 s[sB+1], s[sB+1], 0
    v_writelane_b32 v[vDG], s[sRT], 10
    v_writelane_b32 v[vDG], s[sA], 11
    v_lshlrev_b32 v[vT6], 2, v[vLANE]
    s_mov_b64 exec, 0xfff
    global_store_dword v[vT6], v[vDG], s[sB:sB+1]
  .endif
  s_endpgm
  ; ======================================================================== cold paths
.Lepi:                                              ; O complete, nothing sent yet
  s_cmp_eq_u32 s[sMB], 1
  s_cbranch_scc1 .LepiB
  s_nop 7
  SENDB 2
  SENDB 3
  s_branch .Lepi_sent
.LepiB:
  STAMP 7
  v_mov_b32 v[vT7], 0x71800000
  s_lshl_b32 s[sT3], s[sQR], 12
  s_add_u32 s[sT3], s[sT3], s[sHOFF]
  ROWSUM
  s_nop 0
  v_rcp_f32 v[vFA], v[vL0]
  s_waitcnt vmcnt(0)
  s_barrier
  .set i, 0
  .rept 32
    v_pk_mul_f32 v[vO+2*i:vO+2*i+1], v[vFA:vFA+1], v[vO+2*i:vO+2*i+1] op_sel_hi:[0,1]
    .set i, i+1
  .endr
  OUTHALF 0, vQ-vO
  v_add_u32 v[vSGW], 4096, v[vSGW]
  v_add_u32 v[vSGR], 4096, v[vSGR]
  s_add_u32 s[sT3], s[sT3], 128
  s_nop 0
  OUTHALF 32, vQ-vO+16
  s_branch .Lnext
  ; ======================================================================== mode B loop (two steps per tile)
.Lpro_b:
  .set r, 6
  .rept 5
    EXPE vSA, r
    .set r, r+1
  .endr
  STAMP 6
  s_cmp_eq_u32 s[sNF], 0
  s_cbranch_scc1 .LlB0
.LB0:
  FULLF vSB, vSA, 1, 0, 0
  s_cbranch_scc1 .LlB1
  FULLF vSA, vSB, 1, 0, 2
  s_cbranch_scc0 .LB0
.LlB0:
  LASTF vSA, 1, 0, 0
.LlB1:
  LASTF vSB, 1, 0, 2
  ; ======================================================================== SAFE loop
.Lpro_safe:
  s_cmp_eq_u32 s[sMB], 1
  s_cbranch_scc1 .Lps_b
  KADV
  s_waitcnt vmcnt(8)
  s_barrier
  .set i, 0
  .rept 8
    KREADO i, 0
    .set i, i+1
  .endr
  KADV
.Lps_b:
  .set r, 0
  .rept 16
    EXPE vSA, r
    .set r, r+1
  .endr
  s_cmp_eq_u32 s[sNF], 0
  s_cbranch_scc1 .LlastS0
.LloopS:
  FULLS vSB, vSA, 0
  s_cbranch_scc1 .LlastS1
  FULLS vSA, vSB, 1
  s_cbranch_scc0 .LloopS
.LlastS0:
  LASTS vSA, 0
.LlastS1:
  LASTS vSB, 1

  ; ---- slots in which this wave has nothing to compute
.Lpro_none:
  DALL
  s_cmp_eq_u32 s[sMB], 1
  s_cbranch_scc1 .Lnone
  s_waitcnt vmcnt(8)
  s_barrier
.Lnone:                                             ; wave without steps: keep loading its share of the tiles
  s_cmp_ge_u32 s[sU], s[sNU]
  s_cbranch_scc1 .Lepi
  s_bitcmp1_b32 s[sU], 0
  s_cselect_b32 s[sT1], s[sMB], 0
  s_cmp_eq_u32 s[sT1], 1
  s_cbranch_scc1 .Lnone_skip
  s_cmp_lt_u32 s[sU], s[sND]
  s_cbranch_scc0 .Lnone_fin
  s_waitcnt vmcnt(4) lgkmcnt(0)
  s_barrier
  DALL
  s_branch .Lnone_skip
.Lnone_fin:
  s_waitcnt vmcnt(0) lgkmcnt(0)
  s_barrier
.Lnone_skip:
  s_add_u32 s[sU], s[sU], 1
  s_branch .Lnone
.Lnone_tail:                                        ; after the wave's last step: only barriers remain
  s_cmp_ge_u32 s[sU], s[sNU]
  s_cbranch_scc1 .Lepi
  s_bitcmp1_b32 s[sU], 0
  s_cselect_b32 s[sT1], s[sMB], 0
  s_cmp_eq_u32 s[sT1], 1
  s_cbranch_scc1 .Lnt_skip
  s_barrier
.Lnt_skip:
  s_add_u32 s[sU], s[sU], 1
  s_branch .Lnone_tail

.Lfallback:
  s_waitcnt vmcnt(0) lgkmcnt(0)
  .purgem STAMP
  .purgem STAMPE
  .purgem DK1
  .purgem DK2
  .purgem DV1
  .purgem DV2
  .purgem DALL
  .purgem BARX
  .purgem DMAP
  .purgem KREADO
  .purgem KADV
  .purgem VADDR
  .purgem QKM
  .purgem VREADO
  .purgem PVJ
  .purgem PVM
  .purgem LSUM
  .purgem FMAE
  .purgem XPE
  .purgem EXPE
  .purgem CVTE
  .purgem SUMPK
  .purgem MASK
  .purgem ROWMAX1
  .purgem ROWMAX2
  .purgem RESCALE
  .purgem SENDB
  .purgem LASTV
  .purgem FDMA
  .purgem FDMB
  .purgem FULLF
  .purgem LASTF
  .purgem FULLS
  .purgem LASTS
  .purgem OUTHALF
  .purgem MERGE
  .purgem ROWSUM
  .purgem PREFETCH
  .purgem QK0
  )" :: "s"(__builtin_amdgcn_kernarg_segment_ptr()), "s"(blockIdx.x), "v"(threadIdx.x), "i"(NIT)
     : "memory", "vcc", "m0",
       "s8","s9","s10","s11","s12","s13","s14","s15","s16","s17","s18","s19","s20","s21","s22","s23","s24","s25","s26","s27","s28","s29","s30","s31",
       "s33","s34","s35","s36","s37","s38","s39","s40","s41","s42","s43","s44","s45","s46","s47","s48","s49","s50","s51","s52","s53","s54","s55",
       "s56","s57","s58","s59","s60","s61","s62","s63","s64","s65","s66","s67","s68","s69","s70","s71","s72","s73","s74","s75","s76","s77","s78","s79",
       "s80","s81","s82","s83","s84","s85","s86","s87","s88","s89","s90","s91","s92","s93","s94","s95","s96","s97","s98","s99",
       "v1","v2","v3","v4","v5","v6","v7","v8","v9","v10","v11","v12","v13","v14","v15","v16","v17","v18","v19","v20","v21","v22","v23","v24","v25","v26","v27","v28","v29","v30","v31",
       "v32","v33","v34","v35","v36","v37","v38","v39","v40","v41","v42","v43","v44","v45","v46","v47","v48","v49","v50","v51","v52","v53","v54","v55","v56","v57","v58","v59","v60","v61","v62","v63",
       "v64","v65","v66","v67","v68","v69","v70","v71","v72","v73","v74","v75","v76","v77","v78","v79","v80","v81","v82","v83","v84","v85","v86","v87","v88","v89","v90","v91","v92","v93","v94","v95",
       "v96","v97","v98","v99","v100","v101","v102","v103","v104","v105","v106","v107","v108","v109","v110","v111","v112","v113","v114","v115","v116","v117","v118","v119","v120","v121","v122","v123","v124","v125","v126","v127",
       "v128","v129","v130","v131","v132","v133","v134","v135","v136","v137","v138","v139","v140","v141","v142","v143","v144","v145","v146","v147","v148","v149","v150","v151","v152","v153","v154","v155","v156","v157","v158","v159",
       "v160","v161","v162","v163","v164","v165","v166","v167","v168","v169","v170","v171","v172","v173","v174","v175","v176","v177","v178","v179","v180","v181","v182","v183","v184","v185","v186","v187","v188","v189","v190","v191",
       "v192","v193","v194","v195","v196","v197","v198","v199","v200","v201","v202","v203","v204","v205","v206","v207","v208","v209","v210","v211","v212","v213","v214","v215","v216","v217","v218","v219","v220","v221","v222","v223",
       "v224","v225","v226","v227","v228","v229","v230","v231","v232","v233","v234","v235","v236","v237","v238","v239","v240","v241","v242","v243","v244","v245","v246","v247","v248","v249","v250","v251","v252","v253","v254","v255");
  generic_path((const unsigned short*)Q, (const unsigned short*)K, (const unsigned short*)V, (const int*)seg, (unsigned short*)O, T, lds);
}

__global__ __launch_bounds__(512, 2)
void ragged_attn_generic(const void* Q, const void* K, const void* V, const void* seg, void* O, int T, int pad) {
  __shared__ __attribute__((aligned(16))) char lds[LDS_BYTES];
  generic_path((const unsigned short*)Q, (const unsigned short*)K, (const unsigned short*)V, (const int*)seg, (unsigned short*)O, T, lds);
}

extern "C" void kernel_launch(void* const* d_in, const int* in_sizes, int n_in, void* d_out, int out_size, void* d_ws, size_t ws_size,
                              hipStream_t stream) {
  const int T = in_sizes[3];
  const unsigned nb = (unsigned)T * ROWB, F = 0x00020000u;
  if (T > 2048 && T <= 2560) {
    ragged_attn_kernel<5><<<256, 512, 0, stream>>>(d_in[0], nb, F, d_in[1], nb, F, d_in[2], nb, F, d_in[3], (unsigned)T * 4u, F, d_out, nb, F,
                                                   T, T - 1 - 512 * 4, ((T + 31) / 32) * (int)ROWB, 0, d_ws);
  } else {
    ragged_attn_generic<<<256, 512, 0, stream>>>(d_in[0], d_in[1], d_in[2], d_in[3], d_out, T, 0);
  }
}
```

```cpp
#include <hip/hip_runtime.h>
#include <stdint.h>

typedef short    bf16x8 __attribute__((ext_vector_type(8)));
typedef short    s16x4  __attribute__((ext_vector_type(4)));
typedef float    f32x16 __attribute__((ext_vector_type(16)));
typedef float    f32x4  __attribute__((ext_vector_type(4)));
typedef float    f32x2  __attribute__((ext_vector_type(2)));
typedef __bf16   bf2    __attribute__((ext_vector_type(2)));
typedef unsigned u32x4  __attribute__((ext_vector_type(4)));
typedef unsigned long long u64;
#define LDSP __attribute__((address_space(3)))
typedef LDSP const char* lds_cptr;
typedef LDSP char* lds_ptr;
typedef short v4i16_t __attribute__((ext_vector_type(4)));
#define DI __device__ __forceinline__
#define MFMA(a, b, c) __builtin_amdgcn_mfma_f32_32x32x16_bf16((a), (b), (c), 0, 0, 0)

constexpr int NH = 16, HD = 128;
constexpr unsigned ROWB = NH * HD * 2;
constexpr unsigned LDS_BYTES = 163840;
constexpr float CEXP = 0.08838834764831845f * 1.4426950408889634f;
constexpr float THR_RAW = 8.0f / CEXP;
constexpr float MINIT = -1.0e30f;

DI unsigned rfl(unsigned x) { return __builtin_amdgcn_readfirstlane(x); }
DI unsigned cvtpk(float lo, float hi) { f32x2 v = {lo, hi}; bf2 b = __builtin_convertvector(v, bf2); return __builtin_bit_cast(unsigned, b); }
DI float swap_max(float x) { auto r = __builtin_amdgcn_permlane32_swap(__float_as_uint(x), __float_as_uint(x), false, false); return __builtin_fmaxf(__uint_as_float(r[0]), __uint_as_float(r[1])); }
DI float swap_add(float x) { auto r = __builtin_amdgcn_permlane32_swap(__float_as_uint(x), __float_as_uint(x), false, false); return __uint_as_float(r[0]) + __uint_as_float(r[1]); }
DI s16x4 vtr(lds_cptr p) { return __builtin_bit_cast(s16x4, __builtin_amdgcn_ds_read_tr16_b64_v4i16((LDSP v4i16_t*)p)); }
#define MX3(a, b, c) __builtin_fmaxf(__builtin_fmaxf((a), (b)), (c))

DI void dma16(__amdgpu_buffer_rsrc_t srd, unsigned voff, unsigned soff, unsigned m0v) {
  unsigned keep;
  asm volatile("s_nop 4\n\ts_mov_b32 %0, m0\n\ts_mov_b32 m0, %1\n\ts_nop 0\n\tbuffer_load_dwordx4 %2, %3, %4 offen lds\n\ts_mov_b32 m0, %0"
               : "=&s"(keep) : "s"(m0v), "v"(voff), "s"(srd), "s"(soff) : "memory");
}

__device__ __forceinline__ void generic_path(const unsigned short* __restrict__ Q, const unsigned short* __restrict__ K, const unsigned short* __restrict__ V,
                                          const int* __restrict__ seg, unsigned short* __restrict__ O, int T, char* lds) {
  constexpr unsigned SLOT = 32768, VOFF = 16384, XREG = 4 * SLOT;
  const int tid = threadIdx.x, lane = tid & 63, r32 = lane & 31, hh = lane >> 5;
  const int w = (int)rfl((unsigned)tid >> 6);
  const int grp = blockIdx.x & 7, jj = blockIdx.x >> 3, NJ = 32;
  const unsigned ldsb = (unsigned)(uintptr_t)lds;
  const lds_ptr L = (lds_ptr)lds;
  const int total_items = 2 * ((T + 127) >> 7);
  const __amdgpu_buffer_rsrc_t srdQ = __builtin_amdgcn_make_buffer_rsrc((void*)Q, (short)0, T * (int)ROWB, 0x00020000);
  const __amdgpu_buffer_rsrc_t srdK = __builtin_amdgcn_make_buffer_rsrc((void*)K, (short)0, T * (int)ROWB, 0x00020000);
  const __amdgpu_buffer_rsrc_t srdV = __builtin_amdgcn_make_buffer_rsrc((void*)V, (short)0, T * (int)ROWB, 0x00020000);
  const unsigned prow = (lane >> 2) & 7, pcb = lane >> 5, pc = lane & 3;
  const unsigned voffK = prow * ROWB + pcb * 64 + ((pc ^ ((2u * (unsigned)w + (prow >> 2)) & 3u)) * 16);
  const unsigned voffV = prow * ROWB + pcb * 64 + pc * 16;
  const unsigned kb0 = 2048u * (r32 >> 3) + 64u * (r32 & 7) + 16u * ((unsigned)hh ^ ((r32 >> 2) & 3u));
  const unsigned vb0 = ((unsigned)hh * 4 + ((lane & 15) >> 2)) * 64 + ((lane >> 4) & 1) * 32 + (lane & 3) * 8;
  for (int idx = jj; idx < total_items; idx += NJ) {
    __syncthreads();
    const int head = grp + 8 * (idx & 1);
    const int q0 = (idx >> 1) * 128, qend = min(q0 + 128, T);
    LDSP int* red = (LDSP int*)L;
    if (tid == 0) { red[0] = 0x7fffffff; red[1] = (int)0x80000000; red[2] = 0x7fffffff; red[3] = -1; }
    __syncthreads();
    if (tid < 128 && q0 + tid < qend) { const int id = seg[q0 + tid];
      __hip_atomic_fetch_min((LDSP int*)&red[0], id, __ATOMIC_RELAXED, __HIP_MEMORY_SCOPE_WORKGROUP);
      __hip_atomic_fetch_max((LDSP int*)&red[1], id, __ATOMIC_RELAXED, __HIP_MEMORY_SCOPE_WORKGROUP); }
    __syncthreads();
    const int idlo = red[0], idhi = red[1];
    int lo = 0x7fffffff, hi = -1;
    for (int i = tid; i < T; i += 512) { const int id = seg[i]; if (id >= idlo && id <= idhi) { lo = min(lo, i); hi = max(hi, i); } }
    __hip_atomic_fetch_min((LDSP int*)&red[2], lo, __ATOMIC_RELAXED, __HIP_MEMORY_SCOPE_WORKGROUP);
    __hip_atomic_fetch_max((LDSP int*)&red[3], hi, __ATOMIC_RELAXED, __HIP_MEMORY_SCOPE_WORKGROUP);
    __syncthreads();
    const int k0 = (int)rfl((unsigned)red[2]), kend = (int)rfl((unsigned)red[3]) + 1;
    __syncthreads();
    const int nk = (kend - k0 + 63) >> 6;
    const unsigned hoff = (unsigned)head * 256u;
#pragma unroll
    for (int i = 0; i < 2; ++i) {
      const unsigned m = (unsigned)w + 8u * i;
      const unsigned so = (unsigned)(q0 + 8 * (int)m) * ROWB + hoff;
      dma16(srdQ, voffK, so, ldsb + XREG + m * 2048u);
      dma16(srdQ, voffK, so + 128u, ldsb + XREG + m * 2048u + 1024u);
    }
    const int qb = w & 3, sub = w >> 2;
    const int qrow = q0 + 32 * qb + r32;
    const int segq = seg[qrow < T ? qrow : T - 1];
    bf16x8 qf[8];
    f32x16 o[4];
#pragma unroll
    for (int d = 0; d < 4; ++d) o[d] = f32x16{};
    float m_run = MINIT, l_run = 0.f;
    for (int t = 0; t < nk; ++t) {
      __syncthreads();
      {
        const unsigned so = (unsigned)(k0 + 64 * t + 8 * w) * ROWB + hoff;
        const unsigned d = ldsb + (unsigned)w * 2048u;
        dma16(srdK, voffK, so, d); dma16(srdK, voffK, so + 128u, d + 1024u);
        dma16(srdV, voffV, so, d + VOFF); dma16(srdV, voffV, so + 128u, d + VOFF + 1024u);
      }
      asm volatile("s_waitcnt vmcnt(0)" ::: "memory");
      __syncthreads();
      if (t == 0) {
#pragma unroll
        for (int s = 0; s < 8; ++s)
          qf[s] = *(const LDSP bf16x8*)(L + XREG + (unsigned)qb * 8192u + ((kb0 ^ ((s & 1) * 32u)) + 512u * (s >> 1)));
      }
      const int kbase = k0 + 64 * t + 32 * sub;
      const lds_cptr kp = (lds_cptr)L + (unsigned)sub * 8192u;
      f32x16 sc = f32x16{};
#pragma unroll
      for (int s = 0; s < 8; ++s) {
        const bf16x8 kf = *(const LDSP bf16x8*)(kp + ((kb0 ^ ((s & 1) * 32u)) + 512u * (s >> 1)));
        sc = MFMA(kf, qf[s], sc);
      }
#pragma unroll
      for (int r = 0; r < 16; ++r) {
        const int kk = kbase + (r & 3) + 8 * (r >> 2) + 4 * hh;
        const int id = seg[kk < T ? kk : T - 1];
        sc[r] = (kk < kend && id == segq) ? sc[r] : -__builtin_inff();
      }
      float a = MX3(sc[0], sc[1], sc[2]), b = MX3(sc[3], sc[4], sc[5]);
      a = MX3(a, sc[6], sc[7]); b = MX3(b, sc[8], sc[9]); a = MX3(a, sc[10], sc[11]); b = MX3(b, sc[12], sc[13]);
      a = MX3(a, sc[14], sc[15]);
      const float pm = swap_max(__builtin_fmaxf(a, b));
      const float mn = __builtin_fmaxf(m_run, pm);
      const float al = __builtin_amdgcn_exp2f((m_run - mn) * CEXP);
      m_run = mn; l_run *= al;
#pragma unroll
      for (int d = 0; d < 4; ++d)
#pragma unroll
        for (int r = 0; r < 16; ++r) o[d][r] *= al;
      const float nmc = -m_run * CEXP;
      float ps = 0.f;
#pragma unroll
      for (int r = 0; r < 16; ++r) { sc[r] = __builtin_amdgcn_exp2f(__builtin_fmaf(sc[r], CEXP, nmc)); ps += sc[r]; }
      l_run += ps;
      u32x4 pw0, pw1;
#pragma unroll
      for (int e = 0; e < 4; ++e) { pw0[e] = cvtpk(sc[2 * e], sc[2 * e + 1]); pw1[e] = cvtpk(sc[8 + 2 * e], sc[8 + 2 * e + 1]); }
      const bf16x8 p0 = __builtin_bit_cast(bf16x8, pw0), p1 = __builtin_bit_cast(bf16x8, pw1);
      const lds_cptr vp = kp + VOFF + vb0;
#pragma unroll
      for (int d = 0; d < 4; ++d) {
        const s16x4 a0 = vtr(vp + d * 512), a1 = vtr(vp + d * 512 + 2048), b0 = vtr(vp + d * 512 + 4096), b1 = vtr(vp + d * 512 + 6144);
        const bf16x8 va = __builtin_shufflevector(a0, a1, 0, 1, 2, 3, 4, 5, 6, 7), vb = __builtin_shufflevector(b0, b1, 0, 1, 2, 3, 4, 5, 6, 7);
        o[d] = MFMA(va, p0, o[d]);
        o[d] = MFMA(vb, p1, o[d]);
      }
    }
    l_run = swap_add(l_run);
    unsigned short* orow = O + (size_t)(qrow < T ? qrow : 0) * (NH * HD) + head * HD + 8 * hh;
    const bool row_ok = qrow < qend;
    const int kh = sub;
    __syncthreads();
    const unsigned xa = SLOT, xb = 2 * SLOT, xs = 3 * SLOT;
    const unsigned mine = (kh ? xb : xa) + (unsigned)qb * 8192u + (unsigned)lane * 16u;
    const unsigned theirs = (kh ? xa : xb) + (unsigned)qb * 8192u + (unsigned)lane * 16u;
#pragma unroll
    for (int dd = 0; dd < 2; ++dd)
#pragma unroll
      for (int g = 0; g < 4; ++g) {
        f32x4 v;
#pragma unroll
        for (int e = 0; e < 4; ++e) v[e] = kh ? o[dd][4 * g + e] : o[2 + dd][4 * g + e];
        *(LDSP f32x4*)(L + mine + (unsigned)(dd * 4 + g) * 1024u) = v;
      }
    if (hh == 0) { f32x2 s2 = {m_run, l_run}; *(LDSP f32x2*)(L + xs + (unsigned)w * 256u + (unsigned)r32 * 8u) = s2; }
    __syncthreads();
    const f32x2 o2 = *(const LDSP f32x2*)(L + xs + (unsigned)(w ^ 4) * 256u + (unsigned)r32 * 8u);
    const float mn = __builtin_fmaxf(m_run, o2[0]);
    const float wa = __builtin_amdgcn_exp2f((m_run - mn) * CEXP), wb = __builtin_amdgcn_exp2f((o2[0] - mn) * CEXP);
    const float inv = 1.0f / (wa * l_run + wb * o2[1]);
    const float fa = wa * inv, fb = wb * inv;
#pragma unroll
    for (int dd = 0; dd < 2; ++dd) {
      float r[16];
#pragma unroll
      for (int g = 0; g < 4; ++g) {
        const f32x4 v = *(const LDSP f32x4*)(L + theirs + (unsigned)(dd * 4 + g) * 1024u);
#pragma unroll
        for (int e = 0; e < 4; ++e) r[4 * g + e] = fa * (kh ? o[2 + dd][4 * g + e] : o[dd][4 * g + e]) + fb * v[e];
      }
#pragma unroll
      for (int g = 0; g < 4; g += 2) {
        unsigned ax = cvtpk(r[4 * g + 0], r[4 * g + 1]), ay = cvtpk(r[4 * g + 2], r[4 * g + 3]);
        unsigned bx = cvtpk(r[4 * g + 4], r[4 * g + 5]), by = cvtpk(r[4 * g + 6], r[4 * g + 7]);
        auto rx = __builtin_amdgcn_permlane32_swap(ax, bx, false, false); auto ry = __builtin_amdgcn_permlane32_swap(ay, by, false, false);
        const u32x4 v = {rx[0], ry[0], rx[1], ry[1]};
        if (row_ok) *(u32x4*)(orow + 64 * kh + 32 * dd + 8 * g) = v;
      }
    }
  }
}

template <int NIT>
__global__ __launch_bounds__(512, 2)
void ragged_attn_kernel(const void* Q, unsigned nQ, unsigned fQ, const void* K, unsigned nK, unsigned fK, const void* V, unsigned nV, unsigned fV,
                        const void* seg, unsigned nS, unsigned fS, void* O, unsigned nO, unsigned fO, int T, int lastc, int pfb, int pad) {
  __shared__ __attribute__((aligned(16))) char lds[LDS_BYTES];
  asm volatile(R"(
  .set NIT, %c3
  .set PF1, 3                                       ; Q prefetch instructions issued before / after the segment scan
  .set PF2, 5
  ; ---------------- SGPRs
  .set sQ, 8
  .set sK, 12
  .set sV, 16
  .set sS, 20
  .set sO, 24
  .set sT, 28
  .set sLASTC, 29
  .set sPFB, 30
  .set sF, 30
  .set sLIVE, 30
  .set sTM16, 31
  .set sNDF, 33
  .set sG, 34
  .set sR, 35
  .set sNBIG, 36
  .set sTOT, 37
  .set sHOFF, 38
  .set sQ0, 39
  .set sQEND, 40
  .set sK0, 41
  .set sNK, 42
  .set sLEN, 43
  .set sMB, 44
  .set sNS, 45
  .set sU, 46
  .set sNU, 47
  .set sKOFF, 48
  .set sKSTEP, 49
  .set sVOFF, 50
  .set sKSO, 51
  .set sVSO, 52
  .set sKDST, 53
  .set sVDST, 54
  .set sCNT, 55
  .set sND, 56
  .set sMSTEP, 57
  .set sPEND, 58
  .set sW, 59
  .set sA, 60
  .set sB, 62
  .set sC, 64
  .set sSAFE, 64
  .set sNF, 65
  .set sT1, 66
  .set sT2, 67
  .set sT3, 68
  .set sT4, 69
  .set sCE, 70
  .set sTHR, 71
  .set sWD, 72
  .set sQB, 73
  .set sKH, 74
  .set sLIM, 75
  .set sJ, 76
  .set sQR, 77
  .set sV2, 80
  .set sMK, 86
  .set sMV, 87
  ; ---------------- VGPRs
  .set vLANE, 1
  .set vOK, 2
  .set vOK2, 3
  .set vOV, 4
  .set vOV2, 5
  .set vKB0, 6
  .set vKB1, 7
  .set vVB, 8
  .set vSTART, 9
  .set vLEN, 10
  .set vINCL, 11
  .set vEXCL, 12
  .set vM, 13
  .set vNMC, 14
  .set vL0, 15
  .set vL1, 16
  .set vKA0, 17
  .set vKA1, 18
  .set vVA, 19
  .set vAL, 20
  .set vSO, 21
  .set vHH4, 22
  .set vSW, 23
  .set vT0, 24
  .set vT1, 25
  .set vT2, 26
  .set vT3, 27
  .set vT4, 28
  .set vT5, 29
  .set vT6, 30
  .set vT7, 31
  .set vP, 32
  .set vX, 40
  .set vLS, 40
  .set vXO, 44
  .set vXP, 45
  .set vSTA, 46
  .set vSTP, 47
  .set vSGW, 48
  .set vSGR, 49
  .set vFA, 50
  .set vFB, 52
  .set vSR, 56
  .set vVH, 57
  .set vVBL, 58
  .set vVBH, 59
  .set vONE, 60
  .set vO, 64
  .set vQ, 128
  .set vSA, 160
  .set vSB, 176
  .set vK, 192
  .set vV, 224
  ; ---------------- LDS map: K ring 4 x 16 KiB | V ring 4 x 16 KiB | X (Q stage) 32 KiB
  .set LK, 0
  .set LV, 65536
  .set LX, 131072
  .set LQ2, 49152
  .set LQ3, 114688
  .set LSCR, 98304
  .set TILEB, 64*4096


  ; ======================================================================== macros
  ; ---- DMA pieces (generic form).  sKDST / sVDST: ring offset + w*2048 of the next tile
  .macro DK1
    s_mov_b32 m0, s[sKDST]
    s_add_u32 s[sT1], s[sKDST], 16384
    buffer_load_dwordx4 v[vOK], s[sK:sK+3], s[sKSO] offen lds
  .endm
  .macro DK2
    s_add_u32 m0, s[sKDST], 1024
    s_and_b32 s[sKDST], s[sT1], 0xffff
    buffer_load_dwordx4 v[vOK2], s[sK:sK+3], s[sKSO] offen lds
    s_add_u32 s[sKSO], s[sKSO], TILEB
  .endm
  .macro DV1
    s_add_u32 m0, s[sVDST], LV
    s_add_u32 s[sT3], s[sVDST], 16384
    buffer_load_dwordx4 v[vOV], s[sV:sV+3], s[sVSO] offen lds
  .endm
  .macro DV2
    s_add_u32 m0, s[sVDST], LV+1024
    s_and_b32 s[sVDST], s[sT3], 0xffff
    buffer_load_dwordx4 v[vOV2], s[sV:sV+3], s[sVSO] offen lds
    s_add_u32 s[sVSO], s[sVSO], TILEB
  .endm
  .macro DALL
    DK1
    DK2
    DV1
    DV2
  .endm
  ; m: 0 this slot has a barrier, 1 barrier unless mode B (run time), 2 no barrier
  .macro BARX n, m
    .if \m == 1
      s_cmp_eq_u32 s[sMB], 1
      s_cbranch_scc1 .Lnob\@
    .endif
    .if \m == 2
      s_waitcnt lgkmcnt(0)
    .else
      s_waitcnt vmcnt(\n) lgkmcnt(0)
      s_barrier
    .endif
    .if \m == 1
      s_branch .Lbd\@
  .Lnob\@:
      s_waitcnt lgkmcnt(0)
  .Lbd\@:
    .endif
  .endm
  ; DMA piece k of a generic step.  dma: 0 none, 1 always, 2 run-time flag sF ; m as above
  .macro DMAP k, m, dma
    .if ((\dma) != 0) && ((\m) != 2)
      .if \dma == 2
        s_cmp_eq_u32 s[sF], 0
        s_cbranch_scc1 .Ld\@
      .endif
      .if \m == 1
        s_cmp_eq_u32 s[sMB], 1
        s_cbranch_scc1 .Ld\@
      .endif
      .if \k == 0
        DK1
      .elseif \k == 1
        DK2
      .elseif \k == 2
        DV1
      .else
        DV2
      .endif
  .Ld\@:
    .endif
  .endm
  .macro KREADO i, off
    .if (\i & 1)
      ds_read_b128 v[vK+4*\i:vK+4*\i+3], v[vKA1] offset:(\off)+512*(\i>>1)
    .else
      ds_read_b128 v[vK+4*\i:vK+4*\i+3], v[vKA0] offset:(\off)+512*(\i>>1)
    .endif
  .endm
  .macro KADV                                       ; generic form: advance the K read addresses to the next step
    s_add_u32 s[sKOFF], s[sKOFF], s[sKSTEP]
    s_and_b32 s[sKOFF], s[sKOFF], 0xffff
    v_add_u32 v[vKA0], s[sKOFF], v[vKB0]
    v_add_u32 v[vKA1], s[sKOFF], v[vKB1]
  .endm
  .macro VADDR                                      ; generic form: V read addresses of the coming step
    v_add_u32 v[vVA], s[sVOFF], v[vVBL]
    v_add_u32 v[vVH], s[sVOFF], v[vVBH]
    s_add_u32 s[sVOFF], s[sVOFF], s[sKSTEP]
    s_and_b32 s[sVOFF], s[sVOFF], 0xffff
  .endm
  .macro QKM c, i
    .if \i == 0
      v_mfma_f32_32x32x16_bf16 v[\c:\c+15], v[vK:vK+3], v[vQ:vQ+3], 0
    .else
      v_mfma_f32_32x32x16_bf16 v[\c:\c+15], v[vK+4*\i:vK+4*\i+3], v[vQ+4*\i:vQ+4*\i+3], v[\c:\c+15]
    .endif
  .endm
  .macro VREADO j, off                              ; V fragment j: register block d = j&3 (actual d-block = d ^ 2*kh), ks = j>>2
    .if (\j & 2)
    ds_read_b64_tr_b16 v[vV+4*\j:vV+4*\j+1], v[vVH] offset:(\off)+512*(\j&3)+4096*(\j>>2)
    ds_read_b64_tr_b16 v[vV+4*\j+2:vV+4*\j+3], v[vVH] offset:(\off)+512*(\j&3)+4096*(\j>>2)+2048
    .else
    ds_read_b64_tr_b16 v[vV+4*\j:vV+4*\j+1], v[vVA] offset:(\off)+512*(\j&3)+4096*(\j>>2)
    ds_read_b64_tr_b16 v[vV+4*\j+2:vV+4*\j+3], v[vVA] offset:(\off)+512*(\j&3)+4096*(\j>>2)+2048
    .endif
  .endm
  .macro PVJ d, ks
    v_mfma_f32_32x32x16_bf16 v[vO+16*(\d):vO+16*(\d)+15], v[vV+4*((\d)+4*(\ks)):vV+4*((\d)+4*(\ks))+3], v[vP+4*(\ks):vP+4*(\ks)+3], v[vO+16*(\d):vO+16*(\d)+15]
  .endm
  .macro PVM j
    PVJ (\j&3), (\j>>2)
  .endm
  .macro LSUM ks                                    ; row sums of one packed P fragment
    v_mfma_f32_16x16x32_bf16 v[vLS:vLS+3], v[vONE:vONE+3], v[vP+4*\ks:vP+4*\ks+3], v[vLS:vLS+3]
  .endm
  .macro FMAE c, r
    v_fma_f32 v[\c+(\r)], v[\c+(\r)], s[sCE], v[vNMC]
  .endm
  .macro XPE c, r
    v_exp_f32 v[\c+(\r)], v[\c+(\r)]
  .endm
  .macro EXPE c, r
    FMAE \c, \r
    XPE \c, \r
  .endm
  .macro CVTE p, i                                  ; pack of elements 2i, 2i+1
    v_cvt_pk_bf16_f32 v[vP+\i], v[\p+2*\i], v[\p+2*\i+1]
  .endm
  .macro SUMPK p, i                                 ; SAFE form: row sums + pack of elements 2i, 2i+1
    v_add_f32 v[vL0], v[vL0], v[\p+2*\i]
    v_add_f32 v[vL1], v[vL1], v[\p+2*\i+1]
    v_cvt_pk_bf16_f32 v[vP+\i], v[\p+2*\i], v[\p+2*\i+1]
  .endm
  .macro MASK c                                     ; keys >= lim of this sub-tile -> -inf
    v_sub_u32 v[vT2], s[sLIM], v[vHH4]
    v_mov_b32 v[vT3], 0xff800000
    .set r, 0
    .rept 16
      v_cmp_lt_i32 vcc, (r&3)+8*(r>>2), v[vT2]
      s_nop 1
      v_cndmask_b32 v[\c+r], v[vT3], v[\c+r], vcc
      .set r, r+1
    .endr
  .endm
  .macro ROWMAX1 c
    v_max3_f32 v[vT2], v[\c+0], v[\c+1], v[\c+2]
    v_max3_f32 v[vT3], v[\c+3], v[\c+4], v[\c+5]
    v_max3_f32 v[vT2], v[vT2], v[\c+6], v[\c+7]
    v_max3_f32 v[vT3], v[vT3], v[\c+8], v[\c+9]
    v_max3_f32 v[vT2], v[vT2], v[\c+10], v[\c+11]
    v_max3_f32 v[vT3], v[vT3], v[\c+12], v[\c+13]
    v_max3_f32 v[vT2], v[vT2], v[\c+14], v[\c+15]
    v_max_f32 v[vT2], v[vT2], v[vT3]
    v_mov_b32 v[vT3], v[vT2]
  .endm
  .macro ROWMAX2                                    ; needs 2 wait states after ROWMAX1
    v_permlane32_swap_b32 v[vT2], v[vT3]
    s_nop 0
    v_max_f32 v[vT2], v[vT2], v[vT3]                ; vT2 = row max
  .endm
  .macro RESCALE
    s_nop 7
    s_nop 7
    .set i, 0
    .rept 64
      v_mul_f32 v[vO+i], v[vAL], v[vO+i]
      .set i, i+1
    .endr
    v_mul_f32 v[vL0], v[vAL], v[vL0]
    v_mul_f32 v[vL1], v[vAL], v[vL1]
    s_mov_b32 s[sPEND], 0
  .endm
  .macro SENDB d                                    ; d-block d (16 regs) -> own exchange area
    .set i, 0
    .rept 4
      ds_write_b128 v[vXO], v[vO+16*\d+4*i:vO+16*\d+4*i+3] offset:4096*(\d&1)+1024*i
      .set i, i+1
    .endr
  .endm
  .macro LASTV da, db, dc, dd                       ; last P.V: the blocks to send (da, db) first
    PVJ \da, 0
    PVJ \db, 0
    PVJ \da, 1
    PVJ \db, 1
    PVJ \dc, 0
    s_nop 7
    s_nop 1
    SENDB \da
    PVJ \dd, 0
    SENDB \db
    PVJ \dc, 1
    PVJ \dd, 1
    s_branch .Lepi_sent
  .endm

  ; ---- FAST step body (reference = row max of the wave's first sub-tile; overflow is detected on the row sums at the end of the item,
  ;      which is then redone with the SAFE steps).  prev enters with exp done for e0..e10.
  ;      gen = 1: generic addressing (address registers, generic DMA) ; gen = 0: mode A, ring position r as immediates
  .macro FDMA k, gen, r, m, dma
    .if \gen
      DMAP \k, \m, \dma
    .elseif (\dma)
      .if \k == 0
        s_add_u32 m0, s[sMK], 16384*(\r)
      .elseif \k == 1
        s_add_u32 m0, s[sMK], 16384*(\r)+1024
      .elseif \k == 2
        s_add_u32 m0, s[sMV], 16384*(\r)
      .else
        s_add_u32 m0, s[sMV], 16384*(\r)+1024
      .endif
    .endif
  .endm
  .macro FDMB k, gen, dma                            ; the load itself (mode A form), one instruction behind its M0 write
    .if ((\gen) == 0) && (\dma)
      .if \k == 0
        buffer_load_dwordx4 v[vOK], s[sK:sK+3], s[sKSO] offen lds
      .elseif \k == 1
        buffer_load_dwordx4 v[vOK2], s[sK:sK+3], s[sKSO] offen lds
      .elseif \k == 2
        buffer_load_dwordx4 v[vOV], s[sV2:sV2+3], s[sKSO] offen lds
      .else
        buffer_load_dwordx4 v[vOV2], s[sV2:sV2+3], s[sKSO] offen lds
        s_add_u32 s[sKSO], s[sKSO], TILEB
      .endif
    .endif
  .endm
  .macro FULLF cur, prev, gen, r, m
    .set KO, (16384-8192*\gen)*(\r)
    .set VO, (16384-8192*\gen)*(\r)
    BARX 4, \m
    ; ---------------- phase A
    QKM \cur, 0
    VREADO 0, VO
    EXPE \prev, 11
    QKM \cur, 1
    VREADO 1, VO
    EXPE \prev, 12
    CVTE \prev, 0
    QKM \cur, 2
    VREADO 2, VO
    FDMA 0, \gen, \r, \m, 1
    FMAE \prev, 13
    FDMB 0, \gen, 1
    XPE \prev, 13
    CVTE \prev, 1
    QKM \cur, 3
    VREADO 3, VO
    EXPE \prev, 14
    CVTE \prev, 2
    QKM \cur, 4
    VREADO 4, VO
    EXPE \prev, 15
    CVTE \prev, 3
    QKM \cur, 5
    VREADO 5, VO
    CVTE \prev, 4
    CVTE \prev, 5
    QKM \cur, 6
    VREADO 6, VO
    FDMA 1, \gen, \r, \m, 1
    CVTE \prev, 6
    FDMB 1, \gen, 1
    QKM \cur, 7
    VREADO 7, VO
    CVTE \prev, 7
    .if (\gen == 0) && (\r == 1)
      v_xor_b32 v[vVA], 0x8000, v[vVA]
      v_xor_b32 v[vVH], 0x8000, v[vVH]
    .endif
    ; ---------------- phase B
    s_waitcnt lgkmcnt(4)
    PVM 0
    KREADO 0, KO
    KREADO 1, KO
    FDMA 2, \gen, \r, \m, 1
    KREADO 2, KO
    FDMB 2, \gen, 1
    KREADO 3, KO
    s_nop 2
    PVM 1
    EXPE \cur, 0
    EXPE \cur, 1
    LSUM 0
    EXPE \cur, 2
    PVM 2
    KREADO 4, KO
    EXPE \cur, 3
    PVM 3
    KREADO 5, KO
    EXPE \cur, 4
    FMAE \cur, 5
    PVM 4
    KREADO 6, KO
    XPE \cur, 5
    EXPE \cur, 6
    PVM 5
    KREADO 7, KO
    EXPE \cur, 7
    LSUM 1
    EXPE \cur, 8
    s_waitcnt lgkmcnt(8)
    PVM 6
    FDMA 3, \gen, \r, \m, 1
    FMAE \cur, 9
    FDMB 3, \gen, 1
    XPE \cur, 9
    .if (\gen) && (\r == 1)
      s_add_u32 s[sKOFF], s[sKOFF], 16384
      s_and_b32 s[sKOFF], s[sKOFF], 0xffff
      v_add_u32 v[vKA0], s[sKOFF], v[vKB0]
      v_add_u32 v[vKA1], s[sKOFF], v[vKB1]
      s_sub_u32 s[sT2], s[sKOFF], 16384
      s_and_b32 s[sT2], s[sT2], 0xffff
      v_add_u32 v[vVA], s[sT2], v[vVB]
      v_mov_b32 v[vVH], v[vVA]
    .elseif \gen
    .elseif \r == 1
      v_xor_b32 v[vKA0], 0x8000, v[vKA0]
      v_xor_b32 v[vKA1], 0x8000, v[vKA1]
      s_xor_b32 s[sMK], s[sMK], 0x8000
      s_xor_b32 s[sMV], s[sMV], 0x8000
    .endif
    s_add_u32 s[sCNT], s[sCNT], 1
    PVM 7
    EXPE \cur, 10
  .endm
  .macro LASTF prev, gen, r, m
    .set VO, (16384-8192*\gen)*(\r)
    .if \m == 2
    .else
      BARX 0, \m
    .endif
    .set g, 0
    .rept 8
      VREADO g, VO
      .if g < 5
        EXPE \prev, 11+g
      .endif
      .set g, g+1
    .endr
    s_mov_b32 s[sU], s[sNS]
    s_cmp_eq_i32 s[sMSTEP], -1
    s_cbranch_scc1 .Lnm\@
    ; partial sub-tile: keys >= lim get weight 0
    v_sub_u32 v[vT2], s[sLIM], v[vHH4]
    .set e, 0
    .rept 16
      v_cmp_lt_i32 vcc, (e&3)+8*(e>>2), v[vT2]
      s_nop 1
      v_cndmask_b32 v[\prev+e], 0, v[\prev+e], vcc
      .set e, e+1
    .endr
  .Lnm\@:
    .set g, 0
    .rept 8
      CVTE \prev, g
      .set g, g+1
    .endr
    s_cmp_eq_u32 s[sU], s[sNU]
    s_waitcnt lgkmcnt(0)
    LSUM 0
    LSUM 1
    .if \gen == 0                                    ; mode A, last slot: send the partner's half early
      s_cbranch_scc0 .Lplain\@
      LASTV 2, 3, 0, 1
  .Lplain\@:
    .endif
    .set g, 0
    .rept 8
      PVM g
      .set g, g+1
    .endr
    s_branch .Lnone_tail
  .endm

  ; ---- SAFE step (online max every step), generic addressing, run-time mode
  .macro FULLS cur, prev, m
    s_cmp_lt_u32 s[sU], s[sND]
    s_cselect_b32 s[sF], 1, 0
    s_cbranch_scc0 .Lbn\@
    BARX 4, \m
    s_branch .Lbq\@
  .Lbn\@:
    BARX 0, \m
  .Lbq\@:
    .set g, 0
    .rept 8
      QKM \cur, g
      VREADO g, 0
      SUMPK \prev, g
      .if g == 1
        DMAP 0, \m, 2
      .elseif g == 5
        DMAP 1, \m, 2
      .endif
      .set g, g+1
    .endr
    s_waitcnt lgkmcnt(4)
    PVM 0
    KREADO 0, 0
    KREADO 1, 0
    s_add_u32 s[sU], s[sU], 1
    s_nop 1
    PVM 1
    s_cmp_lg_u32 s[sMSTEP], s[sU]
    s_cbranch_scc1 .Lnm\@
    MASK \cur
  .Lnm\@:
    ROWMAX1 \cur
    PVM 2
    KREADO 2, 0
    ROWMAX2
    v_sub_f32 v[vT3], v[vT2], v[vM]
    v_cmp_lt_f32 vcc, s[sTHR], v[vT3]
    s_cbranch_vccz .Lkeep\@
    v_max_f32 v[vT3], v[vM], v[vT2]
    v_sub_f32 v[vAL], v[vM], v[vT3]
    v_mul_f32 v[vAL], s[sCE], v[vAL]
    v_exp_f32 v[vAL], v[vAL]
    v_mov_b32 v[vM], v[vT3]
    v_mul_f32 v[vNMC], 0xbe0293ee, v[vM]
    s_mov_b32 s[sPEND], 1
  .Lkeep\@:
    PVM 3
    KREADO 3, 0
    DMAP 2, \m, 2
    EXPE \cur, 0
    EXPE \cur, 1
    EXPE \cur, 2
    PVM 4
    KREADO 4, 0
    EXPE \cur, 3
    EXPE \cur, 4
    EXPE \cur, 5
    PVM 5
    KREADO 5, 0
    EXPE \cur, 6
    EXPE \cur, 7
    EXPE \cur, 8
    s_waitcnt lgkmcnt(6)
    PVM 6
    KREADO 6, 0
    DMAP 3, \m, 2
    EXPE \cur, 9
    EXPE \cur, 10
    EXPE \cur, 11
    PVM 7
    KREADO 7, 0
    EXPE \cur, 12
    EXPE \cur, 13
    EXPE \cur, 14
    EXPE \cur, 15
    KADV
    VADDR
    s_cmp_eq_u32 s[sPEND], 0
    s_cbranch_scc1 .Lsend\@
    RESCALE
  .Lsend\@:
    s_cmp_eq_u32 s[sU], s[sNF]
  .endm
  .macro LASTS prev, m
    .if \m == 1
      s_cmp_eq_u32 s[sMB], 1
      s_cbranch_scc1 .Lnob\@
    .endif
    s_waitcnt vmcnt(0) lgkmcnt(0)
    s_barrier
  .Lnob\@:
    .set g, 0
    .rept 8
      VREADO g, 0
      SUMPK \prev, g
      .set g, g+1
    .endr
    s_add_u32 s[sU], s[sU], 1
    s_waitcnt lgkmcnt(0)
    .set g, 0
    .rept 8
      PVM g
      .set g, g+1
    .endr
    s_branch .Lnone_tail
  .endm

  ; ---- epilogue pieces
  ; 32 f32 at vO+b.. (one 64-wide half) -> bf16 -> LDS stage (row-major 128 B rows, 16-B chunks xor row) -> 4 full-line stores
  ; t: 16 dead registers (vO+t..) ; vSGW / vSGR: stage addresses ; sT3 = global offset of row 0 of this half
  .macro OUTHALF b, t
    .set q, 0
    .rept 4
      v_cvt_pk_bf16_f32 v[vO+\t+4*q+0], v[vO+\b+8*q+0], v[vO+\b+8*q+1]
      v_cvt_pk_bf16_f32 v[vO+\t+4*q+1], v[vO+\b+8*q+2], v[vO+\b+8*q+3]
      v_cvt_pk_bf16_f32 v[vO+\t+4*q+2], v[vO+\b+8*q+4], v[vO+\b+8*q+5]
      v_cvt_pk_bf16_f32 v[vO+\t+4*q+3], v[vO+\b+8*q+6], v[vO+\b+8*q+7]
      v_xor_b32 v[vT0+q], q*32, v[vSGW]
      .set q, q+1
    .endr
    .set q, 0
    .rept 4
      v_permlane32_swap_b32 v[vO+\t+4*q+0], v[vO+\t+4*q+2]
      v_permlane32_swap_b32 v[vO+\t+4*q+1], v[vO+\t+4*q+3]
      .set q, q+1
    .endr
    .set q, 0
    .rept 4
      ds_write_b128 v[vT0+q], v[vO+\t+4*q:vO+\t+4*q+3]
      .set q, q+1
    .endr
    s_add_u32 s[sT4], s[sT3], 8*4096
    s_add_u32 s[sT1], s[sT3], 16*4096
    s_add_u32 s[sT2], s[sT3], 24*4096
    s_waitcnt lgkmcnt(0)
    ds_read_b128 v[vO+\t+0:vO+\t+3], v[vSGR]
    ds_read_b128 v[vO+\t+4:vO+\t+7], v[vSGR] offset:1024
    ds_read_b128 v[vO+\t+8:vO+\t+11], v[vSGR] offset:2048
    ds_read_b128 v[vO+\t+12:vO+\t+15], v[vSGR] offset:3072
    s_waitcnt lgkmcnt(3)
    buffer_store_dwordx4 v[vO+\t+0:vO+\t+3], v[vSO], s[sO:sO+3], s[sT3] offen sc1
    s_waitcnt lgkmcnt(2)
    buffer_store_dwordx4 v[vO+\t+4:vO+\t+7], v[vSO], s[sO:sO+3], s[sT4] offen sc1
    s_waitcnt lgkmcnt(1)
    buffer_store_dwordx4 v[vO+\t+8:vO+\t+11], v[vSO], s[sO:sO+3], s[sT1] offen sc1
    s_waitcnt lgkmcnt(0)
    buffer_store_dwordx4 v[vO+\t+12:vO+\t+15], v[vSO], s[sO:sO+3], s[sT2] offen sc1
  .endm
  .macro MERGE kb, rb                               ; kb: kept 32 regs, rb: 32 regs receiving the partner's half
    ds_read_b64 v[vT4:vT5], v[vSTP]
    .set i, 0
    .rept 8
      ds_read_b128 v[vO+\rb+4*i:vO+\rb+4*i+3], v[vXP] offset:1024*i
      .set i, i+1
    .endr
    s_waitcnt lgkmcnt(8)
    v_max_f32 v[vT6], v[vM], v[vT4]
    v_sub_f32 v[vFA], v[vM], v[vT6]
    v_sub_f32 v[vFB], v[vT4], v[vT6]
    v_mul_f32 v[vFA], s[sCE], v[vFA]
    v_mul_f32 v[vFB], s[sCE], v[vFB]
    v_exp_f32 v[vFA], v[vFA]
    v_exp_f32 v[vFB], v[vFB]
    s_nop 0
    v_mul_f32 v[vT6], v[vFA], v[vL0]
    v_fma_f32 v[vT6], v[vFB], v[vT5], v[vT6]
    v_rcp_f32 v[vT6], v[vT6]
    s_nop 0
    v_mul_f32 v[vFA], v[vFA], v[vT6]
    v_mul_f32 v[vFB], v[vFB], v[vT6]
    .set i, 0
    .rept 16
      v_pk_mul_f32 v[vO+\kb+2*i:vO+\kb+2*i+1], v[vFA:vFA+1], v[vO+\kb+2*i:vO+\kb+2*i+1] op_sel_hi:[0,1]
      .set i, i+1
    .endr
    ; per quarter (32 B of every row): add the partner's part, pack, widen, stage
    .set q, 0
    .rept 4
      .if q == 0
        s_waitcnt lgkmcnt(6)
      .elseif q == 1
        s_waitcnt lgkmcnt(5)
      .elseif q == 2
        s_waitcnt lgkmcnt(4)
      .else
        s_waitcnt lgkmcnt(3)
      .endif
      v_pk_fma_f32 v[vO+\kb+8*q+0:vO+\kb+8*q+1], v[vFB:vFB+1], v[vO+\rb+8*q+0:vO+\rb+8*q+1], v[vO+\kb+8*q+0:vO+\kb+8*q+1] op_sel_hi:[0,1,1]
      v_pk_fma_f32 v[vO+\kb+8*q+2:vO+\kb+8*q+3], v[vFB:vFB+1], v[vO+\rb+8*q+2:vO+\rb+8*q+3], v[vO+\kb+8*q+2:vO+\kb+8*q+3] op_sel_hi:[0,1,1]
      v_pk_fma_f32 v[vO+\kb+8*q+4:vO+\kb+8*q+5], v[vFB:vFB+1], v[vO+\rb+8*q+4:vO+\rb+8*q+5], v[vO+\kb+8*q+4:vO+\kb+8*q+5] op_sel_hi:[0,1,1]
      v_pk_fma_f32 v[vO+\kb+8*q+6:vO+\kb+8*q+7], v[vFB:vFB+1], v[vO+\rb+8*q+6:vO+\rb+8*q+7], v[vO+\kb+8*q+6:vO+\kb+8*q+7] op_sel_hi:[0,1,1]
      v_cvt_pk_bf16_f32 v[vO+\rb+8*q+0], v[vO+\kb+8*q+0], v[vO+\kb+8*q+1]
      v_cvt_pk_bf16_f32 v[vO+\rb+8*q+2], v[vO+\kb+8*q+4], v[vO+\kb+8*q+5]
      v_cvt_pk_bf16_f32 v[vO+\rb+8*q+1], v[vO+\kb+8*q+2], v[vO+\kb+8*q+3]
      v_cvt_pk_bf16_f32 v[vO+\rb+8*q+3], v[vO+\kb+8*q+6], v[vO+\kb+8*q+7]
      v_xor_b32 v[vT0+q], q*32, v[vSGW]
      v_permlane32_swap_b32 v[vO+\rb+8*q+0], v[vO+\rb+8*q+2]
      s_nop 0
      v_permlane32_swap_b32 v[vO+\rb+8*q+1], v[vO+\rb+8*q+3]
      s_nop 0
      ds_write_b128 v[vT0+q], v[vO+\rb+8*q:vO+\rb+8*q+3]
      .set q, q+1
    .endr
    s_add_u32 s[sT4], s[sT3], 8*4096
    s_add_u32 s[sT1], s[sT3], 16*4096
    s_add_u32 s[sT2], s[sT3], 24*4096
    s_waitcnt lgkmcnt(0)
    ds_read_b128 v[vO+\rb+0:vO+\rb+3], v[vSGR]
    ds_read_b128 v[vO+\rb+4:vO+\rb+7], v[vSGR] offset:1024
    ds_read_b128 v[vO+\rb+8:vO+\rb+11], v[vSGR] offset:2048
    ds_read_b128 v[vO+\rb+12:vO+\rb+15], v[vSGR] offset:3072
    s_waitcnt lgkmcnt(3)
    buffer_store_dwordx4 v[vO+\rb+0:vO+\rb+3], v[vSO], s[sO:sO+3], s[sT3] offen sc1
    s_waitcnt lgkmcnt(2)
    buffer_store_dwordx4 v[vO+\rb+4:vO+\rb+7], v[vSO], s[sO:sO+3], s[sT4] offen sc1
    s_waitcnt lgkmcnt(1)
    buffer_store_dwordx4 v[vO+\rb+8:vO+\rb+11], v[vSO], s[sO:sO+3], s[sT1] offen sc1
    s_waitcnt lgkmcnt(0)
    buffer_store_dwordx4 v[vO+\rb+12:vO+\rb+15], v[vSO], s[sO:sO+3], s[sT2] offen sc1
  .endm
  .macro ROWSUM                                     ; complete row sum of this wave's keys -> vL0 (all lanes)
    s_cmp_eq_u32 s[sSAFE], 1
    s_cbranch_scc1 .Lss\@
    v_permlane16_swap_b32 v[vLS], v[vLS+1]
    s_nop 0
    v_mov_b32 v[vL0], v[vLS]
    s_nop 1
    v_permlane32_swap_b32 v[vL0], v[vLS]
    s_branch .Lsd\@
  .Lss\@:
    v_add_f32 v[vL0], v[vL0], v[vL1]
    v_mov_b32 v[vL1], v[vL0]
    s_nop 1
    v_permlane32_swap_b32 v[vL0], v[vL1]
    s_nop 0
    v_add_f32 v[vL0], v[vL0], v[vL1]
  .Lsd\@:
  .endm
  .macro PREFETCH lo, hi                            ; Q prefetch into L2, instructions lo..hi-1 (16 rows each)
    .set n, \lo
    .rept \hi-\lo
      s_mul_i32 s[sT3], s[sJ], s[sPFB]
      s_add_u32 s[sT3], s[sT3], n*16*4096
      s_min_u32 s[sT3], s[sT3], s[sTM16]
      buffer_load_dwordx4 v[vV+4*n:vV+4*n+3], v[vV+28], s[sQ:sQ+3], s[sT3] offen
      .set n, n+1
    .endr
  .endm

  ; ======================================================================== entry
  s_load_dwordx16 s[8:23], %0, 0
  s_load_dwordx8 s[24:31], %0, 64
  v_and_b32 v[vLANE], 63, %2
  v_readfirstlane_b32 s[sW], %2
  v_lshlrev_b32 v[vT0], 2, %2                     ; tid*4
  s_and_b32 s[sG], %1, 7
  s_lshl_b32 s[sG], s[sG], 1                      ; this XCD group works on heads 2g and 2g+1
  s_lshr_b32 s[sJ], %1, 3
  s_lshr_b32 s[sW], s[sW], 6
  v_add_u32 v[vT7], 2048*(NIT-1), v[vT0]
  ; prefetch offset: (tid>>5)*4096 + ((tid>>4)&1)*2048 + ((tid>>3)&1)*128 + (tid&7)*16 + g*256
  v_lshrrev_b32 v[vT6], 5, %2
  v_lshlrev_b32 v[vT6], 12, v[vT6]
  v_bfe_u32 v[vT5], %2, 4, 1
  v_lshl_add_u32 v[vT6], v[vT5], 8, v[vT6]
  v_bfe_u32 v[vT5], %2, 3, 1
  v_lshl_add_u32 v[vT6], v[vT5], 7, v[vT6]
  v_and_b32 v[vT5], 7, %2
  v_lshl_add_u32 v[vT6], v[vT5], 4, v[vT6]
  s_lshl_b32 s[sT3], s[sG], 8
  v_add_u32 v[vV+28], s[sT3], v[vT6]
  ; DMA lane offsets
  v_bfe_u32 v[vT1], v[vLANE], 2, 3                ; prow
  v_lshrrev_b32 v[vT2], 5, v[vLANE]               ; hh / pcb
  v_and_b32 v[vT3], 3, v[vLANE]                   ; pc
  v_lshlrev_b32 v[vT4], 12, v[vT1]
  v_lshl_add_u32 v[vT4], v[vT2], 6, v[vT4]        ; prow*4096 + pcb*64
  v_lshl_add_u32 v[vOV], v[vT3], 4, v[vT4]
  v_bfe_u32 v[vT5], v[vLANE], 4, 1
  s_and_b32 s[sT4], s[sW], 1
  s_lshl_b32 s[sT4], s[sT4], 1
  v_or_b32 v[vT5], s[sT4], v[vT5]                 ; x
  v_xor_b32 v[vT5], v[vT5], v[vT3]
  v_lshl_add_u32 v[vOK], v[vT5], 4, v[vT4]
  s_waitcnt lgkmcnt(0)
  ; ======================================================================== S1: segment ends per 64-token chunk
  s_lshl_b32 s[sT2], s[sT], 2
  s_sub_u32 s[sT2], s[sT2], 8                     ; (T-2)*4
  v_min_u32 v[vT7], s[sT2], v[vT7]                ; last chunk: clamped
  .set i, 0
  .rept NIT
    .if i == NIT-1
      buffer_load_dwordx2 v[vX+2*i:vX+2*i+1], v[vT7], s[sS:sS+3], 0 offen
    .elseif i < 2
      buffer_load_dwordx2 v[vX+2*i:vX+2*i+1], v[vT0], s[sS:sS+3], 0 offen offset:2048*i
    .else
      s_movk_i32 s[sT1], 2048*i
      buffer_load_dwordx2 v[vX+2*i:vX+2*i+1], v[vT0], s[sS:sS+3], s[sT1] offen
    .endif
    .set i, i+1
  .endr
  s_sub_u32 s[sTM16], s[sT], 16
  s_lshl_b32 s[sTM16], s[sTM16], 12               ; (T-16)*4096
  ; V descriptor shifted by two tiles: V(u+2) is fetched with the source offset of K(u+4)
  s_sub_u32 s[sV2], s[sV], 2*TILEB
  s_subb_u32 s[sV2+1], s[sV+1], 0
  s_mov_b32 s[sV2+3], s[sV+3]
  ; remaining lane constants (in the shadow of the loads)
  v_add_u32 v[vOK2], 128, v[vOK]
  v_add_u32 v[vOV2], 128, v[vOV]
  v_and_b32 v[vT1], 31, v[vLANE]                  ; r32
  v_lshrrev_b32 v[vT3], 3, v[vT1]
  v_lshlrev_b32 v[vT3], 11, v[vT3]
  v_and_b32 v[vT4], 7, v[vT1]
  v_lshl_add_u32 v[vT3], v[vT4], 6, v[vT3]
  v_bfe_u32 v[vT4], v[vT1], 2, 2
  v_xor_b32 v[vT4], v[vT4], v[vT2]
  v_lshl_add_u32 v[vKB0], v[vT4], 4, v[vT3]
  v_xor_b32 v[vKB1], 32, v[vKB0]
  v_bfe_u32 v[vT3], v[vLANE], 2, 2
  v_lshl_add_u32 v[vT3], v[vT2], 2, v[vT3]
  v_lshlrev_b32 v[vT3], 6, v[vT3]
  v_bfe_u32 v[vT4], v[vLANE], 4, 1
  v_lshl_add_u32 v[vT3], v[vT4], 5, v[vT3]
  v_and_b32 v[vT4], 3, v[vLANE]
  v_lshl_add_u32 v[vVB], v[vT4], 3, v[vT3]
  v_add_u32 v[vVB], LV, v[vVB]
  v_lshlrev_b32 v[vHH4], 2, v[vT2]
  ; output staging: write address r32*128 + ((r32&7)^hh)*16 (chunk pair p: xor p*32)
  v_and_b32 v[vT3], 7, v[vT1]
  v_xor_b32 v[vT3], v[vT3], v[vT2]
  v_lshlrev_b32 v[vT3], 4, v[vT3]
  v_lshl_add_u32 v[vSW], v[vT1], 7, v[vT3]
  ; read address: row = lane>>3, chunk = (lane&7) ^ row ; rows + 8i -> + 1024 i
  v_lshrrev_b32 v[vT3], 3, v[vLANE]
  v_and_b32 v[vT4], 7, v[vLANE]
  v_xor_b32 v[vT5], v[vT4], v[vT3]
  v_lshlrev_b32 v[vT5], 4, v[vT5]
  v_lshl_add_u32 v[vSR], v[vT3], 7, v[vT5]
  ; global store lane offset: (lane>>3)*4096 + (lane&7)*16
  v_lshlrev_b32 v[vT4], 4, v[vT4]
  v_lshl_add_u32 v[vSO], v[vT3], 12, v[vT4]
  ; row-sum MFMA A operand: row (lane&15) 0 takes k-groups 0,2 ; row 1 takes k-groups 1,3
  v_and_b32 v[vT3], 15, v[vLANE]
  v_bfe_u32 v[vT4], v[vLANE], 4, 1
  v_cmp_eq_u32 vcc, v[vT3], v[vT4]
  v_mov_b32 v[vT5], 0x3f803f80
  s_mov_b32 s[sCE], 0x3e0293ee
  v_cndmask_b32 v[vONE], 0, v[vT5], vcc
  v_cndmask_b32 v[vONE+1], 0, v[vT5], vcc
  v_cndmask_b32 v[vONE+2], 0, v[vT5], vcc
  v_cndmask_b32 v[vONE+3], 0, v[vT5], vcc
  s_mov_b32 s[sTHR], 0x427af232
  s_lshl_b32 s[sWD], s[sW], 11                    ; w*2048
  s_mov_b64 s[sC:sC+1], 0
  v_mov_b32 v[vT4], 0
  v_mov_b32 v[vT5], 0
  v_lshlrev_b32 v[vT7], 7, v[vLANE]               ; LDS address of chunk (w + 8*lane): 16 B each
  s_lshl_b32 s[sT1], s[sW], 4
  s_add_u32 s[sT1], s[sT1], LSCR
  v_add_u32 v[vT7], s[sT1], v[vT7]
  PREFETCH 0, PF1
  .set i, 0
  .rept NIT
    s_waitcnt vmcnt(NIT-1-i+PF1)
    v_cmp_ne_u32 s[sA:sA+1], v[vX+2*i], v[vX+2*i+1]
    v_cmp_gt_i32 vcc, v[vX+2*i], v[vX+2*i+1]
    .if i == NIT-1
      v_cmp_gt_i32 s[sB:sB+1], s[sLASTC], %2       ; element < T-1
      s_and_b64 s[sA:sA+1], s[sA:sA+1], s[sB:sB+1]
      s_and_b64 vcc, vcc, s[sB:sB+1]
      v_cmp_eq_u32 s[sB:sB+1], s[sLASTC], %2       ; element == T-1
      s_or_b64 s[sA:sA+1], s[sA:sA+1], s[sB:sB+1]
    .endif
    s_or_b64 s[sC:sC+1], s[sC:sC+1], vcc
    s_nop 0
    v_writelane_b32 v[vT4], s[sA], i
    v_writelane_b32 v[vT5], s[sA+1], i
    .set i, i+1
  .endr
  s_or_b32 s[sT1], s[sC], s[sC+1]
  v_mov_b32 v[vT6], s[sT1]
  s_mov_b64 exec, (1<<NIT)-1
  ds_write_b96 v[vT7], v[vT4:vT6]
  s_mov_b64 exec, -1
  v_mov_b32 v[vT0], 0
  v_mov_b32 v[vT1], 0
  v_mov_b32 v[vT2], 0
  v_lshlrev_b32 v[vT3], 4, v[vLANE]
  v_add_u32 v[vT3], LSCR, v[vT3]
  v_lshlrev_b32 v[vT7], 6, v[vLANE]
  v_add_u32 v[vT7], 1, v[vT7]                     ; lane*64 + 1
  PREFETCH PF1, PF2
  s_waitcnt lgkmcnt(0)
  s_barrier
  ; ======================================================================== S2
  s_bfm_b64 exec, 8*NIT, 0
  ds_read_b96 v[vT0:vT2], v[vT3]
  s_mov_b64 exec, -1
  s_movk_i32 s[sT1], 128
  s_movk_i32 s[sT2], 256
  s_mov_b32 s[sR], 0
  s_waitcnt lgkmcnt(0)
  v_cmp_ne_u32 vcc, 0, v[vT2]
  v_bcnt_u32_b32 v[vT3], v[vT0], 0
  v_bcnt_u32_b32 v[vT3], v[vT1], v[vT3]
  s_cbranch_vccnz .Lfallback
  v_cmp_lt_u32 vcc, 1, v[vT3]
  v_ffbl_b32 v[vT4], v[vT0]
  v_ffbl_b32 v[vT5], v[vT1]
  s_cbranch_vccnz .Lfallback
  v_cmp_ne_u32 s[sA:sA+1], 0, v[vT3]
  v_add_u32 v[vT5], 32, v[vT5]
  v_min_u32 v[vT4], v[vT4], v[vT5]
  v_add_u32 v[vT4], v[vT4], v[vT7]
  v_cndmask_b32 v[vT4], 0, v[vT4], s[sA:sA+1]     ; end (0 if none)
  s_nop 1
  v_mov_b32_dpp v[vSTART], v[vT4] wave_shr:1 row_mask:0xf bank_mask:0xf bound_ctrl:0
  s_nop 1
  v_max_u32_dpp v[vSTART], v[vSTART], v[vSTART] row_shr:1 row_mask:0xf bank_mask:0xf bound_ctrl:0
  s_nop 1
  v_max_u32_dpp v[vSTART], v[vSTART], v[vSTART] row_shr:2 row_mask:0xf bank_mask:0xf bound_ctrl:0
  s_nop 1
  v_max_u32_dpp v[vSTART], v[vSTART], v[vSTART] row_shr:4 row_mask:0xf bank_mask:0xf bound_ctrl:0
  s_nop 1
  v_max_u32_dpp v[vSTART], v[vSTART], v[vSTART] row_shr:8 row_mask:0xf bank_mask:0xf bound_ctrl:0
  s_nop 1
  v_max_u32_dpp v[vSTART], v[vSTART], v[vSTART] row_bcast:15 row_mask:0xa bank_mask:0xf
  s_nop 1
  v_max_u32_dpp v[vSTART], v[vSTART], v[vSTART] row_bcast:31 row_mask:0xc bank_mask:0xf
  s_nop 0
  v_sub_u32 v[vLEN], v[vT4], v[vSTART]
  v_cndmask_b32 v[vLEN], 0, v[vLEN], s[sA:sA+1]   ; len (0 if none)
  v_add_u32 v[vT5], 127, v[vLEN]
  v_lshrrev_b32 v[vT5], 7, v[vT5]                 ; nq (mode A)
  v_cmp_lt_u32 vcc, s[sT1], v[vLEN]               ; len > 128
  v_cmp_ge_u32 s[sB:sB+1], s[sT2], v[vLEN]        ; len <= 256
  s_and_b64 s[sB:sB+1], s[sB:sB+1], vcc           ; mode B
  v_lshlrev_b32 v[vT5], 1, v[vT5]
  s_nop 0
  v_cndmask_b32 v[vT5], v[vT5], 2, s[sB:sB+1]     ; items (2 heads)
  v_lshlrev_b32 v[vT6], 16, v[vT5]
  v_cndmask_b32 v[vINCL], v[vT6], v[vT5], vcc     ; small -> high half
  v_mov_b32 v[vEXCL], v[vINCL]
  s_nop 0
  v_add_u32_dpp v[vINCL], v[vINCL], v[vINCL] row_shr:1 row_mask:0xf bank_mask:0xf bound_ctrl:0
  s_nop 1
  v_add_u32_dpp v[vINCL], v[vINCL], v[vINCL] row_shr:2 row_mask:0xf bank_mask:0xf bound_ctrl:0
  s_nop 1
  v_add_u32_dpp v[vINCL], v[vINCL], v[vINCL] row_shr:4 row_mask:0xf bank_mask:0xf bound_ctrl:0
  s_nop 1
  v_add_u32_dpp v[vINCL], v[vINCL], v[vINCL] row_shr:8 row_mask:0xf bank_mask:0xf bound_ctrl:0
  s_nop 1
  v_add_u32_dpp v[vINCL], v[vINCL], v[vINCL] row_bcast:15 row_mask:0xa bank_mask:0xf
  s_nop 1
  v_add_u32_dpp v[vINCL], v[vINCL], v[vINCL] row_bcast:31 row_mask:0xc bank_mask:0xf
  s_nop 0
  v_sub_u32 v[vEXCL], v[vINCL], v[vEXCL]
  v_readlane_b32 s[sT1], v[vINCL], 63
  s_and_b32 s[sNBIG], s[sT1], 0xffff
  s_lshr_b32 s[sTOT], s[sT1], 16
  s_add_u32 s[sTOT], s[sTOT], s[sNBIG]
  s_mov_b32 s[sSAFE], 0

.Litem:
  ; idx = r*32 + (r odd ? 31-j : j)
  s_lshl_b32 s[sT1], s[sR], 5
  s_sub_u32 s[sT2], 31, s[sJ]
  s_bitcmp1_b32 s[sR], 0
  s_cselect_b32 s[sT2], s[sT2], s[sJ]
  s_add_u32 s[sT1], s[sT1], s[sT2]                ; idx
  s_cmp_ge_u32 s[sT1], s[sTOT]
  s_cbranch_scc1 .Ldone
  s_sub_u32 s[sT2], s[sT1], s[sNBIG]
  s_cmp_lt_u32 s[sT1], s[sNBIG]
  s_cselect_b32 s[sT1], s[sT1], s[sT2]            ; index inside its class
  s_cselect_b32 s[sT2], 0, 16                     ; field shift
  v_bfe_u32 v[vT0], v[vINCL], s[sT2], 16
  v_bfe_u32 v[vT1], v[vEXCL], s[sT2], 16
  v_cmp_lt_u32 s[sA:sA+1], s[sT1], v[vT0]
  v_cmp_ge_u32 vcc, s[sT1], v[vT1]
  s_and_b64 s[sA:sA+1], s[sA:sA+1], vcc
  s_ff1_i32_b64 s[sT2], s[sA:sA+1]
  v_readlane_b32 s[sK0], v[vSTART], s[sT2]
  v_readlane_b32 s[sLEN], v[vLEN], s[sT2]
  v_readlane_b32 s[sT3], v[vT1], s[sT2]
  s_sub_u32 s[sT1], s[sT1], s[sT3]                ; local item
  s_and_b32 s[sT2], s[sT1], 1
  s_add_u32 s[sT2], s[sT2], s[sG]
  s_lshl_b32 s[sHOFF], s[sT2], 8                  ; head*256 bytes
  s_lshr_b32 s[sT1], s[sT1], 1
  s_lshl_b32 s[sT1], s[sT1], 7
  s_add_u32 s[sQ0], s[sK0], s[sT1]
  s_lshl_b32 s[sT1], s[sW], 3
  s_add_u32 s[sT2], s[sQ0], s[sT1]
  s_lshl_b32 s[sT2], s[sT2], 12
  s_add_u32 s[sT2], s[sT2], s[sHOFF]              ; (q0 + 8w)*4096 + hoff
  ; ---- Q DMA
  s_add_u32 m0, s[sWD], LX
  s_add_u32 s[sT3], s[sT2], 64*4096
  buffer_load_dwordx4 v[vOK], s[sQ:sQ+3], s[sT2] offen lds
  s_add_u32 m0, s[sWD], LX+1024
  s_add_u32 s[sT4], s[sT2], 128*4096
  buffer_load_dwordx4 v[vOK2], s[sQ:sQ+3], s[sT2] offen lds
  s_add_u32 m0, s[sWD], LX+16384
  s_add_u32 s[sT1], s[sK0], s[sT1]
  buffer_load_dwordx4 v[vOK], s[sQ:sQ+3], s[sT3] offen lds
  s_add_u32 m0, s[sWD], LX+16384+1024
  s_lshl_b32 s[sT1], s[sT1], 12
  buffer_load_dwordx4 v[vOK2], s[sQ:sQ+3], s[sT3] offen lds
  s_add_u32 s[sKSO], s[sT1], s[sHOFF]             ; (k0 + 8w)*4096 + hoff
  s_mov_b32 s[sVSO], s[sKSO]
  s_add_u32 s[sQEND], s[sK0], s[sLEN]
  s_add_u32 s[sNK], s[sLEN], 63
  s_lshr_b32 s[sNK], s[sNK], 6
  s_lshl_b32 s[sO+2], s[sQEND], 12                ; the output, K and V descriptors end at the segment end
  s_mov_b32 s[sK+2], s[sO+2]
  s_mov_b32 s[sV+2], s[sO+2]
  s_add_u32 s[sV2+2], s[sO+2], 2*TILEB
  s_mov_b32 s[sKDST], s[sWD]
  s_mov_b32 s[sVDST], s[sWD]
  s_sub_u32 s[sT1], s[sLEN], 129
  s_cmp_lt_u32 s[sT1], 128
  s_cselect_b32 s[sMB], 1, 0                      ; mode B: 128 < len <= 256
  s_cbranch_scc0 .Lq_done
  s_add_u32 m0, s[sWD], LQ2
  s_add_u32 s[sT3], s[sT4], 64*4096
  buffer_load_dwordx4 v[vOK], s[sQ:sQ+3], s[sT4] offen lds
  s_add_u32 m0, s[sWD], LQ2+1024
  s_nop 0
  buffer_load_dwordx4 v[vOK2], s[sQ:sQ+3], s[sT4] offen lds
  s_add_u32 m0, s[sWD], LQ3
  s_nop 0
  buffer_load_dwordx4 v[vOK], s[sQ:sQ+3], s[sT3] offen lds
  s_add_u32 m0, s[sWD], LQ3+1024
  s_nop 0
  buffer_load_dwordx4 v[vOK2], s[sQ:sQ+3], s[sT3] offen lds
.Lq_done:
  DK1
  DK2
  ; ---- per-wave geometry
  s_and_b32 s[sT1], s[sW], 3
  s_lshr_b32 s[sT2], s[sW], 2
  s_cmp_eq_u32 s[sMB], 1
  s_cselect_b32 s[sQB], s[sW], s[sT1]             ; q block
  s_cselect_b32 s[sKH], 0, s[sT2]                 ; key half (mode A)
  s_movk_i32 s[sKSTEP], 0x4000
  s_cselect_b32 s[sKSTEP], 8192, s[sKSTEP]
  s_add_u32 s[sT1], s[sLEN], 31
  s_lshr_b32 s[sT1], s[sT1], 5                    ; nst = ceil(len/32)
  s_add_u32 s[sT2], s[sT1], 1
  s_sub_u32 s[sT2], s[sT2], s[sKH]
  s_lshr_b32 s[sT2], s[sT2], 1
  s_cmp_eq_u32 s[sMB], 1
  s_cselect_b32 s[sNS], s[sT1], s[sT2]            ; steps of this wave
  s_cselect_b32 s[sNU], s[sT1], s[sNK]            ; step slots
  s_and_b32 s[sLIM], s[sLEN], 31
  s_sub_u32 s[sT2], s[sT1], 1
  s_and_b32 s[sT2], s[sT2], 1
  s_cmp_eq_u32 s[sT2], s[sKH]
  s_cselect_b32 s[sT3], 1, s[sMB]                 ; owner of the last sub-tile
  s_cmp_lg_u32 s[sLIM], 0
  s_cselect_b32 s[sT3], s[sT3], 0
  s_sub_u32 s[sMSTEP], s[sNS], 1
  s_cmp_eq_u32 s[sT3], 1
  s_cselect_b32 s[sMSTEP], s[sMSTEP], -1
  s_lshl_b32 s[sT1], s[sQB], 5
  s_add_u32 s[sQR], s[sT1], s[sQ0]                ; first q row of this wave
  s_cmp_lt_u32 s[sQR], s[sQEND]
  s_cselect_b32 s[sNS], s[sNS], 0
  ; slots with DMA: tile iteration t <= nk-3
  s_sub_i32 s[sND], s[sNK], 2
  s_max_i32 s[sND], s[sND], 0
  s_lshl_b32 s[sND], s[sND], s[sMB]
  s_sub_u32 s[sNF], s[sNS], 1                     ; FULL slots of this wave
  s_sub_u32 s[sCNT], 0, s[sNF]
  s_mov_b32 s[sMK], s[sWD]
  s_add_u32 s[sMV], s[sWD], LV+0x8000
  ; Q read base
  s_lshl_b32 s[sT1], s[sQB], 13
  s_add_u32 s[sT2], s[sT1], LX                    ; qb < 4
  s_add_u32 s[sT3], s[sT1], LQ2-4*8192            ; qb 4,5
  s_add_u32 s[sT4], s[sT1], LQ3-6*8192            ; qb 6,7
  s_cmp_lt_u32 s[sQB], 6
  s_cselect_b32 s[sT3], s[sT3], s[sT4]
  s_cmp_lt_u32 s[sQB], 4
  s_cselect_b32 s[sT2], s[sT2], s[sT3]
  v_add_u32 v[vT0], s[sT2], v[vKB0]
  v_add_u32 v[vT1], s[sT2], v[vKB1]
  s_lshl_b32 s[sKOFF], s[sKH], 13
  s_mov_b32 s[sVOFF], s[sKOFF]
  v_add_u32 v[vKA0], s[sKOFF], v[vKB0]
  v_add_u32 v[vKA1], s[sKOFF], v[vKB1]
  s_lshl_b32 s[sT1], s[sKH], 10
  v_add_u32 v[vVBL], s[sT1], v[vVB]
  v_subrev_u32 v[vVBH], s[sT1], v[vVB]
  VADDR
  ; epilogue addresses
  v_lshlrev_b32 v[vT2], 4, v[vLANE]
  s_lshl_b32 s[sT1], s[sW], 13
  s_xor_b32 s[sT2], s[sT1], 4*8192
  v_add_u32 v[vXP], s[sT2], v[vT2]                ; partner's exchange area
  v_add_u32 v[vXO], s[sT1], v[vT2]                ; own exchange area
  v_and_b32 v[vT2], 31, v[vLANE]
  v_lshlrev_b32 v[vT2], 3, v[vT2]
  v_add_u32 v[vT2], LX, v[vT2]
  s_lshl_b32 s[sT1], s[sW], 8
  s_xor_b32 s[sT2], s[sT1], 4*256
  v_add_u32 v[vSTP], s[sT2], v[vT2]
  v_add_u32 v[vSTA], s[sT1], v[vT2]
  s_lshl_b32 s[sT1], s[sW], 12                    ; output stage: mode A in the V ring (4 KiB per wave), mode B in the K ring (8 KiB)
  s_add_u32 s[sT2], s[sT1], LV
  s_lshl_b32 s[sT3], s[sW], 13
  s_cmp_eq_u32 s[sMB], 1
  s_cselect_b32 s[sT1], s[sT3], s[sT2]
  v_add_u32 v[vSGW], s[sT1], v[vSW]
  v_add_u32 v[vSGR], s[sT1], v[vSR]
  v_mov_b32 v[vL0], 0
  v_mov_b32 v[vL1], 0
  v_mov_b32 v[vLS], 0
  v_mov_b32 v[vLS+1], 0
  v_mov_b32 v[vLS+2], 0
  v_mov_b32 v[vLS+3], 0
  s_nop 1
  .set i, 0
  .rept 4
    v_mfma_f32_32x32x16_bf16 v[vO+16*i:vO+16*i+15], v[vLS:vLS+3], v[vLS:vLS+3], 0     ; O = 0
    .set i, i+1
  .endr
  v_mov_b32 v[vM], 0xf149f2ca
  s_mov_b32 s[sPEND], 0
  s_mov_b32 s[sU], 0
  ; mid group: mode A: K1, K2, V0 ; mode B: K1, V0
  s_waitcnt vmcnt(2)                                ; own Q pieces landed
  DK1
  DK2
  s_cmp_eq_u32 s[sMB], 1
  s_cbranch_scc1 .Lmid_b
  DK1
  DK2
  DV1
  DV2
  s_waitcnt vmcnt(6)
  s_branch .Lmid_d
.Lmid_b:
  DV1
  DV2
  s_waitcnt vmcnt(4)
.Lmid_d:
  s_barrier                                         ; barrier P: Q, K0 landed
  ; ---- prologue compute: step 0
  s_cmp_eq_u32 s[sNS], 0
  s_cbranch_scc1 .Lpro_none
  .set i, 0
  .rept 8
    KREADO i, 0
    .if (i & 1)
      ds_read_b128 v[vQ+4*i:vQ+4*i+3], v[vT1] offset:512*(i>>1)
    .else
      ds_read_b128 v[vQ+4*i:vQ+4*i+3], v[vT0] offset:512*(i>>1)
    .endif
    .set i, i+1
  .endr
  .macro QK0 rd                                     ; rd: read the next step's K fragments behind each MFMA (mode B: same tile)
    .set i, 0
    .rept 8
      .if \rd
        .if i == 0
          s_waitcnt lgkmcnt(14)
        .elseif i == 1
          s_waitcnt lgkmcnt(13)
        .elseif i == 2
          s_waitcnt lgkmcnt(12)
        .elseif i == 3
          s_waitcnt lgkmcnt(11)
        .elseif i == 4
          s_waitcnt lgkmcnt(10)
        .elseif i == 5
          s_waitcnt lgkmcnt(9)
        .elseif i == 6
          s_waitcnt lgkmcnt(8)
        .else
          s_waitcnt lgkmcnt(7)
        .endif
      .else
        .if i == 0
          s_waitcnt lgkmcnt(14)
        .elseif i == 1
          s_waitcnt lgkmcnt(12)
        .elseif i == 2
          s_waitcnt lgkmcnt(10)
        .elseif i == 3
          s_waitcnt lgkmcnt(8)
        .elseif i == 4
          s_waitcnt lgkmcnt(6)
        .elseif i == 5
          s_waitcnt lgkmcnt(4)
        .elseif i == 6
          s_waitcnt lgkmcnt(2)
        .else
          s_waitcnt lgkmcnt(0)
        .endif
      .endif
      QKM vSA, i
      .if \rd
        KREADO i, 0
      .endif
      .if i == 1
        DK1
      .elseif i == 3
        DK2
      .elseif i == 5
        DV1
      .elseif i == 7
        DV2
      .endif
      .set i, i+1
    .endr
  .endm
  s_cmp_eq_u32 s[sMB], 1
  s_cbranch_scc1 .Lqk0_b
  QK0 0
  s_branch .Lqk0_d
.Lqk0_b:
  KADV
  QK0 1
  KADV
.Lqk0_d:
  s_nop 7
  s_cmp_lg_u32 s[sMSTEP], 0
  s_cbranch_scc1 .Lnomask0
  s_nop 3
  MASK vSA
.Lnomask0:
  s_nop 3
  ROWMAX1 vSA
  s_nop 1
  ROWMAX2
  v_max_f32 v[vM], v[vM], v[vT2]
  v_mul_f32 v[vNMC], 0xbe0293ee, v[vM]
  s_cmp_eq_u32 s[sSAFE], 1
  s_cbranch_scc1 .Lpro_safe
  .set r, 0
  .rept 6
    EXPE vSA, r
    .set r, r+1
  .endr
  s_cmp_eq_u32 s[sMB], 1
  s_cbranch_scc1 .Lpro_b
  ; ---- mode A: K1 landed -> its fragments
  s_waitcnt vmcnt(8)
  s_barrier
  .set i, 0
  .rept 8
    KREADO i, 16384
    .set i, i+1
  .endr
  .set r, 6
  .rept 5
    EXPE vSA, r
    .set r, r+1
  .endr
  v_xor_b32 v[vKA0], 0x8000, v[vKA0]                ; K(2) sits in ring slot 2
  v_xor_b32 v[vKA1], 0x8000, v[vKA1]
  s_cmp_eq_u32 s[sNF], 0
  s_cbranch_scc1 .LlA0
  ; ======================================================================== mode A loop
.LA0:
  FULLF vSB, vSA, 0, 0, 0
  s_cbranch_scc1 .LlA1
  FULLF vSA, vSB, 0, 1, 0
  s_cbranch_scc0 .LA0
.LlA0:
  LASTF vSA, 0, 0, 0
.LlA1:
  LASTF vSB, 0, 1, 0
  ; ======================================================================== epilogue (mode A)
.Lepi_sent:
  v_mov_b32 v[vT7], 0x53800000
  s_lshl_b32 s[sT3], s[sQR], 12
  s_add_u32 s[sT3], s[sT3], s[sHOFF]
  ROWSUM
  v_mov_b32 v[vT4], v[vM]
  v_mov_b32 v[vT5], v[vL0]
  s_lshl_b32 s[sT1], s[sKH], 7
  s_add_u32 s[sT3], s[sT3], s[sT1]
  ds_write_b64 v[vSTA], v[vT4:vT5]
  s_waitcnt lgkmcnt(0)
  s_barrier
  MERGE 0, 32
.Lnext:
  ; guard of the FAST path: its reference is the true max of the wave's first sub-tile (never above the row's true max, so nothing underflows);
  ; a row sum that is not below 2^40 (later keys far above the reference, inf, NaN) -> redo this item with the online (true) max
  v_cmp_nlt_f32 vcc, v[vL0], v[vT7]
  s_lshl_b32 s[sT1], s[sW], 2
  s_add_u32 s[sT1], s[sT1], LX+8192
  s_cmp_lg_u64 vcc, 0
  s_cselect_b32 s[sT2], 1, 0
  v_mov_b32 v[vT0], s[sT1]
  v_mov_b32 v[vT1], s[sT2]
  v_and_b32 v[vT2], 7, v[vLANE]
  v_lshlrev_b32 v[vT2], 2, v[vT2]
  v_add_u32 v[vT2], LX+8192, v[vT2]
  ds_write_b32 v[vT0], v[vT1]
  s_waitcnt lgkmcnt(0)
  s_barrier
  ds_read_b32 v[vT1], v[vT2]
  s_waitcnt lgkmcnt(0)
  v_cmp_ne_u32 vcc, 0, v[vT1]
  s_cmp_eq_u32 s[sSAFE], 1
  s_cbranch_scc1 .Lsafe_done
  s_cbranch_vccz .Lsafe_done
  s_mov_b32 s[sSAFE], 1
  s_barrier
  s_branch .Litem
.Lsafe_done:
  s_mov_b32 s[sSAFE], 0
  s_add_u32 s[sR], s[sR], 1
  s_lshl_b32 s[sT1], s[sR], 5
  s_cmp_ge_u32 s[sT1], s[sTOT]
  s_cbranch_scc1 .Ldone
  s_barrier
  s_branch .Litem
.Ldone:
  s_endpgm
  ; ======================================================================== cold paths
.Lepi:                                              ; O complete, nothing sent yet
  s_cmp_eq_u32 s[sMB], 1
  s_cbranch_scc1 .LepiB
  s_nop 7
  SENDB 2
  SENDB 3
  s_branch .Lepi_sent
.LepiB:
  v_mov_b32 v[vT7], 0x53800000
  s_lshl_b32 s[sT3], s[sQR], 12
  s_add_u32 s[sT3], s[sT3], s[sHOFF]
  ROWSUM
  s_nop 0
  v_rcp_f32 v[vFA], v[vL0]
  s_waitcnt vmcnt(0)
  s_barrier
  .set i, 0
  .rept 32
    v_pk_mul_f32 v[vO+2*i:vO+2*i+1], v[vFA:vFA+1], v[vO+2*i:vO+2*i+1] op_sel_hi:[0,1]
    .set i, i+1
  .endr
  .set q, 0
  .rept 8
    v_cvt_pk_bf16_f32 v[vQ+4*q+0], v[vO+8*q+0], v[vO+8*q+1]
    v_cvt_pk_bf16_f32 v[vQ+4*q+2], v[vO+8*q+4], v[vO+8*q+5]
    v_cvt_pk_bf16_f32 v[vQ+4*q+1], v[vO+8*q+2], v[vO+8*q+3]
    v_cvt_pk_bf16_f32 v[vQ+4*q+3], v[vO+8*q+6], v[vO+8*q+7]
    v_xor_b32 v[vT0+(q&3)], (q&3)*32, v[vSGW]
    v_permlane32_swap_b32 v[vQ+4*q+0], v[vQ+4*q+2]
    s_nop 0
    v_permlane32_swap_b32 v[vQ+4*q+1], v[vQ+4*q+3]
    s_nop 0
    ds_write_b128 v[vT0+(q&3)], v[vQ+4*q:vQ+4*q+3] offset:4096*(q>>2)
    .set q, q+1
  .endr
  s_add_u32 s[sT4], s[sT3], 8*4096
  s_add_u32 s[sT1], s[sT3], 16*4096
  s_add_u32 s[sT2], s[sT3], 24*4096
  s_waitcnt lgkmcnt(0)
  .set q, 0
  .rept 8
    ds_read_b128 v[vQ+4*q:vQ+4*q+3], v[vSGR] offset:4096*(q>>2)+1024*(q&3)
    .set q, q+1
  .endr
  s_waitcnt lgkmcnt(7)
  buffer_store_dwordx4 v[vQ+0:vQ+3], v[vSO], s[sO:sO+3], s[sT3] offen sc1
  s_waitcnt lgkmcnt(6)
  buffer_store_dwordx4 v[vQ+4:vQ+7], v[vSO], s[sO:sO+3], s[sT4] offen sc1
  s_waitcnt lgkmcnt(5)
  buffer_store_dwordx4 v[vQ+8:vQ+11], v[vSO], s[sO:sO+3], s[sT1] offen sc1
  s_waitcnt lgkmcnt(4)
  buffer_store_dwordx4 v[vQ+12:vQ+15], v[vSO], s[sO:sO+3], s[sT2] offen sc1
  s_waitcnt lgkmcnt(3)
  buffer_store_dwordx4 v[vQ+16:vQ+19], v[vSO], s[sO:sO+3], s[sT3] offen offset:128 sc1
  s_waitcnt lgkmcnt(2)
  buffer_store_dwordx4 v[vQ+20:vQ+23], v[vSO], s[sO:sO+3], s[sT4] offen offset:128 sc1
  s_waitcnt lgkmcnt(1)
  buffer_store_dwordx4 v[vQ+24:vQ+27], v[vSO], s[sO:sO+3], s[sT1] offen offset:128 sc1
  s_waitcnt lgkmcnt(0)
  buffer_store_dwordx4 v[vQ+28:vQ+31], v[vSO], s[sO:sO+3], s[sT2] offen offset:128 sc1
  s_branch .Lnext
  ; ======================================================================== mode B loop (two steps per tile)
.Lpro_b:
  .set r, 6
  .rept 5
    EXPE vSA, r
    .set r, r+1
  .endr
  s_cmp_eq_u32 s[sNF], 0
  s_cbranch_scc1 .LlB0
.LB0:
  FULLF vSB, vSA, 1, 0, 0
  s_cbranch_scc1 .LlB1
  FULLF vSA, vSB, 1, 1, 2
  s_cbranch_scc0 .LB0
.LlB0:
  LASTF vSA, 1, 0, 0
.LlB1:
  LASTF vSB, 1, 1, 2
  ; ======================================================================== SAFE loop
.Lpro_safe:
  s_cmp_eq_u32 s[sMB], 1
  s_cbranch_scc1 .Lps_b
  KADV
  s_waitcnt vmcnt(8)
  s_barrier
  .set i, 0
  .rept 8
    KREADO i, 0
    .set i, i+1
  .endr
  KADV
.Lps_b:
  .set r, 0
  .rept 16
    EXPE vSA, r
    .set r, r+1
  .endr
  s_cmp_eq_u32 s[sNF], 0
  s_cbranch_scc1 .LlastS0
.LloopS:
  FULLS vSB, vSA, 0
  s_cbranch_scc1 .LlastS1
  FULLS vSA, vSB, 1
  s_cbranch_scc0 .LloopS
.LlastS0:
  LASTS vSA, 0
.LlastS1:
  LASTS vSB, 1

  ; ---- slots in which this wave has nothing to compute
.Lpro_none:
  DALL
  s_cmp_eq_u32 s[sMB], 1
  s_cbranch_scc1 .Lnone
  s_waitcnt vmcnt(8)
  s_barrier
.Lnone:                                             ; wave without steps: keep loading its share of the tiles
  s_cmp_ge_u32 s[sU], s[sNU]
  s_cbranch_scc1 .Lepi
  s_bitcmp1_b32 s[sU], 0
  s_cselect_b32 s[sT1], s[sMB], 0
  s_cmp_eq_u32 s[sT1], 1
  s_cbranch_scc1 .Lnone_skip
  s_cmp_lt_u32 s[sU], s[sND]
  s_cbranch_scc0 .Lnone_fin
  s_waitcnt vmcnt(4) lgkmcnt(0)
  s_barrier
  DALL
  s_branch .Lnone_skip
.Lnone_fin:
  s_waitcnt vmcnt(0) lgkmcnt(0)
  s_barrier
.Lnone_skip:
  s_add_u32 s[sU], s[sU], 1
  s_branch .Lnone
.Lnone_tail:                                        ; after the wave's last step: only barriers remain
  s_cmp_ge_u32 s[sU], s[sNU]
  s_cbranch_scc1 .Lepi
  s_bitcmp1_b32 s[sU], 0
  s_cselect_b32 s[sT1], s[sMB], 0
  s_cmp_eq_u32 s[sT1], 1
  s_cbranch_scc1 .Lnt_skip
  s_barrier
.Lnt_skip:
  s_add_u32 s[sU], s[sU], 1
  s_branch .Lnone_tail

.Lfallback:
  s_waitcnt vmcnt(0) lgkmcnt(0)
  .purgem DK1
  .purgem DK2
  .purgem DV1
  .purgem DV2
  .purgem DALL
  .purgem BARX
  .purgem DMAP
  .purgem KREADO
  .purgem KADV
  .purgem VADDR
  .purgem QKM
  .purgem VREADO
  .purgem PVJ
  .purgem PVM
  .purgem LSUM
  .purgem FMAE
  .purgem XPE
  .purgem EXPE
  .purgem CVTE
  .purgem SUMPK
  .purgem MASK
  .purgem ROWMAX1
  .purgem ROWMAX2
  .purgem RESCALE
  .purgem SENDB
  .purgem LASTV
  .purgem FDMA
  .purgem FDMB
  .purgem FULLF
  .purgem LASTF
  .purgem FULLS
  .purgem LASTS
  .purgem OUTHALF
  .purgem MERGE
  .purgem ROWSUM
  .purgem PREFETCH
  .purgem QK0
  )" :: "s"(__builtin_amdgcn_kernarg_segment_ptr()), "s"(blockIdx.x), "v"(threadIdx.x), "i"(NIT)
     : "memory", "vcc", "m0",
       "s8","s9","s10","s11","s12","s13","s14","s15","s16","s17","s18","s19","s20","s21","s22","s23","s24","s25","s26","s27","s28","s29","s30","s31",
       "s33","s34","s35","s36","s37","s38","s39","s40","s41","s42","s43","s44","s45","s46","s47","s48","s49","s50","s51","s52","s53","s54","s55",
       "s56","s57","s58","s59","s60","s61","s62","s63","s64","s65","s66","s67","s68","s69","s70","s71","s72","s73","s74","s75","s76","s77","s78","s79",
       "s80","s81","s82","s83","s84","s85","s86","s87","s88","s89","s90","s91","s92","s93","s94","s95","s96","s97","s98","s99",
       "v1","v2","v3","v4","v5","v6","v7","v8","v9","v10","v11","v12","v13","v14","v15","v16","v17","v18","v19","v20","v21","v22","v23","v24","v25","v26","v27","v28","v29","v30","v31",
       "v32","v33","v34","v35","v36","v37","v38","v39","v40","v41","v42","v43","v44","v45","v46","v47","v48","v49","v50","v51","v52","v53","v54","v55","v56","v57","v58","v59","v60","v61","v62","v63",
       "v64","v65","v66","v67","v68","v69","v70","v71","v72","v73","v74","v75","v76","v77","v78","v79","v80","v81","v82","v83","v84","v85","v86","v87","v88","v89","v90","v91","v92","v93","v94","v95",
       "v96","v97","v98","v99","v100","v101","v102","v103","v104","v105","v106","v107","v108","v109","v110","v111","v112","v113","v114","v115","v116","v117","v118","v119","v120","v121","v122","v123","v124","v125","v126","v127",
       "v128","v129","v130","v131","v132","v133","v134","v135","v136","v137","v138","v139","v140","v141","v142","v143","v144","v145","v146","v147","v148","v149","v150","v151","v152","v153","v154","v155","v156","v157","v158","v159",
       "v160","v161","v162","v163","v164","v165","v166","v167","v168","v169","v170","v171","v172","v173","v174","v175","v176","v177","v178","v179","v180","v181","v182","v183","v184","v185","v186","v187","v188","v189","v190","v191",
       "v192","v193","v194","v195","v196","v197","v198","v199","v200","v201","v202","v203","v204","v205","v206","v207","v208","v209","v210","v211","v212","v213","v214","v215","v216","v217","v218","v219","v220","v221","v222","v223",
       "v224","v225","v226","v227","v228","v229","v230","v231","v232","v233","v234","v235","v236","v237","v238","v239","v240","v241","v242","v243","v244","v245","v246","v247","v248","v249","v250","v251","v252","v253","v254","v255");
  generic_path((const unsigned short*)Q, (const unsigned short*)K, (const unsigned short*)V, (const int*)seg, (unsigned short*)O, T, lds);
}

__global__ __launch_bounds__(512, 2)
void ragged_attn_generic(const void* Q, const void* K, const void* V, const void* seg, void* O, int T, int pad) {
  __shared__ __attribute__((aligned(16))) char lds[LDS_BYTES];
  generic_path((const unsigned short*)Q, (const unsigned short*)K, (const unsigned short*)V, (const int*)seg, (unsigned short*)O, T, lds);
}

extern "C" void kernel_launch(void* const* d_in, const int* in_sizes, int n_in, void* d_out, int out_size, void* d_ws, size_t ws_size,
                              hipStream_t stream) {
  const int T = in_sizes[3];
  const unsigned nb = (unsigned)T * ROWB, F = 0x00020000u;
  if (T > 2048 && T <= 2560) {
    ragged_attn_kernel<5><<<256, 512, 0, stream>>>(d_in[0], nb, F, d_in[1], nb, F, d_in[2], nb, F, d_in[3], (unsigned)T * 4u, F, d_out, nb, F,
                                                   T, T - 1 - 512 * 4, ((T + 31) / 32) * (int)ROWB, 0);
  } else {
    ragged_attn_generic<<<256, 512, 0, stream>>>(d_in[0], d_in[1], d_in[2], d_in[3], d_out, T, 0);
  }
}
```
